```python
import torch

def _softmax(x, axis=-1):
    return torch.softmax(x, dim=axis)
def _gelu(x):
    return torch.nn.functional.gelu(x, approximate="tanh")
def _silu(x):
    return torch.nn.functional.silu(x)
def _sigmoid(x):
    return torch.sigmoid(x)
def _layernorm(x, axis=-1, eps=1e-5):
    m = x.mean(dim=axis, keepdim=True)
    v = x.var(dim=axis, keepdim=True, unbiased=False)
    return (x - m) / torch.sqrt(v + eps)
def _scatter_add(tgt, idx, src, axis=0):
    return tgt.clone().index_add_(axis, idx, src)
def _segment_sum(src, seg, n):
    out = torch.zeros((n,) + src.shape[1:], dtype=src.dtype)
    return out.index_add_(0, seg, src)
def _pad(x, pw):
    flat = tuple(v for pair in reversed(pw) for v in pair)
    return torch.nn.functional.pad(x, flat)

kernel_name = 'standalone__gqa_block_cached__sampling'

def setup_inputs():

    _w = (lambda a: (lambda t: t.to(torch.bfloat16) if t.is_floating_point() else t)(torch.as_tensor(a)))
    B, Sq, D = 8, 1, 512
    Hq, Hkv, Sk, Dh = 32, 8, 8192, 128
    assert Hq % Hkv == 0, (Hq, Hkv)
    return {
        "x": _w((lambda *s: torch.randn(*s, dtype=torch.bfloat16) * 0.8)(B, Sq, D)),
        "k_cache": _w((lambda *s: torch.randn(*s, dtype=torch.bfloat16) * 0.8)(B, Hkv, Sk, Dh)),
        "v_cache": _w((lambda *s: torch.randn(*s, dtype=torch.bfloat16) * 0.05)(B, Hkv, Sk, Dh)),
        "wq": _w((lambda *s: torch.randn(*s, dtype=torch.bfloat16) * 0.8)(D, Hq * Dh) * (D ** -0.5)),
        "wo": _w((lambda *s: torch.randn(*s, dtype=torch.bfloat16) * 0.05)(Hq * Dh, D) * ((Hq * Dh) ** -0.5)),
    }

def reference(x, k_cache, v_cache, wq, wo):
    B, Sq, D = x.shape
    Hq, Dh = 32, 128
    Hkv = k_cache.shape[1]
    rep = Hq // Hkv
    q = (lambda x, *d: x.permute(*d))((x @ wq).reshape(B, Sq, Hq, Dh), 0, 2, 1, 3)
    k = (lambda x, n, axis: torch.repeat_interleave(x, n, dim=axis))(k_cache, rep, axis=1)
    v = (lambda x, n, axis: torch.repeat_interleave(x, n, dim=axis))(v_cache, rep, axis=1)
    scores = torch.einsum("bhqd,bhkd->bhqk", q, k) * (Dh ** -0.5)
    p = _softmax(scores, axis=-1)
    o = torch.einsum("bhqk,bhkd->bhqd", p, v)
    return (lambda x, *d: x.permute(*d))(o, 0, 2, 1, 3).reshape(B, Sq, Hq * Dh) @ wo
```

```cpp
#include <hip/hip_runtime.h>
#include <hip/hip_bf16.h>
#include <stdint.h>

#define GAS __attribute__((address_space(1)))
#define LAS __attribute__((address_space(3)))
typedef unsigned v4u __attribute__((ext_vector_type(4)));
typedef unsigned v2u __attribute__((ext_vector_type(2)));
typedef float f32x4 __attribute__((ext_vector_type(4)));
typedef short s16x4 __attribute__((ext_vector_type(4)));
typedef __bf16 bf16x8 __attribute__((ext_vector_type(8)));

constexpr int NB = 8, DM = 512, HQ = 32, HKV = 8, SK = 8192, DH = 128;
constexpr int NSPLIT = 4;
constexpr int NWAVE = 8;
constexpr int KEYS_WG = SK / NSPLIT;
constexpr int QUADS_WG = KEYS_WG / 4;
#define TQ 8
#define NBUF 2
constexpr int BATCH_WG = QUADS_WG / TQ;
constexpr int BATCH_WAVE = BATCH_WG / NWAVE;
constexpr float C2 = 0.088388347648318440f * 1.4426950408889634f;

__device__ __forceinline__ float bf2f(unsigned short b) { return __builtin_bit_cast(float, (unsigned)b << 16); }
__device__ __forceinline__ unsigned cvtpk(float lo, float hi) {
  typedef __bf16 bf16x2_t __attribute__((ext_vector_type(2)));
  typedef float f32x2_t __attribute__((ext_vector_type(2)));
  return __builtin_bit_cast(unsigned, __builtin_convertvector(f32x2_t{lo, hi}, bf16x2_t));
}
template <int CTRL> __device__ __forceinline__ float dpp(float x) {
  return __builtin_bit_cast(float, __builtin_amdgcn_mov_dpp(__builtin_bit_cast(int, x), CTRL, 0xf, 0xf, true));
}
__device__ __forceinline__ float xrow_max(float x) {
  auto s = __builtin_amdgcn_permlane16_swap(__float_as_uint(x), __float_as_uint(x), false, false);
  x = fmaxf(__uint_as_float(s[0]), __uint_as_float(s[1]));
  auto t = __builtin_amdgcn_permlane32_swap(__float_as_uint(x), __float_as_uint(x), false, false);
  return fmaxf(__uint_as_float(t[0]), __uint_as_float(t[1]));
}
__device__ __forceinline__ float xrow_sum(float x) {
  auto s = __builtin_amdgcn_permlane16_swap(__float_as_uint(x), __float_as_uint(x), false, false);
  x = __uint_as_float(s[0]) + __uint_as_float(s[1]);
  auto t = __builtin_amdgcn_permlane32_swap(__float_as_uint(x), __float_as_uint(x), false, false);
  return __uint_as_float(t[0]) + __uint_as_float(t[1]);
}
__device__ __forceinline__ v4u ldnt(const GAS v4u* p) { return __builtin_nontemporal_load(p); }

__global__ void __launch_bounds__(256) k_qproj(const unsigned short* __restrict__ x, const unsigned short* __restrict__ wq,
                                               unsigned short* __restrict__ q) {
  __shared__ float xs[NB][DM];
  __shared__ float red[32][NB][64 + 1];
  const int tid = threadIdx.x;
  for (int i = tid; i < NB * DM; i += 256) xs[i / DM][i % DM] = bf2f(x[i]);
  __syncthreads();
  const int c8 = tid & 7, r = tid >> 3;
  const int col0 = blockIdx.x * 64 + c8 * 8;
  float acc[NB][8];
#pragma unroll
  for (int b = 0; b < NB; ++b)
#pragma unroll
    for (int j = 0; j < 8; ++j) acc[b][j] = 0.f;
  v4u w[16];
#pragma unroll
  for (int i = 0; i < 16; ++i) w[i] = *(const GAS v4u*)(wq + (size_t)(r + 32 * i) * (HQ * DH) + col0);
#pragma unroll
  for (int i = 0; i < 16; ++i) {
    const int k = r + 32 * i;
    float wf[8];
#pragma unroll
    for (int j = 0; j < 4; ++j) { wf[2 * j] = __builtin_bit_cast(float, w[i][j] << 16); wf[2 * j + 1] = __builtin_bit_cast(float, w[i][j] & 0xffff0000u); }
#pragma unroll
    for (int b = 0; b < NB; ++b) { const float xv = xs[b][k];
#pragma unroll
      for (int j = 0; j < 8; ++j) acc[b][j] = fmaf(xv, wf[j], acc[b][j]); }
  }
#pragma unroll
  for (int b = 0; b < NB; ++b)
#pragma unroll
    for (int j = 0; j < 8; ++j) red[r][b][c8 * 8 + j] = acc[b][j];
  __syncthreads();
  for (int o = tid; o < NB * 64; o += 256) {
    const int b = o >> 6, c = o & 63;
    float s = 0.f;
    for (int rr = 0; rr < 32; ++rr) s += red[rr][b][c];
    q[b * (HQ * DH) + blockIdx.x * 64 + c] = (unsigned short)(cvtpk(s, s) & 0xffffu);
  }
}

__global__ void __launch_bounds__(NWAVE * 64, 2) k_attn(const unsigned short* __restrict__ q, const unsigned short* __restrict__ kc,
                                                        const unsigned short* __restrict__ vc, float* __restrict__ po, float* __restrict__ pml) {
  __shared__ __attribute__((aligned(16))) float mrg[NWAVE][4][DH + 4];
  const int tid = threadIdx.x, lane = tid & 63, wave = __builtin_amdgcn_readfirstlane(tid >> 6);
  const int bid = blockIdx.x;
  const int pair = bid >> 2, s = bid & 3;
  const int b = pair >> 3, kvh = pair & 7;
  const size_t base = ((size_t)pair * SK + (size_t)s * KEYS_WG) * DH;
  const int koff = ((lane >> 2) & 3) * 16 + (lane >> 4) * 4 + (lane & 3);
  const GAS v4u* kp = (const GAS v4u*)(kc + base) + koff + (size_t)wave * TQ * 64;
  const GAS v4u* vp = (const GAS v4u*)(vc + base) + lane + (size_t)wave * TQ * 64;
  constexpr int BSTR = NWAVE * TQ * 64;

  const v4u qf = *((const GAS v4u*)(q + (size_t)b * HQ * DH + kvh * 4 * DH) + koff);
  __builtin_amdgcn_sched_barrier(0);
  v4u K[NBUF][TQ], V[NBUF][TQ];
#pragma unroll
  for (int u = 0; u < NBUF; ++u) {
#pragma unroll
    for (int i = 0; i < TQ; ++i) K[u][i] = ldnt(kp + u * BSTR + i * 64);
#pragma unroll
    for (int i = 0; i < TQ; ++i) V[u][i] = ldnt(vp + u * BSTR + i * 64);
  }
  __builtin_amdgcn_sched_barrier(0);
  const unsigned j0 = lane & 3;
  const unsigned mask0 = j0 == 0 ? 0x0000ffffu : (j0 == 1 ? 0xffff0000u : 0u);
  const unsigned mask1 = j0 == 2 ? 0x0000ffffu : (j0 == 3 ? 0xffff0000u : 0u);
  float m = -INFINITY, l = 0.f;
  f32x4 acc0 = {0.f, 0.f, 0.f, 0.f}, acc1 = {0.f, 0.f, 0.f, 0.f};
#pragma unroll
  for (int it = 0; it < BATCH_WAVE; ++it) {
    const int u = it % NBUF;
    float sc[TQ];
#pragma unroll
    for (int i = 0; i < TQ; ++i) {
      f32x4 d = __builtin_amdgcn_mfma_f32_16x16x32_bf16(__builtin_bit_cast(bf16x8, K[u][i]), __builtin_bit_cast(bf16x8, qf), f32x4{0.f, 0.f, 0.f, 0.f}, 0, 0, 0);
      sc[i] = ((dpp<0x00>(d[0]) + dpp<0x55>(d[1])) + dpp<0xAA>(d[2])) + dpp<0xFF>(d[3]);
    }
    if (it + NBUF < BATCH_WAVE) {
#pragma unroll
      for (int i = 0; i < TQ; ++i) K[u][i] = ldnt(kp + (it + NBUF) * BSTR + i * 64);
    }
    float tm = sc[0];
#pragma unroll
    for (int i = 1; i < TQ; ++i) tm = fmaxf(tm, sc[i]);
    tm = xrow_max(tm);
    const float mn = fmaxf(m, tm);
    const float alpha = __builtin_amdgcn_exp2f((m - mn) * C2);
    const float mc = -mn * C2;
    m = mn;
    l *= alpha; acc0 *= alpha; acc1 *= alpha;
#pragma unroll
    for (int i = 0; i < TQ; ++i) {
      const float p = __builtin_amdgcn_exp2f(fmaf(sc[i], C2, mc));
      l += p;
      const unsigned pp = cvtpk(p, p);
      const v2u bo = {pp & mask0, pp & mask1};
      const v2u va = {V[u][i][0], V[u][i][1]}, vb = {V[u][i][2], V[u][i][3]};
      acc0 = __builtin_amdgcn_mfma_f32_16x16x16bf16_1k(__builtin_bit_cast(s16x4, va), __builtin_bit_cast(s16x4, bo), acc0, 0, 0, 0);
      acc1 = __builtin_amdgcn_mfma_f32_16x16x16bf16_1k(__builtin_bit_cast(s16x4, vb), __builtin_bit_cast(s16x4, bo), acc1, 0, 0, 0);
    }
    if (it + NBUF < BATCH_WAVE) {
#pragma unroll
      for (int i = 0; i < TQ; ++i) V[u][i] = ldnt(vp + (it + NBUF) * BSTR + i * 64);
    }
    __builtin_amdgcn_sched_barrier(0);
  }
  l = xrow_sum(l);
  const int h = (lane >> 2) & 3, g = lane >> 4;
#pragma unroll
  for (int r = 0; r < 4; ++r) {
    mrg[wave][h][4 + 32 * g + 8 * r + j0] = acc0[r];
    mrg[wave][h][4 + 32 * g + 8 * r + 4 + j0] = acc1[r];
  }
  if (g == 0 && j0 == 0) { mrg[wave][h][0] = m; mrg[wave][h][1] = l; }
  __syncthreads();
  {
    const int hh = tid >> 7, d = tid & 127;
    float M = -INFINITY;
#pragma unroll
    for (int w = 0; w < NWAVE; ++w) M = fmaxf(M, mrg[w][hh][0]);
    float L = 0.f, o = 0.f;
#pragma unroll
    for (int w = 0; w < NWAVE; ++w) {
      const float wt = __builtin_amdgcn_exp2f((mrg[w][hh][0] - M) * C2);
      L = fmaf(wt, mrg[w][hh][1], L);
      o = fmaf(wt, mrg[w][hh][4 + d], o);
    }
    const size_t slot = ((size_t)b * HQ + kvh * 4 + hh) * NSPLIT + s;
    po[slot * DH + d] = o;
    if (d == 0) { pml[slot * 2] = M; pml[slot * 2 + 1] = L; }
  }
}

__global__ void __launch_bounds__(256) k_out(const float* __restrict__ po, const float* __restrict__ pml, const unsigned short* __restrict__ wo,
                                             unsigned short* __restrict__ out) {
  __shared__ float os[HQ * DH];
  __shared__ float wts[HQ][NSPLIT];
  __shared__ float red[128][17];
  const int tid = threadIdx.x;
  const int b = blockIdx.x >> 5, cb = blockIdx.x & 31;
  const int half = tid & 1, r = tid >> 1;
  v4u w[32];
#pragma unroll
  for (int i = 0; i < 32; ++i) w[i] = *(const GAS v4u*)(wo + (size_t)(r + 128 * i) * DM + cb * 16 + half * 8);
  if (tid < HQ) {
    const float* p = pml + ((size_t)b * HQ + tid) * NSPLIT * 2;
    float M = -INFINITY;
    for (int s = 0; s < NSPLIT; ++s) M = fmaxf(M, p[2 * s]);
    float L = 0.f, wt[NSPLIT];
    for (int s = 0; s < NSPLIT; ++s) { wt[s] = __builtin_amdgcn_exp2f((p[2 * s] - M) * C2); L = fmaf(wt[s], p[2 * s + 1], L); }
    const float inv = 1.f / L;
    for (int s = 0; s < NSPLIT; ++s) wts[tid][s] = wt[s] * inv;
  }
  __syncthreads();
  for (int i = tid; i < HQ * DH; i += 256) {
    const int h = i >> 7, d = i & 127;
    const float* p = po + ((size_t)b * HQ + h) * NSPLIT * DH + d;
    float o = 0.f;
#pragma unroll
    for (int s = 0; s < NSPLIT; ++s) o = fmaf(wts[h][s], p[s * DH], o);
    os[i] = __builtin_bit_cast(float, cvtpk(o, o) << 16);
  }
  __syncthreads();
  float acc[8];
#pragma unroll
  for (int j = 0; j < 8; ++j) acc[j] = 0.f;
#pragma unroll
  for (int i = 0; i < 32; ++i) {
    const float ov = os[r + 128 * i];
#pragma unroll
    for (int j = 0; j < 4; ++j) {
      acc[2 * j] = fmaf(ov, __builtin_bit_cast(float, w[i][j] << 16), acc[2 * j]);
      acc[2 * j + 1] = fmaf(ov, __builtin_bit_cast(float, w[i][j] & 0xffff0000u), acc[2 * j + 1]);
    }
  }
#pragma unroll
  for (int j = 0; j < 8; ++j) red[r][half * 8 + j] = acc[j];
  __syncthreads();
  if (tid < 64) {
    const int c = tid & 15, part = tid >> 4;
    float s = 0.f;
    for (int rr = part * 32; rr < part * 32 + 32; ++rr) s += red[rr][c];
    s = xrow_sum(s);
    if (tid < 16) out[b * DM + cb * 16 + c] = (unsigned short)(cvtpk(s, s) & 0xffffu);
  }
}

extern "C" void kernel_launch(void* const* d_in, const int* in_sizes, int n_in, void* d_out, int out_size, void* d_ws, size_t ws_size,
                              hipStream_t stream) {
  const unsigned short* x = (const unsigned short*)d_in[0];
  const unsigned short* kc = (const unsigned short*)d_in[1];
  const unsigned short* vc = (const unsigned short*)d_in[2];
  const unsigned short* wq = (const unsigned short*)d_in[3];
  const unsigned short* wo = (const unsigned short*)d_in[4];
  unsigned char* ws = (unsigned char*)d_ws;
  unsigned short* q = (unsigned short*)ws;
  float* pml = (float*)(ws + 65536);
  float* po = (float*)(ws + 65536 + 65536);
  k_qproj<<<64, 256, 0, stream>>>(x, wq, q);
  k_attn<<<NB * HKV * NSPLIT, NWAVE * 64, 0, stream>>>(q, kc, vc, po, pml);
  k_out<<<256, 256, 0, stream>>>(po, pml, wo, (unsigned short*)d_out);
}
```

```cpp
#define KNT 1
#define VNT 0
#include <hip/hip_runtime.h>
#include <hip/hip_bf16.h>
#include <stdint.h>

#define GAS __attribute__((address_space(1)))
#define LAS __attribute__((address_space(3)))
typedef unsigned v4u __attribute__((ext_vector_type(4)));
typedef unsigned v2u __attribute__((ext_vector_type(2)));
typedef float f32x4 __attribute__((ext_vector_type(4)));
typedef short s16x4 __attribute__((ext_vector_type(4)));
typedef __bf16 bf16x8 __attribute__((ext_vector_type(8)));

constexpr int NB = 8, DM = 512, HQ = 32, HKV = 8, SK = 8192, DH = 128;
constexpr int NSPLIT = 4;
constexpr int NWAVE = 8;
constexpr int KEYS_WG = SK / NSPLIT;
constexpr int QUADS_WG = KEYS_WG / 4;
#define TQ 8
#define NBUF 2
constexpr int BATCH_WG = QUADS_WG / TQ;
constexpr int BATCH_WAVE = BATCH_WG / NWAVE;
constexpr float C2 = 0.088388347648318440f * 1.4426950408889634f;

__device__ __forceinline__ float bf2f(unsigned short b) { return __builtin_bit_cast(float, (unsigned)b << 16); }
__device__ __forceinline__ unsigned cvtpk(float lo, float hi) {
  typedef __bf16 bf16x2_t __attribute__((ext_vector_type(2)));
  typedef float f32x2_t __attribute__((ext_vector_type(2)));
  return __builtin_bit_cast(unsigned, __builtin_convertvector(f32x2_t{lo, hi}, bf16x2_t));
}
template <int CTRL> __device__ __forceinline__ float dpp(float x) {
  return __builtin_bit_cast(float, __builtin_amdgcn_mov_dpp(__builtin_bit_cast(int, x), CTRL, 0xf, 0xf, true));
}
__device__ __forceinline__ float xrow_max(float x) {
  auto s = __builtin_amdgcn_permlane16_swap(__float_as_uint(x), __float_as_uint(x), false, false);
  x = fmaxf(__uint_as_float(s[0]), __uint_as_float(s[1]));
  auto t = __builtin_amdgcn_permlane32_swap(__float_as_uint(x), __float_as_uint(x), false, false);
  return fmaxf(__uint_as_float(t[0]), __uint_as_float(t[1]));
}
__device__ __forceinline__ float xrow_sum(float x) {
  auto s = __builtin_amdgcn_permlane16_swap(__float_as_uint(x), __float_as_uint(x), false, false);
  x = __uint_as_float(s[0]) + __uint_as_float(s[1]);
  auto t = __builtin_amdgcn_permlane32_swap(__float_as_uint(x), __float_as_uint(x), false, false);
  return __uint_as_float(t[0]) + __uint_as_float(t[1]);
}
__device__ __forceinline__ v4u ldk(const GAS v4u* p) { return KNT ? __builtin_nontemporal_load(p) : *p; }
__device__ __forceinline__ v4u ldv(const GAS v4u* p) { return VNT ? __builtin_nontemporal_load(p) : *p; }

__global__ void __launch_bounds__(256) k_qproj(const unsigned short* __restrict__ x, const unsigned short* __restrict__ wq,
                                               unsigned short* __restrict__ q) {
  __shared__ float xs[NB][DM];
  __shared__ float red[32][NB][64 + 1];
  const int tid = threadIdx.x;
  for (int i = tid; i < NB * DM; i += 256) xs[i / DM][i % DM] = bf2f(x[i]);
  __syncthreads();
  const int c8 = tid & 7, r = tid >> 3;
  const int col0 = blockIdx.x * 64 + c8 * 8;
  float acc[NB][8];
#pragma unroll
  for (int b = 0; b < NB; ++b)
#pragma unroll
    for (int j = 0; j < 8; ++j) acc[b][j] = 0.f;
  v4u w[16];
#pragma unroll
  for (int i = 0; i < 16; ++i) w[i] = *(const GAS v4u*)(wq + (size_t)(r + 32 * i) * (HQ * DH) + col0);
#pragma unroll
  for (int i = 0; i < 16; ++i) {
    const int k = r + 32 * i;
    float wf[8];
#pragma unroll
    for (int j = 0; j < 4; ++j) { wf[2 * j] = __builtin_bit_cast(float, w[i][j] << 16); wf[2 * j + 1] = __builtin_bit_cast(float, w[i][j] & 0xffff0000u); }
#pragma unroll
    for (int b = 0; b < NB; ++b) { const float xv = xs[b][k];
#pragma unroll
      for (int j = 0; j < 8; ++j) acc[b][j] = fmaf(xv, wf[j], acc[b][j]); }
  }
#pragma unroll
  for (int b = 0; b < NB; ++b)
#pragma unroll
    for (int j = 0; j < 8; ++j) red[r][b][c8 * 8 + j] = acc[b][j];
  __syncthreads();
  for (int o = tid; o < NB * 64; o += 256) {
    const int b = o >> 6, c = o & 63;
    float s = 0.f;
    for (int rr = 0; rr < 32; ++rr) s += red[rr][b][c];
    q[b * (HQ * DH) + blockIdx.x * 64 + c] = (unsigned short)(cvtpk(s, s) & 0xffffu);
  }
}

__global__ void __launch_bounds__(NWAVE * 64, 2) k_attn(const unsigned short* __restrict__ q, const unsigned short* __restrict__ kc,
                                                        const unsigned short* __restrict__ vc, float* __restrict__ po, float* __restrict__ pml) {
  __shared__ __attribute__((aligned(16))) float mrg[NWAVE][4][DH + 4];
  const int tid = threadIdx.x, lane = tid & 63, wave = __builtin_amdgcn_readfirstlane(tid >> 6);
  const int bid = blockIdx.x;
  const int pair = bid >> 2, s = bid & 3;
  const int b = pair >> 3, kvh = pair & 7;
  const size_t base = ((size_t)pair * SK + (size_t)s * KEYS_WG) * DH;
  const int koff = ((lane >> 2) & 3) * 16 + (lane >> 4) * 4 + (lane & 3);
  const GAS v4u* kp = (const GAS v4u*)(kc + base) + koff + (size_t)wave * TQ * 64;
  const GAS v4u* vp = (const GAS v4u*)(vc + base) + lane + (size_t)wave * TQ * 64;
  constexpr int BSTR = NWAVE * TQ * 64;

  const v4u qf = *((const GAS v4u*)(q + (size_t)b * HQ * DH + kvh * 4 * DH) + koff);
  __builtin_amdgcn_sched_barrier(0);
  v4u K[NBUF][TQ], V[NBUF][TQ];
#pragma unroll
  for (int u = 0; u < NBUF; ++u) {
#pragma unroll
    for (int i = 0; i < TQ; ++i) K[u][i] = ldk(kp + u * BSTR + i * 64);
#pragma unroll
    for (int i = 0; i < TQ; ++i) V[u][i] = ldv(vp + u * BSTR + i * 64);
  }
  __builtin_amdgcn_sched_barrier(0);
  const unsigned j0 = lane & 3;
  const unsigned mask0 = j0 == 0 ? 0x0000ffffu : (j0 == 1 ? 0xffff0000u : 0u);
  const unsigned mask1 = j0 == 2 ? 0x0000ffffu : (j0 == 3 ? 0xffff0000u : 0u);
  float m = -INFINITY, l = 0.f;
  f32x4 acc0 = {0.f, 0.f, 0.f, 0.f}, acc1 = {0.f, 0.f, 0.f, 0.f};
#pragma unroll
  for (int it = 0; it < BATCH_WAVE; ++it) {
    const int u = it % NBUF;
    float sc[TQ];
#pragma unroll
    for (int i = 0; i < TQ; ++i) {
      f32x4 d = __builtin_amdgcn_mfma_f32_16x16x32_bf16(__builtin_bit_cast(bf16x8, K[u][i]), __builtin_bit_cast(bf16x8, qf), f32x4{0.f, 0.f, 0.f, 0.f}, 0, 0, 0);
      sc[i] = ((dpp<0x00>(d[0]) + dpp<0x55>(d[1])) + dpp<0xAA>(d[2])) + dpp<0xFF>(d[3]);
    }
    if (it + NBUF < BATCH_WAVE) {
#pragma unroll
      for (int i = 0; i < TQ; ++i) K[u][i] = ldk(kp + (it + NBUF) * BSTR + i * 64);
    }
    float tm = sc[0];
#pragma unroll
    for (int i = 1; i < TQ; ++i) tm = fmaxf(tm, sc[i]);
    tm = xrow_max(tm);
    const float mn = fmaxf(m, tm);
    const float alpha = __builtin_amdgcn_exp2f((m - mn) * C2);
    const float mc = -mn * C2;
    m = mn;
    l *= alpha; acc0 *= alpha; acc1 *= alpha;
#pragma unroll
    for (int i = 0; i < TQ; ++i) {
      const float p = __builtin_amdgcn_exp2f(fmaf(sc[i], C2, mc));
      l += p;
      const unsigned pp = cvtpk(p, p);
      const v2u bo = {pp & mask0, pp & mask1};
      const v2u va = {V[u][i][0], V[u][i][1]}, vb = {V[u][i][2], V[u][i][3]};
      acc0 = __builtin_amdgcn_mfma_f32_16x16x16bf16_1k(__builtin_bit_cast(s16x4, va), __builtin_bit_cast(s16x4, bo), acc0, 0, 0, 0);
      acc1 = __builtin_amdgcn_mfma_f32_16x16x16bf16_1k(__builtin_bit_cast(s16x4, vb), __builtin_bit_cast(s16x4, bo), acc1, 0, 0, 0);
    }
    if (it + NBUF < BATCH_WAVE) {
#pragma unroll
      for (int i = 0; i < TQ; ++i) V[u][i] = ldv(vp + (it + NBUF) * BSTR + i * 64);
    }
    __builtin_amdgcn_sched_barrier(0);
  }
  l = xrow_sum(l);
  const int h = (lane >> 2) & 3, g = lane >> 4;
#pragma unroll
  for (int r = 0; r < 4; ++r) {
    mrg[wave][h][4 + 32 * g + 8 * r + j0] = acc0[r];
    mrg[wave][h][4 + 32 * g + 8 * r + 4 + j0] = acc1[r];
  }
  if (g == 0 && j0 == 0) { mrg[wave][h][0] = m; mrg[wave][h][1] = l; }
  __syncthreads();
  {
    const int hh = tid >> 7, d = tid & 127;
    float M = -INFINITY;
#pragma unroll
    for (int w = 0; w < NWAVE; ++w) M = fmaxf(M, mrg[w][hh][0]);
    float L = 0.f, o = 0.f;
#pragma unroll
    for (int w = 0; w < NWAVE; ++w) {
      const float wt = __builtin_amdgcn_exp2f((mrg[w][hh][0] - M) * C2);
      L = fmaf(wt, mrg[w][hh][1], L);
      o = fmaf(wt, mrg[w][hh][4 + d], o);
    }
    const size_t slot = ((size_t)b * HQ + kvh * 4 + hh) * NSPLIT + s;
    po[slot * DH + d] = o;
    if (d == 0) { pml[slot * 2] = M; pml[slot * 2 + 1] = L; }
  }
}

__global__ void __launch_bounds__(256) k_out(const float* __restrict__ po, const float* __restrict__ pml, const unsigned short* __restrict__ wo,
                                             unsigned short* __restrict__ out) {
  __shared__ float os[HQ * DH];
  __shared__ float wts[HQ][NSPLIT];
  __shared__ float red[128][17];
  const int tid = threadIdx.x;
  const int b = blockIdx.x >> 5, cb = blockIdx.x & 31;
  const int half = tid & 1, r = tid >> 1;
  v4u w[32];
#pragma unroll
  for (int i = 0; i < 32; ++i) w[i] = *(const GAS v4u*)(wo + (size_t)(r + 128 * i) * DM + cb * 16 + half * 8);
  if (tid < HQ) {
    const float* p = pml + ((size_t)b * HQ + tid) * NSPLIT * 2;
    float M = -INFINITY;
    for (int s = 0; s < NSPLIT; ++s) M = fmaxf(M, p[2 * s]);
    float L = 0.f, wt[NSPLIT];
    for (int s = 0; s < NSPLIT; ++s) { wt[s] = __builtin_amdgcn_exp2f((p[2 * s] - M) * C2); L = fmaf(wt[s], p[2 * s + 1], L); }
    const float inv = 1.f / L;
    for (int s = 0; s < NSPLIT; ++s) wts[tid][s] = wt[s] * inv;
  }
  __syncthreads();
  for (int i = tid; i < HQ * DH; i += 256) {
    const int h = i >> 7, d = i & 127;
    const float* p = po + ((size_t)b * HQ + h) * NSPLIT * DH + d;
    float o = 0.f;
#pragma unroll
    for (int s = 0; s < NSPLIT; ++s) o = fmaf(wts[h][s], p[s * DH], o);
    os[i] = __builtin_bit_cast(float, cvtpk(o, o) << 16);
  }
  __syncthreads();
  float acc[8];
#pragma unroll
  for (int j = 0; j < 8; ++j) acc[j] = 0.f;
#pragma unroll
  for (int i = 0; i < 32; ++i) {
    const float ov = os[r + 128 * i];
#pragma unroll
    for (int j = 0; j < 4; ++j) {
      acc[2 * j] = fmaf(ov, __builtin_bit_cast(float, w[i][j] << 16), acc[2 * j]);
      acc[2 * j + 1] = fmaf(ov, __builtin_bit_cast(float, w[i][j] & 0xffff0000u), acc[2 * j + 1]);
    }
  }
#pragma unroll
  for (int j = 0; j < 8; ++j) red[r][half * 8 + j] = acc[j];
  __syncthreads();
  if (tid < 64) {
    const int c = tid & 15, part = tid >> 4;
    float s = 0.f;
    for (int rr = part * 32; rr < part * 32 + 32; ++rr) s += red[rr][c];
    s = xrow_sum(s);
    if (tid < 16) out[b * DM + cb * 16 + c] = (unsigned short)(cvtpk(s, s) & 0xffffu);
  }
}

extern "C" void kernel_launch(void* const* d_in, const int* in_sizes, int n_in, void* d_out, int out_size, void* d_ws, size_t ws_size,
                              hipStream_t stream) {
  const unsigned short* x = (const unsigned short*)d_in[0];
  const unsigned short* kc = (const unsigned short*)d_in[1];
  const unsigned short* vc = (const unsigned short*)d_in[2];
  const unsigned short* wq = (const unsigned short*)d_in[3];
  const unsigned short* wo = (const unsigned short*)d_in[4];
  unsigned char* ws = (unsigned char*)d_ws;
  unsigned short* q = (unsigned short*)ws;
  float* pml = (float*)(ws + 65536);
  float* po = (float*)(ws + 65536 + 65536);
  k_qproj<<<64, 256, 0, stream>>>(x, wq, q);
  k_attn<<<NB * HKV * NSPLIT, NWAVE * 64, 0, stream>>>(q, kc, vc, po, pml);
  k_out<<<256, 256, 0, stream>>>(po, pml, wo, (unsigned short*)d_out);
}
```

```cpp
#include <hip/hip_runtime.h>
#include <hip/hip_bf16.h>
#include <stdint.h>

#define GAS __attribute__((address_space(1)))
#define LAS __attribute__((address_space(3)))
#define CAS __attribute__((address_space(4)))
typedef unsigned v4u __attribute__((ext_vector_type(4)));
typedef unsigned v2u __attribute__((ext_vector_type(2)));
typedef int v4i __attribute__((ext_vector_type(4)));
typedef float f32x4 __attribute__((ext_vector_type(4)));
typedef short s16x4 __attribute__((ext_vector_type(4)));
typedef __bf16 bf16x8 __attribute__((ext_vector_type(8)));

constexpr int NB = 8, DM = 512, HQ = 32, HKV = 8, SK = 8192, DH = 128;
constexpr int NWG = 256, NTHR = 320;
constexpr int NROW = 32;
constexpr int SLOT = 8192, NSLOT = 4, RING = SLOT * NSLOT;
constexpr float C2 = 0.088388347648318440f * 1.4426950408889634f;
#define TAU16 (108 * 16)
#define POFF 0
#define PROD_DELAY 120
constexpr unsigned SPIN_CAP = 1u << 16;
constexpr int L_RING = 0, L_Q = 4 * RING, L_MRG = L_Q + 1024, MREC = 132 * 4, L_OT = L_MRG + 16 * MREC, OT_ROW = 272, L_END = L_OT + 8 * OT_ROW;
constexpr int L_X = L_MRG;
static_assert(16 * MREC >= 8192 && L_END <= 160 * 1024, "lds map");
constexpr int W_E = 0, W_QG = 4096, W_PA = W_QG + NB * HQ * DH * 4, PA_REC = 2176, W_YB = W_PA + NWG * PA_REC, W_END = W_YB + NWG * 2048;
#define STAMP(i)
#define STAMP_OUT()

__device__ __forceinline__ unsigned cvtpk(float lo, float hi) {
  typedef __bf16 bf16x2_t __attribute__((ext_vector_type(2)));
  typedef float f32x2_t __attribute__((ext_vector_type(2)));
  return __builtin_bit_cast(unsigned, __builtin_convertvector(f32x2_t{lo, hi}, bf16x2_t));
}
template <int CTRL> __device__ __forceinline__ float dpp(float x) {
  return __builtin_bit_cast(float, __builtin_amdgcn_mov_dpp(__builtin_bit_cast(int, x), CTRL, 0xf, 0xf, true));
}
__device__ __forceinline__ float xrow_max(float x) {
  auto s = __builtin_amdgcn_permlane16_swap(__float_as_uint(x), __float_as_uint(x), false, false);
  x = fmaxf(__uint_as_float(s[0]), __uint_as_float(s[1]));
  auto t = __builtin_amdgcn_permlane32_swap(__float_as_uint(x), __float_as_uint(x), false, false);
  return fmaxf(__uint_as_float(t[0]), __uint_as_float(t[1]));
}
__device__ __forceinline__ float xrow_sum(float x) {
  auto s = __builtin_amdgcn_permlane16_swap(__float_as_uint(x), __float_as_uint(x), false, false);
  x = __uint_as_float(s[0]) + __uint_as_float(s[1]);
  auto t = __builtin_amdgcn_permlane32_swap(__float_as_uint(x), __float_as_uint(x), false, false);
  return __uint_as_float(t[0]) + __uint_as_float(t[1]);
}
__device__ __forceinline__ v4i mkrsrc(const void* p, unsigned bytes) {
  const unsigned long long a = (unsigned long long)p;
  v4i r; r[0] = (int)(unsigned)a; r[1] = (int)((unsigned)(a >> 32) & 0xffffu); r[2] = (int)bytes; r[3] = 0x00020000; return r;
}
__device__ __forceinline__ unsigned now() { return (unsigned)__builtin_amdgcn_s_memrealtime(); }
#define WAITVM(n) asm volatile("s_waitcnt vmcnt(" #n ")" ::: "memory")
#define BARRIER() asm volatile("s_waitcnt lgkmcnt(0)\n\ts_barrier" ::: "memory")
__device__ __forceinline__ void issue_row(v4i rk, v4i rv, unsigned vk, unsigned vv, unsigned soff, unsigned lds) {
  asm volatile("s_mov_b32 m0, %5\n\ts_nop 0\n\t"
               "buffer_load_dwordx4 %2, %0, %4 offen nt lds\n\t"
               "buffer_load_dwordx4 %2, %0, %4 offen offset:1024 nt lds\n\t"
               "buffer_load_dwordx4 %2, %0, %4 offen offset:2048 nt lds\n\t"
               "buffer_load_dwordx4 %2, %0, %4 offen offset:3072 nt lds\n\t"
               "s_add_u32 m0, m0, 0x1000\n\ts_nop 0\n\t"
               "buffer_load_dwordx4 %3, %1, %4 offen nt lds\n\t"
               "buffer_load_dwordx4 %3, %1, %4 offen offset:1024 nt lds\n\t"
               "buffer_load_dwordx4 %3, %1, %4 offen offset:2048 nt lds\n\t"
               "buffer_load_dwordx4 %3, %1, %4 offen offset:3072 nt lds"
               :: "s"(rk), "s"(rv), "v"(vk), "v"(vv), "s"(soff), "s"(lds) : "memory", "scc");
}
__device__ __forceinline__ void dma1_nt(v4i r, unsigned voff, unsigned soff, unsigned lds) {
  asm volatile("s_mov_b32 m0, %3\n\ts_nop 0\n\tbuffer_load_dwordx4 %1, %0, %2 offen nt lds" :: "s"(r), "v"(voff), "s"(soff), "s"(lds) : "memory");
}
__device__ __forceinline__ void dma1(v4i r, unsigned voff, unsigned soff, unsigned lds) {
  asm volatile("s_mov_b32 m0, %3\n\ts_nop 0\n\tbuffer_load_dwordx4 %1, %0, %2 offen lds" :: "s"(r), "v"(voff), "s"(soff), "s"(lds) : "memory");
}
__device__ __forceinline__ f32x4 mfma32(v4u a, v4u b, f32x4 c) {
  return __builtin_amdgcn_mfma_f32_16x16x32_bf16(__builtin_bit_cast(bf16x8, a), __builtin_bit_cast(bf16x8, b), c, 0, 0, 0);
}
__device__ __forceinline__ f32x4 mfma16(unsigned a0, unsigned a1, v2u b, f32x4 c) {
  return __builtin_amdgcn_mfma_f32_16x16x16bf16_1k(__builtin_bit_cast(s16x4, v2u{a0, a1}), __builtin_bit_cast(s16x4, b), c, 0, 0, 0);
}
__device__ __forceinline__ v4u tr_frag(LAS unsigned char* p) {
  const v2u lo = __builtin_bit_cast(v2u, __builtin_amdgcn_ds_read_tr16_b64_v4i16((LAS s16x4*)p));
  const v2u hi = __builtin_bit_cast(v2u, __builtin_amdgcn_ds_read_tr16_b64_v4i16((LAS s16x4*)(p + 512)));
  return v4u{lo[0], lo[1], hi[0], hi[1]};
}
constexpr int AUX_SC1 = 16;
__device__ __forceinline__ v4u tag4(f32x4 v, unsigned tag) {
  const v4u u = __builtin_bit_cast(v4u, v);
  return v4u{(u[0] & 0xffffff00u) | (tag & 0xffu), (u[1] & 0xffffff00u) | ((tag >> 8) & 0xffu), (u[2] & 0xffffff00u) | ((tag >> 16) & 0xffu), (u[3] & 0xffffff00u) | (tag >> 24)};
}
__device__ __forceinline__ v4u untag4(v4u u, unsigned tag) {
  return v4u{u[0] ^ (tag & 0xffu), u[1] ^ ((tag >> 8) & 0xffu), u[2] ^ ((tag >> 16) & 0xffu), u[3] ^ (tag >> 24)};
}
__device__ __forceinline__ unsigned load_tag(const unsigned char* ws) {
  unsigned e0;
  asm volatile("s_load_dword %0, %1, 0x0\n\ts_waitcnt lgkmcnt(0)" : "=&s"(e0) : "s"(ws) : "memory");
  return ((e0 & 0x7f7f7f7fu) + 0x51276b35u) ^ (e0 & 0x80808080u);
}
#define RELOAD() asm volatile("" ::: "memory")
__device__ __forceinline__ unsigned or4(v4u u) { return (u[0] | u[1]) | (u[2] | u[3]); }

__global__ void __launch_bounds__(NTHR, 1) gqa_fused(const unsigned char* __restrict__ x, const unsigned char* __restrict__ kc, const unsigned char* __restrict__ vc,
                                                     const unsigned char* __restrict__ wq, const unsigned char* __restrict__ wo, unsigned char* __restrict__ out,
                                                     unsigned char* ws) {
  __shared__ __attribute__((aligned(16))) unsigned char smem[L_END];
  LAS unsigned char* L = (LAS unsigned char*)smem;
  const unsigned lbase = (unsigned)(size_t)L;
  const unsigned t0 = now();
  const int lane = threadIdx.x & 63, wv = __builtin_amdgcn_readfirstlane(threadIdx.x >> 6);
  const int j = blockIdx.x, xg = j & 7, idx = j >> 3;
  const int b = idx >> 2, s = idx & 3, kvh = xg;
  const int ch = 4 * xg + (idx >> 3), nb = idx & 7;
  const bool producer = j < 64;
  const int g = lane >> 4;
  STAMP(0);
  const __amdgpu_buffer_rsrc_t rw = __builtin_amdgcn_make_buffer_rsrc((void*)ws, 0, W_END, 0x00020000);

  if (wv != 0) {
    const int w = wv - 1;
    const size_t pbase = (size_t)(b * HKV + kvh) * (SK * DH * 2);
    const v4i rk = mkrsrc(kc + pbase, SK * DH * 2), rv = mkrsrc(vc + pbase, SK * DH * 2);
    const unsigned vk = (((lane >> 2) & 3) * 16 + g * 4 + (lane & 3)) * 16, vv = lane * 16;
    const unsigned ring = lbase + L_RING + w * RING;
    const unsigned so0 = (s * 4 + w) * 4096;
    const int toff = POFF + (w * TAU16 >> 6);
    int i = 0;
    {
      const int npre = producer ? 2 : NSLOT;
      if (producer) { while ((int)(now() - t0) < PROD_DELAY) __builtin_amdgcn_s_sleep(2); }
#pragma unroll 1
      for (; i < npre; ++i) {
        if (i) { const int tt = toff + (i * TAU16 >> 4); while ((int)(now() - t0) < tt) __builtin_amdgcn_s_sleep(1); }
        WAITVM(8);
        issue_row(rk, rv, vk, vv, so0 + i * 65536, ring + i * SLOT);
      }
    }
    STAMP(1);
    const unsigned tag = load_tag(ws);
    BARRIER();
    STAMP(2);
    const v4u qf = *(LAS v4u*)(L + L_Q + vk);
    const unsigned j0 = lane & 3;
    const unsigned mask0 = j0 == 0 ? 0x0000ffffu : (j0 == 1 ? 0xffff0000u : 0u);
    const unsigned mask1 = j0 == 2 ? 0x0000ffffu : (j0 == 3 ? 0xffff0000u : 0u);
    float m = -INFINITY, l = 0.f;
    f32x4 acc0 = {0.f, 0.f, 0.f, 0.f}, acc1 = {0.f, 0.f, 0.f, 0.f};
    int n = 0;
#pragma unroll 1
    while (true) {
      if (i < NROW && i - n < NSLOT) {
        const int tt = toff + (i * TAU16 >> 4);
        const bool due = (int)(now() - t0) >= tt;
        if (due) {
          WAITVM(8);
          issue_row(rk, rv, vk, vv, so0 + i * 65536, ring + (i & (NSLOT - 1)) * SLOT);
          ++i;
          continue;
        }
        if (i - n < 2) { __builtin_amdgcn_s_sleep(1); continue; }
      }
      if (i == NROW && i - n < NSLOT) {
        const v4i ro = mkrsrc(wo, HQ * DH * DM * 2);
        const unsigned vo = (lane >> 3) * 1024 + (lane & 7) * 16;
#pragma unroll
        for (int u = 0; u < 4; ++u) dma1_nt(ro, vo, (ch * 128 + w * 32 + u * 8) * 1024 + nb * 128, ring + (NROW & (NSLOT - 1)) * SLOT + u * 1024);
        ++i;
      }
      if (n == NROW - 1) WAITVM(4);
      else if (n == NROW - 2 && i > NROW) WAITVM(12);
      else if (i - n == 2) WAITVM(8);
      LAS v4u* src = (LAS v4u*)(L + L_RING + w * RING + (n & (NSLOT - 1)) * SLOT) + lane;
      v4u K[4], V[4];
#pragma unroll
      for (int u = 0; u < 4; ++u) K[u] = src[u * 64];
#pragma unroll
      for (int u = 0; u < 4; ++u) V[u] = src[256 + u * 64];
      float sc[4];
#pragma unroll
      for (int u = 0; u < 4; ++u) {
        const f32x4 d = mfma32(K[u], qf, f32x4{0.f, 0.f, 0.f, 0.f});
        sc[u] = (dpp<0x00>(d[0]) + dpp<0x55>(d[1])) + (dpp<0xAA>(d[2]) + dpp<0xFF>(d[3]));
      }
      const float tm = xrow_max(fmaxf(fmaxf(sc[0], sc[1]), fmaxf(sc[2], sc[3])));
      const float mn = fmaxf(m, tm);
      const float alpha = __builtin_amdgcn_exp2f((m - mn) * C2);
      const float mc = -mn * C2;
      m = mn;
      l *= alpha; acc0 *= alpha; acc1 *= alpha;
#pragma unroll
      for (int u = 0; u < 4; ++u) {
        const float p = __builtin_amdgcn_exp2f(fmaf(sc[u], C2, mc));
        l += p;
        const unsigned pp = cvtpk(p, p);
        const v2u bo = {pp & mask0, pp & mask1};
        acc0 = mfma16(V[u][0], V[u][1], bo, acc0);
        acc1 = mfma16(V[u][2], V[u][3], bo, acc1);
      }
      if (++n == NROW) break;
    }
    STAMP(3);
    l = xrow_sum(l);
    {
      LAS float* rec = (LAS float*)(L + L_MRG + (w * 4 + ((lane >> 2) & 3)) * MREC);
#pragma unroll
      for (int r = 0; r < 4; ++r) { rec[32 * g + 8 * r + j0] = acc0[r]; rec[32 * g + 8 * r + 4 + j0] = acc1[r]; }
      if (g == 0 && j0 == 0) { rec[128] = m; rec[129] = l; }
    }
    BARRIER();
    STAMP(4);
    {
      const int c = lane < 32 ? lane : 32;
      f32x4 a[4]; float mw[4];
#pragma unroll
      for (int u = 0; u < 4; ++u) { LAS float* rec = (LAS float*)(L + L_MRG + (u * 4 + w) * MREC); a[u] = *(LAS f32x4*)(rec + 4 * c); mw[u] = rec[128]; }
      float M = fmaxf(fmaxf(mw[0], mw[1]), fmaxf(mw[2], mw[3]));
      M = __builtin_bit_cast(float, __builtin_bit_cast(unsigned, M) & 0xffffff00u);
      f32x4 o = {0.f, 0.f, 0.f, 0.f};
#pragma unroll
      for (int u = 0; u < 4; ++u) o += a[u] * __builtin_amdgcn_exp2f((mw[u] - M) * C2);
      if (c == 32) { o[0] = M; o[2] = 0.f; o[3] = 0.f; }
      if (lane <= 32) __builtin_amdgcn_raw_buffer_store_b128(tag4(o, tag), rw, c * 16, W_PA + ((b * HKV + kvh) * 4 + s) * PA_REC + w * MREC, AUX_SC1);
    }
    STAMP(5);
    {
      const int bb = 2 * w + (lane >> 5), c = lane & 31;
      const int vo = bb * (HKV * 4 * PA_REC) + c * 16, vm = bb * (HKV * 4 * PA_REC) + 512;
      const int so = W_PA + (ch >> 2) * (4 * PA_REC) + (ch & 3) * MREC;
      v4u a[4], ml[4];
      for (unsigned spin = 0; spin < SPIN_CAP; ++spin) {
        RELOAD();
#pragma unroll
        for (int u = 0; u < 4; ++u) { a[u] = __builtin_amdgcn_raw_buffer_load_b128(rw, vo, so + u * PA_REC, AUX_SC1); ml[u] = __builtin_amdgcn_raw_buffer_load_b128(rw, vm, so + u * PA_REC, AUX_SC1); }
        unsigned bad = 0;
#pragma unroll
        for (int u = 0; u < 4; ++u) { a[u] = untag4(a[u], tag); ml[u] = untag4(ml[u], tag); bad |= or4(a[u]) | or4(ml[u]); }
        if (__all((bad & 0xffu) == 0u)) break;
      }
      STAMP(6);
      float ms[4], M = -INFINITY;
#pragma unroll
      for (int u = 0; u < 4; ++u) { ms[u] = __uint_as_float(ml[u][0]); M = fmaxf(M, ms[u]); }
      float Ls = 0.f; f32x4 o = {0.f, 0.f, 0.f, 0.f};
#pragma unroll
      for (int u = 0; u < 4; ++u) { const float e = __builtin_amdgcn_exp2f((ms[u] - M) * C2); Ls = fmaf(e, __uint_as_float(ml[u][1]), Ls); o += __builtin_bit_cast(f32x4, a[u]) * e; }
      o *= __builtin_amdgcn_rcpf(Ls);
      *(LAS v2u*)(L + L_OT + bb * OT_ROW + c * 8) = v2u{cvtpk(o[0], o[1]), cvtpk(o[2], o[3])};
    }
    BARRIER();
    STAMP(7);
    {
      f32x4 y0 = {0.f, 0.f, 0.f, 0.f}, y1 = {0.f, 0.f, 0.f, 0.f};
      LAS unsigned char* ap = L + L_RING + (NROW & (NSLOT - 1)) * SLOT + (8 * g + ((lane & 15) >> 2)) * 128 + (16 * w + 4 * (lane & 3)) * 2;
      LAS unsigned char* bp = L + L_OT + (lane & 7) * OT_ROW + g * 16;
#pragma unroll
      for (int ks = 0; ks < 4; ks += 2) {
        y0 = mfma32(tr_frag(ap + ks * RING), *(LAS v4u*)(bp + ks * 64), y0);
        y1 = mfma32(tr_frag(ap + (ks + 1) * RING), *(LAS v4u*)(bp + (ks + 1) * 64), y1);
      }
      y0 += y1;
      if ((lane & 15) < 8) __builtin_amdgcn_raw_buffer_store_b128(tag4(y0, tag), rw, (lane & 7) * (32 * 2048) + g * 16, W_YB + (nb * 4 + w) * 2048 + ch * 64, AUX_SC1);
    }
    STAMP(8);
    STAMP_OUT();
    return;
  }

  __builtin_amdgcn_s_setprio(3);
  unsigned tag;
  if (!producer) tag = load_tag(ws);
  else {
    const v4i rx = mkrsrc(x, NB * DM * 2), rq = mkrsrc(wq, DM * HQ * DH * 2);
    const unsigned vx = (lane & 7) * 1024 + (lane >> 3) * 16;
#pragma unroll 1
    for (int u = 0; u < 8; ++u) dma1(rx, vx, u * 128, lbase + L_X + u * 1024);
    const unsigned vq = (lane >> 3) * 8192 + (lane & 7) * 16;
#pragma unroll 1
    for (int u = 0; u < 48; ++u) dma1_nt(rq, vq, u * 65536 + j * 128, lbase + L_RING + (u >> 4) * RING + 2 * SLOT + (u & 15) * 1024);
    tag = load_tag(ws);
    f32x4 qa[4];
#pragma unroll
    for (int t = 0; t < 4; ++t) qa[t] = f32x4{0.f, 0.f, 0.f, 0.f};
    LAS unsigned char* ap = L + L_RING + 2 * SLOT + (8 * g + ((lane & 15) >> 2)) * 128 + 4 * (lane & 3) * 2;
    LAS unsigned char* bp = L + L_X + g * 128 + (lane & 7) * 16;
#pragma unroll 1
    for (int c = 0; c < 4; ++c) {
      if (c < 2) WAITVM(32); else if (c == 2) WAITVM(16); else WAITVM(0);
      if (c == 0) {
#pragma unroll 1
        for (int u = 48; u < 64; ++u) dma1_nt(rq, vq, u * 65536 + j * 128, lbase + L_RING + 3 * RING + 2 * SLOT + (u & 15) * 1024);
      }
#pragma unroll
      for (int ks = 0; ks < 4; ++ks) {
        const v4u xb = *(LAS v4u*)(bp + (c * 4 + ks) * 512);
#pragma unroll
        for (int t = 0; t < 4; ++t) qa[t] = mfma32(tr_frag(ap + c * RING + ks * 4096 + t * 32), xb, qa[t]);
      }
    }
    STAMP(1);
    const bool hi = (lane & 8) != 0;
#pragma unroll
    for (int t = 0; t < 4; t += 2) {
      const unsigned pa0 = cvtpk(qa[t][0], qa[t][1]), pa1 = cvtpk(qa[t][2], qa[t][3]), pb0 = cvtpk(qa[t + 1][0], qa[t + 1][1]), pb1 = cvtpk(qa[t + 1][2], qa[t + 1][3]);
      const unsigned p0 = hi ? pb0 : pa0, p1 = hi ? pb1 : pa1;
      const v4u o = {(p0 << 16) | (tag & 0xffffu), (p0 & 0xffff0000u) | (tag >> 16), (p1 << 16) | (tag & 0xffffu), (p1 & 0xffff0000u) | (tag >> 16)};
      __builtin_amdgcn_raw_buffer_store_b128(o, rw, (lane & 7) * (HQ * DH * 4) + (hi ? 64 : 0) + g * 16, W_QG + (j * 64 + t * 16) * 4, AUX_SC1);
    }
  }
  STAMP(2);
  {
    const int so = W_QG + (b * HQ * DH + kvh * 512) * 4;
    v4u a0, a1;
    for (unsigned spin = 0; spin < SPIN_CAP; ++spin) {
      RELOAD();
      a0 = __builtin_amdgcn_raw_buffer_load_b128(rw, lane * 16, so, AUX_SC1);
      a1 = __builtin_amdgcn_raw_buffer_load_b128(rw, lane * 16 + 1024, so, AUX_SC1);
      const unsigned lo = tag & 0xffffu, hi = tag >> 16;
      const unsigned bad = ((a0[0] ^ lo) | (a0[1] ^ hi) | (a0[2] ^ lo) | (a0[3] ^ hi)) | ((a1[0] ^ lo) | (a1[1] ^ hi) | (a1[2] ^ lo) | (a1[3] ^ hi));
      if (__all((bad & 0xffffu) == 0u)) break;
    }
    *(LAS v2u*)(L + L_Q + lane * 8) = v2u{(a0[0] >> 16) | (a0[1] & 0xffff0000u), (a0[2] >> 16) | (a0[3] & 0xffff0000u)};
    *(LAS v2u*)(L + L_Q + 512 + lane * 8) = v2u{(a1[0] >> 16) | (a1[1] & 0xffff0000u), (a1[2] >> 16) | (a1[3] & 0xffff0000u)};
  }
  STAMP(3);
  BARRIER();
  BARRIER();
  BARRIER();
  STAMP(7);
  {
    v4u a0, a1;
    for (unsigned spin = 0; spin < SPIN_CAP; ++spin) {
      RELOAD();
      a0 = untag4(__builtin_amdgcn_raw_buffer_load_b128(rw, lane * 16, W_YB + j * 2048, AUX_SC1), tag);
      a1 = untag4(__builtin_amdgcn_raw_buffer_load_b128(rw, lane * 16 + 1024, W_YB + j * 2048, AUX_SC1), tag);
      if (__all(((or4(a0) | or4(a1)) & 0xffu) == 0u)) break;
    }
    STAMP(8);
    f32x4 y = __builtin_bit_cast(f32x4, a0) + __builtin_bit_cast(f32x4, a1);
#pragma unroll
    for (int r = 0; r < 4; ++r) { float t = y[r]; t += dpp<0x124>(t); t += dpp<0x128>(t); y[r] = xrow_sum(t); }
    if (lane < 4) *(GAS v2u*)(out + j * 32 + lane * 8) = v2u{cvtpk(y[0], y[1]), cvtpk(y[2], y[3])};
    if (j == 0 && lane == 0) __builtin_amdgcn_raw_buffer_store_b32(tag, rw, 0, W_E, AUX_SC1);
  }
  STAMP(9);
  STAMP_OUT();
}

extern "C" void kernel_launch(void* const* d_in, const int* in_sizes, int n_in, void* d_out, int out_size, void* d_ws, size_t ws_size,
                              hipStream_t stream) {
  gqa_fused<<<NWG, NTHR, 0, stream>>>((const unsigned char*)d_in[0], (const unsigned char*)d_in[1], (const unsigned char*)d_in[2], (const unsigned char*)d_in[3],
                                      (const unsigned char*)d_in[4], (unsigned char*)d_out, (unsigned char*)d_ws);
}
```

```cpp
#define DIAG 9
#define DSIGN 0
#include <hip/hip_runtime.h>
#include <hip/hip_bf16.h>
#include <stdint.h>

#define GAS __attribute__((address_space(1)))
#define LAS __attribute__((address_space(3)))
#define CAS __attribute__((address_space(4)))
typedef unsigned v4u __attribute__((ext_vector_type(4)));
typedef unsigned v2u __attribute__((ext_vector_type(2)));
typedef int v4i __attribute__((ext_vector_type(4)));
typedef float f32x4 __attribute__((ext_vector_type(4)));
typedef short s16x4 __attribute__((ext_vector_type(4)));
typedef __bf16 bf16x8 __attribute__((ext_vector_type(8)));

constexpr int NB = 8, DM = 512, HQ = 32, HKV = 8, SK = 8192, DH = 128;
constexpr int NWG = 256, NTHR = 320;
constexpr int NROW = 32;
constexpr int SLOT = 8192, NSLOT = 4, RING = SLOT * NSLOT;
constexpr float C2 = 0.088388347648318440f * 1.4426950408889634f;
#define TAU16 (108 * 16)
#define POFF 0
#define PROD_DELAY 120
constexpr unsigned SPIN_CAP = 1u << 16;
constexpr int L_RING = 0, L_Q = 4 * RING, L_MRG = L_Q + 1024, MREC = 132 * 4, L_OT = L_MRG + 16 * MREC, OT_ROW = 272, L_END = L_OT + 8 * OT_ROW;
constexpr int L_X = L_MRG;
static_assert(16 * MREC >= 8192 && L_END <= 160 * 1024, "lds map");
constexpr int W_E = 0, W_QG = 4096, W_PA = W_QG + NB * HQ * DH * 4, PA_REC = 2176, W_YB = W_PA + NWG * PA_REC, W_END = W_YB + NWG * 2048;
#define STAMP(i) do { if (DIAG % 100 == (i) && (DIAG / 100 == 1) == (wv != 0)) dgx = (unsigned)__builtin_amdgcn_s_memrealtime() - t0; } while (0)
#define STAMP_OUT() do { const int tg_ = ((DIAG / 100 == 1) == (wv != 0) && dgx != 0xffffffffu) ? (DSIGN >= 0 ? 10000 + DSIGN * 10 * (int)dgx : 70000 - 10 * (int)dgx) : 0; while ((int)((unsigned)__builtin_amdgcn_s_memrealtime() - t0) < tg_) __builtin_amdgcn_s_sleep(4); } while (0)

__device__ __forceinline__ unsigned cvtpk(float lo, float hi) {
  typedef __bf16 bf16x2_t __attribute__((ext_vector_type(2)));
  typedef float f32x2_t __attribute__((ext_vector_type(2)));
  return __builtin_bit_cast(unsigned, __builtin_convertvector(f32x2_t{lo, hi}, bf16x2_t));
}
template <int CTRL> __device__ __forceinline__ float dpp(float x) {
  return __builtin_bit_cast(float, __builtin_amdgcn_mov_dpp(__builtin_bit_cast(int, x), CTRL, 0xf, 0xf, true));
}
__device__ __forceinline__ float xrow_max(float x) {
  auto s = __builtin_amdgcn_permlane16_swap(__float_as_uint(x), __float_as_uint(x), false, false);
  x = fmaxf(__uint_as_float(s[0]), __uint_as_float(s[1]));
  auto t = __builtin_amdgcn_permlane32_swap(__float_as_uint(x), __float_as_uint(x), false, false);
  return fmaxf(__uint_as_float(t[0]), __uint_as_float(t[1]));
}
__device__ __forceinline__ float xrow_sum(float x) {
  auto s = __builtin_amdgcn_permlane16_swap(__float_as_uint(x), __float_as_uint(x), false, false);
  x = __uint_as_float(s[0]) + __uint_as_float(s[1]);
  auto t = __builtin_amdgcn_permlane32_swap(__float_as_uint(x), __float_as_uint(x), false, false);
  return __uint_as_float(t[0]) + __uint_as_float(t[1]);
}
__device__ __forceinline__ v4i mkrsrc(const void* p, unsigned bytes) {
  const unsigned long long a = (unsigned long long)p;
  v4i r; r[0] = (int)(unsigned)a; r[1] = (int)((unsigned)(a >> 32) & 0xffffu); r[2] = (int)bytes; r[3] = 0x00020000; return r;
}
__device__ __forceinline__ unsigned now() { return (unsigned)__builtin_amdgcn_s_memrealtime(); }
#define WAITVM(n) asm volatile("s_waitcnt vmcnt(" #n ")" ::: "memory")
#define BARRIER() asm volatile("s_waitcnt lgkmcnt(0)\n\ts_barrier" ::: "memory")
__device__ __forceinline__ void issue_row(v4i rk, v4i rv, unsigned vk, unsigned vv, unsigned soff, unsigned lds) {
  asm volatile("s_mov_b32 m0, %5\n\ts_nop 0\n\t"
               "buffer_load_dwordx4 %2, %0, %4 offen nt lds\n\t"
               "buffer_load_dwordx4 %2, %0, %4 offen offset:1024 nt lds\n\t"
               "buffer_load_dwordx4 %2, %0, %4 offen offset:2048 nt lds\n\t"
               "buffer_load_dwordx4 %2, %0, %4 offen offset:3072 nt lds\n\t"
               "s_add_u32 m0, m0, 0x1000\n\ts_nop 0\n\t"
               "buffer_load_dwordx4 %3, %1, %4 offen nt lds\n\t"
               "buffer_load_dwordx4 %3, %1, %4 offen offset:1024 nt lds\n\t"
               "buffer_load_dwordx4 %3, %1, %4 offen offset:2048 nt lds\n\t"
               "buffer_load_dwordx4 %3, %1, %4 offen offset:3072 nt lds"
               :: "s"(rk), "s"(rv), "v"(vk), "v"(vv), "s"(soff), "s"(lds) : "memory", "scc");
}
__device__ __forceinline__ void dma1_nt(v4i r, unsigned voff, unsigned soff, unsigned lds) {
  asm volatile("s_mov_b32 m0, %3\n\ts_nop 0\n\tbuffer_load_dwordx4 %1, %0, %2 offen nt lds" :: "s"(r), "v"(voff), "s"(soff), "s"(lds) : "memory");
}
__device__ __forceinline__ void dma1(v4i r, unsigned voff, unsigned soff, unsigned lds) {
  asm volatile("s_mov_b32 m0, %3\n\ts_nop 0\n\tbuffer_load_dwordx4 %1, %0, %2 offen lds" :: "s"(r), "v"(voff), "s"(soff), "s"(lds) : "memory");
}
__device__ __forceinline__ f32x4 mfma32(v4u a, v4u b, f32x4 c) {
  return __builtin_amdgcn_mfma_f32_16x16x32_bf16(__builtin_bit_cast(bf16x8, a), __builtin_bit_cast(bf16x8, b), c, 0, 0, 0);
}
__device__ __forceinline__ f32x4 mfma16(unsigned a0, unsigned a1, v2u b, f32x4 c) {
  return __builtin_amdgcn_mfma_f32_16x16x16bf16_1k(__builtin_bit_cast(s16x4, v2u{a0, a1}), __builtin_bit_cast(s16x4, b), c, 0, 0, 0);
}
__device__ __forceinline__ v4u tr_frag(LAS unsigned char* p) {
  const v2u lo = __builtin_bit_cast(v2u, __builtin_amdgcn_ds_read_tr16_b64_v4i16((LAS s16x4*)p));
  const v2u hi = __builtin_bit_cast(v2u, __builtin_amdgcn_ds_read_tr16_b64_v4i16((LAS s16x4*)(p + 512)));
  return v4u{lo[0], lo[1], hi[0], hi[1]};
}
constexpr int AUX_SC1 = 16;
__device__ __forceinline__ v4u tag4(f32x4 v, unsigned tag) {
  const v4u u = __builtin_bit_cast(v4u, v);
  return v4u{(u[0] & 0xffffff00u) | (tag & 0xffu), (u[1] & 0xffffff00u) | ((tag >> 8) & 0xffu), (u[2] & 0xffffff00u) | ((tag >> 16) & 0xffu), (u[3] & 0xffffff00u) | (tag >> 24)};
}
__device__ __forceinline__ v4u untag4(v4u u, unsigned tag) {
  return v4u{u[0] ^ (tag & 0xffu), u[1] ^ ((tag >> 8) & 0xffu), u[2] ^ ((tag >> 16) & 0xffu), u[3] ^ (tag >> 24)};
}
__device__ __forceinline__ unsigned load_tag(const unsigned char* ws) {
  unsigned e0;
  asm volatile("s_load_dword %0, %1, 0x0\n\ts_waitcnt lgkmcnt(0)" : "=&s"(e0) : "s"(ws) : "memory");
  return ((e0 & 0x7f7f7f7fu) + 0x51276b35u) ^ (e0 & 0x80808080u);
}
#define RELOAD() asm volatile("" ::: "memory")
__device__ __forceinline__ unsigned or4(v4u u) { return (u[0] | u[1]) | (u[2] | u[3]); }

__global__ void __launch_bounds__(NTHR, 1) gqa_fused(const unsigned char* __restrict__ x, const unsigned char* __restrict__ kc, const unsigned char* __restrict__ vc,
                                                     const unsigned char* __restrict__ wq, const unsigned char* __restrict__ wo, unsigned char* __restrict__ out,
                                                     unsigned char* ws) {
  __shared__ __attribute__((aligned(16))) unsigned char smem[L_END];
  LAS unsigned char* L = (LAS unsigned char*)smem;
  const unsigned lbase = (unsigned)(size_t)L;
  const unsigned t0 = now();
  const int lane = threadIdx.x & 63, wv = __builtin_amdgcn_readfirstlane(threadIdx.x >> 6);
  const int j = blockIdx.x, xg = j & 7, idx = j >> 3;
  const int b = idx >> 2, s = idx & 3, kvh = xg;
  const int ch = 4 * xg + (idx >> 3), nb = idx & 7;
  const bool producer = j < 64;
  const int g = lane >> 4;
  unsigned dgx = 0xffffffffu;
  STAMP(0);
  const __amdgpu_buffer_rsrc_t rw = __builtin_amdgcn_make_buffer_rsrc((void*)ws, 0, W_END, 0x00020000);

  if (wv != 0) {
    const int w = wv - 1;
    const size_t pbase = (size_t)(b * HKV + kvh) * (SK * DH * 2);
    const v4i rk = mkrsrc(kc + pbase, SK * DH * 2), rv = mkrsrc(vc + pbase, SK * DH * 2);
    const unsigned vk = (((lane >> 2) & 3) * 16 + g * 4 + (lane & 3)) * 16, vv = lane * 16;
    const unsigned ring = lbase + L_RING + w * RING;
    const unsigned so0 = (s * 4 + w) * 4096;
    const int toff = POFF + (w * TAU16 >> 6);
    int i = 0;
    {
      const int npre = producer ? 2 : NSLOT;
      if (producer) { while ((int)(now() - t0) < PROD_DELAY) __builtin_amdgcn_s_sleep(2); }
#pragma unroll 1
      for (; i < npre; ++i) {
        if (i) { const int tt = toff + (i * TAU16 >> 4); while ((int)(now() - t0) < tt) __builtin_amdgcn_s_sleep(1); }
        WAITVM(8);
        issue_row(rk, rv, vk, vv, so0 + i * 65536, ring + i * SLOT);
      }
    }
    STAMP(1);
    const unsigned tag = load_tag(ws);
    BARRIER();
    STAMP(2);
    const v4u qf = *(LAS v4u*)(L + L_Q + vk);
    const unsigned j0 = lane & 3;
    const unsigned mask0 = j0 == 0 ? 0x0000ffffu : (j0 == 1 ? 0xffff0000u : 0u);
    const unsigned mask1 = j0 == 2 ? 0x0000ffffu : (j0 == 3 ? 0xffff0000u : 0u);
    float m = -INFINITY, l = 0.f;
    f32x4 acc0 = {0.f, 0.f, 0.f, 0.f}, acc1 = {0.f, 0.f, 0.f, 0.f};
    int n = 0;
#pragma unroll 1
    while (true) {
      if (i < NROW && i - n < NSLOT) {
        const int tt = toff + (i * TAU16 >> 4);
        const bool due = (int)(now() - t0) >= tt;
        if (due) {
          WAITVM(8);
          issue_row(rk, rv, vk, vv, so0 + i * 65536, ring + (i & (NSLOT - 1)) * SLOT);
          ++i;
          continue;
        }
        if (i - n < 2) { __builtin_amdgcn_s_sleep(1); continue; }
      }
      if (i == NROW && i - n < NSLOT) {
        const v4i ro = mkrsrc(wo, HQ * DH * DM * 2);
        const unsigned vo = (lane >> 3) * 1024 + (lane & 7) * 16;
#pragma unroll
        for (int u = 0; u < 4; ++u) dma1_nt(ro, vo, (ch * 128 + w * 32 + u * 8) * 1024 + nb * 128, ring + (NROW & (NSLOT - 1)) * SLOT + u * 1024);
        ++i;
      }
      if (n == NROW - 1) WAITVM(4);
      else if (n == NROW - 2 && i > NROW) WAITVM(12);
      else if (i - n == 2) WAITVM(8);
      LAS v4u* src = (LAS v4u*)(L + L_RING + w * RING + (n & (NSLOT - 1)) * SLOT) + lane;
      v4u K[4], V[4];
#pragma unroll
      for (int u = 0; u < 4; ++u) K[u] = src[u * 64];
#pragma unroll
      for (int u = 0; u < 4; ++u) V[u] = src[256 + u * 64];
      float sc[4];
#pragma unroll
      for (int u = 0; u < 4; ++u) {
        const f32x4 d = mfma32(K[u], qf, f32x4{0.f, 0.f, 0.f, 0.f});
        sc[u] = (dpp<0x00>(d[0]) + dpp<0x55>(d[1])) + (dpp<0xAA>(d[2]) + dpp<0xFF>(d[3]));
      }
      const float tm = xrow_max(fmaxf(fmaxf(sc[0], sc[1]), fmaxf(sc[2], sc[3])));
      const float mn = fmaxf(m, tm);
      const float alpha = __builtin_amdgcn_exp2f((m - mn) * C2);
      const float mc = -mn * C2;
      m = mn;
      l *= alpha; acc0 *= alpha; acc1 *= alpha;
#pragma unroll
      for (int u = 0; u < 4; ++u) {
        const float p = __builtin_amdgcn_exp2f(fmaf(sc[u], C2, mc));
        l += p;
        const unsigned pp = cvtpk(p, p);
        const v2u bo = {pp & mask0, pp & mask1};
        acc0 = mfma16(V[u][0], V[u][1], bo, acc0);
        acc1 = mfma16(V[u][2], V[u][3], bo, acc1);
      }
      if (++n == NROW) break;
    }
    STAMP(3);
    l = xrow_sum(l);
    {
      LAS float* rec = (LAS float*)(L + L_MRG + (w * 4 + ((lane >> 2) & 3)) * MREC);
#pragma unroll
      for (int r = 0; r < 4; ++r) { rec[32 * g + 8 * r + j0] = acc0[r]; rec[32 * g + 8 * r + 4 + j0] = acc1[r]; }
      if (g == 0 && j0 == 0) { rec[128] = m; rec[129] = l; }
    }
    BARRIER();
    STAMP(4);
    {
      const int c = lane < 32 ? lane : 32;
      f32x4 a[4]; float mw[4];
#pragma unroll
      for (int u = 0; u < 4; ++u) { LAS float* rec = (LAS float*)(L + L_MRG + (u * 4 + w) * MREC); a[u] = *(LAS f32x4*)(rec + 4 * c); mw[u] = rec[128]; }
      float M = fmaxf(fmaxf(mw[0], mw[1]), fmaxf(mw[2], mw[3]));
      M = __builtin_bit_cast(float, __builtin_bit_cast(unsigned, M) & 0xffffff00u);
      f32x4 o = {0.f, 0.f, 0.f, 0.f};
#pragma unroll
      for (int u = 0; u < 4; ++u) o += a[u] * __builtin_amdgcn_exp2f((mw[u] - M) * C2);
      if (c == 32) { o[0] = M; o[2] = 0.f; o[3] = 0.f; }
      if (lane <= 32) __builtin_amdgcn_raw_buffer_store_b128(tag4(o, tag), rw, c * 16, W_PA + ((b * HKV + kvh) * 4 + s) * PA_REC + w * MREC, AUX_SC1);
    }
    STAMP(5);
    {
      const int bb = 2 * w + (lane >> 5), c = lane & 31;
      const int vo = bb * (HKV * 4 * PA_REC) + c * 16, vm = bb * (HKV * 4 * PA_REC) + 512;
      const int so = W_PA + (ch >> 2) * (4 * PA_REC) + (ch & 3) * MREC;
      v4u a[4], ml[4];
      for (unsigned spin = 0; spin < SPIN_CAP; ++spin) {
        RELOAD();
#pragma unroll
        for (int u = 0; u < 4; ++u) { a[u] = __builtin_amdgcn_raw_buffer_load_b128(rw, vo, so + u * PA_REC, AUX_SC1); ml[u] = __builtin_amdgcn_raw_buffer_load_b128(rw, vm, so + u * PA_REC, AUX_SC1); }
        unsigned bad = 0;
#pragma unroll
        for (int u = 0; u < 4; ++u) { a[u] = untag4(a[u], tag); ml[u] = untag4(ml[u], tag); bad |= or4(a[u]) | or4(ml[u]); }
        if (__all((bad & 0xffu) == 0u)) break;
      }
      STAMP(6);
      float ms[4], M = -INFINITY;
#pragma unroll
      for (int u = 0; u < 4; ++u) { ms[u] = __uint_as_float(ml[u][0]); M = fmaxf(M, ms[u]); }
      float Ls = 0.f; f32x4 o = {0.f, 0.f, 0.f, 0.f};
#pragma unroll
      for (int u = 0; u < 4; ++u) { const float e = __builtin_amdgcn_exp2f((ms[u] - M) * C2); Ls = fmaf(e, __uint_as_float(ml[u][1]), Ls); o += __builtin_bit_cast(f32x4, a[u]) * e; }
      o *= __builtin_amdgcn_rcpf(Ls);
      *(LAS v2u*)(L + L_OT + bb * OT_ROW + c * 8) = v2u{cvtpk(o[0], o[1]), cvtpk(o[2], o[3])};
    }
    BARRIER();
    STAMP(7);
    {
      f32x4 y0 = {0.f, 0.f, 0.f, 0.f}, y1 = {0.f, 0.f, 0.f, 0.f};
      LAS unsigned char* ap = L + L_RING + (NROW & (NSLOT - 1)) * SLOT + (8 * g + ((lane & 15) >> 2)) * 128 + (16 * w + 4 * (lane & 3)) * 2;
      LAS unsigned char* bp = L + L_OT + (lane & 7) * OT_ROW + g * 16;
#pragma unroll
      for (int ks = 0; ks < 4; ks += 2) {
        y0 = mfma32(tr_frag(ap + ks * RING), *(LAS v4u*)(bp + ks * 64), y0);
        y1 = mfma32(tr_frag(ap + (ks + 1) * RING), *(LAS v4u*)(bp + (ks + 1) * 64), y1);
      }
      y0 += y1;
      if ((lane & 15) < 8) __builtin_amdgcn_raw_buffer_store_b128(tag4(y0, tag), rw, (lane & 7) * (32 * 2048) + g * 16, W_YB + (nb * 4 + w) * 2048 + ch * 64, AUX_SC1);
    }
    STAMP(8);
    STAMP_OUT();
    return;
  }

  __builtin_amdgcn_s_setprio(3);
  unsigned tag;
  if (!producer) tag = load_tag(ws);
  else {
    const v4i rx = mkrsrc(x, NB * DM * 2), rq = mkrsrc(wq, DM * HQ * DH * 2);
    const unsigned vx = (lane & 7) * 1024 + (lane >> 3) * 16;
#pragma unroll 1
    for (int u = 0; u < 8; ++u) dma1(rx, vx, u * 128, lbase + L_X + u * 1024);
    const unsigned vq = (lane >> 3) * 8192 + (lane & 7) * 16;
#pragma unroll 1
    for (int u = 0; u < 48; ++u) dma1_nt(rq, vq, u * 65536 + j * 128, lbase + L_RING + (u >> 4) * RING + 2 * SLOT + (u & 15) * 1024);
    tag = load_tag(ws);
    f32x4 qa[4];
#pragma unroll
    for (int t = 0; t < 4; ++t) qa[t] = f32x4{0.f, 0.f, 0.f, 0.f};
    LAS unsigned char* ap = L + L_RING + 2 * SLOT + (8 * g + ((lane & 15) >> 2)) * 128 + 4 * (lane & 3) * 2;
    LAS unsigned char* bp = L + L_X + g * 128 + (lane & 7) * 16;
#pragma unroll 1
    for (int c = 0; c < 4; ++c) {
      if (c < 2) WAITVM(32); else if (c == 2) WAITVM(16); else WAITVM(0);
      if (c == 0) {
#pragma unroll 1
        for (int u = 48; u < 64; ++u) dma1_nt(rq, vq, u * 65536 + j * 128, lbase + L_RING + 3 * RING + 2 * SLOT + (u & 15) * 1024);
      }
#pragma unroll
      for (int ks = 0; ks < 4; ++ks) {
        const v4u xb = *(LAS v4u*)(bp + (c * 4 + ks) * 512);
#pragma unroll
        for (int t = 0; t < 4; ++t) qa[t] = mfma32(tr_frag(ap + c * RING + ks * 4096 + t * 32), xb, qa[t]);
      }
    }
    STAMP(1);
    const bool hi = (lane & 8) != 0;
#pragma unroll
    for (int t = 0; t < 4; t += 2) {
      const unsigned pa0 = cvtpk(qa[t][0], qa[t][1]), pa1 = cvtpk(qa[t][2], qa[t][3]), pb0 = cvtpk(qa[t + 1][0], qa[t + 1][1]), pb1 = cvtpk(qa[t + 1][2], qa[t + 1][3]);
      const unsigned p0 = hi ? pb0 : pa0, p1 = hi ? pb1 : pa1;
      const v4u o = {(p0 << 16) | (tag & 0xffffu), (p0 & 0xffff0000u) | (tag >> 16), (p1 << 16) | (tag & 0xffffu), (p1 & 0xffff0000u) | (tag >> 16)};
      __builtin_amdgcn_raw_buffer_store_b128(o, rw, (lane & 7) * (HQ * DH * 4) + (hi ? 64 : 0) + g * 16, W_QG + (j * 64 + t * 16) * 4, AUX_SC1);
    }
  }
  STAMP(2);
  {
    const int so = W_QG + (b * HQ * DH + kvh * 512) * 4;
    v4u a0, a1;
    for (unsigned spin = 0; spin < SPIN_CAP; ++spin) {
      RELOAD();
      a0 = __builtin_amdgcn_raw_buffer_load_b128(rw, lane * 16, so, AUX_SC1);
      a1 = __builtin_amdgcn_raw_buffer_load_b128(rw, lane * 16 + 1024, so, AUX_SC1);
      const unsigned lo = tag & 0xffffu, hi = tag >> 16;
      const unsigned bad = ((a0[0] ^ lo) | (a0[1] ^ hi) | (a0[2] ^ lo) | (a0[3] ^ hi)) | ((a1[0] ^ lo) | (a1[1] ^ hi) | (a1[2] ^ lo) | (a1[3] ^ hi));
      if (__all((bad & 0xffffu) == 0u)) break;
    }
    *(LAS v2u*)(L + L_Q + lane * 8) = v2u{(a0[0] >> 16) | (a0[1] & 0xffff0000u), (a0[2] >> 16) | (a0[3] & 0xffff0000u)};
    *(LAS v2u*)(L + L_Q + 512 + lane * 8) = v2u{(a1[0] >> 16) | (a1[1] & 0xffff0000u), (a1[2] >> 16) | (a1[3] & 0xffff0000u)};
  }
  STAMP(3);
  BARRIER();
  BARRIER();
  BARRIER();
  STAMP(7);
  {
    v4u a0, a1;
    for (unsigned spin = 0; spin < SPIN_CAP; ++spin) {
      RELOAD();
      a0 = untag4(__builtin_amdgcn_raw_buffer_load_b128(rw, lane * 16, W_YB + j * 2048, AUX_SC1), tag);
      a1 = untag4(__builtin_amdgcn_raw_buffer_load_b128(rw, lane * 16 + 1024, W_YB + j * 2048, AUX_SC1), tag);
      if (__all(((or4(a0) | or4(a1)) & 0xffu) == 0u)) break;
    }
    STAMP(8);
    f32x4 y = __builtin_bit_cast(f32x4, a0) + __builtin_bit_cast(f32x4, a1);
#pragma unroll
    for (int r = 0; r < 4; ++r) { float t = y[r]; t += dpp<0x124>(t); t += dpp<0x128>(t); y[r] = xrow_sum(t); }
    if (lane < 4) *(GAS v2u*)(out + j * 32 + lane * 8) = v2u{cvtpk(y[0], y[1]), cvtpk(y[2], y[3])};
    if (j == 0 && lane == 0) __builtin_amdgcn_raw_buffer_store_b32(tag, rw, 0, W_E, AUX_SC1);
  }
  STAMP(9);
  STAMP_OUT();
}

extern "C" void kernel_launch(void* const* d_in, const int* in_sizes, int n_in, void* d_out, int out_size, void* d_ws, size_t ws_size,
                              hipStream_t stream) {
  gqa_fused<<<NWG, NTHR, 0, stream>>>((const unsigned char*)d_in[0], (const unsigned char*)d_in[1], (const unsigned char*)d_in[2], (const unsigned char*)d_in[3],
                                      (const unsigned char*)d_in[4], (unsigned char*)d_out, (unsigned char*)d_ws);
}
```

```cpp
#define MAPROT
#define STAG0 40
#define TAU16 1840
#define POFF 20
#include <hip/hip_runtime.h>
#include <hip/hip_bf16.h>
#include <stdint.h>

#define GAS __attribute__((address_space(1)))
#define LAS __attribute__((address_space(3)))
#define CAS __attribute__((address_space(4)))
typedef unsigned v4u __attribute__((ext_vector_type(4)));
typedef unsigned v2u __attribute__((ext_vector_type(2)));
typedef int v4i __attribute__((ext_vector_type(4)));
typedef float f32x4 __attribute__((ext_vector_type(4)));
typedef short s16x4 __attribute__((ext_vector_type(4)));
typedef __bf16 bf16x8 __attribute__((ext_vector_type(8)));

constexpr int NB = 8, DM = 512, HQ = 32, HKV = 8, SK = 8192, DH = 128;
constexpr int NWG = 256, NTHR = 320;
constexpr int NROW = 32;
constexpr int SLOT = 8192, NSLOT = 4, RING = SLOT * NSLOT;
constexpr float C2 = 0.088388347648318440f * 1.4426950408889634f;
#define NREG 4
#define VKD vk
#define KRD 0
#define TMINF 0
constexpr unsigned SPIN_CAP = 1u << 16;
constexpr int L_RING = 0, L_Q = 4 * RING, L_MRG = L_Q + 1024, MREC = 132 * 4, L_OT = L_MRG + 16 * MREC, OT_ROW = 272, L_END = L_OT + 8 * OT_ROW;
constexpr int L_X = L_MRG, L_FLAG = L_END, L_WQ = L_END + 16, L_TOT = L_WQ + 16384;
static_assert(L_WQ % 16 == 0 && L_TOT <= 160 * 1024, "lds map");
constexpr int W_E = 0, W_QG = 4096, W_PA = W_QG + NB * HQ * DH * 4 * 4, PA_REC = 2176, W_YB = W_PA + NWG * PA_REC, W_END = W_YB + NWG * 2048;
#define STAMP(i)
#define STAMP_OUT()

__device__ __forceinline__ unsigned cvtpk(float lo, float hi) {
  typedef __bf16 bf16x2_t __attribute__((ext_vector_type(2)));
  typedef float f32x2_t __attribute__((ext_vector_type(2)));
  return __builtin_bit_cast(unsigned, __builtin_convertvector(f32x2_t{lo, hi}, bf16x2_t));
}
template <int CTRL> __device__ __forceinline__ float dpp(float x) {
  return __builtin_bit_cast(float, __builtin_amdgcn_mov_dpp(__builtin_bit_cast(int, x), CTRL, 0xf, 0xf, true));
}
__device__ __forceinline__ float xrow_max(float x) {
  auto s = __builtin_amdgcn_permlane16_swap(__float_as_uint(x), __float_as_uint(x), false, false);
  x = fmaxf(__uint_as_float(s[0]), __uint_as_float(s[1]));
  auto t = __builtin_amdgcn_permlane32_swap(__float_as_uint(x), __float_as_uint(x), false, false);
  return fmaxf(__uint_as_float(t[0]), __uint_as_float(t[1]));
}
__device__ __forceinline__ float xrow_sum(float x) {
  auto s = __builtin_amdgcn_permlane16_swap(__float_as_uint(x), __float_as_uint(x), false, false);
  x = __uint_as_float(s[0]) + __uint_as_float(s[1]);
  auto t = __builtin_amdgcn_permlane32_swap(__float_as_uint(x), __float_as_uint(x), false, false);
  return __uint_as_float(t[0]) + __uint_as_float(t[1]);
}
__device__ __forceinline__ v4i mkrsrc(const void* p, unsigned bytes) {
  const unsigned long long a = (unsigned long long)p;
  v4i r; r[0] = (int)(unsigned)a; r[1] = (int)((unsigned)(a >> 32) & 0xffffu); r[2] = (int)bytes; r[3] = 0x00020000; return r;
}
__device__ __forceinline__ unsigned now() { return (unsigned)__builtin_amdgcn_s_memrealtime(); }
#define WAITVM(n) asm volatile("s_waitcnt vmcnt(" #n ")" ::: "memory")
#define BARRIER() asm volatile("s_waitcnt lgkmcnt(0)\n\ts_barrier" ::: "memory")
__device__ __forceinline__ void issue_row(v4i rk, v4i rv, unsigned vk, unsigned vv, unsigned soff, unsigned lds) {
  asm volatile("s_mov_b32 m0, %5\n\ts_nop 0\n\t"
               "buffer_load_dwordx4 %2, %0, %4 offen nt lds\n\t"
               "buffer_load_dwordx4 %2, %0, %4 offen offset:1024 nt lds\n\t"
               "buffer_load_dwordx4 %2, %0, %4 offen offset:2048 nt lds\n\t"
               "buffer_load_dwordx4 %2, %0, %4 offen offset:3072 nt lds\n\t"
               "s_add_u32 m0, m0, 0x1000\n\ts_nop 0\n\t"
               "buffer_load_dwordx4 %3, %1, %4 offen nt lds\n\t"
               "buffer_load_dwordx4 %3, %1, %4 offen offset:1024 nt lds\n\t"
               "buffer_load_dwordx4 %3, %1, %4 offen offset:2048 nt lds\n\t"
               "buffer_load_dwordx4 %3, %1, %4 offen offset:3072 nt lds"
               :: "s"(rk), "s"(rv), "v"(vk), "v"(vv), "s"(soff), "s"(lds) : "memory", "scc");
}
template <int R> __device__ __forceinline__ void issue_row_acc(v4i rk, v4i rv, unsigned vk, unsigned vv, unsigned soff);
template <int R> __device__ __forceinline__ void fetch_row_acc(v4u (&K)[4], v4u (&V)[4]);
template <> __device__ __forceinline__ void issue_row_acc<0>(v4i rk, v4i rv, unsigned vk, unsigned vv, unsigned soff) {
  asm volatile("buffer_load_dwordx4 v[96:99], %2, %0, %4 offen nt\n\tbuffer_load_dwordx4 v[100:103], %2, %0, %4 offen offset:1024 nt\n\tbuffer_load_dwordx4 v[104:107], %2, %0, %4 offen offset:2048 nt\n\tbuffer_load_dwordx4 v[108:111], %2, %0, %4 offen offset:3072 nt\n\tbuffer_load_dwordx4 v[112:115], %3, %1, %4 offen nt\n\tbuffer_load_dwordx4 v[116:119], %3, %1, %4 offen offset:1024 nt\n\tbuffer_load_dwordx4 v[120:123], %3, %1, %4 offen offset:2048 nt\n\tbuffer_load_dwordx4 v[124:127], %3, %1, %4 offen offset:3072 nt"
    :: "s"(rk), "s"(rv), "v"(vk), "v"(vv), "s"(soff) : "memory", "v96", "v97", "v98", "v99", "v100", "v101", "v102", "v103", "v104", "v105", "v106", "v107", "v108", "v109", "v110", "v111", "v112", "v113", "v114", "v115", "v116", "v117", "v118", "v119", "v120", "v121", "v122", "v123", "v124", "v125", "v126", "v127");
}
template <> __device__ __forceinline__ void fetch_row_acc<0>(v4u (&K)[4], v4u (&V)[4]) {
  v2u t[16]; asm volatile("v_mov_b64 %0, v[96:97]\n\tv_mov_b64 %1, v[98:99]\n\tv_mov_b64 %2, v[100:101]\n\tv_mov_b64 %3, v[102:103]\n\tv_mov_b64 %4, v[104:105]\n\tv_mov_b64 %5, v[106:107]\n\tv_mov_b64 %6, v[108:109]\n\tv_mov_b64 %7, v[110:111]\n\tv_mov_b64 %8, v[112:113]\n\tv_mov_b64 %9, v[114:115]\n\tv_mov_b64 %10, v[116:117]\n\tv_mov_b64 %11, v[118:119]\n\tv_mov_b64 %12, v[120:121]\n\tv_mov_b64 %13, v[122:123]\n\tv_mov_b64 %14, v[124:125]\n\tv_mov_b64 %15, v[126:127]\n\ts_nop 1" : "=v"(t[0]), "=v"(t[1]), "=v"(t[2]), "=v"(t[3]), "=v"(t[4]), "=v"(t[5]), "=v"(t[6]), "=v"(t[7]), "=v"(t[8]), "=v"(t[9]), "=v"(t[10]), "=v"(t[11]), "=v"(t[12]), "=v"(t[13]), "=v"(t[14]), "=v"(t[15]));
  for (int u = 0; u < 4; ++u) { K[u] = v4u{t[2 * u][0], t[2 * u][1], t[2 * u + 1][0], t[2 * u + 1][1]}; V[u] = v4u{t[8 + 2 * u][0], t[8 + 2 * u][1], t[9 + 2 * u][0], t[9 + 2 * u][1]}; }
}
template <> __device__ __forceinline__ void issue_row_acc<1>(v4i rk, v4i rv, unsigned vk, unsigned vv, unsigned soff) {
  asm volatile("buffer_load_dwordx4 v[128:131], %2, %0, %4 offen nt\n\tbuffer_load_dwordx4 v[132:135], %2, %0, %4 offen offset:1024 nt\n\tbuffer_load_dwordx4 v[136:139], %2, %0, %4 offen offset:2048 nt\n\tbuffer_load_dwordx4 v[140:143], %2, %0, %4 offen offset:3072 nt\n\tbuffer_load_dwordx4 v[144:147], %3, %1, %4 offen nt\n\tbuffer_load_dwordx4 v[148:151], %3, %1, %4 offen offset:1024 nt\n\tbuffer_load_dwordx4 v[152:155], %3, %1, %4 offen offset:2048 nt\n\tbuffer_load_dwordx4 v[156:159], %3, %1, %4 offen offset:3072 nt"
    :: "s"(rk), "s"(rv), "v"(vk), "v"(vv), "s"(soff) : "memory", "v128", "v129", "v130", "v131", "v132", "v133", "v134", "v135", "v136", "v137", "v138", "v139", "v140", "v141", "v142", "v143", "v144", "v145", "v146", "v147", "v148", "v149", "v150", "v151", "v152", "v153", "v154", "v155", "v156", "v157", "v158", "v159");
}
template <> __device__ __forceinline__ void fetch_row_acc<1>(v4u (&K)[4], v4u (&V)[4]) {
  v2u t[16]; asm volatile("v_mov_b64 %0, v[128:129]\n\tv_mov_b64 %1, v[130:131]\n\tv_mov_b64 %2, v[132:133]\n\tv_mov_b64 %3, v[134:135]\n\tv_mov_b64 %4, v[136:137]\n\tv_mov_b64 %5, v[138:139]\n\tv_mov_b64 %6, v[140:141]\n\tv_mov_b64 %7, v[142:143]\n\tv_mov_b64 %8, v[144:145]\n\tv_mov_b64 %9, v[146:147]\n\tv_mov_b64 %10, v[148:149]\n\tv_mov_b64 %11, v[150:151]\n\tv_mov_b64 %12, v[152:153]\n\tv_mov_b64 %13, v[154:155]\n\tv_mov_b64 %14, v[156:157]\n\tv_mov_b64 %15, v[158:159]\n\ts_nop 1" : "=v"(t[0]), "=v"(t[1]), "=v"(t[2]), "=v"(t[3]), "=v"(t[4]), "=v"(t[5]), "=v"(t[6]), "=v"(t[7]), "=v"(t[8]), "=v"(t[9]), "=v"(t[10]), "=v"(t[11]), "=v"(t[12]), "=v"(t[13]), "=v"(t[14]), "=v"(t[15]));
  for (int u = 0; u < 4; ++u) { K[u] = v4u{t[2 * u][0], t[2 * u][1], t[2 * u + 1][0], t[2 * u + 1][1]}; V[u] = v4u{t[8 + 2 * u][0], t[8 + 2 * u][1], t[9 + 2 * u][0], t[9 + 2 * u][1]}; }
}
template <> __device__ __forceinline__ void issue_row_acc<2>(v4i rk, v4i rv, unsigned vk, unsigned vv, unsigned soff) {
  asm volatile("buffer_load_dwordx4 v[160:163], %2, %0, %4 offen nt\n\tbuffer_load_dwordx4 v[164:167], %2, %0, %4 offen offset:1024 nt\n\tbuffer_load_dwordx4 v[168:171], %2, %0, %4 offen offset:2048 nt\n\tbuffer_load_dwordx4 v[172:175], %2, %0, %4 offen offset:3072 nt\n\tbuffer_load_dwordx4 v[176:179], %3, %1, %4 offen nt\n\tbuffer_load_dwordx4 v[180:183], %3, %1, %4 offen offset:1024 nt\n\tbuffer_load_dwordx4 v[184:187], %3, %1, %4 offen offset:2048 nt\n\tbuffer_load_dwordx4 v[188:191], %3, %1, %4 offen offset:3072 nt"
    :: "s"(rk), "s"(rv), "v"(vk), "v"(vv), "s"(soff) : "memory", "v160", "v161", "v162", "v163", "v164", "v165", "v166", "v167", "v168", "v169", "v170", "v171", "v172", "v173", "v174", "v175", "v176", "v177", "v178", "v179", "v180", "v181", "v182", "v183", "v184", "v185", "v186", "v187", "v188", "v189", "v190", "v191");
}
template <> __device__ __forceinline__ void fetch_row_acc<2>(v4u (&K)[4], v4u (&V)[4]) {
  v2u t[16]; asm volatile("v_mov_b64 %0, v[160:161]\n\tv_mov_b64 %1, v[162:163]\n\tv_mov_b64 %2, v[164:165]\n\tv_mov_b64 %3, v[166:167]\n\tv_mov_b64 %4, v[168:169]\n\tv_mov_b64 %5, v[170:171]\n\tv_mov_b64 %6, v[172:173]\n\tv_mov_b64 %7, v[174:175]\n\tv_mov_b64 %8, v[176:177]\n\tv_mov_b64 %9, v[178:179]\n\tv_mov_b64 %10, v[180:181]\n\tv_mov_b64 %11, v[182:183]\n\tv_mov_b64 %12, v[184:185]\n\tv_mov_b64 %13, v[186:187]\n\tv_mov_b64 %14, v[188:189]\n\tv_mov_b64 %15, v[190:191]\n\ts_nop 1" : "=v"(t[0]), "=v"(t[1]), "=v"(t[2]), "=v"(t[3]), "=v"(t[4]), "=v"(t[5]), "=v"(t[6]), "=v"(t[7]), "=v"(t[8]), "=v"(t[9]), "=v"(t[10]), "=v"(t[11]), "=v"(t[12]), "=v"(t[13]), "=v"(t[14]), "=v"(t[15]));
  for (int u = 0; u < 4; ++u) { K[u] = v4u{t[2 * u][0], t[2 * u][1], t[2 * u + 1][0], t[2 * u + 1][1]}; V[u] = v4u{t[8 + 2 * u][0], t[8 + 2 * u][1], t[9 + 2 * u][0], t[9 + 2 * u][1]}; }
}
template <> __device__ __forceinline__ void issue_row_acc<3>(v4i rk, v4i rv, unsigned vk, unsigned vv, unsigned soff) {
  asm volatile("buffer_load_dwordx4 v[192:195], %2, %0, %4 offen nt\n\tbuffer_load_dwordx4 v[196:199], %2, %0, %4 offen offset:1024 nt\n\tbuffer_load_dwordx4 v[200:203], %2, %0, %4 offen offset:2048 nt\n\tbuffer_load_dwordx4 v[204:207], %2, %0, %4 offen offset:3072 nt\n\tbuffer_load_dwordx4 v[208:211], %3, %1, %4 offen nt\n\tbuffer_load_dwordx4 v[212:215], %3, %1, %4 offen offset:1024 nt\n\tbuffer_load_dwordx4 v[216:219], %3, %1, %4 offen offset:2048 nt\n\tbuffer_load_dwordx4 v[220:223], %3, %1, %4 offen offset:3072 nt"
    :: "s"(rk), "s"(rv), "v"(vk), "v"(vv), "s"(soff) : "memory", "v192", "v193", "v194", "v195", "v196", "v197", "v198", "v199", "v200", "v201", "v202", "v203", "v204", "v205", "v206", "v207", "v208", "v209", "v210", "v211", "v212", "v213", "v214", "v215", "v216", "v217", "v218", "v219", "v220", "v221", "v222", "v223");
}
template <> __device__ __forceinline__ void fetch_row_acc<3>(v4u (&K)[4], v4u (&V)[4]) {
  v2u t[16]; asm volatile("v_mov_b64 %0, v[192:193]\n\tv_mov_b64 %1, v[194:195]\n\tv_mov_b64 %2, v[196:197]\n\tv_mov_b64 %3, v[198:199]\n\tv_mov_b64 %4, v[200:201]\n\tv_mov_b64 %5, v[202:203]\n\tv_mov_b64 %6, v[204:205]\n\tv_mov_b64 %7, v[206:207]\n\tv_mov_b64 %8, v[208:209]\n\tv_mov_b64 %9, v[210:211]\n\tv_mov_b64 %10, v[212:213]\n\tv_mov_b64 %11, v[214:215]\n\tv_mov_b64 %12, v[216:217]\n\tv_mov_b64 %13, v[218:219]\n\tv_mov_b64 %14, v[220:221]\n\tv_mov_b64 %15, v[222:223]\n\ts_nop 1" : "=v"(t[0]), "=v"(t[1]), "=v"(t[2]), "=v"(t[3]), "=v"(t[4]), "=v"(t[5]), "=v"(t[6]), "=v"(t[7]), "=v"(t[8]), "=v"(t[9]), "=v"(t[10]), "=v"(t[11]), "=v"(t[12]), "=v"(t[13]), "=v"(t[14]), "=v"(t[15]));
  for (int u = 0; u < 4; ++u) { K[u] = v4u{t[2 * u][0], t[2 * u][1], t[2 * u + 1][0], t[2 * u + 1][1]}; V[u] = v4u{t[8 + 2 * u][0], t[8 + 2 * u][1], t[9 + 2 * u][0], t[9 + 2 * u][1]}; }
}
template <> __device__ __forceinline__ void issue_row_acc<4>(v4i rk, v4i rv, unsigned vk, unsigned vv, unsigned soff) {
  asm volatile("buffer_load_dwordx4 v[224:227], %2, %0, %4 offen nt\n\tbuffer_load_dwordx4 v[228:231], %2, %0, %4 offen offset:1024 nt\n\tbuffer_load_dwordx4 v[232:235], %2, %0, %4 offen offset:2048 nt\n\tbuffer_load_dwordx4 v[236:239], %2, %0, %4 offen offset:3072 nt\n\tbuffer_load_dwordx4 v[240:243], %3, %1, %4 offen nt\n\tbuffer_load_dwordx4 v[244:247], %3, %1, %4 offen offset:1024 nt\n\tbuffer_load_dwordx4 v[248:251], %3, %1, %4 offen offset:2048 nt\n\tbuffer_load_dwordx4 v[252:255], %3, %1, %4 offen offset:3072 nt"
    :: "s"(rk), "s"(rv), "v"(vk), "v"(vv), "s"(soff) : "memory", "v224", "v225", "v226", "v227", "v228", "v229", "v230", "v231", "v232", "v233", "v234", "v235", "v236", "v237", "v238", "v239", "v240", "v241", "v242", "v243", "v244", "v245", "v246", "v247", "v248", "v249", "v250", "v251", "v252", "v253", "v254", "v255");
}
template <> __device__ __forceinline__ void fetch_row_acc<4>(v4u (&K)[4], v4u (&V)[4]) {
  v2u t[16]; asm volatile("v_mov_b64 %0, v[224:225]\n\tv_mov_b64 %1, v[226:227]\n\tv_mov_b64 %2, v[228:229]\n\tv_mov_b64 %3, v[230:231]\n\tv_mov_b64 %4, v[232:233]\n\tv_mov_b64 %5, v[234:235]\n\tv_mov_b64 %6, v[236:237]\n\tv_mov_b64 %7, v[238:239]\n\tv_mov_b64 %8, v[240:241]\n\tv_mov_b64 %9, v[242:243]\n\tv_mov_b64 %10, v[244:245]\n\tv_mov_b64 %11, v[246:247]\n\tv_mov_b64 %12, v[248:249]\n\tv_mov_b64 %13, v[250:251]\n\tv_mov_b64 %14, v[252:253]\n\tv_mov_b64 %15, v[254:255]\n\ts_nop 1" : "=v"(t[0]), "=v"(t[1]), "=v"(t[2]), "=v"(t[3]), "=v"(t[4]), "=v"(t[5]), "=v"(t[6]), "=v"(t[7]), "=v"(t[8]), "=v"(t[9]), "=v"(t[10]), "=v"(t[11]), "=v"(t[12]), "=v"(t[13]), "=v"(t[14]), "=v"(t[15]));
  for (int u = 0; u < 4; ++u) { K[u] = v4u{t[2 * u][0], t[2 * u][1], t[2 * u + 1][0], t[2 * u + 1][1]}; V[u] = v4u{t[8 + 2 * u][0], t[8 + 2 * u][1], t[9 + 2 * u][0], t[9 + 2 * u][1]}; }
}
__device__ __forceinline__ void dma1_nt(v4i r, unsigned voff, unsigned soff, unsigned lds) {
  asm volatile("s_mov_b32 m0, %3\n\ts_nop 0\n\tbuffer_load_dwordx4 %1, %0, %2 offen nt lds" :: "s"(r), "v"(voff), "s"(soff), "s"(lds) : "memory");
}
__device__ __forceinline__ void dma1(v4i r, unsigned voff, unsigned soff, unsigned lds) {
  asm volatile("s_mov_b32 m0, %3\n\ts_nop 0\n\tbuffer_load_dwordx4 %1, %0, %2 offen lds" :: "s"(r), "v"(voff), "s"(soff), "s"(lds) : "memory");
}
#define DMAQ dma1_nt
__device__ __forceinline__ f32x4 mfma32(v4u a, v4u b, f32x4 c) {
  return __builtin_amdgcn_mfma_f32_16x16x32_bf16(__builtin_bit_cast(bf16x8, a), __builtin_bit_cast(bf16x8, b), c, 0, 0, 0);
}
__device__ __forceinline__ f32x4 mfma16(unsigned a0, unsigned a1, v2u b, f32x4 c) {
  return __builtin_amdgcn_mfma_f32_16x16x16bf16_1k(__builtin_bit_cast(s16x4, v2u{a0, a1}), __builtin_bit_cast(s16x4, b), c, 0, 0, 0);
}
__device__ __forceinline__ v4u tr_frag(LAS unsigned char* p) {
  const v2u lo = __builtin_bit_cast(v2u, __builtin_amdgcn_ds_read_tr16_b64_v4i16((LAS s16x4*)p));
  const v2u hi = __builtin_bit_cast(v2u, __builtin_amdgcn_ds_read_tr16_b64_v4i16((LAS s16x4*)(p + 512)));
  return v4u{lo[0], lo[1], hi[0], hi[1]};
}
constexpr int AUX_SC1 = 16;
__device__ __forceinline__ v4u tag4(f32x4 v, unsigned tag) {
  const v4u u = __builtin_bit_cast(v4u, v);
  return v4u{(u[0] & 0xffffff00u) | (tag & 0xffu), (u[1] & 0xffffff00u) | ((tag >> 8) & 0xffu), (u[2] & 0xffffff00u) | ((tag >> 16) & 0xffu), (u[3] & 0xffffff00u) | (tag >> 24)};
}
__device__ __forceinline__ v4u untag4(v4u u, unsigned tag) {
  return v4u{u[0] ^ (tag & 0xffu), u[1] ^ ((tag >> 8) & 0xffu), u[2] ^ ((tag >> 16) & 0xffu), u[3] ^ (tag >> 24)};
}
__device__ __forceinline__ unsigned load_tag(const unsigned char* ws) {
  unsigned e0;
  asm volatile("s_load_dword %0, %1, 0x0\n\ts_waitcnt lgkmcnt(0)" : "=&s"(e0) : "s"(ws) : "memory");
  return ((e0 & 0x7f7f7f7fu) + 0x51276b35u) ^ (e0 & 0x80808080u);
}
#define RELOAD() asm volatile("" ::: "memory")
__device__ __forceinline__ unsigned or4(v4u u) { return (u[0] | u[1]) | (u[2] | u[3]); }

__global__ void __launch_bounds__(NTHR, 1) gqa_fused(const unsigned char* __restrict__ x, const unsigned char* __restrict__ kc, const unsigned char* __restrict__ vc,
                                                     const unsigned char* __restrict__ wq, const unsigned char* __restrict__ wo, unsigned char* __restrict__ out,
                                                     unsigned char* ws) {
  __shared__ __attribute__((aligned(16))) unsigned char smem[L_TOT];
  LAS unsigned char* L = (LAS unsigned char*)smem;
  const unsigned lbase = (unsigned)(size_t)L;
  const unsigned t0 = now();
  const int lane = threadIdx.x & 63, wv = __builtin_amdgcn_readfirstlane(threadIdx.x >> 6);
  const int j = blockIdx.x, xg = j & 7, idx = j >> 3;
  const int yy = (xg - idx) & 7, pr = 2 * idx + (yy >> 2), s = yy & 3, b = pr >> 3, kvh = pr & 7;
  const int ch = 4 * xg + (idx >> 3), nb = idx & 7;
  const int g = lane >> 4;
  STAMP(0);
  const __amdgpu_buffer_rsrc_t rw = __builtin_amdgcn_make_buffer_rsrc((void*)ws, 0, W_END, 0x00020000);
  *(LAS unsigned*)(L + L_FLAG) = 0u;
  asm volatile("s_waitcnt lgkmcnt(0)\n\ts_barrier" :: "s"(x), "s"(kc), "s"(vc), "s"(wq), "s"(wo), "s"(out), "s"(ws) : "memory");

  if (wv != 0) {
    const int w = wv - 1;
    const size_t pbase = (size_t)(b * HKV + kvh) * (SK * DH * 2);
    const v4i rk = mkrsrc(kc + pbase, SK * DH * 2), rv = mkrsrc(vc + pbase, SK * DH * 2);
    const unsigned vk = (((lane >> 2) & 3) * 16 + g * 4 + (lane & 3)) * 16, vv = lane * 16;
    const unsigned ring = lbase + L_RING + w * RING;
    const unsigned so0 = (s * 4 + w) * 4096;
    constexpr unsigned RSTR = 65536;
    const int toff = POFF + (w * TAU16 >> 6);
    while ((int)(now() - t0) < w * STAG0) __builtin_amdgcn_s_sleep(1);
    STAMP(32);
    issue_row_acc<0>(rk, rv, vk, vv, so0);
    const unsigned j0 = lane & 3;
    const unsigned mask0 = j0 == 0 ? 0x0000ffffu : (j0 == 1 ? 0xffff0000u : 0u);
    const unsigned mask1 = j0 == 2 ? 0x0000ffffu : (j0 == 3 ? 0xffff0000u : 0u);
    float m = -INFINITY, l = 0.f;
    f32x4 acc0 = {0.f, 0.f, 0.f, 0.f}, acc1 = {0.f, 0.f, 0.f, 0.f};
    v4u qf = {0u, 0u, 0u, 0u};
    int i = 1, n = NREG, r = 0, tmin = 0;
    bool ready = false;
#pragma unroll 1
    while (true) {
      if (i < NROW && i - n < (n > NREG + NSLOT ? 3 : NSLOT)) {
        const int tt = max(toff + (i * TAU16 >> 4), tmin);
        if ((int)(now() - t0) >= tt) {
          WAITVM(8);
          if (TMINF) tmin = (int)(now() - t0) + (TAU16 * TMINF >> 10);
          STAMP(32 + i);
          const unsigned so = so0 + i * RSTR;
          if (i >= NREG) issue_row(rk, rv, VKD, vv, so, ring + (i & (NSLOT - 1)) * SLOT);
          else if (NREG > 1 && (i == 1 || NREG == 2)) issue_row_acc<1>(rk, rv, vk, vv, so);
          else if (NREG > 2 && (i == 2 || NREG == 3)) issue_row_acc<2>(rk, rv, vk, vv, so);
          else if (NREG > 3 && (i == 3 || NREG == 4)) issue_row_acc<3>(rk, rv, vk, vv, so);
          else issue_row_acc<4>(rk, rv, vk, vv, so);
          ++i;
          continue;
        }
      }
      if (!ready) {
        RELOAD();
        if (__builtin_amdgcn_readfirstlane(*(LAS unsigned*)(L + L_FLAG)) == 0u) { __builtin_amdgcn_s_sleep(1); continue; }
        RELOAD();
        qf = *(LAS v4u*)(L + L_Q + vk);
        ready = true;
        STAMP(2);
      }
      const bool fromacc = r < NREG && r + 3 <= i && (i - n < 2 || n >= NREG + 8 + 2 * r);
      if (!fromacc && i < NROW && i - n < 2) { __builtin_amdgcn_s_sleep(1); continue; }
      if (i == NROW && i - n < NSLOT) {
        const v4i ro = mkrsrc(wo, HQ * DH * DM * 2);
        const unsigned vo = (lane >> 3) * 1024 + (lane & 7) * 16;
#pragma unroll
        for (int u = 0; u < 4; ++u) dma1_nt(ro, vo, (ch * 128 + w * 32 + u * 8) * 1024 + nb * 128, ring + (NROW & (NSLOT - 1)) * SLOT + u * 1024);
        ++i;
      }
      v4u K[4], V[4];
      if (fromacc) {
        if (r == 0) fetch_row_acc<0>(K, V);
        else if (NREG > 1 && (r == 1 || NREG == 2)) fetch_row_acc<1>(K, V);
        else if (NREG > 2 && (r == 2 || NREG == 3)) fetch_row_acc<2>(K, V);
        else if (NREG > 3 && (r == 3 || NREG == 4)) fetch_row_acc<3>(K, V);
        else fetch_row_acc<4>(K, V);
        ++r;
      } else {
        if (n == NROW - 1) WAITVM(4);
        else if (n == NROW - 2 && i > NROW) WAITVM(12);
        else if (i - n == 2) WAITVM(8);
        LAS v4u* src = (LAS v4u*)(L + L_RING + w * RING + (n & (NSLOT - 1)) * SLOT) + lane;
#pragma unroll
        for (int u = 0; u < 4; ++u) K[u] = src[u * 64 + KRD];
#pragma unroll
        for (int u = 0; u < 4; ++u) V[u] = src[256 + u * 64];
        ++n;
      }
      float sc[4];
#pragma unroll
      for (int u = 0; u < 4; ++u) {
        const f32x4 d = mfma32(K[u], qf, f32x4{0.f, 0.f, 0.f, 0.f});
        sc[u] = (dpp<0x00>(d[0]) + dpp<0x55>(d[1])) + (dpp<0xAA>(d[2]) + dpp<0xFF>(d[3]));
      }
      const float tm = xrow_max(fmaxf(fmaxf(sc[0], sc[1]), fmaxf(sc[2], sc[3])));
      const float mn = fmaxf(m, tm);
      const float alpha = __builtin_amdgcn_exp2f((m - mn) * C2);
      const float mc = -mn * C2;
      m = mn;
      l *= alpha; acc0 *= alpha; acc1 *= alpha;
#pragma unroll
      for (int u = 0; u < 4; ++u) {
        const float p = __builtin_amdgcn_exp2f(fmaf(sc[u], C2, mc));
        l += p;
        const unsigned pp = cvtpk(p, p);
        const v2u bo = {pp & mask0, pp & mask1};
        acc0 = mfma16(V[u][0], V[u][1], bo, acc0);
        acc1 = mfma16(V[u][2], V[u][3], bo, acc1);
      }
      if (n == NROW) break;
    }
    STAMP(3);
    const unsigned tag = __builtin_amdgcn_readfirstlane(*(LAS unsigned*)(L + L_FLAG + 4));
    l = xrow_sum(l);
    {
      LAS float* rec = (LAS float*)(L + L_MRG + (w * 4 + ((lane >> 2) & 3)) * MREC);
#pragma unroll
      for (int r = 0; r < 4; ++r) { rec[32 * g + 8 * r + j0] = acc0[r]; rec[32 * g + 8 * r + 4 + j0] = acc1[r]; }
      if (g == 0 && j0 == 0) { rec[128] = m; rec[129] = l; }
    }
    BARRIER();
    STAMP(4);
    {
      const int c = lane < 32 ? lane : 32;
      f32x4 a[4]; float mw[4];
#pragma unroll
      for (int u = 0; u < 4; ++u) { LAS float* rec = (LAS float*)(L + L_MRG + (u * 4 + w) * MREC); a[u] = *(LAS f32x4*)(rec + 4 * c); mw[u] = rec[128]; }
      float M = fmaxf(fmaxf(mw[0], mw[1]), fmaxf(mw[2], mw[3]));
      M = __builtin_bit_cast(float, __builtin_bit_cast(unsigned, M) & 0xffffff00u);
      f32x4 o = {0.f, 0.f, 0.f, 0.f};
#pragma unroll
      for (int u = 0; u < 4; ++u) o += a[u] * __builtin_amdgcn_exp2f((mw[u] - M) * C2);
      if (c == 32) { o[0] = M; o[2] = 0.f; o[3] = 0.f; }
      if (lane <= 32) __builtin_amdgcn_raw_buffer_store_b128(tag4(o, tag), rw, c * 16, W_PA + ((b * HKV + kvh) * 4 + s) * PA_REC + w * MREC, AUX_SC1);
    }
    STAMP(5);
    {
      const int bb = 2 * w + (lane >> 5), c = lane & 31;
      const int vo = bb * (HKV * 4 * PA_REC) + c * 16, vm = bb * (HKV * 4 * PA_REC) + 512;
      const int so = W_PA + (ch >> 2) * (4 * PA_REC) + (ch & 3) * MREC;
      v4u a[4], ml[4];
      for (unsigned spin = 0; spin < SPIN_CAP; ++spin) {
        RELOAD();
#pragma unroll
        for (int u = 0; u < 4; ++u) { a[u] = __builtin_amdgcn_raw_buffer_load_b128(rw, vo, so + u * PA_REC, AUX_SC1); ml[u] = __builtin_amdgcn_raw_buffer_load_b128(rw, vm, so + u * PA_REC, AUX_SC1); }
        unsigned bad = 0;
#pragma unroll
        for (int u = 0; u < 4; ++u) { a[u] = untag4(a[u], tag); ml[u] = untag4(ml[u], tag); bad |= or4(a[u]) | or4(ml[u]); }
        if (__all((bad & 0xffu) == 0u)) break;
      }
      STAMP(6);
      float ms[4], M = -INFINITY;
#pragma unroll
      for (int u = 0; u < 4; ++u) { ms[u] = __uint_as_float(ml[u][0]); M = fmaxf(M, ms[u]); }
      float Ls = 0.f; f32x4 o = {0.f, 0.f, 0.f, 0.f};
#pragma unroll
      for (int u = 0; u < 4; ++u) { const float e = __builtin_amdgcn_exp2f((ms[u] - M) * C2); Ls = fmaf(e, __uint_as_float(ml[u][1]), Ls); o += __builtin_bit_cast(f32x4, a[u]) * e; }
      o *= __builtin_amdgcn_rcpf(Ls);
      *(LAS v2u*)(L + L_OT + bb * OT_ROW + c * 8) = v2u{cvtpk(o[0], o[1]), cvtpk(o[2], o[3])};
    }
    BARRIER();
    STAMP(7);
    {
      f32x4 y0 = {0.f, 0.f, 0.f, 0.f}, y1 = {0.f, 0.f, 0.f, 0.f};
      LAS unsigned char* ap = L + L_RING + (NROW & (NSLOT - 1)) * SLOT + (8 * g + ((lane & 15) >> 2)) * 128 + (16 * w + 4 * (lane & 3)) * 2;
      LAS unsigned char* bp = L + L_OT + (lane & 7) * OT_ROW + g * 16;
#pragma unroll
      for (int ks = 0; ks < 4; ks += 2) {
        y0 = mfma32(tr_frag(ap + ks * RING), *(LAS v4u*)(bp + ks * 64), y0);
        y1 = mfma32(tr_frag(ap + (ks + 1) * RING), *(LAS v4u*)(bp + (ks + 1) * 64), y1);
      }
      y0 += y1;
      if ((lane & 15) < 8) __builtin_amdgcn_raw_buffer_store_b128(tag4(y0, tag), rw, (lane & 7) * (32 * 2048) + g * 16, W_YB + (nb * 4 + w) * 2048 + ch * 64, AUX_SC1);
    }
    STAMP(8);
    STAMP_OUT();
    return;
  }

  __builtin_amdgcn_s_setprio(3);
  unsigned tag;
  {
    const int cb = j & 63, kq = j >> 6;
    const v4i rx = mkrsrc(x, NB * DM * 2), rq = mkrsrc(wq, DM * HQ * DH * 2);
    const unsigned vx = (lane & 7) * 1024 + (lane >> 3) * 16;
    const unsigned vq = (lane >> 3) * 8192 + (lane & 7) * 16;
    asm volatile("" :: "s"(rx), "s"(rq));
    STAMP(4);
#pragma unroll 1
    for (int u = 0; u < 16; ++u) DMAQ(rq, vq, (kq * 16 + u) * 65536 + cb * 128, lbase + L_WQ + u * 1024);
    dma1(rx, vx, kq * 256, lbase + L_X);
    dma1(rx, vx, kq * 256 + 128, lbase + L_X + 1024);
    STAMP(5);
    tag = load_tag(ws);
    STAMP(6);
    f32x4 qa[4];
#pragma unroll
    for (int t = 0; t < 4; ++t) qa[t] = f32x4{0.f, 0.f, 0.f, 0.f};
    LAS unsigned char* ap = L + L_WQ + (8 * g + ((lane & 15) >> 2)) * 128 + 4 * (lane & 3) * 2;
    LAS unsigned char* bp = L + L_X + g * 128 + (lane & 7) * 16;
    WAITVM(0);
    STAMP(10);
#pragma unroll
    for (int ks = 0; ks < 4; ++ks) {
      const v4u xb = *(LAS v4u*)(bp + ks * 512);
#pragma unroll
      for (int t = 0; t < 4; ++t) qa[t] = mfma32(tr_frag(ap + ks * 4096 + t * 32), xb, qa[t]);
    }
    STAMP(1);
    const bool hi = (lane & 8) != 0;
#pragma unroll
    for (int t = 0; t < 4; t += 2) {
      const f32x4 v = {hi ? qa[t + 1][0] : qa[t][0], hi ? qa[t + 1][1] : qa[t][1], hi ? qa[t + 1][2] : qa[t][2], hi ? qa[t + 1][3] : qa[t][3]};
      __builtin_amdgcn_raw_buffer_store_b128(tag4(v, tag), rw, (lane & 7) * (HKV * 8192) + (hi ? 64 : 0) + g * 16, W_QG + (cb >> 3) * 8192 + kq * 2048 + (cb & 7) * 256 + t * 64, AUX_SC1);
    }
  }
  STAMP(2);
  {
    const int so = W_QG + (b * HKV + kvh) * 8192;
    v4u a[8];
    for (unsigned spin = 0; spin < SPIN_CAP; ++spin) {
      RELOAD();
#pragma unroll
      for (int u = 0; u < 8; ++u) a[u] = __builtin_amdgcn_raw_buffer_load_b128(rw, lane * 16 + (u & 3) * 1024, so + (u >> 2) * 4096, AUX_SC1);
      unsigned bad = 0;
#pragma unroll
      for (int u = 0; u < 8; ++u) { a[u] = untag4(a[u], tag); bad |= or4(a[u]); }
      if (spin == 0) STAMP(14);
      if (spin == 1) STAMP(15);
      if (__all((bad & 0xffu) == 0u)) break;
    }
#pragma unroll
    for (int u = 0; u < 2; ++u) {
      const f32x4 v = (__builtin_bit_cast(f32x4, a[u]) + __builtin_bit_cast(f32x4, a[2 + u])) + (__builtin_bit_cast(f32x4, a[4 + u]) + __builtin_bit_cast(f32x4, a[6 + u]));
      *(LAS v2u*)(L + L_Q + u * 512 + lane * 8) = v2u{cvtpk(v[0], v[1]), cvtpk(v[2], v[3])};
    }
  }
  asm volatile("s_waitcnt lgkmcnt(0)" ::: "memory");
  *(LAS v2u*)(L + L_FLAG) = v2u{1u, tag};
  STAMP(3);
  BARRIER();
  BARRIER();
  STAMP(7);
  {
    v4u a0, a1;
    for (unsigned spin = 0; spin < SPIN_CAP; ++spin) {
      RELOAD();
      a0 = untag4(__builtin_amdgcn_raw_buffer_load_b128(rw, lane * 16, W_YB + j * 2048, AUX_SC1), tag);
      a1 = untag4(__builtin_amdgcn_raw_buffer_load_b128(rw, lane * 16 + 1024, W_YB + j * 2048, AUX_SC1), tag);
      if (__all(((or4(a0) | or4(a1)) & 0xffu) == 0u)) break;
    }
    STAMP(8);
    f32x4 y = __builtin_bit_cast(f32x4, a0) + __builtin_bit_cast(f32x4, a1);
#pragma unroll
    for (int r = 0; r < 4; ++r) { float t = y[r]; t += dpp<0x124>(t); t += dpp<0x128>(t); y[r] = xrow_sum(t); }
    if (lane < 4) *(GAS v2u*)(out + j * 32 + lane * 8) = v2u{cvtpk(y[0], y[1]), cvtpk(y[2], y[3])};
    if (j == 0 && lane == 0) __builtin_amdgcn_raw_buffer_store_b32(tag, rw, 0, W_E, AUX_SC1);
  }
  STAMP(9);
  STAMP_OUT();
}

extern "C" void kernel_launch(void* const* d_in, const int* in_sizes, int n_in, void* d_out, int out_size, void* d_ws, size_t ws_size,
                              hipStream_t stream) {
  gqa_fused<<<NWG, NTHR, 0, stream>>>((const unsigned char*)d_in[0], (const unsigned char*)d_in[1], (const unsigned char*)d_in[2], (const unsigned char*)d_in[3],
                                      (const unsigned char*)d_in[4], (unsigned char*)d_out, (unsigned char*)d_ws);
}
```

```cpp
#define UXOR 0
#define D1 50
#define D2 50
#define CODEPF 63
#include <hip/hip_runtime.h>
#include <hip/hip_bf16.h>
#include <stdint.h>

#define GAS __attribute__((address_space(1)))
#define LAS __attribute__((address_space(3)))
typedef unsigned v4u __attribute__((ext_vector_type(4)));
typedef unsigned v2u __attribute__((ext_vector_type(2)));
typedef int v4i __attribute__((ext_vector_type(4)));
typedef float f32x4 __attribute__((ext_vector_type(4)));
typedef short s16x4 __attribute__((ext_vector_type(4)));
typedef __bf16 bf16x8 __attribute__((ext_vector_type(8)));

constexpr int NB = 8, DM = 512, HQ = 32, HKV = 8, SK = 8192, DH = 128;
constexpr int NWG = 256, NTHR = 384;
constexpr int NROW = 32, NUNIT = 128;
constexpr int SLOT = 8192;
constexpr float C2 = 0.088388347648318440f * 1.4426950408889634f;
#define NREG 4
#define TH 16
#define FB_ON 1
#define FB_USE 1
#define FB_STR 8
#define FB_P 192
#define FB_NREP 32
#define PHASE 28
#define HOLD 70
#define DELTA 20
#define TH_HI 24
#define QPOLL 450
#define PRETOUCH 1
constexpr unsigned SPIN_CAP = 1u << 16;
constexpr int L_RING = 0, L_Q = 16 * SLOT, L_MRG = L_Q + 1024, MREC = 132 * 4, L_OT = L_MRG + 16 * MREC, OT_ROW = 272, L_CTL = L_OT + 8 * OT_ROW;
constexpr int L_X = L_MRG, L_W = L_CTL + 64, L_TOT = L_W + 16384;
constexpr int C_READY = L_CTL, C_TAG = L_CTL + 4, C_LINE = L_CTL + 8, C_DONE = L_CTL + 16, C_LAND = L_CTL + 32;
static_assert(L_CTL % 16 == 0 && L_TOT <= 160 * 1024, "lds map");
constexpr int W_E = 0, W_FB = 1024, FB_SLOT = 64 * FB_STR, W_QG = W_FB + 64 * FB_SLOT + 1024, W_PA = W_QG + NB * HQ * DH * 4 * 4, PA_REC = 2176, W_YB = W_PA + NWG * PA_REC, W_END = W_YB + NWG * 2048;
#define STAMP(i)
#define STAMP_OUT()

__device__ __forceinline__ unsigned cvtpk(float lo, float hi) {
  typedef __bf16 bf16x2_t __attribute__((ext_vector_type(2)));
  typedef float f32x2_t __attribute__((ext_vector_type(2)));
  return __builtin_bit_cast(unsigned, __builtin_convertvector(f32x2_t{lo, hi}, bf16x2_t));
}
template <int CTRL> __device__ __forceinline__ float dpp(float x) {
  return __builtin_bit_cast(float, __builtin_amdgcn_mov_dpp(__builtin_bit_cast(int, x), CTRL, 0xf, 0xf, true));
}
__device__ __forceinline__ float xrow_max(float x) {
  auto s = __builtin_amdgcn_permlane16_swap(__float_as_uint(x), __float_as_uint(x), false, false);
  x = fmaxf(__uint_as_float(s[0]), __uint_as_float(s[1]));
  auto t = __builtin_amdgcn_permlane32_swap(__float_as_uint(x), __float_as_uint(x), false, false);
  return fmaxf(__uint_as_float(t[0]), __uint_as_float(t[1]));
}
__device__ __forceinline__ float xrow_sum(float x) {
  auto s = __builtin_amdgcn_permlane16_swap(__float_as_uint(x), __float_as_uint(x), false, false);
  x = __uint_as_float(s[0]) + __uint_as_float(s[1]);
  auto t = __builtin_amdgcn_permlane32_swap(__float_as_uint(x), __float_as_uint(x), false, false);
  return __uint_as_float(t[0]) + __uint_as_float(t[1]);
}
__device__ __forceinline__ v4i mkrsrc(const void* p, unsigned bytes) {
  const unsigned long long a = (unsigned long long)p;
  v4i r; r[0] = (int)(unsigned)a; r[1] = (int)((unsigned)(a >> 32) & 0xffffu); r[2] = (int)bytes; r[3] = 0x00020000; return r;
}
__device__ __forceinline__ unsigned now() { return (unsigned)__builtin_amdgcn_s_memrealtime(); }
#define STR_(x) #x
#define WAITVM(n) asm volatile("s_waitcnt vmcnt(" STR_(n) ")" ::: "memory")
#define BARRIER() asm volatile("s_waitcnt lgkmcnt(0)\n\ts_barrier" ::: "memory")
#define RELOAD() asm volatile("" ::: "memory")
__device__ __forceinline__ void issue_unit(v4i rk, v4i rv, unsigned vk, unsigned vv, unsigned soff, unsigned lds) {
  asm volatile("s_mov_b32 m0, %5\n\ts_nop 0\n\t"
               "buffer_load_dwordx4 %2, %0, %4 offen nt lds\n\t"
               "buffer_load_dwordx4 %2, %0, %4 offen offset:1024 nt lds\n\t"
               "buffer_load_dwordx4 %2, %0, %4 offen offset:2048 nt lds\n\t"
               "buffer_load_dwordx4 %2, %0, %4 offen offset:3072 nt lds\n\t"
               "s_add_u32 m0, m0, 0x1000\n\ts_nop 0\n\t"
               "buffer_load_dwordx4 %3, %1, %4 offen nt lds\n\t"
               "buffer_load_dwordx4 %3, %1, %4 offen offset:1024 nt lds\n\t"
               "buffer_load_dwordx4 %3, %1, %4 offen offset:2048 nt lds\n\t"
               "buffer_load_dwordx4 %3, %1, %4 offen offset:3072 nt lds"
               :: "s"(rk), "s"(rv), "v"(vk), "v"(vv), "s"(soff), "s"(lds) : "memory", "scc");
}
__device__ __forceinline__ void dma1_nt(v4i r, unsigned voff, unsigned soff, unsigned lds) {
  asm volatile("s_mov_b32 m0, %3\n\ts_nop 0\n\tbuffer_load_dwordx4 %1, %0, %2 offen nt lds" :: "s"(r), "v"(voff), "s"(soff), "s"(lds) : "memory");
}
__device__ __forceinline__ void dma1(v4i r, unsigned voff, unsigned soff, unsigned lds) {
  asm volatile("s_mov_b32 m0, %3\n\ts_nop 0\n\tbuffer_load_dwordx4 %1, %0, %2 offen lds" :: "s"(r), "v"(voff), "s"(soff), "s"(lds) : "memory");
}
template <int R> __device__ __forceinline__ void park_row(unsigned ak, unsigned av);
template <int R> __device__ __forceinline__ void fetch_row(v4u (&K)[4], v4u (&V)[4]);
template <> __device__ __forceinline__ void park_row<0>(unsigned ak, unsigned av) {
  asm volatile("ds_read_b128 v[96:99], %0 offset:0\n\tds_read_b128 v[100:103], %0 offset:1024\n\tds_read_b128 v[104:107], %0 offset:2048\n\tds_read_b128 v[108:111], %0 offset:3072\n\tds_read_b128 v[112:115], %1 offset:4096\n\tds_read_b128 v[116:119], %1 offset:5120\n\tds_read_b128 v[120:123], %1 offset:6144\n\tds_read_b128 v[124:127], %1 offset:7168\n\ts_waitcnt lgkmcnt(0)"
    :: "v"(ak), "v"(av) : "memory", "v96", "v97", "v98", "v99", "v100", "v101", "v102", "v103", "v104", "v105", "v106", "v107", "v108", "v109", "v110", "v111", "v112", "v113", "v114", "v115", "v116", "v117", "v118", "v119", "v120", "v121", "v122", "v123", "v124", "v125", "v126", "v127");
}
template <> __device__ __forceinline__ void fetch_row<0>(v4u (&K)[4], v4u (&V)[4]) {
  v2u t[16]; asm volatile("v_mov_b64 %0, v[96:97]\n\tv_mov_b64 %1, v[98:99]\n\tv_mov_b64 %2, v[100:101]\n\tv_mov_b64 %3, v[102:103]\n\tv_mov_b64 %4, v[104:105]\n\tv_mov_b64 %5, v[106:107]\n\tv_mov_b64 %6, v[108:109]\n\tv_mov_b64 %7, v[110:111]\n\tv_mov_b64 %8, v[112:113]\n\tv_mov_b64 %9, v[114:115]\n\tv_mov_b64 %10, v[116:117]\n\tv_mov_b64 %11, v[118:119]\n\tv_mov_b64 %12, v[120:121]\n\tv_mov_b64 %13, v[122:123]\n\tv_mov_b64 %14, v[124:125]\n\tv_mov_b64 %15, v[126:127]\n\ts_nop 1" : "=v"(t[0]), "=v"(t[1]), "=v"(t[2]), "=v"(t[3]), "=v"(t[4]), "=v"(t[5]), "=v"(t[6]), "=v"(t[7]), "=v"(t[8]), "=v"(t[9]), "=v"(t[10]), "=v"(t[11]), "=v"(t[12]), "=v"(t[13]), "=v"(t[14]), "=v"(t[15]));
  for (int u = 0; u < 4; ++u) { K[u] = v4u{t[2 * u][0], t[2 * u][1], t[2 * u + 1][0], t[2 * u + 1][1]}; V[u] = v4u{t[8 + 2 * u][0], t[8 + 2 * u][1], t[9 + 2 * u][0], t[9 + 2 * u][1]}; }
}
template <> __device__ __forceinline__ void park_row<1>(unsigned ak, unsigned av) {
  asm volatile("ds_read_b128 v[128:131], %0 offset:0\n\tds_read_b128 v[132:135], %0 offset:1024\n\tds_read_b128 v[136:139], %0 offset:2048\n\tds_read_b128 v[140:143], %0 offset:3072\n\tds_read_b128 v[144:147], %1 offset:4096\n\tds_read_b128 v[148:151], %1 offset:5120\n\tds_read_b128 v[152:155], %1 offset:6144\n\tds_read_b128 v[156:159], %1 offset:7168\n\ts_waitcnt lgkmcnt(0)"
    :: "v"(ak), "v"(av) : "memory", "v128", "v129", "v130", "v131", "v132", "v133", "v134", "v135", "v136", "v137", "v138", "v139", "v140", "v141", "v142", "v143", "v144", "v145", "v146", "v147", "v148", "v149", "v150", "v151", "v152", "v153", "v154", "v155", "v156", "v157", "v158", "v159");
}
template <> __device__ __forceinline__ void fetch_row<1>(v4u (&K)[4], v4u (&V)[4]) {
  v2u t[16]; asm volatile("v_mov_b64 %0, v[128:129]\n\tv_mov_b64 %1, v[130:131]\n\tv_mov_b64 %2, v[132:133]\n\tv_mov_b64 %3, v[134:135]\n\tv_mov_b64 %4, v[136:137]\n\tv_mov_b64 %5, v[138:139]\n\tv_mov_b64 %6, v[140:141]\n\tv_mov_b64 %7, v[142:143]\n\tv_mov_b64 %8, v[144:145]\n\tv_mov_b64 %9, v[146:147]\n\tv_mov_b64 %10, v[148:149]\n\tv_mov_b64 %11, v[150:151]\n\tv_mov_b64 %12, v[152:153]\n\tv_mov_b64 %13, v[154:155]\n\tv_mov_b64 %14, v[156:157]\n\tv_mov_b64 %15, v[158:159]\n\ts_nop 1" : "=v"(t[0]), "=v"(t[1]), "=v"(t[2]), "=v"(t[3]), "=v"(t[4]), "=v"(t[5]), "=v"(t[6]), "=v"(t[7]), "=v"(t[8]), "=v"(t[9]), "=v"(t[10]), "=v"(t[11]), "=v"(t[12]), "=v"(t[13]), "=v"(t[14]), "=v"(t[15]));
  for (int u = 0; u < 4; ++u) { K[u] = v4u{t[2 * u][0], t[2 * u][1], t[2 * u + 1][0], t[2 * u + 1][1]}; V[u] = v4u{t[8 + 2 * u][0], t[8 + 2 * u][1], t[9 + 2 * u][0], t[9 + 2 * u][1]}; }
}
template <> __device__ __forceinline__ void park_row<2>(unsigned ak, unsigned av) {
  asm volatile("ds_read_b128 v[160:163], %0 offset:0\n\tds_read_b128 v[164:167], %0 offset:1024\n\tds_read_b128 v[168:171], %0 offset:2048\n\tds_read_b128 v[172:175], %0 offset:3072\n\tds_read_b128 v[176:179], %1 offset:4096\n\tds_read_b128 v[180:183], %1 offset:5120\n\tds_read_b128 v[184:187], %1 offset:6144\n\tds_read_b128 v[188:191], %1 offset:7168\n\ts_waitcnt lgkmcnt(0)"
    :: "v"(ak), "v"(av) : "memory", "v160", "v161", "v162", "v163", "v164", "v165", "v166", "v167", "v168", "v169", "v170", "v171", "v172", "v173", "v174", "v175", "v176", "v177", "v178", "v179", "v180", "v181", "v182", "v183", "v184", "v185", "v186", "v187", "v188", "v189", "v190", "v191");
}
template <> __device__ __forceinline__ void fetch_row<2>(v4u (&K)[4], v4u (&V)[4]) {
  v2u t[16]; asm volatile("v_mov_b64 %0, v[160:161]\n\tv_mov_b64 %1, v[162:163]\n\tv_mov_b64 %2, v[164:165]\n\tv_mov_b64 %3, v[166:167]\n\tv_mov_b64 %4, v[168:169]\n\tv_mov_b64 %5, v[170:171]\n\tv_mov_b64 %6, v[172:173]\n\tv_mov_b64 %7, v[174:175]\n\tv_mov_b64 %8, v[176:177]\n\tv_mov_b64 %9, v[178:179]\n\tv_mov_b64 %10, v[180:181]\n\tv_mov_b64 %11, v[182:183]\n\tv_mov_b64 %12, v[184:185]\n\tv_mov_b64 %13, v[186:187]\n\tv_mov_b64 %14, v[188:189]\n\tv_mov_b64 %15, v[190:191]\n\ts_nop 1" : "=v"(t[0]), "=v"(t[1]), "=v"(t[2]), "=v"(t[3]), "=v"(t[4]), "=v"(t[5]), "=v"(t[6]), "=v"(t[7]), "=v"(t[8]), "=v"(t[9]), "=v"(t[10]), "=v"(t[11]), "=v"(t[12]), "=v"(t[13]), "=v"(t[14]), "=v"(t[15]));
  for (int u = 0; u < 4; ++u) { K[u] = v4u{t[2 * u][0], t[2 * u][1], t[2 * u + 1][0], t[2 * u + 1][1]}; V[u] = v4u{t[8 + 2 * u][0], t[8 + 2 * u][1], t[9 + 2 * u][0], t[9 + 2 * u][1]}; }
}
template <> __device__ __forceinline__ void park_row<3>(unsigned ak, unsigned av) {
  asm volatile("ds_read_b128 v[192:195], %0 offset:0\n\tds_read_b128 v[196:199], %0 offset:1024\n\tds_read_b128 v[200:203], %0 offset:2048\n\tds_read_b128 v[204:207], %0 offset:3072\n\tds_read_b128 v[208:211], %1 offset:4096\n\tds_read_b128 v[212:215], %1 offset:5120\n\tds_read_b128 v[216:219], %1 offset:6144\n\tds_read_b128 v[220:223], %1 offset:7168\n\ts_waitcnt lgkmcnt(0)"
    :: "v"(ak), "v"(av) : "memory", "v192", "v193", "v194", "v195", "v196", "v197", "v198", "v199", "v200", "v201", "v202", "v203", "v204", "v205", "v206", "v207", "v208", "v209", "v210", "v211", "v212", "v213", "v214", "v215", "v216", "v217", "v218", "v219", "v220", "v221", "v222", "v223");
}
template <> __device__ __forceinline__ void fetch_row<3>(v4u (&K)[4], v4u (&V)[4]) {
  v2u t[16]; asm volatile("v_mov_b64 %0, v[192:193]\n\tv_mov_b64 %1, v[194:195]\n\tv_mov_b64 %2, v[196:197]\n\tv_mov_b64 %3, v[198:199]\n\tv_mov_b64 %4, v[200:201]\n\tv_mov_b64 %5, v[202:203]\n\tv_mov_b64 %6, v[204:205]\n\tv_mov_b64 %7, v[206:207]\n\tv_mov_b64 %8, v[208:209]\n\tv_mov_b64 %9, v[210:211]\n\tv_mov_b64 %10, v[212:213]\n\tv_mov_b64 %11, v[214:215]\n\tv_mov_b64 %12, v[216:217]\n\tv_mov_b64 %13, v[218:219]\n\tv_mov_b64 %14, v[220:221]\n\tv_mov_b64 %15, v[222:223]\n\ts_nop 1" : "=v"(t[0]), "=v"(t[1]), "=v"(t[2]), "=v"(t[3]), "=v"(t[4]), "=v"(t[5]), "=v"(t[6]), "=v"(t[7]), "=v"(t[8]), "=v"(t[9]), "=v"(t[10]), "=v"(t[11]), "=v"(t[12]), "=v"(t[13]), "=v"(t[14]), "=v"(t[15]));
  for (int u = 0; u < 4; ++u) { K[u] = v4u{t[2 * u][0], t[2 * u][1], t[2 * u + 1][0], t[2 * u + 1][1]}; V[u] = v4u{t[8 + 2 * u][0], t[8 + 2 * u][1], t[9 + 2 * u][0], t[9 + 2 * u][1]}; }
}
template <> __device__ __forceinline__ void park_row<4>(unsigned ak, unsigned av) {
  asm volatile("ds_read_b128 v[224:227], %0 offset:0\n\tds_read_b128 v[228:231], %0 offset:1024\n\tds_read_b128 v[232:235], %0 offset:2048\n\tds_read_b128 v[236:239], %0 offset:3072\n\tds_read_b128 v[240:243], %1 offset:4096\n\tds_read_b128 v[244:247], %1 offset:5120\n\tds_read_b128 v[248:251], %1 offset:6144\n\tds_read_b128 v[252:255], %1 offset:7168\n\ts_waitcnt lgkmcnt(0)"
    :: "v"(ak), "v"(av) : "memory", "v224", "v225", "v226", "v227", "v228", "v229", "v230", "v231", "v232", "v233", "v234", "v235", "v236", "v237", "v238", "v239", "v240", "v241", "v242", "v243", "v244", "v245", "v246", "v247", "v248", "v249", "v250", "v251", "v252", "v253", "v254", "v255");
}
template <> __device__ __forceinline__ void fetch_row<4>(v4u (&K)[4], v4u (&V)[4]) {
  v2u t[16]; asm volatile("v_mov_b64 %0, v[224:225]\n\tv_mov_b64 %1, v[226:227]\n\tv_mov_b64 %2, v[228:229]\n\tv_mov_b64 %3, v[230:231]\n\tv_mov_b64 %4, v[232:233]\n\tv_mov_b64 %5, v[234:235]\n\tv_mov_b64 %6, v[236:237]\n\tv_mov_b64 %7, v[238:239]\n\tv_mov_b64 %8, v[240:241]\n\tv_mov_b64 %9, v[242:243]\n\tv_mov_b64 %10, v[244:245]\n\tv_mov_b64 %11, v[246:247]\n\tv_mov_b64 %12, v[248:249]\n\tv_mov_b64 %13, v[250:251]\n\tv_mov_b64 %14, v[252:253]\n\tv_mov_b64 %15, v[254:255]\n\ts_nop 1" : "=v"(t[0]), "=v"(t[1]), "=v"(t[2]), "=v"(t[3]), "=v"(t[4]), "=v"(t[5]), "=v"(t[6]), "=v"(t[7]), "=v"(t[8]), "=v"(t[9]), "=v"(t[10]), "=v"(t[11]), "=v"(t[12]), "=v"(t[13]), "=v"(t[14]), "=v"(t[15]));
  for (int u = 0; u < 4; ++u) { K[u] = v4u{t[2 * u][0], t[2 * u][1], t[2 * u + 1][0], t[2 * u + 1][1]}; V[u] = v4u{t[8 + 2 * u][0], t[8 + 2 * u][1], t[9 + 2 * u][0], t[9 + 2 * u][1]}; }
}

__device__ __forceinline__ f32x4 mfma32(v4u a, v4u b, f32x4 c) {
  return __builtin_amdgcn_mfma_f32_16x16x32_bf16(__builtin_bit_cast(bf16x8, a), __builtin_bit_cast(bf16x8, b), c, 0, 0, 0);
}
__device__ __forceinline__ f32x4 mfma16(unsigned a0, unsigned a1, v2u b, f32x4 c) {
  return __builtin_amdgcn_mfma_f32_16x16x16bf16_1k(__builtin_bit_cast(s16x4, v2u{a0, a1}), __builtin_bit_cast(s16x4, b), c, 0, 0, 0);
}
__device__ __forceinline__ v4u tr_frag(LAS unsigned char* p) {
  const v2u lo = __builtin_bit_cast(v2u, __builtin_amdgcn_ds_read_tr16_b64_v4i16((LAS s16x4*)p));
  const v2u hi = __builtin_bit_cast(v2u, __builtin_amdgcn_ds_read_tr16_b64_v4i16((LAS s16x4*)(p + 512)));
  return v4u{lo[0], lo[1], hi[0], hi[1]};
}
constexpr int AUX_SC1 = 16;
__device__ __forceinline__ v4u tag4(f32x4 v, unsigned tag) {
  const v4u u = __builtin_bit_cast(v4u, v);
  return v4u{(u[0] & 0xffffff00u) | (tag & 0xffu), (u[1] & 0xffffff00u) | ((tag >> 8) & 0xffu), (u[2] & 0xffffff00u) | ((tag >> 16) & 0xffu), (u[3] & 0xffffff00u) | (tag >> 24)};
}
__device__ __forceinline__ v4u untag4(v4u u, unsigned tag) {
  return v4u{u[0] ^ (tag & 0xffu), u[1] ^ ((tag >> 8) & 0xffu), u[2] ^ ((tag >> 16) & 0xffu), u[3] ^ (tag >> 24)};
}
__device__ __forceinline__ unsigned load_tag(const unsigned char* ws) {
  unsigned e0;
  asm volatile("s_load_dword %0, %1, 0x0\n\ts_waitcnt lgkmcnt(0)" : "=&s"(e0) : "s"(ws) : "memory");
  return ((e0 & 0x7f7f7f7fu) + 0x51276b35u) ^ (e0 & 0x80808080u);
}
__device__ __forceinline__ unsigned or4(v4u u) { return (u[0] | u[1]) | (u[2] | u[3]); }
__device__ __forceinline__ int lds_word(LAS unsigned char* p) { RELOAD(); const int v = __builtin_amdgcn_readfirstlane(*(LAS int*)p); RELOAD(); return v; }

__global__ void __launch_bounds__(NTHR, 1) gqa_fused(const unsigned char* __restrict__ x, const unsigned char* __restrict__ kc, const unsigned char* __restrict__ vc,
                                                     const unsigned char* __restrict__ wq, const unsigned char* __restrict__ wo, unsigned char* __restrict__ out,
                                                     unsigned char* ws) {
  __shared__ __attribute__((aligned(16))) unsigned char smem[L_TOT];
  LAS unsigned char* L = (LAS unsigned char*)smem;
  const unsigned lbase = (unsigned)(size_t)L;
  unsigned long long pc0;
  asm volatile("s_getpc_b64 %0" : "=s"(pc0));
  const unsigned t0 = now();
  const int lane = threadIdx.x & 63, wv = __builtin_amdgcn_readfirstlane(threadIdx.x >> 6);
  const int j = blockIdx.x, xg = j & 7, idx = j >> 3;
  const int yy = (xg - idx) & 7, pr = 2 * idx + (yy >> 2), s = yy & 3;
  const int ch = 4 * xg + (idx >> 3), nb = idx & 7;
  const int cb = j & 63, kq = j >> 6;
  const int g = lane >> 4;
  const int ux = UXOR ? (idx & 3) : 0;
  const unsigned vk = (((lane >> 2) & 3) * 16 + g * 4 + (lane & 3)) * 16, vv = lane * 16;
  STAMP(0);

  asm volatile("" :: "s"(x), "s"(kc), "s"(vc), "s"(wq), "s"(wo), "s"(out), "s"(ws));
  const size_t pbase = (size_t)pr * (SK * DH * 2);
  const v4i rk = mkrsrc(kc + pbase, SK * DH * 2), rv = mkrsrc(vc + pbase, SK * DH * 2);
  if (wv == 0) {
    __builtin_amdgcn_s_setprio(3);
    const v4i rx = mkrsrc(x, NB * DM * 2), rq = mkrsrc(wq, DM * HQ * DH * 2);
    const unsigned vx = (lane & 7) * 1024 + (lane >> 3) * 16, vq = (lane >> 3) * 8192 + (lane & 7) * 16;
#pragma unroll 1
    for (int u = 0; u < 16; ++u) dma1_nt(rq, vq, (kq * 16 + u) * 65536 + cb * 128, lbase + L_W + u * 1024);
    dma1(rx, vx, kq * 256, lbase + L_X);
    dma1(rx, vx, kq * 256 + 128, lbase + L_X + 1024);
    issue_unit(rk, rv, vv, vv, s * 16384 + ux * 4096, lbase);
    STAMP(1);
  }
  if (CODEPF && wv == 5) {
    const unsigned long long pa = pc0 & ~127ull;
    asm volatile("global_load_dword v95, %0, %1" :: "v"((lane < CODEPF ? lane : 0) * 128), "s"(pa) : "memory", "v95");
    if (CODEPF > 64) asm volatile("global_load_dword v95, %0, %1" :: "v"((lane + 64 < CODEPF ? lane + 64 : 0) * 128), "s"(pa) : "memory", "v95");
  }
  if (wv == 1 && lane < 12) *(LAS int*)(L + L_CTL + lane * 4) = lane == 8 ? -1 : 0;
  BARRIER();

  if (wv == 0) {
    unsigned due = 0, tl = 0;
    bool led = false;
    int dn = 0;
    const unsigned ph = (((unsigned)(j * 37) & 31u) * PHASE >> 5) - PHASE / 2 - DELTA;
#pragma unroll 1
    for (int u = 1; u < NUNIT; ++u) {
      while (dn < (u >> 2) - 3) { __builtin_amdgcn_s_sleep(1); dn = lds_word(L + C_DONE + (u & 3) * 4); }
      if (led) {
        while ((int)(due - tl) > 0) tl = now();
        WAITVM(TH_HI);
      } else WAITVM(TH);
      issue_unit(rk, rv, vv, vv, (u >> 2) * 65536 + s * 16384 + ((u ^ ux) & 3) * 4096, lbase + (u & 15) * SLOT);
      *(LAS int*)(L + C_LAND) = u - (led ? (TH_HI + 7) / 8 : (TH + 7) / 8);
      const int dv = *(LAS int*)(L + C_DONE + ((u + 1) & 3) * 4);
      if (FB_ON) {
        const v2u ln = *(LAS v2u*)(L + C_LINE);
        tl = now();
        *(LAS v2u*)(L + C_LAND + 8) = v2u{(unsigned)(8 * u), tl};
        const unsigned la = __builtin_amdgcn_readfirstlane(ln[0]), lt = __builtin_amdgcn_readfirstlane(ln[1]);
        led = FB_USE && lt != 0u;
        due = la + ((unsigned)(u + 1) * lt >> 5) + ph;
        const unsigned cap = tl + (lt * HOLD >> 11);
        if ((int)(due - cap) > 0) due = cap;
      }
      dn = __builtin_amdgcn_readfirstlane(dv);
    }
    STAMP(2);
    WAITVM(24); *(LAS int*)(L + C_LAND) = NUNIT - 3;
    WAITVM(16); *(LAS int*)(L + C_LAND) = NUNIT - 2;
    WAITVM(8); *(LAS int*)(L + C_LAND) = NUNIT - 1;
    WAITVM(0); *(LAS int*)(L + C_LAND) = NUNIT;
    STAMP(3);
    {
      const v4i ro = mkrsrc(wo, HQ * DH * DM * 2);
      const unsigned vo = (lane >> 3) * 1024 + (lane & 7) * 16;
#pragma unroll 1
      for (int u = 0; u < 16; ++u) dma1_nt(ro, vo, (ch * 128 + u * 8) * 1024 + nb * 128, lbase + L_W + u * 1024);
    }
    BARRIER();
    WAITVM(0);
    STAMP(6);
    BARRIER();
    STAMP_OUT();
    return;
  }

  const __amdgpu_buffer_rsrc_t rw = __builtin_amdgcn_make_buffer_rsrc((void*)ws, 0, W_END, 0x00020000);
  if (wv >= 2) {
    const int w = wv - 2;
    const unsigned rk0 = vk;
#define PARK(R) if (NREG > R) { while (lds_word(L + C_LAND) <= 4 * R + w) __builtin_amdgcn_s_sleep(2); \
      park_row<R>(lbase + ((4 * R + w) & 15) * SLOT + rk0, lbase + ((4 * R + w) & 15) * SLOT + vv); *(LAS int*)(L + C_DONE + w * 4) = R + 1; }
    PARK(0) PARK(1) PARK(2) PARK(3) PARK(4)
    STAMP(1);
    while (lds_word(L + C_READY) == 0) __builtin_amdgcn_s_sleep(2);
    STAMP(2);
    const v4u qf = *(LAS v4u*)(L + L_Q + vk);
    const unsigned tag = (unsigned)lds_word(L + C_TAG);
    const unsigned j0 = lane & 3;
    const unsigned mask0 = j0 == 0 ? 0x0000ffffu : (j0 == 1 ? 0xffff0000u : 0u);
    const unsigned mask1 = j0 == 2 ? 0x0000ffffu : (j0 == 3 ? 0xffff0000u : 0u);
    float m = -INFINITY, l = 0.f;
    f32x4 acc0 = {0.f, 0.f, 0.f, 0.f}, acc1 = {0.f, 0.f, 0.f, 0.f};
    int n = NREG, r = 0;
#pragma unroll 1
    while (true) {
      v4u K[4], V[4];
      if (r < NREG && n >= NREG + 4 + r) {
        if (r == 0) fetch_row<0>(K, V);
        else if (NREG > 1 && (r == 1 || NREG == 2)) fetch_row<1>(K, V);
        else if (NREG > 2 && (r == 2 || NREG == 3)) fetch_row<2>(K, V);
        else if (NREG > 3 && (r == 3 || NREG == 4)) fetch_row<3>(K, V);
        else fetch_row<4>(K, V);
        ++r;
      } else {
        while (lds_word(L + C_LAND) <= 4 * n + w) __builtin_amdgcn_s_sleep(1);
        if (n == NROW - 1) STAMP(9);
        LAS unsigned char* src = L + ((4 * n + w) & 15) * SLOT;
#pragma unroll
        for (int u = 0; u < 4; ++u) K[u] = *(LAS v4u*)(src + rk0 + u * 1024);
#pragma unroll
        for (int u = 0; u < 4; ++u) V[u] = *(LAS v4u*)(src + vv + 4096 + u * 1024);
        ++n;
      }
      float sc[4];
#pragma unroll
      for (int u = 0; u < 4; ++u) {
        const f32x4 d = mfma32(K[u], qf, f32x4{0.f, 0.f, 0.f, 0.f});
        sc[u] = (dpp<0x00>(d[0]) + dpp<0x55>(d[1])) + (dpp<0xAA>(d[2]) + dpp<0xFF>(d[3]));
      }
      asm volatile("" :: "v"(V[0]), "v"(V[1]), "v"(V[2]), "v"(V[3]), "v"(sc[0]), "v"(sc[1]), "v"(sc[2]), "v"(sc[3]) : "memory");
      *(LAS int*)(L + C_DONE + w * 4) = n;
      const float tm = xrow_max(fmaxf(fmaxf(sc[0], sc[1]), fmaxf(sc[2], sc[3])));
      const float mn = fmaxf(m, tm);
      const float alpha = __builtin_amdgcn_exp2f((m - mn) * C2);
      const float mc = -mn * C2;
      m = mn;
      l *= alpha; acc0 *= alpha; acc1 *= alpha;
#pragma unroll
      for (int u = 0; u < 4; ++u) {
        const float p = __builtin_amdgcn_exp2f(fmaf(sc[u], C2, mc));
        l += p;
        const unsigned pp = cvtpk(p, p);
        const v2u bo = {pp & mask0, pp & mask1};
        acc0 = mfma16(V[u][0], V[u][1], bo, acc0);
        acc1 = mfma16(V[u][2], V[u][3], bo, acc1);
      }
      if (n == NROW) break;
    }
    STAMP(3);
    l = xrow_sum(l);
    {
      LAS float* rec = (LAS float*)(L + L_MRG + (w * 4 + ((lane >> 2) & 3)) * MREC);
#pragma unroll
      for (int q = 0; q < 4; ++q) { rec[32 * g + 8 * q + j0] = acc0[q]; rec[32 * g + 8 * q + 4 + j0] = acc1[q]; }
      if (g == 0 && j0 == 0) { rec[128] = m; rec[129] = l; }
    }
    BARRIER();
    STAMP(4);
    {
      const int c = lane < 32 ? lane : 32;
      f32x4 a[4]; float mw[4];
#pragma unroll
      for (int u = 0; u < 4; ++u) { LAS float* rec = (LAS float*)(L + L_MRG + (u * 4 + w) * MREC); a[u] = *(LAS f32x4*)(rec + 4 * c); mw[u] = rec[128]; }
      float M = fmaxf(fmaxf(mw[0], mw[1]), fmaxf(mw[2], mw[3]));
      M = __uint_as_float(__float_as_uint(M) & 0xffffff00u);
      f32x4 o = {0.f, 0.f, 0.f, 0.f};
#pragma unroll
      for (int u = 0; u < 4; ++u) o += a[u] * __builtin_amdgcn_exp2f((mw[u] - M) * C2);
      if (c == 32) { o[0] = M; o[2] = 0.f; o[3] = 0.f; }
      if (lane <= 32) __builtin_amdgcn_raw_buffer_store_b128(tag4(o, tag), rw, c * 16, W_PA + (pr * 4 + s) * PA_REC + w * MREC, AUX_SC1);
    }
    STAMP(5);
    { const unsigned tp = now(); while ((int)(now() - tp) < D1) {} }
    {
      const int bb = 2 * w + (lane >> 5), c = lane & 31;
      const int vo = bb * (HKV * 4 * PA_REC) + c * 16, vm = bb * (HKV * 4 * PA_REC) + 512;
      const int so = W_PA + (ch >> 2) * (4 * PA_REC) + (ch & 3) * MREC;
      v4u a[4], ml[4];
      for (unsigned spin = 0; spin < SPIN_CAP; ++spin) {
        RELOAD();
#pragma unroll
        for (int u = 0; u < 4; ++u) { a[u] = __builtin_amdgcn_raw_buffer_load_b128(rw, vo, so + u * PA_REC, AUX_SC1); ml[u] = __builtin_amdgcn_raw_buffer_load_b128(rw, vm, so + u * PA_REC, AUX_SC1); }
        unsigned bad = 0;
#pragma unroll
        for (int u = 0; u < 4; ++u) { a[u] = untag4(a[u], tag); ml[u] = untag4(ml[u], tag); bad |= or4(a[u]) | or4(ml[u]); }
        if (__all((bad & 0xffu) == 0u)) break;
      }
      STAMP(6);
      float ms[4], M = -INFINITY;
#pragma unroll
      for (int u = 0; u < 4; ++u) { ms[u] = __uint_as_float(ml[u][0]); M = fmaxf(M, ms[u]); }
      float Ls = 0.f; f32x4 o = {0.f, 0.f, 0.f, 0.f};
#pragma unroll
      for (int u = 0; u < 4; ++u) { const float e = __builtin_amdgcn_exp2f((ms[u] - M) * C2); Ls = fmaf(e, __uint_as_float(ml[u][1]), Ls); o += __builtin_bit_cast(f32x4, a[u]) * e; }
      o *= __builtin_amdgcn_rcpf(Ls);
      *(LAS v2u*)(L + L_OT + bb * OT_ROW + c * 8) = v2u{cvtpk(o[0], o[1]), cvtpk(o[2], o[3])};
    }
    BARRIER();
    STAMP(7);
    {
      f32x4 y0 = {0.f, 0.f, 0.f, 0.f}, y1 = {0.f, 0.f, 0.f, 0.f};
      LAS unsigned char* ap = L + L_W + (8 * g + ((lane & 15) >> 2)) * 128 + (16 * w + 4 * (lane & 3)) * 2;
      LAS unsigned char* bp = L + L_OT + (lane & 7) * OT_ROW + g * 16;
#pragma unroll
      for (int ks = 0; ks < 4; ks += 2) {
        y0 = mfma32(tr_frag(ap + ks * 4096), *(LAS v4u*)(bp + ks * 64), y0);
        y1 = mfma32(tr_frag(ap + (ks + 1) * 4096), *(LAS v4u*)(bp + (ks + 1) * 64), y1);
      }
      y0 += y1;
      if ((lane & 15) < 8) __builtin_amdgcn_raw_buffer_store_b128(tag4(y0, tag), rw, (lane & 7) * (32 * 2048) + g * 16, W_YB + (nb * 4 + w) * 2048 + ch * 64, AUX_SC1);
    }
    STAMP(8);
    STAMP_OUT();
    return;
  }

  __builtin_amdgcn_s_setprio(2);
  const unsigned tag = load_tag(ws);
  STAMP(6);
  {
    while (lds_word(L + C_LAND) < 0) __builtin_amdgcn_s_sleep(1);
    STAMP(10);
    f32x4 qa[4];
#pragma unroll
    for (int t = 0; t < 4; ++t) qa[t] = f32x4{0.f, 0.f, 0.f, 0.f};
    LAS unsigned char* ap = L + L_W + (8 * g + ((lane & 15) >> 2)) * 128 + 4 * (lane & 3) * 2;
    LAS unsigned char* bp = L + L_X + g * 128 + (lane & 7) * 16;
#pragma unroll
    for (int ks = 0; ks < 4; ++ks) {
      const v4u xb = *(LAS v4u*)(bp + ks * 512);
#pragma unroll
      for (int t = 0; t < 4; ++t) qa[t] = mfma32(tr_frag(ap + ks * 4096 + t * 32), xb, qa[t]);
    }
    STAMP(1);
    const bool hi = (lane & 8) != 0;
#pragma unroll
    for (int t = 0; t < 4; t += 2) {
      const f32x4 v = {hi ? qa[t + 1][0] : qa[t][0], hi ? qa[t + 1][1] : qa[t][1], hi ? qa[t + 1][2] : qa[t][2], hi ? qa[t + 1][3] : qa[t][3]};
      __builtin_amdgcn_raw_buffer_store_b128(tag4(v, tag), rw, (lane & 7) * (HKV * 8192) + (hi ? 64 : 0) + g * 16, W_QG + (cb >> 3) * 8192 + kq * 2048 + (cb & 7) * 256 + t * 64, AUX_SC1);
    }
  }
  STAMP(2);
  while ((int)(now() - t0) < QPOLL) __builtin_amdgcn_s_sleep(2);
  {
    const int so = W_QG + pr * 8192;
    v4u a[8];
    for (unsigned spin = 0; spin < SPIN_CAP; ++spin) {
      RELOAD();
#pragma unroll
      for (int u = 0; u < 8; ++u) a[u] = __builtin_amdgcn_raw_buffer_load_b128(rw, lane * 16 + (u & 3) * 1024, so + (u >> 2) * 4096, AUX_SC1);
      unsigned bad = 0;
#pragma unroll
      for (int u = 0; u < 8; ++u) { a[u] = untag4(a[u], tag); bad |= or4(a[u]); }
      if (__all((bad & 0xffu) == 0u)) break;
      STAMP(15);
    }
#pragma unroll
    for (int u = 0; u < 2; ++u) {
      const f32x4 v = (__builtin_bit_cast(f32x4, a[u]) + __builtin_bit_cast(f32x4, a[2 + u])) + (__builtin_bit_cast(f32x4, a[4 + u]) + __builtin_bit_cast(f32x4, a[6 + u]));
      *(LAS v2u*)(L + L_Q + u * 512 + lane * 8) = v2u{cvtpk(v[0], v[1]), cvtpk(v[2], v[3])};
    }
    asm volatile("s_waitcnt lgkmcnt(0)" ::: "memory");
    *(LAS v2u*)(L + C_READY) = v2u{1u, tag};
  }
  STAMP(3);
  if (PRETOUCH) {
    const unsigned p0 = __builtin_amdgcn_raw_buffer_load_b32(rw, (lane < 17 ? lane : 0) * 128, W_PA + (pr * 4 + s) * PA_REC, 0);
    const unsigned p1 = __builtin_amdgcn_raw_buffer_load_b32(rw, (lane & 7) * (32 * 2048) + ((lane >> 3) & 3) * 2048, W_YB + nb * 4 * 2048 + ch * 64, 0);
    asm volatile("" :: "v"(p0), "v"(p1));
  }
  if (FB_ON) {
    unsigned k = now() / FB_P + 1u;
    float p0 = 0.f, dt0 = -1.f;
#pragma unroll 1
    for (int it = 0; it < 40; ++it, ++k) {
      const unsigned tk = k * FB_P;
      while ((int)(now() - tk) < 0) __builtin_amdgcn_s_sleep(2);
      RELOAD();
      const v4u pr4 = *(LAS v4u*)(L + C_LAND);
      if (__builtin_amdgcn_readfirstlane(pr4[0]) >= NUNIT - 12) break;
      const v2u fb = __builtin_amdgcn_raw_buffer_load_b64(rw, (lane & (FB_NREP - 1)) * FB_STR, W_FB + ((k - 1u) & 63u) * FB_SLOT, AUX_SC1);
      if (j < FB_NREP && lane == 0) {
        const float p = (float)(int)pr4[2] + fminf((float)(int)(tk - pr4[3]) * (1.f / 3.4f), 8.f);
        __builtin_amdgcn_raw_buffer_store_b64(v2u{__float_as_uint(p), tag ^ k}, rw, 0, W_FB + (k & 63u) * FB_SLOT + j * FB_STR, AUX_SC1);
      }
      const bool ok = fb[1] == (tag ^ (k - 1u));
      float ps = ok ? __uint_as_float(fb[0]) : 0.f, pc = ok ? 1.f : 0.f;
      ps += dpp<0xB1>(ps); ps += dpp<0x4E>(ps); ps += dpp<0x124>(ps); ps += dpp<0x128>(ps); ps = xrow_sum(ps);
      pc += dpp<0xB1>(pc); pc += dpp<0x4E>(pc); pc += dpp<0x124>(pc); pc += dpp<0x128>(pc); pc = xrow_sum(pc);
      if (dt0 >= 0.f) dt0 += (float)FB_P;
      if (__builtin_amdgcn_readfirstlane(pc < 48.f)) continue;
      const float pbar = ps * __builtin_amdgcn_rcpf(pc);
      if (dt0 < 0.f) { dt0 = 0.f; p0 = pbar; }
      const float tl = fminf(fmaxf((dt0 + 300.f) * __builtin_amdgcn_rcpf(pbar - p0 + 300.f / 3.45f), 3.f), 5.f);
      *(LAS v2u*)(L + C_LINE) = v2u{tk - FB_P - (unsigned)(int)(pbar * tl), (unsigned)(int)(tl * 256.f)};
    }
  }
  BARRIER();
  BARRIER();
  STAMP(7);
  { const unsigned tp = now(); while ((int)(now() - tp) < D2) {} }
  {
    v4u a0, a1;
    for (unsigned spin = 0; spin < SPIN_CAP; ++spin) {
      RELOAD();
      a0 = untag4(__builtin_amdgcn_raw_buffer_load_b128(rw, lane * 16, W_YB + j * 2048, AUX_SC1), tag);
      a1 = untag4(__builtin_amdgcn_raw_buffer_load_b128(rw, lane * 16 + 1024, W_YB + j * 2048, AUX_SC1), tag);
      if (__all(((or4(a0) | or4(a1)) & 0xffu) == 0u)) break;
    }
    STAMP(8);
    f32x4 y = __builtin_bit_cast(f32x4, a0) + __builtin_bit_cast(f32x4, a1);
#pragma unroll
    for (int q = 0; q < 4; ++q) { float t = y[q]; t += dpp<0x124>(t); t += dpp<0x128>(t); y[q] = xrow_sum(t); }
    if (lane < 4) *(GAS v2u*)(out + j * 32 + lane * 8) = v2u{cvtpk(y[0], y[1]), cvtpk(y[2], y[3])};
    if (j == 0 && lane == 0) __builtin_amdgcn_raw_buffer_store_b32(tag, rw, 0, W_E, AUX_SC1);
  }
  STAMP(9);
  STAMP_OUT();
}

extern "C" void kernel_launch(void* const* d_in, const int* in_sizes, int n_in, void* d_out, int out_size, void* d_ws, size_t ws_size,
                              hipStream_t stream) {
  gqa_fused<<<NWG, NTHR, 0, stream>>>((const unsigned char*)d_in[0], (const unsigned char*)d_in[1], (const unsigned char*)d_in[2], (const unsigned char*)d_in[3],
                                      (const unsigned char*)d_in[4], (unsigned char*)d_out, (unsigned char*)d_ws);
}
```

```cpp
#define ROW0 1
#define ROW1 28
#define NFIN 3
#define MINSP 20
#define DELTA 0
#define FB_G 0.4f
#define FB_KI 0.f
#define TL0 3.35f
#define ROTM 1
#define ROTS 1
#define D1 10
#define D2 45
#define D1G 122
#define WOPOS 125
#define PRETOUCH 0
#include <hip/hip_runtime.h>
#include <hip/hip_bf16.h>
#include <stdint.h>

#define GAS __attribute__((address_space(1)))
#define LAS __attribute__((address_space(3)))
typedef unsigned v4u __attribute__((ext_vector_type(4)));
typedef unsigned v2u __attribute__((ext_vector_type(2)));
typedef int v4i __attribute__((ext_vector_type(4)));
typedef float f32x4 __attribute__((ext_vector_type(4)));
typedef float f32x32 __attribute__((ext_vector_type(32)));
typedef unsigned v8u __attribute__((ext_vector_type(8)));
typedef short s16x4 __attribute__((ext_vector_type(4)));
typedef __bf16 bf16x8 __attribute__((ext_vector_type(8)));

constexpr int NB = 8, DM = 512, HQ = 32, HKV = 8, SK = 8192, DH = 128;
constexpr int NWG = 256, NTHR = 384;
constexpr int NUNIT = 128;
constexpr int SLOT = 8192;
constexpr float C2 = 0.088388347648318440f * 1.4426950408889634f;
#define NREG 4
#define TH 16
#define FB_ON 1
#define FB_USE 1
#define FB_STR 8
#define FB_P 192
#define FB_NREP 32
#define FB_I0 3
#define FB_REF 0
#define PHASE 28
#define HOLD 33
#define TH_HI 24
#define QPOLL 450
#define CODEPF 1
#define XF 0
#define TUNE_ARG
#define TUNE_PASS
#define LINE_END ((unsigned)(TL0 * 1024.f))
constexpr unsigned SPIN_CAP = 1u << 16;
constexpr int L_RING = 0, L_Q = 16 * SLOT, L_ACC = L_Q + 1024, L_PL = L_ACC + 4 * 2048, L_OT = L_PL + 2048, OT_ROW = 272, L_ML = L_OT + 8 * OT_ROW, L_CTL = L_ML + 144;
constexpr int L_X = L_ACC, L_W = L_CTL + 64, L_TOT = L_W + 16384;
constexpr int C_READY = L_CTL, C_TAG = L_CTL + 4, C_LAND = L_CTL + 8, C_DONE = L_CTL + 16, C_NM = L_CTL + 48, C_NL = L_CTL + 52, C_NA = L_CTL + 56, C_WO = L_CTL + 60;
static_assert(L_CTL % 16 == 0 && L_TOT <= 160 * 1024, "lds map");
constexpr int W_E = 0, W_FB = 1024, FB_SLOT = 64 * FB_STR, W_ML = W_FB + 64 * FB_SLOT + 1024, W_QG = W_ML + NWG * 128, W_PA = W_QG + NB * HQ * DH * 4 * 4, PA_REC = 2048, W_YB = W_PA + NWG * PA_REC,
              W_END = W_YB + NWG * 2048;
#define STAMP(i)
#define STAMP_OUT()

__device__ __forceinline__ unsigned cvtpk(float lo, float hi) {
  typedef __bf16 bf16x2_t __attribute__((ext_vector_type(2)));
  typedef float f32x2_t __attribute__((ext_vector_type(2)));
  return __builtin_bit_cast(unsigned, __builtin_convertvector(f32x2_t{lo, hi}, bf16x2_t));
}
template <int CTRL> __device__ __forceinline__ float dpp(float x) {
  return __builtin_bit_cast(float, __builtin_amdgcn_mov_dpp(__builtin_bit_cast(int, x), CTRL, 0xf, 0xf, true));
}
__device__ __forceinline__ float xrow_max(float x) {
  auto s = __builtin_amdgcn_permlane16_swap(__float_as_uint(x), __float_as_uint(x), false, false);
  x = fmaxf(__uint_as_float(s[0]), __uint_as_float(s[1]));
  auto t = __builtin_amdgcn_permlane32_swap(__float_as_uint(x), __float_as_uint(x), false, false);
  return fmaxf(__uint_as_float(t[0]), __uint_as_float(t[1]));
}
__device__ __forceinline__ float xrow_sum(float x) {
  auto s = __builtin_amdgcn_permlane16_swap(__float_as_uint(x), __float_as_uint(x), false, false);
  x = __uint_as_float(s[0]) + __uint_as_float(s[1]);
  auto t = __builtin_amdgcn_permlane32_swap(__float_as_uint(x), __float_as_uint(x), false, false);
  return __uint_as_float(t[0]) + __uint_as_float(t[1]);
}
__device__ __forceinline__ v4i mkrsrc(const void* p, unsigned bytes) {
  const unsigned long long a = (unsigned long long)p;
  v4i r; r[0] = (int)(unsigned)a; r[1] = (int)((unsigned)(a >> 32) & 0xffffu); r[2] = (int)bytes; r[3] = 0x00020000; return r;
}
__device__ __forceinline__ unsigned now() { return (unsigned)__builtin_amdgcn_s_memrealtime(); }
#define RFL(x) ((unsigned)__builtin_amdgcn_readfirstlane((int)(x)))
#define STR_(x) #x
#define STRX_(x) STR_(x)
#define WAITVM(n) asm volatile("s_waitcnt vmcnt(" STR_(n) ")" ::: "memory")
#define BARRIER() asm volatile("s_waitcnt lgkmcnt(0)\n\ts_barrier" ::: "memory")
#define RELOAD() asm volatile("" ::: "memory")
__device__ __forceinline__ void issue_unit(v4i r, unsigned vv, unsigned so0, unsigned so1, unsigned lds) {
  asm volatile("s_mov_b32 m0, %4\n\ts_nop 0\n\t"
               "buffer_load_dwordx4 %1, %0, %2 offen nt lds\n\t"
               "buffer_load_dwordx4 %1, %0, %2 offen offset:1024 nt lds\n\t"
               "buffer_load_dwordx4 %1, %0, %2 offen offset:2048 nt lds\n\t"
               "buffer_load_dwordx4 %1, %0, %2 offen offset:3072 nt lds\n\t"
               "s_add_u32 m0, m0, 0x1000\n\ts_nop 0\n\t"
               "buffer_load_dwordx4 %1, %0, %3 offen nt lds\n\t"
               "buffer_load_dwordx4 %1, %0, %3 offen offset:1024 nt lds\n\t"
               "buffer_load_dwordx4 %1, %0, %3 offen offset:2048 nt lds\n\t"
               "buffer_load_dwordx4 %1, %0, %3 offen offset:3072 nt lds"
               :: "s"(r), "v"(vv), "s"(so0), "s"(so1), "s"(lds) : "memory", "scc");
}
#define AM_A 1
#define AM_B 16
#define AM_C 14
#define AM_D 13
#define AM_E 12
#define unit_off(u, s) ((unsigned)((((u) >> AM_A) << AM_B) + ((s) << AM_C) + (((u) & ((1 << AM_A) - 1)) << AM_D)))
#define PIECE2 (1u << AM_E)
__device__ __forceinline__ void dma1_nt(v4i r, unsigned voff, unsigned soff, unsigned lds) {
  asm volatile("s_mov_b32 m0, %3\n\ts_nop 0\n\tbuffer_load_dwordx4 %1, %0, %2 offen nt lds" :: "s"(r), "v"(voff), "s"(soff), "s"(lds) : "memory");
}
__device__ __forceinline__ void dma1(v4i r, unsigned voff, unsigned soff, unsigned lds) {
  asm volatile("s_mov_b32 m0, %3\n\ts_nop 0\n\tbuffer_load_dwordx4 %1, %0, %2 offen lds" :: "s"(r), "v"(voff), "s"(soff), "s"(lds) : "memory");
}
__device__ __forceinline__ f32x4 mfma32(v4u a, v4u b, f32x4 c) {
  return __builtin_amdgcn_mfma_f32_16x16x32_bf16(__builtin_bit_cast(bf16x8, a), __builtin_bit_cast(bf16x8, b), c, 0, 0, 0);
}
__device__ __forceinline__ f32x4 mfma16(unsigned a0, unsigned a1, v2u b, f32x4 c) {
  return __builtin_amdgcn_mfma_f32_16x16x16bf16_1k(__builtin_bit_cast(s16x4, v2u{a0, a1}), __builtin_bit_cast(s16x4, b), c, 0, 0, 0);
}
__device__ __forceinline__ v4u tr_frag(LAS unsigned char* p) {
  const v2u lo = __builtin_bit_cast(v2u, __builtin_amdgcn_ds_read_tr16_b64_v4i16((LAS s16x4*)p));
  const v2u hi = __builtin_bit_cast(v2u, __builtin_amdgcn_ds_read_tr16_b64_v4i16((LAS s16x4*)(p + 512)));
  return v4u{lo[0], lo[1], hi[0], hi[1]};
}
__device__ __forceinline__ float score16(const v4u* K, v4u qf, unsigned j0) {
  float sc[4];
#pragma unroll
  for (int u = 0; u < 4; ++u) {
    const f32x4 d = mfma32(K[u], qf, f32x4{0.f, 0.f, 0.f, 0.f});
    sc[u] = (dpp<0x00>(d[0]) + dpp<0x55>(d[1])) + (dpp<0xAA>(d[2]) + dpp<0xFF>(d[3]));
  }
  return (j0 & 2) ? ((j0 & 1) ? sc[3] : sc[2]) : ((j0 & 1) ? sc[1] : sc[0]);
}
template <int CTRL> __device__ __forceinline__ unsigned dppu(unsigned x) { return (unsigned)__builtin_amdgcn_mov_dpp((int)x, CTRL, 0xf, 0xf, true); }
__device__ __forceinline__ void p_operands(unsigned k0, unsigned k1, unsigned mask0, unsigned mask1, v4u (&bo)[4]) {
  bo[0] = v4u{dppu<0x00>(k0) & mask0, dppu<0x00>(k0) & mask1, dppu<0x55>(k0) & mask0, dppu<0x55>(k0) & mask1};
  bo[1] = v4u{dppu<0xAA>(k0) & mask0, dppu<0xAA>(k0) & mask1, dppu<0xFF>(k0) & mask0, dppu<0xFF>(k0) & mask1};
  bo[2] = v4u{dppu<0x00>(k1) & mask0, dppu<0x00>(k1) & mask1, dppu<0x55>(k1) & mask0, dppu<0x55>(k1) & mask1};
  bo[3] = v4u{dppu<0xAA>(k1) & mask0, dppu<0xAA>(k1) & mask1, dppu<0xFF>(k1) & mask0, dppu<0xFF>(k1) & mask1};
  asm volatile("" : "+v"(bo[0]), "+v"(bo[1]), "+v"(bo[2]), "+v"(bo[3]));
}
__device__ __forceinline__ void pv32(unsigned p, const v4u (&bo)[4], f32x4& acc0, f32x4& acc1) {
  v4u a0, a1, a2, a3, a4, a5, a6, a7;
  asm volatile(
      "ds_read2st64_b64 %[a0], %[p0] offset0:0 offset1:2\n\t"
      "ds_read2st64_b64 %[a1], %[p1] offset0:0 offset1:2\n\t"
      "ds_read2st64_b64 %[a2], %[p0] offset0:4 offset1:6\n\t"
      "ds_read2st64_b64 %[a3], %[p1] offset0:4 offset1:6\n\t"
      "ds_read2st64_b64 %[a4], %[p0] offset0:8 offset1:10\n\t"
      "ds_read2st64_b64 %[a5], %[p1] offset0:8 offset1:10\n\t"
      "ds_read2st64_b64 %[a6], %[p0] offset0:12 offset1:14\n\t"
      "ds_read2st64_b64 %[a7], %[p1] offset0:12 offset1:14\n\t"
      "s_waitcnt lgkmcnt(7)\n\tv_mfma_f32_16x16x32_bf16 %[c0], %[a0], %[b0], %[c0]\n\t"
      "s_waitcnt lgkmcnt(6)\n\tv_mfma_f32_16x16x32_bf16 %[c1], %[a1], %[b0], %[c1]\n\t"
      "s_waitcnt lgkmcnt(5)\n\tv_mfma_f32_16x16x32_bf16 %[c0], %[a2], %[b1], %[c0]\n\t"
      "s_waitcnt lgkmcnt(4)\n\tv_mfma_f32_16x16x32_bf16 %[c1], %[a3], %[b1], %[c1]\n\t"
      "s_waitcnt lgkmcnt(3)\n\tv_mfma_f32_16x16x32_bf16 %[c0], %[a4], %[b2], %[c0]\n\t"
      "s_waitcnt lgkmcnt(2)\n\tv_mfma_f32_16x16x32_bf16 %[c1], %[a5], %[b2], %[c1]\n\t"
      "s_waitcnt lgkmcnt(1)\n\tv_mfma_f32_16x16x32_bf16 %[c0], %[a6], %[b3], %[c0]\n\t"
      "s_waitcnt lgkmcnt(0)\n\tv_mfma_f32_16x16x32_bf16 %[c1], %[a7], %[b3], %[c1]\n\t"
      "s_nop 7"
      : [c0] "+v"(acc0), [c1] "+v"(acc1), [a0] "=&v"(a0), [a1] "=&v"(a1), [a2] "=&v"(a2), [a3] "=&v"(a3), [a4] "=&v"(a4), [a5] "=&v"(a5), [a6] "=&v"(a6), [a7] "=&v"(a7)
      : [p0] "v"(p), [p1] "v"(p + 8), [b0] "v"(bo[0]), [b1] "v"(bo[1]), [b2] "v"(bo[2]), [b3] "v"(bo[3]) : "memory");
}
constexpr int AUX_SC1 = 16;
__device__ __forceinline__ v4u tag4(f32x4 v, unsigned tag) {
  const v4u u = __builtin_bit_cast(v4u, v);
  return v4u{(u[0] & 0xffffff00u) | (tag & 0xffu), (u[1] & 0xffffff00u) | ((tag >> 8) & 0xffu), (u[2] & 0xffffff00u) | ((tag >> 16) & 0xffu), (u[3] & 0xffffff00u) | (tag >> 24)};
}
__device__ __forceinline__ v4u untag4(v4u u, unsigned tag) {
  return v4u{u[0] ^ (tag & 0xffu), u[1] ^ ((tag >> 8) & 0xffu), u[2] ^ ((tag >> 16) & 0xffu), u[3] ^ (tag >> 24)};
}
__device__ __forceinline__ unsigned load_tag(const unsigned char* ws) {
  unsigned e0;
  asm volatile("s_load_dword %0, %1, 0x0\n\ts_waitcnt lgkmcnt(0)" : "=&s"(e0) : "s"(ws) : "memory");
  return ((e0 & 0x7f7f7f7fu) + 0x51276b35u) ^ (e0 & 0x80808080u);
}
__device__ __forceinline__ unsigned or4(v4u u) { return (u[0] | u[1]) | (u[2] | u[3]); }
__device__ __forceinline__ void lds_inc(LAS unsigned char* p, int lane) {
  RELOAD(); if (lane == 0) (void)__hip_atomic_fetch_add((LAS int*)p, 1, __ATOMIC_RELAXED, __HIP_MEMORY_SCOPE_WORKGROUP); RELOAD();
}
__device__ __forceinline__ int lds_word(LAS unsigned char* p) { RELOAD(); const int v = __builtin_amdgcn_readfirstlane(*(LAS int*)p); RELOAD(); return v; }

__global__ void __launch_bounds__(NTHR, 1) gqa_fused(const unsigned char* __restrict__ x, const unsigned char* __restrict__ kc, const unsigned char* __restrict__ vc,
                                                     const unsigned char* __restrict__ wq, const unsigned char* __restrict__ wo, unsigned char* __restrict__ out,
                                                     unsigned char* ws TUNE_ARG) {
  __shared__ __attribute__((aligned(16))) unsigned char smem[L_TOT];
  LAS unsigned char* L = (LAS unsigned char*)smem;
  const unsigned lbase = (unsigned)(size_t)L;
  unsigned long long pc0;
  unsigned clen;
  asm volatile("s_getpc_b64 %0\nLgqa_pc:\n\ts_mov_b32 %1, Lgqa_end_a-Lgqa_pc\n\ts_max_u32 %1, %1, Lgqa_end_b-Lgqa_pc\n\ts_max_u32 %1, %1, Lgqa_end_c-Lgqa_pc" : "=s"(pc0), "=s"(clen) :: "scc");
  const unsigned t0 = now();
  const int lane = threadIdx.x & 63, wv = __builtin_amdgcn_readfirstlane(threadIdx.x >> 6);
  const int j = blockIdx.x, xg = j & 7, idx = j >> 3;
  const int yy = (xg - idx) & 7, pr = (XF & 16) ? (idx >> 2) * 8 + xg : 2 * idx + (yy >> 2), s = (XF & 16) ? (idx & 3) : yy & 3;
  const int ch = 4 * xg + (idx >> 3), nb = idx & 7;
  const int cb = j & 63, kq = j >> 6;
  const int g = lane >> 4;
  const unsigned rot = ((unsigned)(pr * ROTM >> ROTS) << AM_B) & 0x1fffffu;
  const unsigned vk = (((lane >> 2) & 3) * 16 + g * 4 + (lane & 3)) * 16, vv = lane * 16;
  STAMP(0);

  asm volatile("" :: "s"(x), "s"(kc), "s"(vc), "s"(wq), "s"(wo), "s"(out), "s"(ws));
  const size_t pbase = (size_t)pr * (SK * DH * 2);
  const v4i rs = mkrsrc(kc + pbase, SK * DH * 2);
  if (wv == 0) {
    __builtin_amdgcn_s_setprio(3);
    const v4i rx = mkrsrc(x, NB * DM * 2), rq = mkrsrc(wq, (XF & 8) ? 0 : DM * HQ * DH * 2);
    const unsigned vx = (lane & 7) * 1024 + (lane >> 3) * 16, vq = (lane >> 3) * 8192 + (lane & 7) * 16;
#pragma unroll 1
    for (int u = 0; u < 16; ++u) dma1_nt(rq, vq, (kq * 16 + u) * 65536 + cb * 128, lbase + L_W + u * 1024);
    dma1(rx, vx, kq * 256, lbase + L_X);
    dma1(rx, vx, kq * 256 + 128, lbase + L_X + 1024);
    issue_unit(rs, vv, unit_off(0, s) + rot, unit_off(0, s) + rot + PIECE2, lbase);
    STAMP(1);
  }
  unsigned pf = 0;
  if (CODEPF && wv >= 3) {
    const unsigned co = (unsigned)(lane + (5 - wv) * 64) * 128u;
    if (co < clen + 128u) asm volatile("global_load_dword %0, %1, %2" : "=&v"(pf) : "v"(co), "s"(pc0 & ~127ull) : "memory");
  }
  if (wv == 1 && lane < 16) *(LAS unsigned*)(L + L_CTL + lane * 4) = lane == 2 ? ~0u : (lane >= 4 && lane < 12 && (lane & 1)) ? t0 - (1u << 20) : 0u;
  BARRIER();
  const __amdgpu_buffer_rsrc_t rw = __builtin_amdgcn_make_buffer_rsrc((void*)ws, 0, W_END, 0x00020000);
  const unsigned j0 = lane & 3;
  const int h = (lane >> 2) & 3;
  const unsigned mask0 = j0 == 0 ? 0x0000ffffu : (j0 == 1 ? 0xffff0000u : 0u);
  const unsigned mask1 = j0 == 2 ? 0x0000ffffu : (j0 == 3 ? 0xffff0000u : 0u);

  if (wv == 0) {
#pragma unroll 1
    for (int u = 1; u < 4 * ROW0; ++u) {
      WAITVM(TH);
      const unsigned so = (unit_off(u, s) + rot) & 0x1fffffu;
      issue_unit(rs, vv, so, so + PIECE2, lbase + u * SLOT);
      *(LAS int*)(L + C_LAND) = u - (TH + 7) / 8;
    }
    v4u bo[NFIN][4];
    unsigned vo, tg0, tg1, tg2, tg3, hm = 0xffffff00u;
    float scale;
    {
      constexpr unsigned rc = (unsigned)(TL0 * 512.f), uc = rc >> 6;
      unsigned ra = RFL(ROW0 * rc + (((unsigned)(j * 37) & 31u) * PHASE >> 1)), bs = ra >> 4, so = RFL((unit_off(4 * ROW0, s) + rot) & 0x1fffffu), ta, tb;
      int nd = ROW0 - 3;
      const unsigned long long vb = (unsigned long long)(vc + pbase) & 0xffffffffffffull;
      const unsigned long long kb = (unsigned long long)(kc + pbase) & 0xffffffffffffull;
#define STAMP_ASM
#define STAMP_OPS
#define STAMP_INS
#pragma unroll 1
      for (int pass = 0; pass < 2; ++pass) {
      while (lds_word(L + C_DONE) < nd) __builtin_amdgcn_s_sleep(1);
      asm volatile(
      "s_mov_b64 s[36:37], %[kb]\n\t"
      "s_mov_b32 s38, 0x200000\n\t"
      "s_mov_b32 s39, 0x20000\n\t"
      "s_mov_b32 m0, %[m0i]\n\t"
      "v_mov_b32 v2, %[ld0]\n"
      "LT%=:\n\t"
      "s_waitcnt vmcnt(" STRX_(TH_HI) ")\n\t"
      "buffer_load_dwordx4 %[v0], s[36:39], %[so] offen nt lds\n\t"
      "buffer_load_dwordx4 %[v0], s[36:39], %[so] offen offset:1024 nt lds\n\t"
      "buffer_load_dwordx4 %[v0], s[36:39], %[so] offen offset:2048 nt lds\n\t"
      "buffer_load_dwordx4 %[v0], s[36:39], %[so] offen offset:3072 nt lds\n\t"
      "s_add_u32 m0, m0, 0x1000\n\t"
      "s_memrealtime s[40:41]\n\t"
      "buffer_load_dwordx4 %[v1], s[36:39], %[so] offen nt lds\n\t"
      "buffer_load_dwordx4 %[v1], s[36:39], %[so] offen offset:1024 nt lds\n\t"
      "buffer_load_dwordx4 %[v1], s[36:39], %[so] offen offset:2048 nt lds\n\t"
      "buffer_load_dwordx4 %[v1], s[36:39], %[so] offen offset:3072 nt lds\n\t"
      "ds_read_b64 v[4:5], %[vc] offset:24\n\t"
      "s_add_u32 m0, m0, 0x1000\n\t"
      "s_add_u32 %[so], %[so], %[st0]\n\t"
      "s_and_b32 %[so], %[so], 0x1fffff\n\t"
      "v_add_u32 v2, 1, v2\n\t"
      "s_add_u32 %[bs], %[bs], %[uc]\n\t"
      "s_waitcnt lgkmcnt(0)\n\t"
      "v_mov_b32 v3, s40\n\t"
      "v_readfirstlane_b32 %[a], v5\n\t"
      "v_readfirstlane_b32 %[b], v4\n\t"
      "ds_write_b64 %[vc], v[2:3] offset:8\n\t"
      STAMP_ASM
      "s_add_u32 %[a], %[a], %[bs]\n\t"
      "s_sub_i32 %[a], %[a], s40\n\t"
      "s_min_i32 %[a], %[a], %[hold]\n\t"
      "s_max_i32 %[a], %[a], %[msp]\n\t"
      "s_cmp_lt_i32 %[b], %[nd]\n\t"
      "s_cbranch_scc1 LS0%=\n"
      "LB0%=:\n\t"
      "s_cmp_lt_i32 %[a], 8\n\t"
      "s_cbranch_scc1 LN0%=\n\t"
      "s_add_u32 %[a], %[a], s40\n"
      "LW0%=:\n\t"
      "s_memrealtime s[40:41]\n\t"
      "s_waitcnt lgkmcnt(0)\n\t"
      "s_sub_i32 %[b], %[a], s40\n\t"
      "s_cmp_gt_i32 %[b], 0\n\t"
      "s_cbranch_scc1 LW0%=\n"
      "LN0%=:\n\t"
      "s_waitcnt vmcnt(" STRX_(TH_HI) ")\n\t"
      "buffer_load_dwordx4 %[v0], s[36:39], %[so] offen nt lds\n\t"
      "buffer_load_dwordx4 %[v0], s[36:39], %[so] offen offset:1024 nt lds\n\t"
      "buffer_load_dwordx4 %[v0], s[36:39], %[so] offen offset:2048 nt lds\n\t"
      "buffer_load_dwordx4 %[v0], s[36:39], %[so] offen offset:3072 nt lds\n\t"
      "s_add_u32 m0, m0, 0x1000\n\t"
      "s_memrealtime s[40:41]\n\t"
      "buffer_load_dwordx4 %[v1], s[36:39], %[so] offen nt lds\n\t"
      "buffer_load_dwordx4 %[v1], s[36:39], %[so] offen offset:1024 nt lds\n\t"
      "buffer_load_dwordx4 %[v1], s[36:39], %[so] offen offset:2048 nt lds\n\t"
      "buffer_load_dwordx4 %[v1], s[36:39], %[so] offen offset:3072 nt lds\n\t"
      "ds_read_b64 v[4:5], %[vc] offset:32\n\t"
      "s_add_u32 m0, m0, 0x1000\n\t"
      "s_add_u32 %[so], %[so], %[st1]\n\t"
      "s_and_b32 %[so], %[so], 0x1fffff\n\t"
      "v_add_u32 v2, 1, v2\n\t"
      "s_add_u32 %[bs], %[bs], %[uc]\n\t"
      "s_waitcnt lgkmcnt(0)\n\t"
      "v_mov_b32 v3, s40\n\t"
      "v_readfirstlane_b32 %[a], v5\n\t"
      "v_readfirstlane_b32 %[b], v4\n\t"
      "ds_write_b64 %[vc], v[2:3] offset:8\n\t"
      STAMP_ASM
      "s_add_u32 %[a], %[a], %[bs]\n\t"
      "s_sub_i32 %[a], %[a], s40\n\t"
      "s_min_i32 %[a], %[a], %[hold]\n\t"
      "s_max_i32 %[a], %[a], %[msp]\n\t"
      "s_cmp_lt_i32 %[b], %[nd]\n\t"
      "s_cbranch_scc1 LS1%=\n"
      "LB1%=:\n\t"
      "s_cmp_lt_i32 %[a], 8\n\t"
      "s_cbranch_scc1 LN1%=\n\t"
      "s_add_u32 %[a], %[a], s40\n"
      "LW1%=:\n\t"
      "s_memrealtime s[40:41]\n\t"
      "s_waitcnt lgkmcnt(0)\n\t"
      "s_sub_i32 %[b], %[a], s40\n\t"
      "s_cmp_gt_i32 %[b], 0\n\t"
      "s_cbranch_scc1 LW1%=\n"
      "LN1%=:\n\t"
      "s_waitcnt vmcnt(" STRX_(TH_HI) ")\n\t"
      "buffer_load_dwordx4 %[v0], s[36:39], %[so] offen nt lds\n\t"
      "buffer_load_dwordx4 %[v0], s[36:39], %[so] offen offset:1024 nt lds\n\t"
      "buffer_load_dwordx4 %[v0], s[36:39], %[so] offen offset:2048 nt lds\n\t"
      "buffer_load_dwordx4 %[v0], s[36:39], %[so] offen offset:3072 nt lds\n\t"
      "s_add_u32 m0, m0, 0x1000\n\t"
      "s_memrealtime s[40:41]\n\t"
      "buffer_load_dwordx4 %[v1], s[36:39], %[so] offen nt lds\n\t"
      "buffer_load_dwordx4 %[v1], s[36:39], %[so] offen offset:1024 nt lds\n\t"
      "buffer_load_dwordx4 %[v1], s[36:39], %[so] offen offset:2048 nt lds\n\t"
      "buffer_load_dwordx4 %[v1], s[36:39], %[so] offen offset:3072 nt lds\n\t"
      "ds_read_b64 v[4:5], %[vc] offset:40\n\t"
      "s_add_u32 m0, m0, 0x1000\n\t"
      "s_add_u32 %[so], %[so], %[st2]\n\t"
      "s_and_b32 %[so], %[so], 0x1fffff\n\t"
      "v_add_u32 v2, 1, v2\n\t"
      "s_add_u32 %[bs], %[bs], %[uc]\n\t"
      "s_waitcnt lgkmcnt(0)\n\t"
      "v_mov_b32 v3, s40\n\t"
      "v_readfirstlane_b32 %[a], v5\n\t"
      "v_readfirstlane_b32 %[b], v4\n\t"
      "ds_write_b64 %[vc], v[2:3] offset:8\n\t"
      STAMP_ASM
      "s_add_u32 %[a], %[a], %[bs]\n\t"
      "s_sub_i32 %[a], %[a], s40\n\t"
      "s_min_i32 %[a], %[a], %[hold]\n\t"
      "s_max_i32 %[a], %[a], %[msp]\n\t"
      "s_cmp_lt_i32 %[b], %[nd]\n\t"
      "s_cbranch_scc1 LS2%=\n"
      "LB2%=:\n\t"
      "s_cmp_lt_i32 %[a], 8\n\t"
      "s_cbranch_scc1 LN2%=\n\t"
      "s_add_u32 %[a], %[a], s40\n"
      "LW2%=:\n\t"
      "s_memrealtime s[40:41]\n\t"
      "s_waitcnt lgkmcnt(0)\n\t"
      "s_sub_i32 %[b], %[a], s40\n\t"
      "s_cmp_gt_i32 %[b], 0\n\t"
      "s_cbranch_scc1 LW2%=\n"
      "LN2%=:\n\t"
      "s_waitcnt vmcnt(" STRX_(TH_HI) ")\n\t"
      "buffer_load_dwordx4 %[v0], s[36:39], %[so] offen nt lds\n\t"
      "buffer_load_dwordx4 %[v0], s[36:39], %[so] offen offset:1024 nt lds\n\t"
      "buffer_load_dwordx4 %[v0], s[36:39], %[so] offen offset:2048 nt lds\n\t"
      "buffer_load_dwordx4 %[v0], s[36:39], %[so] offen offset:3072 nt lds\n\t"
      "s_add_u32 m0, m0, 0x1000\n\t"
      "s_memrealtime s[40:41]\n\t"
      "buffer_load_dwordx4 %[v1], s[36:39], %[so] offen nt lds\n\t"
      "buffer_load_dwordx4 %[v1], s[36:39], %[so] offen offset:1024 nt lds\n\t"
      "buffer_load_dwordx4 %[v1], s[36:39], %[so] offen offset:2048 nt lds\n\t"
      "buffer_load_dwordx4 %[v1], s[36:39], %[so] offen offset:3072 nt lds\n\t"
      "ds_read_b64 v[4:5], %[vc] offset:16\n\t"
      "s_add_u32 m0, m0, 0x1000\n\t"
      "s_add_u32 %[so], %[so], %[st3]\n\t"
      "s_and_b32 %[so], %[so], 0x1fffff\n\t"
      "v_add_u32 v2, 1, v2\n\t"
      "s_add_u32 %[ra], %[ra], %[rc]\n\t"
      "s_lshr_b32 %[bs], %[ra], 4\n\t"
      "s_add_i32 %[nd], %[nd], 1\n\t"
      "s_and_b32 m0, m0, 0x1ffff\n\t"
      "s_cmp_eq_u32 %[nd], 13\n\t"
      "s_cselect_b64 s[36:37], %[vb], s[36:37]\n\t"
      "s_cselect_b32 %[so], %[so0], %[so]\n\t"
      "s_waitcnt lgkmcnt(0)\n\t"
      "v_mov_b32 v3, s40\n\t"
      "v_readfirstlane_b32 %[a], v5\n\t"
      "v_readfirstlane_b32 %[b], v4\n\t"
      "ds_write_b64 %[vc], v[2:3] offset:8\n\t"
      STAMP_ASM
      "s_cmp_eq_u32 %[nd], %[ne]\n\t"
      "s_cbranch_scc1 LE%=\n\t"
      "s_add_u32 %[a], %[a], %[bs]\n\t"
      "s_sub_i32 %[a], %[a], s40\n\t"
      "s_min_i32 %[a], %[a], %[hold]\n\t"
      "s_max_i32 %[a], %[a], %[msp]\n\t"
      "s_cmp_lt_i32 %[b], %[nd]\n\t"
      "s_cbranch_scc1 LS3%=\n"
      "LB3%=:\n\t"
      "s_cmp_lt_i32 %[a], 8\n\t"
      "s_cbranch_scc1 LN3%=\n\t"
      "s_add_u32 %[a], %[a], s40\n"
      "LW3%=:\n\t"
      "s_memrealtime s[40:41]\n\t"
      "s_waitcnt lgkmcnt(0)\n\t"
      "s_sub_i32 %[b], %[a], s40\n\t"
      "s_cmp_gt_i32 %[b], 0\n\t"
      "s_cbranch_scc1 LW3%=\n"
      "LN3%=:\n\t"
      "s_branch LT%=\n"
      "LS0%=:\n\t"
      "s_sleep 1\n\t"
      "ds_read_b32 v4, %[vc] offset:24\n\t"
      "s_waitcnt lgkmcnt(0)\n\t"
      "v_readfirstlane_b32 %[b], v4\n\t"
      "s_cmp_lt_i32 %[b], %[nd]\n\t"
      "s_cbranch_scc1 LS0%=\n\t"
      "s_branch LB0%=\n"
      "LS1%=:\n\t"
      "s_sleep 1\n\t"
      "ds_read_b32 v4, %[vc] offset:32\n\t"
      "s_waitcnt lgkmcnt(0)\n\t"
      "v_readfirstlane_b32 %[b], v4\n\t"
      "s_cmp_lt_i32 %[b], %[nd]\n\t"
      "s_cbranch_scc1 LS1%=\n\t"
      "s_branch LB1%=\n"
      "LS2%=:\n\t"
      "s_sleep 1\n\t"
      "ds_read_b32 v4, %[vc] offset:40\n\t"
      "s_waitcnt lgkmcnt(0)\n\t"
      "v_readfirstlane_b32 %[b], v4\n\t"
      "s_cmp_lt_i32 %[b], %[nd]\n\t"
      "s_cbranch_scc1 LS2%=\n\t"
      "s_branch LB2%=\n"
      "LS3%=:\n\t"
      "s_sleep 1\n\t"
      "ds_read_b32 v4, %[vc] offset:16\n\t"
      "s_waitcnt lgkmcnt(0)\n\t"
      "v_readfirstlane_b32 %[b], v4\n\t"
      "s_cmp_lt_i32 %[b], %[nd]\n\t"
      "s_cbranch_scc1 LS3%=\n\t"
      "s_branch LB3%=\n"
      "LE%=:"
      : [so] "+s"(so), [ra] "+s"(ra), [bs] "+s"(bs), [nd] "+s"(nd), [a] "=&s"(ta), [b] "=&s"(tb) STAMP_OPS
      : [v0] "v"(vv), [v1] "v"(vv + PIECE2), [vc] "v"(lbase + L_CTL), [kb] "s"(pass ? vb : kb), [ne] "s"(pass ? 29 : ROW1 - 3), [vb] "s"(vb), [so0] "s"(RFL(unit_off(0, s) + rot)),
        [st0] "s"(RFL(unit_off(1, 0) - unit_off(0, 0))), [st1] "s"(RFL(unit_off(2, 0) - unit_off(1, 0))), [st2] "s"(RFL(unit_off(3, 0) - unit_off(2, 0))), [st3] "s"(RFL(unit_off(4, 0) - unit_off(3, 0))),
        [rc] "s"(rc), [uc] "s"(uc), [hold] "s"(HOLD), [msp] "s"(MINSP), [m0i] "s"(((pass ? 4 * ROW1 : 4 * ROW0) & 15) * SLOT), [ld0] "s"((pass ? 4 * ROW1 : 4 * ROW0) - 1 - (TH_HI + 7) / 8) STAMP_INS
      : "memory", "scc", "vcc", "v2", "v3", "v4", "v5", "s36", "s37", "s38", "s39", "s40", "s41");
      if (pass == 0) {
        while (lds_word(L + C_DONE + 24) < 26) __builtin_amdgcn_s_sleep(1);
        const unsigned tag = *(LAS unsigned*)(L + C_TAG);
        v2u pl[NFIN];
#pragma unroll
        for (int k = 0; k < NFIN; ++k) pl[k] = *(LAS v2u*)(L + L_PL + (4 - NFIN + k) * 512 + lane * 8);
        scale = *(LAS float*)(L + L_ML + 128 + h * 4);
#pragma unroll
        for (int k = 0; k < NFIN; ++k) p_operands(pl[k][0], pl[k][1], mask0, mask1, bo[k]);
        vo = h * 512 + g * 64 + j0 * 16; tg0 = tag & 0xffu; tg1 = (tag >> 8) & 0xffu; tg2 = (tag >> 16) & 0xffu; tg3 = tag >> 24;
        asm volatile("" : "+v"(vo), "+v"(tg0), "+v"(tg1), "+v"(tg2), "+v"(tg3), "+v"(scale), "+s"(hm));
      }
      }
    }
    STAMP(2);
    const int so = W_PA + (pr * 4 + s) * PA_REC;
    LAS unsigned char* asrc = L + L_ACC + vv;
    asm volatile("" : "+v"(asrc));
    f32x4 acc0 = {0.f, 0.f, 0.f, 0.f}, acc1 = {0.f, 0.f, 0.f, 0.f};
#pragma unroll
    for (int k = 0; k < 3; ++k) {
      if (k == 0) WAITVM(24); else if (k == 1) WAITVM(16); else WAITVM(8);
      *(LAS int*)(L + C_LAND) = NUNIT - 3 + k;
      if (k >= 4 - NFIN) pv32(lbase + (12 + k) * SLOT + vv, bo[k >= 4 - NFIN ? k - (4 - NFIN) : 0], acc0, acc1);
    }
    while (lds_word(L + C_NA) < 4) {}
    f32x4 s0, s1;
    {
      f32x4 a[8];
#pragma unroll
      for (int u = 0; u < 8; ++u) a[u] = *(LAS f32x4*)(asrc + u * 1024);
      __builtin_amdgcn_sched_barrier(0);
      s0 = ((a[0] + a[2]) + (a[4] + a[6])) * scale; s1 = ((a[1] + a[3]) + (a[5] + a[7])) * scale;
      asm volatile("" : "+v"(s0), "+v"(s1) : "v"(lbase + 15 * SLOT + vv), "v"(lbase + 15 * SLOT + vv + 8), "s"(so));
    }
    STAMP(4);
    WAITVM(0);
    STAMP(3);
    {
      pv32(lbase + 15 * SLOT + vv, bo[NFIN - 1], acc0, acc1);
      const v4u o0 = __builtin_bit_cast(v4u, acc0 * scale + s0), o1 = __builtin_bit_cast(v4u, acc1 * scale + s1);
      v4u t0v = {(o0[0] & hm) | tg0, (o0[1] & hm) | tg1, (o0[2] & hm) | tg2, (o0[3] & hm) | tg3};
      __builtin_amdgcn_raw_buffer_store_b128(t0v, rw, vo, so, AUX_SC1);
      v4u t1v = {(o1[0] & hm) | tg0, (o1[1] & hm) | tg1, (o1[2] & hm) | tg2, (o1[3] & hm) | tg3};
      __builtin_amdgcn_raw_buffer_store_b128(t1v, rw, vo + 256, so, AUX_SC1);
      asm volatile("s_nop 1" :: "v"(t0v), "v"(t1v) : "memory");
    }
    RELOAD();
    *(LAS int*)(L + C_LAND) = NUNIT;
    asm volatile("Lgqa_end_a:" ::: "memory");
    STAMP(5);
    STAMP_OUT();
    return;
  }

  if (wv >= 2) {
    const int w = wv - 2;
    if (PRETOUCH && !(XF & 4) && w == 0) {
      const int off = lane < 16 ? W_PA + (pr * 4 + s) * PA_REC + lane * 128 : W_YB + nb * 4 * 2048 + ch * 64 + (lane & 7) * (32 * 2048) + ((lane >> 3) & 3) * 2048;
      unsigned pt = 0;
      if (lane < 16 || (lane >= 32 && !(ch & 1))) pt = __builtin_amdgcn_raw_buffer_load_b32(rw, off, 0, 0);
      asm volatile("" :: "v"(pt));
    }
    asm volatile("s_waitcnt vmcnt(0)" :: "v"(pf) : "memory");
    f32x32 sc;
    float m;
    {
      v4u P[NREG][8];
#pragma unroll
      for (int r = 0; r < NREG; ++r) {
        while (lds_word(L + C_LAND) <= 4 * r + w) __builtin_amdgcn_s_sleep(2);
#pragma unroll
        for (int u = 0; u < 8; ++u) P[r][u] = *(LAS v4u*)(L + ((4 * r + w) & 15) * SLOT + vk + u * 1024);
        asm volatile("" :: "v"(P[r][0]), "v"(P[r][1]), "v"(P[r][2]), "v"(P[r][3]), "v"(P[r][4]), "v"(P[r][5]), "v"(P[r][6]), "v"(P[r][7]) : "memory");
        *(LAS int*)(L + C_DONE + w * 8) = r + 1;
      }
      STAMP(1);
      while (lds_word(L + C_READY) == 0) __builtin_amdgcn_s_sleep(2);
      STAMP(2);
      const v4u qf = *(LAS v4u*)(L + L_Q + vk);
      m = -INFINITY;
#pragma unroll
      for (int r = 0; r < NREG; ++r) {
        const float s0 = score16(&P[r][0], qf, j0), s1 = score16(&P[r][4], qf, j0);
        sc[2 * r] = s0; sc[2 * r + 1] = s1; m = fmaxf(m, fmaxf(s0, s1));
      }
#pragma unroll 1
      for (int n = NREG; n < 16; ++n) {
        while (lds_word(L + C_LAND) <= 4 * n + w) __builtin_amdgcn_s_sleep(2);
        v4u K[8];
#pragma unroll
        for (int u = 0; u < 8; ++u) K[u] = *(LAS v4u*)(L + ((4 * n + w) & 15) * SLOT + vk + u * 1024);
        asm volatile("" :: "v"(K[0]), "v"(K[1]), "v"(K[2]), "v"(K[3]), "v"(K[4]), "v"(K[5]), "v"(K[6]), "v"(K[7]) : "memory");
        *(LAS int*)(L + C_DONE + w * 8) = n + 1;
        const float s0 = score16(&K[0], qf, j0), s1 = score16(&K[4], qf, j0);
        sc[2 * n] = s0; sc[2 * n + 1] = s1; m = fmaxf(m, fmaxf(s0, s1));
      }
    }
    STAMP(10);
    const unsigned tag = (unsigned)lds_word(L + C_TAG);
    m = fmaxf(m, dpp<0xB1>(m)); m = fmaxf(m, dpp<0x4E>(m)); m = xrow_max(m);
    *(LAS float*)(L + L_ML + h * 16 + w * 4) = m;
    lds_inc(L + C_NM, lane);
    while (lds_word(L + C_NM) < 4) __builtin_amdgcn_s_sleep(1);
    {
      const f32x4 mw = *(LAS f32x4*)(L + L_ML + h * 16);
      m = __uint_as_float(__float_as_uint(fmaxf(fmaxf(mw[0], mw[1]), fmaxf(mw[2], mw[3]))) & 0xffffff00u);
      const float mc = -m * C2;
      float l = 0.f;
#pragma unroll
      for (int n = 0; n < 32; ++n) { const float p = __builtin_amdgcn_exp2f(fmaf(sc[n], C2, mc)); l += p; sc[n] = __uint_as_float(cvtpk(p, p)); }
      l += dpp<0xB1>(l); l += dpp<0x4E>(l); l = xrow_sum(l);
      *(LAS float*)(L + L_ML + 64 + h * 16 + w * 4) = l;
      *(LAS v2u*)(L + L_PL + w * 512 + lane * 8) = v2u{__float_as_uint(sc[30]), __float_as_uint(sc[31])};
      lds_inc(L + C_NL, lane);
    }
    STAMP(11);
    f32x4 acc0 = {0.f, 0.f, 0.f, 0.f}, acc1 = {0.f, 0.f, 0.f, 0.f};
#pragma unroll 1
    for (int n = 0; n < (w < 4 - NFIN ? 16 : 15); ++n) {
      if (w == 3 && n == 1) {
        while (lds_word(L + C_NL) < 4) __builtin_amdgcn_s_sleep(1);
        const f32x4 lw = *(LAS f32x4*)(L + L_ML + 64 + h * 16);
        if (lane < 16 && j0 == 0) __builtin_amdgcn_raw_buffer_store_b128(tag4(f32x4{m, (lw[0] + lw[1]) + (lw[2] + lw[3]), 0.f, 0.f}, tag), rw, h * 16, W_ML + (pr * 4 + s) * 128, AUX_SC1);
      }
      if (w == 3 && n == 8) {
        v4u ml;
        for (unsigned spin = 0; spin < SPIN_CAP; ++spin) {
          RELOAD();
          ml = untag4(__builtin_amdgcn_raw_buffer_load_b128(rw, j0 * 128 + h * 16, W_ML + pr * 512, AUX_SC1), tag);
          if (__all((or4(ml) & 0xffu) == 0u)) break;
        }
        const float ms = __uint_as_float(ml[0]);
        float M = fmaxf(ms, dpp<0xB1>(ms)); M = fmaxf(M, dpp<0x4E>(M));
        float ls = __uint_as_float(ml[1]) * __builtin_amdgcn_exp2f((ms - M) * C2);
        ls += dpp<0xB1>(ls); ls += dpp<0x4E>(ls);
        if (lane < 16 && j0 == 0) *(LAS float*)(L + L_ML + 128 + h * 4) = __builtin_amdgcn_exp2f((m - M) * C2) * __builtin_amdgcn_rcpf(ls);
      }
      v4u bo[4];
      p_operands(__float_as_uint(sc[2 * n]), __float_as_uint(sc[2 * n + 1]), mask0, mask1, bo);
      while (lds_word(L + C_LAND) <= 64 + 4 * n + w) __builtin_amdgcn_s_sleep(1);
      pv32(lbase + ((4 * n + w) & 15) * SLOT + vv, bo, acc0, acc1);
      *(LAS int*)(L + C_DONE + w * 8) = 17 + n;
    }
    *(LAS f32x4*)(L + L_ACC + w * 2048 + vv) = acc0; *(LAS f32x4*)(L + L_ACC + w * 2048 + 1024 + vv) = acc1;
    lds_inc(L + C_NA, lane);
    STAMP(3);
    while (lds_word(L + C_LAND) < NUNIT) __builtin_amdgcn_s_sleep(1);
    STAMP(5);
    {
      const unsigned tp = now() + D1, tg = (unsigned)lds_word(L + C_DONE + 4) + LINE_END + D1G;
      unsigned tc;
      do tc = now(); while ((int)(tc - tp) < 0 || ((int)(tc - tg) < 0 && (int)(tc - tp) < 150));
    }
    STAMP(4);
    v4u af[4];
    {
      const int bb = 2 * w + (lane >> 5), c = lane & 31;
      const int vo = bb * (HKV * 4 * PA_REC) + c * 16, so = W_PA + (ch >> 2) * (4 * PA_REC) + (ch & 3) * 512;
      v4u a[4];
#pragma unroll
      for (int u = 0; u < 4; ++u) a[u] = __builtin_amdgcn_raw_buffer_load_b128(rw, vo, so + u * PA_REC, AUX_SC1);
      while (lds_word(L + C_WO) == 0) __builtin_amdgcn_s_sleep(1);
      LAS unsigned char* ap = L + L_W + (8 * g + ((lane & 15) >> 2)) * 128 + (16 * w + 4 * (lane & 3)) * 2;
#pragma unroll
      for (int ks = 0; ks < 4; ++ks) af[ks] = tr_frag(ap + ks * 4096);
      asm volatile("" : "+v"(af[0]), "+v"(af[1]), "+v"(af[2]), "+v"(af[3]));
      for (unsigned spin = 0; spin < SPIN_CAP; ++spin) {
        unsigned bad = 0;
#pragma unroll
        for (int u = 0; u < 4; ++u) { a[u] = untag4(a[u], tag); bad |= or4(a[u]); }
        if (__all((bad & 0xffu) == 0u)) break;
        RELOAD();
#pragma unroll
        for (int u = 0; u < 4; ++u) a[u] = __builtin_amdgcn_raw_buffer_load_b128(rw, vo, so + u * PA_REC, AUX_SC1);
      }
      STAMP(6);
      const f32x4 o = (__builtin_bit_cast(f32x4, a[0]) + __builtin_bit_cast(f32x4, a[1])) + (__builtin_bit_cast(f32x4, a[2]) + __builtin_bit_cast(f32x4, a[3]));
      *(LAS v2u*)(L + L_OT + bb * OT_ROW + c * 8) = v2u{cvtpk(o[0], o[1]), cvtpk(o[2], o[3])};
    }
    BARRIER();
    STAMP(7);
    {
      LAS unsigned char* bp = L + L_OT + (lane & 7) * OT_ROW + g * 16;
      f32x4 y = {0.f, 0.f, 0.f, 0.f};
#pragma unroll
      for (int ks = 0; ks < 4; ++ks) y = mfma32(af[ks], *(LAS v4u*)(bp + ks * 64), y);
      if ((lane & 15) < 8) __builtin_amdgcn_raw_buffer_store_b128(tag4(y, tag), rw, (lane & 7) * (32 * 2048) + g * 16, W_YB + (nb * 4 + w) * 2048 + ch * 64, AUX_SC1);
    }
    asm volatile("Lgqa_end_b:" ::: "memory");
    STAMP(8);
    STAMP_OUT();
    return;
  }

  __builtin_amdgcn_s_setprio(2);
  const unsigned tag = load_tag(ws);
  STAMP(6);
  {
    while (lds_word(L + C_LAND) < 0) __builtin_amdgcn_s_sleep(1);
    STAMP(10);
    f32x4 qa[4];
#pragma unroll
    for (int t = 0; t < 4; ++t) qa[t] = f32x4{0.f, 0.f, 0.f, 0.f};
    LAS unsigned char* ap = L + L_W + (8 * g + ((lane & 15) >> 2)) * 128 + 4 * (lane & 3) * 2;
    LAS unsigned char* bp = L + L_X + g * 128 + (lane & 7) * 16;
#pragma unroll
    for (int ks = 0; ks < 4; ++ks) {
      const v4u xb = *(LAS v4u*)(bp + ks * 512);
#pragma unroll
      for (int t = 0; t < 4; ++t) qa[t] = mfma32(tr_frag(ap + ks * 4096 + t * 32), xb, qa[t]);
    }
    STAMP(1);
    const bool hi = (lane & 8) != 0;
#pragma unroll
    for (int t = 0; t < 4; t += 2) {
      const f32x4 v = {hi ? qa[t + 1][0] : qa[t][0], hi ? qa[t + 1][1] : qa[t][1], hi ? qa[t + 1][2] : qa[t][2], hi ? qa[t + 1][3] : qa[t][3]};
      if (!(XF & 1)) __builtin_amdgcn_raw_buffer_store_b128(tag4(v, tag), rw, (lane & 7) * (HKV * 8192) + (hi ? 64 : 0) + g * 16, W_QG + (cb >> 3) * 8192 + kq * 2048 + (cb & 7) * 256 + t * 64, AUX_SC1);
    }
  }
  STAMP(2);
  while ((int)(now() - t0) < QPOLL) __builtin_amdgcn_s_sleep(2);
  {
    const int so = W_QG + pr * 8192;
    v4u a[8];
    for (unsigned spin = 0; spin < SPIN_CAP; ++spin) {
      RELOAD();
#pragma unroll
      for (int u = 0; u < 8; ++u) a[u] = __builtin_amdgcn_raw_buffer_load_b128(rw, (XF & 2) ? 0x7000000 : lane * 16 + (u & 3) * 1024, so + (u >> 2) * 4096, AUX_SC1);
      unsigned bad = 0;
#pragma unroll
      for (int u = 0; u < 8; ++u) { a[u] = untag4(a[u], tag); bad |= or4(a[u]); }
      if ((XF & 3) || __all((bad & 0xffu) == 0u)) break;
      STAMP(15);
    }
#pragma unroll
    for (int u = 0; u < 2; ++u) {
      const f32x4 v = (__builtin_bit_cast(f32x4, a[u]) + __builtin_bit_cast(f32x4, a[2 + u])) + (__builtin_bit_cast(f32x4, a[4 + u]) + __builtin_bit_cast(f32x4, a[6 + u]));
      *(LAS v2u*)(L + L_Q + u * 512 + lane * 8) = v2u{cvtpk(v[0], v[1]), cvtpk(v[2], v[3])};
    }
    asm volatile("s_waitcnt lgkmcnt(0)" ::: "memory");
    *(LAS v2u*)(L + C_READY) = v2u{1u, tag};
  }
  STAMP(3);
  if (!(XF & 32) && lane == 0) {
    const unsigned a = __builtin_nontemporal_load((const GAS unsigned*)(vc + pbase + unit_off(0, s) + rot));
    const unsigned b2 = __builtin_nontemporal_load((const GAS unsigned*)(wo + ch * 128 * 1024 + nb * 128));
    const unsigned c = __builtin_nontemporal_load((const GAS unsigned*)(out + j * 32));
    asm volatile("" :: "v"(a), "v"(b2), "v"(c));
  }
  if (FB_ON) {
    unsigned k = now() / FB_P + 1u;
    float af = 0.f, a1 = 0.f, a2 = 0.f, dr = 0.f;
    int nv = 0;
#pragma unroll 1
    for (int it = 0; it < 40; ++it, ++k) {
      const unsigned tk = k * FB_P;
      while ((int)(now() - tk) < 0) __builtin_amdgcn_s_sleep(2);
      RELOAD();
      const v2u pr2 = *(LAS v2u*)(L + C_LAND);
      if (__builtin_amdgcn_readfirstlane(pr2[0]) >= NUNIT - 12) break;
      const v2u fb = __builtin_amdgcn_raw_buffer_load_b64(rw, (lane & (FB_NREP - 1)) * FB_STR, W_FB + ((k - 1u) & 63u) * FB_SLOT, AUX_SC1);
      if (j < FB_NREP && lane == 0 && pr2[1] != 0u) {
        const float p = (float)(8 * ((int)pr2[0] + (TH_HI + 7) / 8)) + fminf((float)(int)(tk - pr2[1]) * (1.f / 3.4f), 8.f);
        __builtin_amdgcn_raw_buffer_store_b64(v2u{__float_as_uint(p), tag ^ k}, rw, 0, W_FB + (k & 63u) * FB_SLOT + j * FB_STR, AUX_SC1);
      }
      const bool ok = fb[1] == (tag ^ (k - 1u));
      float ps = ok ? __uint_as_float(fb[0]) : 0.f, pc = ok ? 1.f : 0.f;
      ps += dpp<0xB1>(ps); ps += dpp<0x4E>(ps); ps += dpp<0x124>(ps); ps += dpp<0x128>(ps); ps = xrow_sum(ps);
      pc += dpp<0xB1>(pc); pc += dpp<0x4E>(pc); pc += dpp<0x124>(pc); pc += dpp<0x128>(pc); pc = xrow_sum(pc);
      if (__builtin_amdgcn_readfirstlane(pc < 48.f)) continue;
      const float pbar = ps * __builtin_amdgcn_rcpf(pc);
      const float tl = TL0;
      const float as = (float)(int)(tk - FB_P - t0) - pbar * tl - (float)(DELTA + PHASE / 2);
      if (nv == 0) { af = as; a1 = as; a2 = as; }
      else {
        const float er = as - (FB_REF ? a2 : af);
        if (nv >= FB_I0) dr = fminf(fmaxf(dr + FB_KI * fminf(er, 30.f), -6.f), 30.f);
        a2 = a1; a1 = af; af += dr + FB_G * er;
      }
      ++nv;
      if (FB_USE) *(LAS unsigned*)(L + C_DONE + 4 + (lane & 3) * 8) = t0 + (unsigned)(int)af;
    }
  }
  {
    const v4i ro = mkrsrc(wo, HQ * DH * DM * 2);
    const unsigned vo = ((lane >> 5) + 8 * ((lane >> 3) & 3)) * 1024 + (lane & 7) * 16;
    while (lds_word(L + C_LAND) < WOPOS) __builtin_amdgcn_s_sleep(1);
#pragma unroll 1
    for (int u = 0; u < 16; ++u) dma1_nt(ro, vo, (ch * 128 + 32 * ((u >> 1) & 3) + 4 * (u >> 3) + 2 * (u & 1)) * 1024 + nb * 128, lbase + L_W + u * 1024);
    WAITVM(0);
    *(LAS int*)(L + C_WO) = 1;
    STAMP(11);
  }
  BARRIER();
  STAMP(7);
  { const unsigned tp = now(); while ((int)(now() - tp) < D2) {} }
  {
    v4u a0, a1;
    for (unsigned spin = 0; spin < SPIN_CAP; ++spin) {
      RELOAD();
      a0 = untag4(__builtin_amdgcn_raw_buffer_load_b128(rw, lane * 16, W_YB + j * 2048, AUX_SC1), tag);
      a1 = untag4(__builtin_amdgcn_raw_buffer_load_b128(rw, lane * 16 + 1024, W_YB + j * 2048, AUX_SC1), tag);
      if (__all(((or4(a0) | or4(a1)) & 0xffu) == 0u)) break;
    }
    STAMP(8);
    f32x4 y = __builtin_bit_cast(f32x4, a0) + __builtin_bit_cast(f32x4, a1);
#pragma unroll
    for (int q = 0; q < 4; ++q) { float t = y[q]; t += dpp<0x124>(t); t += dpp<0x128>(t); y[q] = xrow_sum(t); }
    if (lane < 4) *(GAS v2u*)(out + j * 32 + lane * 8) = v2u{cvtpk(y[0], y[1]), cvtpk(y[2], y[3])};
    if (j == 0 && lane == 0) __builtin_amdgcn_raw_buffer_store_b32(tag, rw, 0, W_E, AUX_SC1);
  }
  asm volatile("Lgqa_end_c:" ::: "memory");
  STAMP(9);
  STAMP_OUT();
}

extern "C" void kernel_launch(void* const* d_in, const int* in_sizes, int n_in, void* d_out, int out_size, void* d_ws, size_t ws_size,
                              hipStream_t stream) {
  gqa_fused<<<NWG, NTHR, 0, stream>>>((const unsigned char*)d_in[0], (const unsigned char*)d_in[1], (const unsigned char*)d_in[2], (const unsigned char*)d_in[3],
                                      (const unsigned char*)d_in[4], (unsigned char*)d_out, (unsigned char*)d_ws TUNE_PASS);
}
```

```cpp
#define ROW0 1
#define ROW1 28
#define NFIN 3
#define MINSP 20
#define DELTA 0
#define FB_G 0.4f
#define FB_KI 0.f
#define TL0 3.35f
#define ROTM 1
#define ROTS 1
#define D1 10
#define D2 45
#define D1G 122
#define WOPOS 125
#define PRETOUCH 0
#define DIAG 102
#define DSIGN 1
#include <hip/hip_runtime.h>
#include <hip/hip_bf16.h>
#include <stdint.h>

#define GAS __attribute__((address_space(1)))
#define LAS __attribute__((address_space(3)))
typedef unsigned v4u __attribute__((ext_vector_type(4)));
typedef unsigned v2u __attribute__((ext_vector_type(2)));
typedef int v4i __attribute__((ext_vector_type(4)));
typedef float f32x4 __attribute__((ext_vector_type(4)));
typedef float f32x32 __attribute__((ext_vector_type(32)));
typedef unsigned v8u __attribute__((ext_vector_type(8)));
typedef short s16x4 __attribute__((ext_vector_type(4)));
typedef __bf16 bf16x8 __attribute__((ext_vector_type(8)));

constexpr int NB = 8, DM = 512, HQ = 32, HKV = 8, SK = 8192, DH = 128;
constexpr int NWG = 256, NTHR = 384;
constexpr int NUNIT = 128;
constexpr int SLOT = 8192;
constexpr float C2 = 0.088388347648318440f * 1.4426950408889634f;
#define NREG 4
#define TH 16
#define FB_ON 1
#define FB_USE 1
#define FB_STR 8
#define FB_P 192
#define FB_NREP 32
#define FB_I0 3
#define FB_REF 0
#define PHASE 28
#define HOLD 33
#define TH_HI 24
#define QPOLL 450
#define CODEPF 1
#define XF 0
#define TUNE_ARG
#define TUNE_PASS
#define LINE_END ((unsigned)(TL0 * 1024.f))
constexpr unsigned SPIN_CAP = 1u << 16;
constexpr int L_RING = 0, L_Q = 16 * SLOT, L_ACC = L_Q + 1024, L_PL = L_ACC + 4 * 2048, L_OT = L_PL + 2048 + 16, OT_ROW = 272, L_ML = L_OT + 8 * OT_ROW, L_CTL = L_ML + 144;
constexpr int L_X = L_ACC, L_W = L_CTL + 64, L_TOT = L_W + 16384;
constexpr int C_READY = L_CTL, C_TAG = L_CTL + 4, C_LAND = L_CTL + 8, C_DONE = L_CTL + 16, C_NM = L_CTL + 48, C_NL = L_CTL + 52, C_NA = L_CTL + 56, C_WO = L_CTL + 60;
static_assert(L_CTL % 16 == 0 && L_TOT <= 160 * 1024, "lds map");
constexpr int W_E = 0, W_FB = 1024, FB_SLOT = 64 * FB_STR, W_ML = W_FB + 64 * FB_SLOT + 1024, W_QG = W_ML + NWG * 128, W_PA = W_QG + NB * HQ * DH * 4 * 4, PA_REC = 2048, W_YB = W_PA + NWG * PA_REC,
              W_END = W_YB + NWG * 2048;
#define STAMP(i) do { if (DIAG % 100 == (i) && DIAG / 100 == (wv > 2 ? 2 : wv) && lane == 0) (void)__hip_atomic_fetch_max((LAS int*)(L + L_OT - 16), (int)(now() - t0), __ATOMIC_RELAXED, __HIP_MEMORY_SCOPE_WORKGROUP); } while (0)
#define STAMP_OUT() do { if (wv == 1) { while ((int)(now() - t0) < 9000) __builtin_amdgcn_s_sleep(8); const int x_ = lds_word(L + L_OT - 16); const int tg_ = DSIGN >= 0 ? 10000 + DSIGN * 10 * x_ : 70000 - 10 * x_; while ((int)(now() - t0) < tg_) __builtin_amdgcn_s_sleep(4); } } while (0)

__device__ __forceinline__ unsigned cvtpk(float lo, float hi) {
  typedef __bf16 bf16x2_t __attribute__((ext_vector_type(2)));
  typedef float f32x2_t __attribute__((ext_vector_type(2)));
  return __builtin_bit_cast(unsigned, __builtin_convertvector(f32x2_t{lo, hi}, bf16x2_t));
}
template <int CTRL> __device__ __forceinline__ float dpp(float x) {
  return __builtin_bit_cast(float, __builtin_amdgcn_mov_dpp(__builtin_bit_cast(int, x), CTRL, 0xf, 0xf, true));
}
__device__ __forceinline__ float xrow_max(float x) {
  auto s = __builtin_amdgcn_permlane16_swap(__float_as_uint(x), __float_as_uint(x), false, false);
  x = fmaxf(__uint_as_float(s[0]), __uint_as_float(s[1]));
  auto t = __builtin_amdgcn_permlane32_swap(__float_as_uint(x), __float_as_uint(x), false, false);
  return fmaxf(__uint_as_float(t[0]), __uint_as_float(t[1]));
}
__device__ __forceinline__ float xrow_sum(float x) {
  auto s = __builtin_amdgcn_permlane16_swap(__float_as_uint(x), __float_as_uint(x), false, false);
  x = __uint_as_float(s[0]) + __uint_as_float(s[1]);
  auto t = __builtin_amdgcn_permlane32_swap(__float_as_uint(x), __float_as_uint(x), false, false);
  return __uint_as_float(t[0]) + __uint_as_float(t[1]);
}
__device__ __forceinline__ v4i mkrsrc(const void* p, unsigned bytes) {
  const unsigned long long a = (unsigned long long)p;
  v4i r; r[0] = (int)(unsigned)a; r[1] = (int)((unsigned)(a >> 32) & 0xffffu); r[2] = (int)bytes; r[3] = 0x00020000; return r;
}
__device__ __forceinline__ unsigned now() { return (unsigned)__builtin_amdgcn_s_memrealtime(); }
#define RFL(x) ((unsigned)__builtin_amdgcn_readfirstlane((int)(x)))
#define STR_(x) #x
#define STRX_(x) STR_(x)
#define WAITVM(n) asm volatile("s_waitcnt vmcnt(" STR_(n) ")" ::: "memory")
#define BARRIER() asm volatile("s_waitcnt lgkmcnt(0)\n\ts_barrier" ::: "memory")
#define RELOAD() asm volatile("" ::: "memory")
__device__ __forceinline__ void issue_unit(v4i r, unsigned vv, unsigned so0, unsigned so1, unsigned lds) {
  asm volatile("s_mov_b32 m0, %4\n\ts_nop 0\n\t"
               "buffer_load_dwordx4 %1, %0, %2 offen nt lds\n\t"
               "buffer_load_dwordx4 %1, %0, %2 offen offset:1024 nt lds\n\t"
               "buffer_load_dwordx4 %1, %0, %2 offen offset:2048 nt lds\n\t"
               "buffer_load_dwordx4 %1, %0, %2 offen offset:3072 nt lds\n\t"
               "s_add_u32 m0, m0, 0x1000\n\ts_nop 0\n\t"
               "buffer_load_dwordx4 %1, %0, %3 offen nt lds\n\t"
               "buffer_load_dwordx4 %1, %0, %3 offen offset:1024 nt lds\n\t"
               "buffer_load_dwordx4 %1, %0, %3 offen offset:2048 nt lds\n\t"
               "buffer_load_dwordx4 %1, %0, %3 offen offset:3072 nt lds"
               :: "s"(r), "v"(vv), "s"(so0), "s"(so1), "s"(lds) : "memory", "scc");
}
#define AM_A 1
#define AM_B 16
#define AM_C 14
#define AM_D 13
#define AM_E 12
#define unit_off(u, s) ((unsigned)((((u) >> AM_A) << AM_B) + ((s) << AM_C) + (((u) & ((1 << AM_A) - 1)) << AM_D)))
#define PIECE2 (1u << AM_E)
__device__ __forceinline__ void dma1_nt(v4i r, unsigned voff, unsigned soff, unsigned lds) {
  asm volatile("s_mov_b32 m0, %3\n\ts_nop 0\n\tbuffer_load_dwordx4 %1, %0, %2 offen nt lds" :: "s"(r), "v"(voff), "s"(soff), "s"(lds) : "memory");
}
__device__ __forceinline__ void dma1(v4i r, unsigned voff, unsigned soff, unsigned lds) {
  asm volatile("s_mov_b32 m0, %3\n\ts_nop 0\n\tbuffer_load_dwordx4 %1, %0, %2 offen lds" :: "s"(r), "v"(voff), "s"(soff), "s"(lds) : "memory");
}
__device__ __forceinline__ f32x4 mfma32(v4u a, v4u b, f32x4 c) {
  return __builtin_amdgcn_mfma_f32_16x16x32_bf16(__builtin_bit_cast(bf16x8, a), __builtin_bit_cast(bf16x8, b), c, 0, 0, 0);
}
__device__ __forceinline__ f32x4 mfma16(unsigned a0, unsigned a1, v2u b, f32x4 c) {
  return __builtin_amdgcn_mfma_f32_16x16x16bf16_1k(__builtin_bit_cast(s16x4, v2u{a0, a1}), __builtin_bit_cast(s16x4, b), c, 0, 0, 0);
}
__device__ __forceinline__ v4u tr_frag(LAS unsigned char* p) {
  const v2u lo = __builtin_bit_cast(v2u, __builtin_amdgcn_ds_read_tr16_b64_v4i16((LAS s16x4*)p));
  const v2u hi = __builtin_bit_cast(v2u, __builtin_amdgcn_ds_read_tr16_b64_v4i16((LAS s16x4*)(p + 512)));
  return v4u{lo[0], lo[1], hi[0], hi[1]};
}
__device__ __forceinline__ float score16(const v4u* K, v4u qf, unsigned j0) {
  float sc[4];
#pragma unroll
  for (int u = 0; u < 4; ++u) {
    const f32x4 d = mfma32(K[u], qf, f32x4{0.f, 0.f, 0.f, 0.f});
    sc[u] = (dpp<0x00>(d[0]) + dpp<0x55>(d[1])) + (dpp<0xAA>(d[2]) + dpp<0xFF>(d[3]));
  }
  return (j0 & 2) ? ((j0 & 1) ? sc[3] : sc[2]) : ((j0 & 1) ? sc[1] : sc[0]);
}
template <int CTRL> __device__ __forceinline__ unsigned dppu(unsigned x) { return (unsigned)__builtin_amdgcn_mov_dpp((int)x, CTRL, 0xf, 0xf, true); }
__device__ __forceinline__ void p_operands(unsigned k0, unsigned k1, unsigned mask0, unsigned mask1, v4u (&bo)[4]) {
  bo[0] = v4u{dppu<0x00>(k0) & mask0, dppu<0x00>(k0) & mask1, dppu<0x55>(k0) & mask0, dppu<0x55>(k0) & mask1};
  bo[1] = v4u{dppu<0xAA>(k0) & mask0, dppu<0xAA>(k0) & mask1, dppu<0xFF>(k0) & mask0, dppu<0xFF>(k0) & mask1};
  bo[2] = v4u{dppu<0x00>(k1) & mask0, dppu<0x00>(k1) & mask1, dppu<0x55>(k1) & mask0, dppu<0x55>(k1) & mask1};
  bo[3] = v4u{dppu<0xAA>(k1) & mask0, dppu<0xAA>(k1) & mask1, dppu<0xFF>(k1) & mask0, dppu<0xFF>(k1) & mask1};
  asm volatile("" : "+v"(bo[0]), "+v"(bo[1]), "+v"(bo[2]), "+v"(bo[3]));
}
__device__ __forceinline__ void pv32(unsigned p, const v4u (&bo)[4], f32x4& acc0, f32x4& acc1) {
  v4u a0, a1, a2, a3, a4, a5, a6, a7;
  asm volatile(
      "ds_read2st64_b64 %[a0], %[p0] offset0:0 offset1:2\n\t"
      "ds_read2st64_b64 %[a1], %[p1] offset0:0 offset1:2\n\t"
      "ds_read2st64_b64 %[a2], %[p0] offset0:4 offset1:6\n\t"
      "ds_read2st64_b64 %[a3], %[p1] offset0:4 offset1:6\n\t"
      "ds_read2st64_b64 %[a4], %[p0] offset0:8 offset1:10\n\t"
      "ds_read2st64_b64 %[a5], %[p1] offset0:8 offset1:10\n\t"
      "ds_read2st64_b64 %[a6], %[p0] offset0:12 offset1:14\n\t"
      "ds_read2st64_b64 %[a7], %[p1] offset0:12 offset1:14\n\t"
      "s_waitcnt lgkmcnt(7)\n\tv_mfma_f32_16x16x32_bf16 %[c0], %[a0], %[b0], %[c0]\n\t"
      "s_waitcnt lgkmcnt(6)\n\tv_mfma_f32_16x16x32_bf16 %[c1], %[a1], %[b0], %[c1]\n\t"
      "s_waitcnt lgkmcnt(5)\n\tv_mfma_f32_16x16x32_bf16 %[c0], %[a2], %[b1], %[c0]\n\t"
      "s_waitcnt lgkmcnt(4)\n\tv_mfma_f32_16x16x32_bf16 %[c1], %[a3], %[b1], %[c1]\n\t"
      "s_waitcnt lgkmcnt(3)\n\tv_mfma_f32_16x16x32_bf16 %[c0], %[a4], %[b2], %[c0]\n\t"
      "s_waitcnt lgkmcnt(2)\n\tv_mfma_f32_16x16x32_bf16 %[c1], %[a5], %[b2], %[c1]\n\t"
      "s_waitcnt lgkmcnt(1)\n\tv_mfma_f32_16x16x32_bf16 %[c0], %[a6], %[b3], %[c0]\n\t"
      "s_waitcnt lgkmcnt(0)\n\tv_mfma_f32_16x16x32_bf16 %[c1], %[a7], %[b3], %[c1]\n\t"
      "s_nop 7"
      : [c0] "+v"(acc0), [c1] "+v"(acc1), [a0] "=&v"(a0), [a1] "=&v"(a1), [a2] "=&v"(a2), [a3] "=&v"(a3), [a4] "=&v"(a4), [a5] "=&v"(a5), [a6] "=&v"(a6), [a7] "=&v"(a7)
      : [p0] "v"(p), [p1] "v"(p + 8), [b0] "v"(bo[0]), [b1] "v"(bo[1]), [b2] "v"(bo[2]), [b3] "v"(bo[3]) : "memory");
}
constexpr int AUX_SC1 = 16;
__device__ __forceinline__ v4u tag4(f32x4 v, unsigned tag) {
  const v4u u = __builtin_bit_cast(v4u, v);
  return v4u{(u[0] & 0xffffff00u) | (tag & 0xffu), (u[1] & 0xffffff00u) | ((tag >> 8) & 0xffu), (u[2] & 0xffffff00u) | ((tag >> 16) & 0xffu), (u[3] & 0xffffff00u) | (tag >> 24)};
}
__device__ __forceinline__ v4u untag4(v4u u, unsigned tag) {
  return v4u{u[0] ^ (tag & 0xffu), u[1] ^ ((tag >> 8) & 0xffu), u[2] ^ ((tag >> 16) & 0xffu), u[3] ^ (tag >> 24)};
}
__device__ __forceinline__ unsigned load_tag(const unsigned char* ws) {
  unsigned e0;
  asm volatile("s_load_dword %0, %1, 0x0\n\ts_waitcnt lgkmcnt(0)" : "=&s"(e0) : "s"(ws) : "memory");
  return ((e0 & 0x7f7f7f7fu) + 0x51276b35u) ^ (e0 & 0x80808080u);
}
__device__ __forceinline__ unsigned or4(v4u u) { return (u[0] | u[1]) | (u[2] | u[3]); }
__device__ __forceinline__ void lds_inc(LAS unsigned char* p, int lane) {
  RELOAD(); if (lane == 0) (void)__hip_atomic_fetch_add((LAS int*)p, 1, __ATOMIC_RELAXED, __HIP_MEMORY_SCOPE_WORKGROUP); RELOAD();
}
__device__ __forceinline__ int lds_word(LAS unsigned char* p) { RELOAD(); const int v = __builtin_amdgcn_readfirstlane(*(LAS int*)p); RELOAD(); return v; }

__global__ void __launch_bounds__(NTHR, 1) gqa_fused(const unsigned char* __restrict__ x, const unsigned char* __restrict__ kc, const unsigned char* __restrict__ vc,
                                                     const unsigned char* __restrict__ wq, const unsigned char* __restrict__ wo, unsigned char* __restrict__ out,
                                                     unsigned char* ws TUNE_ARG) {
  __shared__ __attribute__((aligned(16))) unsigned char smem[L_TOT];
  LAS unsigned char* L = (LAS unsigned char*)smem;
  const unsigned lbase = (unsigned)(size_t)L;
  unsigned long long pc0;
  unsigned clen;
  asm volatile("s_getpc_b64 %0\nLgqa_pc:\n\ts_mov_b32 %1, Lgqa_end_a-Lgqa_pc\n\ts_max_u32 %1, %1, Lgqa_end_b-Lgqa_pc\n\ts_max_u32 %1, %1, Lgqa_end_c-Lgqa_pc" : "=s"(pc0), "=s"(clen) :: "scc");
  const unsigned t0 = now();
  const int lane = threadIdx.x & 63, wv = __builtin_amdgcn_readfirstlane(threadIdx.x >> 6);
  const int j = blockIdx.x, xg = j & 7, idx = j >> 3;
  const int yy = (xg - idx) & 7, pr = (XF & 16) ? (idx >> 2) * 8 + xg : 2 * idx + (yy >> 2), s = (XF & 16) ? (idx & 3) : yy & 3;
  const int ch = 4 * xg + (idx >> 3), nb = idx & 7;
  const int cb = j & 63, kq = j >> 6;
  const int g = lane >> 4;
  const unsigned rot = ((unsigned)(pr * ROTM >> ROTS) << AM_B) & 0x1fffffu;
  const unsigned vk = (((lane >> 2) & 3) * 16 + g * 4 + (lane & 3)) * 16, vv = lane * 16;

  asm volatile("" :: "s"(x), "s"(kc), "s"(vc), "s"(wq), "s"(wo), "s"(out), "s"(ws));
  const size_t pbase = (size_t)pr * (SK * DH * 2);
  const v4i rs = mkrsrc(kc + pbase, SK * DH * 2);
  if (wv == 0) {
    __builtin_amdgcn_s_setprio(3);
    const v4i rx = mkrsrc(x, NB * DM * 2), rq = mkrsrc(wq, (XF & 8) ? 0 : DM * HQ * DH * 2);
    const unsigned vx = (lane & 7) * 1024 + (lane >> 3) * 16, vq = (lane >> 3) * 8192 + (lane & 7) * 16;
#pragma unroll 1
    for (int u = 0; u < 16; ++u) dma1_nt(rq, vq, (kq * 16 + u) * 65536 + cb * 128, lbase + L_W + u * 1024);
    dma1(rx, vx, kq * 256, lbase + L_X);
    dma1(rx, vx, kq * 256 + 128, lbase + L_X + 1024);
    issue_unit(rs, vv, unit_off(0, s) + rot, unit_off(0, s) + rot + PIECE2, lbase);
    STAMP(1);
  }
  unsigned pf = 0;
  if (CODEPF && wv >= 3) {
    const unsigned co = (unsigned)(lane + (5 - wv) * 64) * 128u;
    if (co < clen + 128u) asm volatile("global_load_dword %0, %1, %2" : "=&v"(pf) : "v"(co), "s"(pc0 & ~127ull) : "memory");
  }
  if (wv == 1 && DIAG != 1) *(LAS int*)(L + L_OT - 16) = 0;
  if (wv == 1 && lane < 16) *(LAS unsigned*)(L + L_CTL + lane * 4) = lane == 2 ? ~0u : (lane >= 4 && lane < 12 && (lane & 1)) ? t0 - (1u << 20) : 0u;
  BARRIER();
  const __amdgpu_buffer_rsrc_t rw = __builtin_amdgcn_make_buffer_rsrc((void*)ws, 0, W_END, 0x00020000);
  const unsigned j0 = lane & 3;
  const int h = (lane >> 2) & 3;
  const unsigned mask0 = j0 == 0 ? 0x0000ffffu : (j0 == 1 ? 0xffff0000u : 0u);
  const unsigned mask1 = j0 == 2 ? 0x0000ffffu : (j0 == 3 ? 0xffff0000u : 0u);

  if (wv == 0) {
#pragma unroll 1
    for (int u = 1; u < 4 * ROW0; ++u) {
      WAITVM(TH);
      const unsigned so = (unit_off(u, s) + rot) & 0x1fffffu;
      issue_unit(rs, vv, so, so + PIECE2, lbase + u * SLOT);
      *(LAS int*)(L + C_LAND) = u - (TH + 7) / 8;
    }
    v4u bo[NFIN][4];
    unsigned vo, tg0, tg1, tg2, tg3, hm = 0xffffff00u;
    float scale;
    {
      constexpr unsigned rc = (unsigned)(TL0 * 512.f), uc = rc >> 6;
      unsigned ra = RFL(ROW0 * rc + (((unsigned)(j * 37) & 31u) * PHASE >> 1)), bs = ra >> 4, so = RFL((unit_off(4 * ROW0, s) + rot) & 0x1fffffu), ta, tb;
      int nd = ROW0 - 3;
      const unsigned long long vb = (unsigned long long)(vc + pbase) & 0xffffffffffffull;
      const unsigned long long kb = (unsigned long long)(kc + pbase) & 0xffffffffffffull;
#define STAMP_ASM
#define STAMP_OPS
#define STAMP_INS
#pragma unroll 1
      for (int pass = 0; pass < 2; ++pass) {
      while (lds_word(L + C_DONE) < nd) __builtin_amdgcn_s_sleep(1);
      asm volatile(
      "s_mov_b64 s[36:37], %[kb]\n\t"
      "s_mov_b32 s38, 0x200000\n\t"
      "s_mov_b32 s39, 0x20000\n\t"
      "s_mov_b32 m0, %[m0i]\n\t"
      "v_mov_b32 v2, %[ld0]\n"
      "LT%=:\n\t"
      "s_waitcnt vmcnt(" STRX_(TH_HI) ")\n\t"
      "buffer_load_dwordx4 %[v0], s[36:39], %[so] offen nt lds\n\t"
      "buffer_load_dwordx4 %[v0], s[36:39], %[so] offen offset:1024 nt lds\n\t"
      "buffer_load_dwordx4 %[v0], s[36:39], %[so] offen offset:2048 nt lds\n\t"
      "buffer_load_dwordx4 %[v0], s[36:39], %[so] offen offset:3072 nt lds\n\t"
      "s_add_u32 m0, m0, 0x1000\n\t"
      "s_memrealtime s[40:41]\n\t"
      "buffer_load_dwordx4 %[v1], s[36:39], %[so] offen nt lds\n\t"
      "buffer_load_dwordx4 %[v1], s[36:39], %[so] offen offset:1024 nt lds\n\t"
      "buffer_load_dwordx4 %[v1], s[36:39], %[so] offen offset:2048 nt lds\n\t"
      "buffer_load_dwordx4 %[v1], s[36:39], %[so] offen offset:3072 nt lds\n\t"
      "ds_read_b64 v[4:5], %[vc] offset:24\n\t"
      "s_add_u32 m0, m0, 0x1000\n\t"
      "s_add_u32 %[so], %[so], %[st0]\n\t"
      "s_and_b32 %[so], %[so], 0x1fffff\n\t"
      "v_add_u32 v2, 1, v2\n\t"
      "s_add_u32 %[bs], %[bs], %[uc]\n\t"
      "s_waitcnt lgkmcnt(0)\n\t"
      "v_mov_b32 v3, s40\n\t"
      "v_readfirstlane_b32 %[a], v5\n\t"
      "v_readfirstlane_b32 %[b], v4\n\t"
      "ds_write_b64 %[vc], v[2:3] offset:8\n\t"
      STAMP_ASM
      "s_add_u32 %[a], %[a], %[bs]\n\t"
      "s_sub_i32 %[a], %[a], s40\n\t"
      "s_min_i32 %[a], %[a], %[hold]\n\t"
      "s_max_i32 %[a], %[a], %[msp]\n\t"
      "s_cmp_lt_i32 %[b], %[nd]\n\t"
      "s_cbranch_scc1 LS0%=\n"
      "LB0%=:\n\t"
      "s_cmp_lt_i32 %[a], 8\n\t"
      "s_cbranch_scc1 LN0%=\n\t"
      "s_add_u32 %[a], %[a], s40\n"
      "LW0%=:\n\t"
      "s_memrealtime s[40:41]\n\t"
      "s_waitcnt lgkmcnt(0)\n\t"
      "s_sub_i32 %[b], %[a], s40\n\t"
      "s_cmp_gt_i32 %[b], 0\n\t"
      "s_cbranch_scc1 LW0%=\n"
      "LN0%=:\n\t"
      "s_waitcnt vmcnt(" STRX_(TH_HI) ")\n\t"
      "buffer_load_dwordx4 %[v0], s[36:39], %[so] offen nt lds\n\t"
      "buffer_load_dwordx4 %[v0], s[36:39], %[so] offen offset:1024 nt lds\n\t"
      "buffer_load_dwordx4 %[v0], s[36:39], %[so] offen offset:2048 nt lds\n\t"
      "buffer_load_dwordx4 %[v0], s[36:39], %[so] offen offset:3072 nt lds\n\t"
      "s_add_u32 m0, m0, 0x1000\n\t"
      "s_memrealtime s[40:41]\n\t"
      "buffer_load_dwordx4 %[v1], s[36:39], %[so] offen nt lds\n\t"
      "buffer_load_dwordx4 %[v1], s[36:39], %[so] offen offset:1024 nt lds\n\t"
      "buffer_load_dwordx4 %[v1], s[36:39], %[so] offen offset:2048 nt lds\n\t"
      "buffer_load_dwordx4 %[v1], s[36:39], %[so] offen offset:3072 nt lds\n\t"
      "ds_read_b64 v[4:5], %[vc] offset:32\n\t"
      "s_add_u32 m0, m0, 0x1000\n\t"
      "s_add_u32 %[so], %[so], %[st1]\n\t"
      "s_and_b32 %[so], %[so], 0x1fffff\n\t"
      "v_add_u32 v2, 1, v2\n\t"
      "s_add_u32 %[bs], %[bs], %[uc]\n\t"
      "s_waitcnt lgkmcnt(0)\n\t"
      "v_mov_b32 v3, s40\n\t"
      "v_readfirstlane_b32 %[a], v5\n\t"
      "v_readfirstlane_b32 %[b], v4\n\t"
      "ds_write_b64 %[vc], v[2:3] offset:8\n\t"
      STAMP_ASM
      "s_add_u32 %[a], %[a], %[bs]\n\t"
      "s_sub_i32 %[a], %[a], s40\n\t"
      "s_min_i32 %[a], %[a], %[hold]\n\t"
      "s_max_i32 %[a], %[a], %[msp]\n\t"
      "s_cmp_lt_i32 %[b], %[nd]\n\t"
      "s_cbranch_scc1 LS1%=\n"
      "LB1%=:\n\t"
      "s_cmp_lt_i32 %[a], 8\n\t"
      "s_cbranch_scc1 LN1%=\n\t"
      "s_add_u32 %[a], %[a], s40\n"
      "LW1%=:\n\t"
      "s_memrealtime s[40:41]\n\t"
      "s_waitcnt lgkmcnt(0)\n\t"
      "s_sub_i32 %[b], %[a], s40\n\t"
      "s_cmp_gt_i32 %[b], 0\n\t"
      "s_cbranch_scc1 LW1%=\n"
      "LN1%=:\n\t"
      "s_waitcnt vmcnt(" STRX_(TH_HI) ")\n\t"
      "buffer_load_dwordx4 %[v0], s[36:39], %[so] offen nt lds\n\t"
      "buffer_load_dwordx4 %[v0], s[36:39], %[so] offen offset:1024 nt lds\n\t"
      "buffer_load_dwordx4 %[v0], s[36:39], %[so] offen offset:2048 nt lds\n\t"
      "buffer_load_dwordx4 %[v0], s[36:39], %[so] offen offset:3072 nt lds\n\t"
      "s_add_u32 m0, m0, 0x1000\n\t"
      "s_memrealtime s[40:41]\n\t"
      "buffer_load_dwordx4 %[v1], s[36:39], %[so] offen nt lds\n\t"
      "buffer_load_dwordx4 %[v1], s[36:39], %[so] offen offset:1024 nt lds\n\t"
      "buffer_load_dwordx4 %[v1], s[36:39], %[so] offen offset:2048 nt lds\n\t"
      "buffer_load_dwordx4 %[v1], s[36:39], %[so] offen offset:3072 nt lds\n\t"
      "ds_read_b64 v[4:5], %[vc] offset:40\n\t"
      "s_add_u32 m0, m0, 0x1000\n\t"
      "s_add_u32 %[so], %[so], %[st2]\n\t"
      "s_and_b32 %[so], %[so], 0x1fffff\n\t"
      "v_add_u32 v2, 1, v2\n\t"
      "s_add_u32 %[bs], %[bs], %[uc]\n\t"
      "s_waitcnt lgkmcnt(0)\n\t"
      "v_mov_b32 v3, s40\n\t"
      "v_readfirstlane_b32 %[a], v5\n\t"
      "v_readfirstlane_b32 %[b], v4\n\t"
      "ds_write_b64 %[vc], v[2:3] offset:8\n\t"
      STAMP_ASM
      "s_add_u32 %[a], %[a], %[bs]\n\t"
      "s_sub_i32 %[a], %[a], s40\n\t"
      "s_min_i32 %[a], %[a], %[hold]\n\t"
      "s_max_i32 %[a], %[a], %[msp]\n\t"
      "s_cmp_lt_i32 %[b], %[nd]\n\t"
      "s_cbranch_scc1 LS2%=\n"
      "LB2%=:\n\t"
      "s_cmp_lt_i32 %[a], 8\n\t"
      "s_cbranch_scc1 LN2%=\n\t"
      "s_add_u32 %[a], %[a], s40\n"
      "LW2%=:\n\t"
      "s_memrealtime s[40:41]\n\t"
      "s_waitcnt lgkmcnt(0)\n\t"
      "s_sub_i32 %[b], %[a], s40\n\t"
      "s_cmp_gt_i32 %[b], 0\n\t"
      "s_cbranch_scc1 LW2%=\n"
      "LN2%=:\n\t"
      "s_waitcnt vmcnt(" STRX_(TH_HI) ")\n\t"
      "buffer_load_dwordx4 %[v0], s[36:39], %[so] offen nt lds\n\t"
      "buffer_load_dwordx4 %[v0], s[36:39], %[so] offen offset:1024 nt lds\n\t"
      "buffer_load_dwordx4 %[v0], s[36:39], %[so] offen offset:2048 nt lds\n\t"
      "buffer_load_dwordx4 %[v0], s[36:39], %[so] offen offset:3072 nt lds\n\t"
      "s_add_u32 m0, m0, 0x1000\n\t"
      "s_memrealtime s[40:41]\n\t"
      "buffer_load_dwordx4 %[v1], s[36:39], %[so] offen nt lds\n\t"
      "buffer_load_dwordx4 %[v1], s[36:39], %[so] offen offset:1024 nt lds\n\t"
      "buffer_load_dwordx4 %[v1], s[36:39], %[so] offen offset:2048 nt lds\n\t"
      "buffer_load_dwordx4 %[v1], s[36:39], %[so] offen offset:3072 nt lds\n\t"
      "ds_read_b64 v[4:5], %[vc] offset:16\n\t"
      "s_add_u32 m0, m0, 0x1000\n\t"
      "s_add_u32 %[so], %[so], %[st3]\n\t"
      "s_and_b32 %[so], %[so], 0x1fffff\n\t"
      "v_add_u32 v2, 1, v2\n\t"
      "s_add_u32 %[ra], %[ra], %[rc]\n\t"
      "s_lshr_b32 %[bs], %[ra], 4\n\t"
      "s_add_i32 %[nd], %[nd], 1\n\t"
      "s_and_b32 m0, m0, 0x1ffff\n\t"
      "s_cmp_eq_u32 %[nd], 13\n\t"
      "s_cselect_b64 s[36:37], %[vb], s[36:37]\n\t"
      "s_cselect_b32 %[so], %[so0], %[so]\n\t"
      "s_waitcnt lgkmcnt(0)\n\t"
      "v_mov_b32 v3, s40\n\t"
      "v_readfirstlane_b32 %[a], v5\n\t"
      "v_readfirstlane_b32 %[b], v4\n\t"
      "ds_write_b64 %[vc], v[2:3] offset:8\n\t"
      STAMP_ASM
      "s_cmp_eq_u32 %[nd], %[ne]\n\t"
      "s_cbranch_scc1 LE%=\n\t"
      "s_add_u32 %[a], %[a], %[bs]\n\t"
      "s_sub_i32 %[a], %[a], s40\n\t"
      "s_min_i32 %[a], %[a], %[hold]\n\t"
      "s_max_i32 %[a], %[a], %[msp]\n\t"
      "s_cmp_lt_i32 %[b], %[nd]\n\t"
      "s_cbranch_scc1 LS3%=\n"
      "LB3%=:\n\t"
      "s_cmp_lt_i32 %[a], 8\n\t"
      "s_cbranch_scc1 LN3%=\n\t"
      "s_add_u32 %[a], %[a], s40\n"
      "LW3%=:\n\t"
      "s_memrealtime s[40:41]\n\t"
      "s_waitcnt lgkmcnt(0)\n\t"
      "s_sub_i32 %[b], %[a], s40\n\t"
      "s_cmp_gt_i32 %[b], 0\n\t"
      "s_cbranch_scc1 LW3%=\n"
      "LN3%=:\n\t"
      "s_branch LT%=\n"
      "LS0%=:\n\t"
      "s_sleep 1\n\t"
      "ds_read_b32 v4, %[vc] offset:24\n\t"
      "s_waitcnt lgkmcnt(0)\n\t"
      "v_readfirstlane_b32 %[b], v4\n\t"
      "s_cmp_lt_i32 %[b], %[nd]\n\t"
      "s_cbranch_scc1 LS0%=\n\t"
      "s_branch LB0%=\n"
      "LS1%=:\n\t"
      "s_sleep 1\n\t"
      "ds_read_b32 v4, %[vc] offset:32\n\t"
      "s_waitcnt lgkmcnt(0)\n\t"
      "v_readfirstlane_b32 %[b], v4\n\t"
      "s_cmp_lt_i32 %[b], %[nd]\n\t"
      "s_cbranch_scc1 LS1%=\n\t"
      "s_branch LB1%=\n"
      "LS2%=:\n\t"
      "s_sleep 1\n\t"
      "ds_read_b32 v4, %[vc] offset:40\n\t"
      "s_waitcnt lgkmcnt(0)\n\t"
      "v_readfirstlane_b32 %[b], v4\n\t"
      "s_cmp_lt_i32 %[b], %[nd]\n\t"
      "s_cbranch_scc1 LS2%=\n\t"
      "s_branch LB2%=\n"
      "LS3%=:\n\t"
      "s_sleep 1\n\t"
      "ds_read_b32 v4, %[vc] offset:16\n\t"
      "s_waitcnt lgkmcnt(0)\n\t"
      "v_readfirstlane_b32 %[b], v4\n\t"
      "s_cmp_lt_i32 %[b], %[nd]\n\t"
      "s_cbranch_scc1 LS3%=\n\t"
      "s_branch LB3%=\n"
      "LE%=:"
      : [so] "+s"(so), [ra] "+s"(ra), [bs] "+s"(bs), [nd] "+s"(nd), [a] "=&s"(ta), [b] "=&s"(tb) STAMP_OPS
      : [v0] "v"(vv), [v1] "v"(vv + PIECE2), [vc] "v"(lbase + L_CTL), [kb] "s"(pass ? vb : kb), [ne] "s"(pass ? 29 : ROW1 - 3), [vb] "s"(vb), [so0] "s"(RFL(unit_off(0, s) + rot)),
        [st0] "s"(RFL(unit_off(1, 0) - unit_off(0, 0))), [st1] "s"(RFL(unit_off(2, 0) - unit_off(1, 0))), [st2] "s"(RFL(unit_off(3, 0) - unit_off(2, 0))), [st3] "s"(RFL(unit_off(4, 0) - unit_off(3, 0))),
        [rc] "s"(rc), [uc] "s"(uc), [hold] "s"(HOLD), [msp] "s"(MINSP), [m0i] "s"(((pass ? 4 * ROW1 : 4 * ROW0) & 15) * SLOT), [ld0] "s"((pass ? 4 * ROW1 : 4 * ROW0) - 1 - (TH_HI + 7) / 8) STAMP_INS
      : "memory", "scc", "vcc", "v2", "v3", "v4", "v5", "s36", "s37", "s38", "s39", "s40", "s41");
      if (pass == 0) {
        while (lds_word(L + C_DONE + 24) < 26) __builtin_amdgcn_s_sleep(1);
        const unsigned tag = *(LAS unsigned*)(L + C_TAG);
        v2u pl[NFIN];
#pragma unroll
        for (int k = 0; k < NFIN; ++k) pl[k] = *(LAS v2u*)(L + L_PL + (4 - NFIN + k) * 512 + lane * 8);
        scale = *(LAS float*)(L + L_ML + 128 + h * 4);
#pragma unroll
        for (int k = 0; k < NFIN; ++k) p_operands(pl[k][0], pl[k][1], mask0, mask1, bo[k]);
        vo = h * 512 + g * 64 + j0 * 16; tg0 = tag & 0xffu; tg1 = (tag >> 8) & 0xffu; tg2 = (tag >> 16) & 0xffu; tg3 = tag >> 24;
        asm volatile("" : "+v"(vo), "+v"(tg0), "+v"(tg1), "+v"(tg2), "+v"(tg3), "+v"(scale), "+s"(hm));
      }
      }
    }
    STAMP(2);
    const int so = W_PA + (pr * 4 + s) * PA_REC;
    LAS unsigned char* asrc = L + L_ACC + vv;
    asm volatile("" : "+v"(asrc));
    f32x4 acc0 = {0.f, 0.f, 0.f, 0.f}, acc1 = {0.f, 0.f, 0.f, 0.f};
#pragma unroll
    for (int k = 0; k < 3; ++k) {
      if (k == 0) WAITVM(24); else if (k == 1) WAITVM(16); else WAITVM(8);
      *(LAS int*)(L + C_LAND) = NUNIT - 3 + k;
      if (k >= 4 - NFIN) pv32(lbase + (12 + k) * SLOT + vv, bo[k >= 4 - NFIN ? k - (4 - NFIN) : 0], acc0, acc1);
    }
    while (lds_word(L + C_NA) < 4) {}
    f32x4 s0, s1;
    {
      f32x4 a[8];
#pragma unroll
      for (int u = 0; u < 8; ++u) a[u] = *(LAS f32x4*)(asrc + u * 1024);
      __builtin_amdgcn_sched_barrier(0);
      s0 = ((a[0] + a[2]) + (a[4] + a[6])) * scale; s1 = ((a[1] + a[3]) + (a[5] + a[7])) * scale;
      asm volatile("" : "+v"(s0), "+v"(s1) : "v"(lbase + 15 * SLOT + vv), "v"(lbase + 15 * SLOT + vv + 8), "s"(so));
    }
    STAMP(4);
    WAITVM(0);
    STAMP(3);
    {
      pv32(lbase + 15 * SLOT + vv, bo[NFIN - 1], acc0, acc1);
      const v4u o0 = __builtin_bit_cast(v4u, acc0 * scale + s0), o1 = __builtin_bit_cast(v4u, acc1 * scale + s1);
      v4u t0v = {(o0[0] & hm) | tg0, (o0[1] & hm) | tg1, (o0[2] & hm) | tg2, (o0[3] & hm) | tg3};
      __builtin_amdgcn_raw_buffer_store_b128(t0v, rw, vo, so, AUX_SC1);
      v4u t1v = {(o1[0] & hm) | tg0, (o1[1] & hm) | tg1, (o1[2] & hm) | tg2, (o1[3] & hm) | tg3};
      __builtin_amdgcn_raw_buffer_store_b128(t1v, rw, vo + 256, so, AUX_SC1);
      asm volatile("s_nop 1" :: "v"(t0v), "v"(t1v) : "memory");
    }
    RELOAD();
    *(LAS int*)(L + C_LAND) = NUNIT;
    asm volatile("Lgqa_end_a:" ::: "memory");
    STAMP(5);
    STAMP_OUT();
    return;
  }

  if (wv >= 2) {
    const int w = wv - 2;
    if (PRETOUCH && !(XF & 4) && w == 0) {
      const int off = lane < 16 ? W_PA + (pr * 4 + s) * PA_REC + lane * 128 : W_YB + nb * 4 * 2048 + ch * 64 + (lane & 7) * (32 * 2048) + ((lane >> 3) & 3) * 2048;
      unsigned pt = 0;
      if (lane < 16 || (lane >= 32 && !(ch & 1))) pt = __builtin_amdgcn_raw_buffer_load_b32(rw, off, 0, 0);
      asm volatile("" :: "v"(pt));
    }
    asm volatile("s_waitcnt vmcnt(0)" :: "v"(pf) : "memory");
    f32x32 sc;
    float m;
    {
      v4u P[NREG][8];
#pragma unroll
      for (int r = 0; r < NREG; ++r) {
        while (lds_word(L + C_LAND) <= 4 * r + w) __builtin_amdgcn_s_sleep(2);
#pragma unroll
        for (int u = 0; u < 8; ++u) P[r][u] = *(LAS v4u*)(L + ((4 * r + w) & 15) * SLOT + vk + u * 1024);
        asm volatile("" :: "v"(P[r][0]), "v"(P[r][1]), "v"(P[r][2]), "v"(P[r][3]), "v"(P[r][4]), "v"(P[r][5]), "v"(P[r][6]), "v"(P[r][7]) : "memory");
        *(LAS int*)(L + C_DONE + w * 8) = r + 1;
      }
      STAMP(1);
      while (lds_word(L + C_READY) == 0) __builtin_amdgcn_s_sleep(2);
      STAMP(2);
      const v4u qf = *(LAS v4u*)(L + L_Q + vk);
      m = -INFINITY;
#pragma unroll
      for (int r = 0; r < NREG; ++r) {
        const float s0 = score16(&P[r][0], qf, j0), s1 = score16(&P[r][4], qf, j0);
        sc[2 * r] = s0; sc[2 * r + 1] = s1; m = fmaxf(m, fmaxf(s0, s1));
      }
#pragma unroll 1
      for (int n = NREG; n < 16; ++n) {
        while (lds_word(L + C_LAND) <= 4 * n + w) __builtin_amdgcn_s_sleep(2);
        v4u K[8];
#pragma unroll
        for (int u = 0; u < 8; ++u) K[u] = *(LAS v4u*)(L + ((4 * n + w) & 15) * SLOT + vk + u * 1024);
        asm volatile("" :: "v"(K[0]), "v"(K[1]), "v"(K[2]), "v"(K[3]), "v"(K[4]), "v"(K[5]), "v"(K[6]), "v"(K[7]) : "memory");
        *(LAS int*)(L + C_DONE + w * 8) = n + 1;
        const float s0 = score16(&K[0], qf, j0), s1 = score16(&K[4], qf, j0);
        sc[2 * n] = s0; sc[2 * n + 1] = s1; m = fmaxf(m, fmaxf(s0, s1));
      }
    }
    STAMP(10);
    const unsigned tag = (unsigned)lds_word(L + C_TAG);
    m = fmaxf(m, dpp<0xB1>(m)); m = fmaxf(m, dpp<0x4E>(m)); m = xrow_max(m);
    *(LAS float*)(L + L_ML + h * 16 + w * 4) = m;
    lds_inc(L + C_NM, lane);
    while (lds_word(L + C_NM) < 4) __builtin_amdgcn_s_sleep(1);
    {
      const f32x4 mw = *(LAS f32x4*)(L + L_ML + h * 16);
      m = __uint_as_float(__float_as_uint(fmaxf(fmaxf(mw[0], mw[1]), fmaxf(mw[2], mw[3]))) & 0xffffff00u);
      const float mc = -m * C2;
      float l = 0.f;
#pragma unroll
      for (int n = 0; n < 32; ++n) { const float p = __builtin_amdgcn_exp2f(fmaf(sc[n], C2, mc)); l += p; sc[n] = __uint_as_float(cvtpk(p, p)); }
      l += dpp<0xB1>(l); l += dpp<0x4E>(l); l = xrow_sum(l);
      *(LAS float*)(L + L_ML + 64 + h * 16 + w * 4) = l;
      *(LAS v2u*)(L + L_PL + w * 512 + lane * 8) = v2u{__float_as_uint(sc[30]), __float_as_uint(sc[31])};
      lds_inc(L + C_NL, lane);
    }
    STAMP(11);
    f32x4 acc0 = {0.f, 0.f, 0.f, 0.f}, acc1 = {0.f, 0.f, 0.f, 0.f};
#pragma unroll 1
    for (int n = 0; n < (w < 4 - NFIN ? 16 : 15); ++n) {
      if (w == 3 && n == 1) {
        while (lds_word(L + C_NL) < 4) __builtin_amdgcn_s_sleep(1);
        const f32x4 lw = *(LAS f32x4*)(L + L_ML + 64 + h * 16);
        if (lane < 16 && j0 == 0) __builtin_amdgcn_raw_buffer_store_b128(tag4(f32x4{m, (lw[0] + lw[1]) + (lw[2] + lw[3]), 0.f, 0.f}, tag), rw, h * 16, W_ML + (pr * 4 + s) * 128, AUX_SC1);
      }
      if (w == 3 && n == 8) {
        v4u ml;
        for (unsigned spin = 0; spin < SPIN_CAP; ++spin) {
          RELOAD();
          ml = untag4(__builtin_amdgcn_raw_buffer_load_b128(rw, j0 * 128 + h * 16, W_ML + pr * 512, AUX_SC1), tag);
          if (__all((or4(ml) & 0xffu) == 0u)) break;
        }
        const float ms = __uint_as_float(ml[0]);
        float M = fmaxf(ms, dpp<0xB1>(ms)); M = fmaxf(M, dpp<0x4E>(M));
        float ls = __uint_as_float(ml[1]) * __builtin_amdgcn_exp2f((ms - M) * C2);
        ls += dpp<0xB1>(ls); ls += dpp<0x4E>(ls);
        if (lane < 16 && j0 == 0) *(LAS float*)(L + L_ML + 128 + h * 4) = __builtin_amdgcn_exp2f((m - M) * C2) * __builtin_amdgcn_rcpf(ls);
      }
      v4u bo[4];
      p_operands(__float_as_uint(sc[2 * n]), __float_as_uint(sc[2 * n + 1]), mask0, mask1, bo);
      while (lds_word(L + C_LAND) <= 64 + 4 * n + w) __builtin_amdgcn_s_sleep(1);
      pv32(lbase + ((4 * n + w) & 15) * SLOT + vv, bo, acc0, acc1);
      *(LAS int*)(L + C_DONE + w * 8) = 17 + n;
    }
    *(LAS f32x4*)(L + L_ACC + w * 2048 + vv) = acc0; *(LAS f32x4*)(L + L_ACC + w * 2048 + 1024 + vv) = acc1;
    lds_inc(L + C_NA, lane);
    STAMP(3);
    while (lds_word(L + C_LAND) < NUNIT) __builtin_amdgcn_s_sleep(1);
    STAMP(5);
    {
      const unsigned tp = now() + D1, tg = (unsigned)lds_word(L + C_DONE + 4) + LINE_END + D1G;
      unsigned tc;
      do tc = now(); while ((int)(tc - tp) < 0 || ((int)(tc - tg) < 0 && (int)(tc - tp) < 150));
    }
    STAMP(4);
    v4u af[4];
    {
      const int bb = 2 * w + (lane >> 5), c = lane & 31;
      const int vo = bb * (HKV * 4 * PA_REC) + c * 16, so = W_PA + (ch >> 2) * (4 * PA_REC) + (ch & 3) * 512;
      v4u a[4];
#pragma unroll
      for (int u = 0; u < 4; ++u) a[u] = __builtin_amdgcn_raw_buffer_load_b128(rw, vo, so + u * PA_REC, AUX_SC1);
      while (lds_word(L + C_WO) == 0) __builtin_amdgcn_s_sleep(1);
      LAS unsigned char* ap = L + L_W + (8 * g + ((lane & 15) >> 2)) * 128 + (16 * w + 4 * (lane & 3)) * 2;
#pragma unroll
      for (int ks = 0; ks < 4; ++ks) af[ks] = tr_frag(ap + ks * 4096);
      asm volatile("" : "+v"(af[0]), "+v"(af[1]), "+v"(af[2]), "+v"(af[3]));
      for (unsigned spin = 0; spin < SPIN_CAP; ++spin) {
        unsigned bad = 0;
#pragma unroll
        for (int u = 0; u < 4; ++u) { a[u] = untag4(a[u], tag); bad |= or4(a[u]); }
        if (__all((bad & 0xffu) == 0u)) break;
        RELOAD();
#pragma unroll
        for (int u = 0; u < 4; ++u) a[u] = __builtin_amdgcn_raw_buffer_load_b128(rw, vo, so + u * PA_REC, AUX_SC1);
      }
      STAMP(6);
      const f32x4 o = (__builtin_bit_cast(f32x4, a[0]) + __builtin_bit_cast(f32x4, a[1])) + (__builtin_bit_cast(f32x4, a[2]) + __builtin_bit_cast(f32x4, a[3]));
      *(LAS v2u*)(L + L_OT + bb * OT_ROW + c * 8) = v2u{cvtpk(o[0], o[1]), cvtpk(o[2], o[3])};
    }
    BARRIER();
    STAMP(7);
    {
      LAS unsigned char* bp = L + L_OT + (lane & 7) * OT_ROW + g * 16;
      f32x4 y = {0.f, 0.f, 0.f, 0.f};
#pragma unroll
      for (int ks = 0; ks < 4; ++ks) y = mfma32(af[ks], *(LAS v4u*)(bp + ks * 64), y);
      if ((lane & 15) < 8) __builtin_amdgcn_raw_buffer_store_b128(tag4(y, tag), rw, (lane & 7) * (32 * 2048) + g * 16, W_YB + (nb * 4 + w) * 2048 + ch * 64, AUX_SC1);
    }
    asm volatile("Lgqa_end_b:" ::: "memory");
    STAMP(8);
    STAMP_OUT();
    return;
  }

  __builtin_amdgcn_s_setprio(2);
  const unsigned tag = load_tag(ws);
  STAMP(6);
  {
    while (lds_word(L + C_LAND) < 0) __builtin_amdgcn_s_sleep(1);
    STAMP(10);
    f32x4 qa[4];
#pragma unroll
    for (int t = 0; t < 4; ++t) qa[t] = f32x4{0.f, 0.f, 0.f, 0.f};
    LAS unsigned char* ap = L + L_W + (8 * g + ((lane & 15) >> 2)) * 128 + 4 * (lane & 3) * 2;
    LAS unsigned char* bp = L + L_X + g * 128 + (lane & 7) * 16;
#pragma unroll
    for (int ks = 0; ks < 4; ++ks) {
      const v4u xb = *(LAS v4u*)(bp + ks * 512);
#pragma unroll
      for (int t = 0; t < 4; ++t) qa[t] = mfma32(tr_frag(ap + ks * 4096 + t * 32), xb, qa[t]);
    }
    STAMP(1);
    const bool hi = (lane & 8) != 0;
#pragma unroll
    for (int t = 0; t < 4; t += 2) {
      const f32x4 v = {hi ? qa[t + 1][0] : qa[t][0], hi ? qa[t + 1][1] : qa[t][1], hi ? qa[t + 1][2] : qa[t][2], hi ? qa[t + 1][3] : qa[t][3]};
      if (!(XF & 1)) __builtin_amdgcn_raw_buffer_store_b128(tag4(v, tag), rw, (lane & 7) * (HKV * 8192) + (hi ? 64 : 0) + g * 16, W_QG + (cb >> 3) * 8192 + kq * 2048 + (cb & 7) * 256 + t * 64, AUX_SC1);
    }
  }
  STAMP(2);
  while ((int)(now() - t0) < QPOLL) __builtin_amdgcn_s_sleep(2);
  {
    const int so = W_QG + pr * 8192;
    v4u a[8];
    for (unsigned spin = 0; spin < SPIN_CAP; ++spin) {
      RELOAD();
#pragma unroll
      for (int u = 0; u < 8; ++u) a[u] = __builtin_amdgcn_raw_buffer_load_b128(rw, (XF & 2) ? 0x7000000 : lane * 16 + (u & 3) * 1024, so + (u >> 2) * 4096, AUX_SC1);
      unsigned bad = 0;
#pragma unroll
      for (int u = 0; u < 8; ++u) { a[u] = untag4(a[u], tag); bad |= or4(a[u]); }
      if ((XF & 3) || __all((bad & 0xffu) == 0u)) break;
      STAMP(15);
    }
#pragma unroll
    for (int u = 0; u < 2; ++u) {
      const f32x4 v = (__builtin_bit_cast(f32x4, a[u]) + __builtin_bit_cast(f32x4, a[2 + u])) + (__builtin_bit_cast(f32x4, a[4 + u]) + __builtin_bit_cast(f32x4, a[6 + u]));
      *(LAS v2u*)(L + L_Q + u * 512 + lane * 8) = v2u{cvtpk(v[0], v[1]), cvtpk(v[2], v[3])};
    }
    asm volatile("s_waitcnt lgkmcnt(0)" ::: "memory");
    *(LAS v2u*)(L + C_READY) = v2u{1u, tag};
  }
  STAMP(3);
  if (!(XF & 32) && lane == 0) {
    const unsigned a = __builtin_nontemporal_load((const GAS unsigned*)(vc + pbase + unit_off(0, s) + rot));
    const unsigned b2 = __builtin_nontemporal_load((const GAS unsigned*)(wo + ch * 128 * 1024 + nb * 128));
    const unsigned c = __builtin_nontemporal_load((const GAS unsigned*)(out + j * 32));
    asm volatile("" :: "v"(a), "v"(b2), "v"(c));
  }
  if (FB_ON) {
    unsigned k = now() / FB_P + 1u;
    float af = 0.f, a1 = 0.f, a2 = 0.f, dr = 0.f;
    int nv = 0;
#pragma unroll 1
    for (int it = 0; it < 40; ++it, ++k) {
      const unsigned tk = k * FB_P;
      while ((int)(now() - tk) < 0) __builtin_amdgcn_s_sleep(2);
      RELOAD();
      const v2u pr2 = *(LAS v2u*)(L + C_LAND);
      if (__builtin_amdgcn_readfirstlane(pr2[0]) >= NUNIT - 12) break;
      const v2u fb = __builtin_amdgcn_raw_buffer_load_b64(rw, (lane & (FB_NREP - 1)) * FB_STR, W_FB + ((k - 1u) & 63u) * FB_SLOT, AUX_SC1);
      if (j < FB_NREP && lane == 0 && pr2[1] != 0u) {
        const float p = (float)(8 * ((int)pr2[0] + (TH_HI + 7) / 8)) + fminf((float)(int)(tk - pr2[1]) * (1.f / 3.4f), 8.f);
        __builtin_amdgcn_raw_buffer_store_b64(v2u{__float_as_uint(p), tag ^ k}, rw, 0, W_FB + (k & 63u) * FB_SLOT + j * FB_STR, AUX_SC1);
      }
      const bool ok = fb[1] == (tag ^ (k - 1u));
      float ps = ok ? __uint_as_float(fb[0]) : 0.f, pc = ok ? 1.f : 0.f;
      ps += dpp<0xB1>(ps); ps += dpp<0x4E>(ps); ps += dpp<0x124>(ps); ps += dpp<0x128>(ps); ps = xrow_sum(ps);
      pc += dpp<0xB1>(pc); pc += dpp<0x4E>(pc); pc += dpp<0x124>(pc); pc += dpp<0x128>(pc); pc = xrow_sum(pc);
      if (__builtin_amdgcn_readfirstlane(pc < 48.f)) continue;
      const float pbar = ps * __builtin_amdgcn_rcpf(pc);
      const float tl = TL0;
      const float as = (float)(int)(tk - FB_P - t0) - pbar * tl - (float)(DELTA + PHASE / 2);
      if (nv == 0) { af = as; a1 = as; a2 = as; }
      else {
        const float er = as - (FB_REF ? a2 : af);
        if (nv >= FB_I0) dr = fminf(fmaxf(dr + FB_KI * fminf(er, 30.f), -6.f), 30.f);
        a2 = a1; a1 = af; af += dr + FB_G * er;
      }
      ++nv;
      if (FB_USE) *(LAS unsigned*)(L + C_DONE + 4 + (lane & 3) * 8) = t0 + (unsigned)(int)af;
    }
  }
  {
    const v4i ro = mkrsrc(wo, HQ * DH * DM * 2);
    const unsigned vo = ((lane >> 5) + 8 * ((lane >> 3) & 3)) * 1024 + (lane & 7) * 16;
    while (lds_word(L + C_LAND) < WOPOS) __builtin_amdgcn_s_sleep(1);
#pragma unroll 1
    for (int u = 0; u < 16; ++u) dma1_nt(ro, vo, (ch * 128 + 32 * ((u >> 1) & 3) + 4 * (u >> 3) + 2 * (u & 1)) * 1024 + nb * 128, lbase + L_W + u * 1024);
    WAITVM(0);
    *(LAS int*)(L + C_WO) = 1;
    STAMP(11);
  }
  BARRIER();
  STAMP(7);
  { const unsigned tp = now(); while ((int)(now() - tp) < D2) {} }
  {
    v4u a0, a1;
    for (unsigned spin = 0; spin < SPIN_CAP; ++spin) {
      RELOAD();
      a0 = untag4(__builtin_amdgcn_raw_buffer_load_b128(rw, lane * 16, W_YB + j * 2048, AUX_SC1), tag);
      a1 = untag4(__builtin_amdgcn_raw_buffer_load_b128(rw, lane * 16 + 1024, W_YB + j * 2048, AUX_SC1), tag);
      if (__all(((or4(a0) | or4(a1)) & 0xffu) == 0u)) break;
    }
    STAMP(8);
    f32x4 y = __builtin_bit_cast(f32x4, a0) + __builtin_bit_cast(f32x4, a1);
#pragma unroll
    for (int q = 0; q < 4; ++q) { float t = y[q]; t += dpp<0x124>(t); t += dpp<0x128>(t); y[q] = xrow_sum(t); }
    if (lane < 4) *(GAS v2u*)(out + j * 32 + lane * 8) = v2u{cvtpk(y[0], y[1]), cvtpk(y[2], y[3])};
    if (j == 0 && lane == 0) __builtin_amdgcn_raw_buffer_store_b32(tag, rw, 0, W_E, AUX_SC1);
  }
  asm volatile("Lgqa_end_c:" ::: "memory");
  STAMP(9);
  STAMP_OUT();
}

extern "C" void kernel_launch(void* const* d_in, const int* in_sizes, int n_in, void* d_out, int out_size, void* d_ws, size_t ws_size,
                              hipStream_t stream) {
  gqa_fused<<<NWG, NTHR, 0, stream>>>((const unsigned char*)d_in[0], (const unsigned char*)d_in[1], (const unsigned char*)d_in[2], (const unsigned char*)d_in[3],
                                      (const unsigned char*)d_in[4], (unsigned char*)d_out, (unsigned char*)d_ws TUNE_PASS);
}
```

```cpp
#define TL0 3.33f
#include <hip/hip_runtime.h>
#include <hip/hip_bf16.h>
#include <stdint.h>

#define GAS __attribute__((address_space(1)))
#define LAS __attribute__((address_space(3)))
typedef unsigned v4u __attribute__((ext_vector_type(4)));
typedef unsigned v2u __attribute__((ext_vector_type(2)));
typedef int v4i __attribute__((ext_vector_type(4)));
typedef float f32x4 __attribute__((ext_vector_type(4)));
typedef float f32x32 __attribute__((ext_vector_type(32)));
typedef short s16x4 __attribute__((ext_vector_type(4)));
typedef __bf16 bf16x8 __attribute__((ext_vector_type(8)));

constexpr int NB = 8, DM = 512, HQ = 32, HKV = 8, SK = 8192, DH = 128;
constexpr int NWG = 256, NTHR = 384;
constexpr int NUNIT = 128;
constexpr int SLOT = 8192;
constexpr float C2 = 0.088388347648318440f * 1.4426950408889634f;

#define NREG 4
#define NFIRST 4
#define WIN 24
#define UPREP 112
#define NFIN 3
#define MPOS 1
#define WOLAZY 1
#define PF2ROW 11
#define FB_NREP 32
#define QMODE 4
#define XF 256
#define TUNE_ARG
#define TUNE_PASS
#define UC16 ((unsigned)(TL0 * 128.f))
#define DELTA 0
#define HOLD 30
#define MINSP 20
#define LINE0 120
#define LATE 40
#define MINSP2 16
#define PHASE 28
#define FB_P 192u
#define FB_G 0.4f
#define FB_KI 0.f
#define FB_I0 3
#define WQT 0
#define WQSP 12
#define QPOLL 480
#define WOPOS 125
#define D1 30
#define D1G 138
#define GPOS 126
#define D2G 0
#define D2 40
constexpr unsigned SPIN_CAP = 1u << 16;
constexpr int LAG = (WIN + 7) / 8;

constexpr int L_Q = 16 * SLOT, L_ACC = L_Q + 1024, L_PL = L_ACC + 4 * 2048, L_DG = L_PL + 2048, L_OT = L_DG + 16, OT_ROW = 272, L_ML = L_OT + 8 * OT_ROW, L_CTL = (L_ML + 144 + 63) / 64 * 64;
constexpr int L_X = L_ACC, L_W = L_CTL + 64, L_TOT = L_W + 16384;
constexpr int C_FREE = L_CTL, C_LAND = L_CTL + 32, C_READY = L_CTL + 40, C_TAG = L_CTL + 44, C_NM = L_CTL + 48, C_NL = L_CTL + 52, C_NA = L_CTL + 56, C_WO = L_CTL + 60;
static_assert(L_CTL % 32 == 0 && L_TOT <= 160 * 1024, "lds map");
constexpr int W_E = 0, W_FB = 1024, FB_SLOT = 512, W_ML = W_FB + 64 * FB_SLOT + 1024, W_QG = W_ML + NWG * 128, W_PA = W_QG + NWG * 2048, PA_REC = 2048, W_YB = W_PA + NWG * PA_REC,
              W_END = W_YB + NWG * 2048;

#define STAMP(i)
#define STAMP_OUT()

#define STR_(x) #x
#define STRX_(x) STR_(x)
#define WAITVM(n) asm volatile("s_waitcnt vmcnt(" STR_(n) ")" ::: "memory")
#define BARRIER() asm volatile("s_waitcnt lgkmcnt(0)\n\ts_barrier" ::: "memory")
#define RELOAD() asm volatile("" ::: "memory")
constexpr int AUX_SC1 = 16;

__device__ __forceinline__ unsigned now() { return (unsigned)__builtin_amdgcn_s_memrealtime(); }
__device__ __forceinline__ unsigned cvtpk(float lo, float hi) {
  typedef __bf16 bf16x2_t __attribute__((ext_vector_type(2)));
  typedef float f32x2_t __attribute__((ext_vector_type(2)));
  return __builtin_bit_cast(unsigned, __builtin_convertvector(f32x2_t{lo, hi}, bf16x2_t));
}
template <int CTRL> __device__ __forceinline__ unsigned dppu(unsigned x) { return (unsigned)__builtin_amdgcn_mov_dpp((int)x, CTRL, 0xf, 0xf, true); }
template <int CTRL> __device__ __forceinline__ float dpp(float x) { return __uint_as_float(dppu<CTRL>(__float_as_uint(x))); }
__device__ __forceinline__ float xrow_max(float x) {
  auto s = __builtin_amdgcn_permlane16_swap(__float_as_uint(x), __float_as_uint(x), false, false);
  x = fmaxf(__uint_as_float(s[0]), __uint_as_float(s[1]));
  auto t = __builtin_amdgcn_permlane32_swap(__float_as_uint(x), __float_as_uint(x), false, false);
  return fmaxf(__uint_as_float(t[0]), __uint_as_float(t[1]));
}
__device__ __forceinline__ float xrow_sum(float x) {
  auto s = __builtin_amdgcn_permlane16_swap(__float_as_uint(x), __float_as_uint(x), false, false);
  x = __uint_as_float(s[0]) + __uint_as_float(s[1]);
  auto t = __builtin_amdgcn_permlane32_swap(__float_as_uint(x), __float_as_uint(x), false, false);
  return __uint_as_float(t[0]) + __uint_as_float(t[1]);
}
__device__ __forceinline__ v4i mkrsrc(const void* p, unsigned bytes) {
  const unsigned long long a = (unsigned long long)p;
  v4i r; r[0] = (int)(unsigned)a; r[1] = (int)((unsigned)(a >> 32) & 0xffffu); r[2] = (int)bytes; r[3] = 0x00020000; return r;
}
__device__ __forceinline__ int lds_word(LAS unsigned char* p) { RELOAD(); const int v = __builtin_amdgcn_readfirstlane(*(LAS int*)p); RELOAD(); return v; }
__device__ __forceinline__ void lds_inc(LAS unsigned char* p) {
  unsigned long long ex;
  asm volatile("s_mov_b64 %0, exec\n\ts_mov_b64 exec, 1\n\tds_add_u32 %1, %2\n\ts_mov_b64 exec, %0" : "=&s"(ex) : "v"((unsigned)(size_t)p), "v"(1u) : "memory");
}
#define DMA4(V, R, S) "buffer_load_dwordx4 " V ", " R ", " S " offen nt lds\n\tbuffer_load_dwordx4 " V ", " R ", " S " offen offset:1024 nt lds\n\t" \
                      "buffer_load_dwordx4 " V ", " R ", " S " offen offset:2048 nt lds\n\tbuffer_load_dwordx4 " V ", " R ", " S " offen offset:3072 nt lds\n\t"
__device__ __forceinline__ void issue_two(v4i r, unsigned vv, unsigned so, unsigned lds) {
  unsigned t;
  asm volatile("s_mov_b32 m0, %4\n\ts_add_u32 %0, %3, 0x1000\n\t" DMA4("%2", "%1", "%3") "s_add_u32 m0, m0, 0x1000\n\ts_nop 0\n\t" DMA4("%2", "%1", "%0")
               "s_add_u32 m0, m0, 0x1000\n\ts_add_u32 %0, %0, 0x1000\n\t" DMA4("%2", "%1", "%0") "s_add_u32 m0, m0, 0x1000\n\ts_add_u32 %0, %0, 0x1000\n\t" DMA4("%2", "%1", "%0")
               : "=&s"(t) : "s"(r), "v"(vv), "s"(so), "s"(lds) : "memory", "scc");
}
__device__ __forceinline__ void dma1_nt(v4i r, unsigned voff, unsigned soff, unsigned lds) {
  asm volatile("s_mov_b32 m0, %3\n\ts_nop 0\n\tbuffer_load_dwordx4 %1, %0, %2 offen nt lds" :: "s"(r), "v"(voff), "s"(soff), "s"(lds) : "memory");
}
__device__ __forceinline__ void dma1(v4i r, unsigned voff, unsigned soff, unsigned lds) {
  asm volatile("s_mov_b32 m0, %3\n\ts_nop 0\n\tbuffer_load_dwordx4 %1, %0, %2 offen lds" :: "s"(r), "v"(voff), "s"(soff), "s"(lds) : "memory");
}
struct Dbg { unsigned d0, d1; };
__device__ __forceinline__ void stream_units(v4i rs, unsigned vv, unsigned u, unsigned ue, unsigned so, unsigned ra, unsigned uc16, int hold, int msp, int lat, int ms2, Dbg& dbg, int lane) {
  unsigned ta, tb, st = 8192u, fo = ((u + 1u) & 3u) * 8u + C_FREE;
#define STAMP_ASM
#define STAMP_OPS
#define STAMP_INS
  asm volatile(
      "s_mov_b32 m0, %[m0i]\n\t"
      "v_mov_b32 v2, %[ld0]\n"
      "LT%=:\n\t"
      "s_waitcnt vmcnt(" STRX_(WIN) ")\n\t"
      DMA4("%[v0]", "%[rs]", "%[so]")
      "s_add_u32 m0, m0, 0x1000\n\t"
      "s_memrealtime s[40:41]\n\t"
      DMA4("%[v1]", "%[rs]", "%[so]")
      "ds_read_b64 v[4:5], %[fo]\n\t"
      "s_add_u32 m0, m0, 0x1000\n\t"
      "s_add_u32 %[so], %[so], %[st]\n\t"
      "s_and_b32 m0, m0, 0x1ffff\n\t"
      "s_and_b32 %[so], %[so], 0x1fffff\n\t"
      "s_xor_b32 %[st], %[st], 0xc000\n\t"
      "s_add_u32 %[ra], %[ra], %[uc]\n\t"
      "v_add_u32 v2, 1, v2\n\t"
      "v_add_u32 %[fo], 8, %[fo]\n\t"
      "s_add_u32 %[u], %[u], 1\n\t"
      "v_and_b32 %[fo], %[fm], %[fo]\n\t"
      "s_waitcnt lgkmcnt(0)\n\t"
      "v_mov_b32 v3, s40\n\t"
      "v_readfirstlane_b32 %[a], v5\n\t"
      "s_lshr_b32 s41, %[ra], 4\n\t"
      "v_readfirstlane_b32 %[b], v4\n\t"
      "ds_write_b64 %[vl], v[2:3]\n\t"
      STAMP_ASM
      "s_cmp_eq_u32 %[u], %[ue]\n\t"
      "s_cbranch_scc1 LE%=\n\t"
      "s_add_u32 %[a], %[a], s41\n\t"
      "s_sub_i32 %[a], %[a], s40\n\t"
      "s_cmp_lt_i32 %[a], %[lat]\n\t"
      "s_cselect_b32 s41, %[ms2], %[msp]\n\t"
      "s_min_i32 %[a], %[a], %[hold]\n\t"
      "s_max_i32 %[a], %[a], s41\n\t"
      "s_add_u32 %[a], %[a], s40\n\t"
      "s_cmp_lt_u32 %[b], %[u]\n\t"
      "s_cbranch_scc1 LS%=\n"
      "LW%=:\n\t"
      "s_memrealtime s[40:41]\n\t"
      "s_waitcnt lgkmcnt(0)\n\t"
      "s_sub_i32 %[b], %[a], s40\n\t"
      "s_cmp_gt_i32 %[b], 0\n\t"
      "s_cbranch_scc0 LT%=\n\t"
      "s_branch LW%=\n"
      "LS%=:\n\t"
      "s_and_b32 %[b], %[u], 3\n\t"
      "s_lshl_b32 %[b], %[b], 3\n\t"
      "s_add_u32 %[b], %[b], %[fb]\n\t"
      "v_mov_b32 v5, %[b]\n"
      "LR%=:\n\t"
      "s_sleep 1\n\t"
      "ds_read_b32 v4, v5\n\t"
      "s_waitcnt lgkmcnt(0)\n\t"
      "v_readfirstlane_b32 %[b], v4\n\t"
      "s_cmp_lt_u32 %[b], %[u]\n\t"
      "s_cbranch_scc1 LR%=\n\t"
      "s_branch LW%=\n"
      "LE%=:"
      : [so] "+s"(so), [ra] "+s"(ra), [u] "+s"(u), [st] "+s"(st), [fo] "+v"(fo), [a] "=&s"(ta), [b] "=&s"(tb) STAMP_OPS
      : [rs] "s"(rs), [v0] "v"(vv), [v1] "v"(vv + 4096u), [vl] "v"((unsigned)C_LAND), [ue] "s"(ue), [uc] "s"(uc16), [hold] "s"(hold), [msp] "s"(msp), [lat] "s"(lat), [ms2] "s"(ms2),
        [m0i] "s"((u & 15u) * SLOT), [ld0] "s"(u - 1u - LAG), [fm] "s"(~32u), [fb] "s"((unsigned)C_FREE) STAMP_INS
      : "memory", "scc", "vcc", "v2", "v3", "v4", "v5", "s40", "s41");
}
static_assert((C_FREE & 32) == 0, "the record pointer wraps by clearing bit 5");

__device__ __forceinline__ f32x4 mfma32(v4u a, v4u b, f32x4 c) {
  return __builtin_amdgcn_mfma_f32_16x16x32_bf16(__builtin_bit_cast(bf16x8, a), __builtin_bit_cast(bf16x8, b), c, 0, 0, 0);
}
__device__ __forceinline__ v4u tr_frag(LAS unsigned char* p, int hi) {
  const v2u lo = __builtin_bit_cast(v2u, __builtin_amdgcn_ds_read_tr16_b64_v4i16((LAS s16x4*)p));
  const v2u up = __builtin_bit_cast(v2u, __builtin_amdgcn_ds_read_tr16_b64_v4i16((LAS s16x4*)(p + hi)));
  return v4u{lo[0], lo[1], up[0], up[1]};
}
__device__ __forceinline__ float score16(const v4u* K, v4u qf, unsigned j0) {
  float sc[4];
#pragma unroll
  for (int u = 0; u < 4; ++u) {
    const f32x4 d = mfma32(K[u], qf, f32x4{0.f, 0.f, 0.f, 0.f});
    sc[u] = ((dpp<0x00>(d[0]) + dpp<0x55>(d[1])) + dpp<0xAA>(d[2])) + dpp<0xFF>(d[3]);
  }
  return (j0 & 2) ? ((j0 & 1) ? sc[3] : sc[2]) : ((j0 & 1) ? sc[1] : sc[0]);
}
__device__ __forceinline__ void p_operands(unsigned k0, unsigned k1, unsigned mask0, unsigned mask1, v4u (&bo)[4]) {
  bo[0] = v4u{dppu<0x00>(k0) & mask0, dppu<0x00>(k0) & mask1, dppu<0x55>(k0) & mask0, dppu<0x55>(k0) & mask1};
  bo[1] = v4u{dppu<0xAA>(k0) & mask0, dppu<0xAA>(k0) & mask1, dppu<0xFF>(k0) & mask0, dppu<0xFF>(k0) & mask1};
  bo[2] = v4u{dppu<0x00>(k1) & mask0, dppu<0x00>(k1) & mask1, dppu<0x55>(k1) & mask0, dppu<0x55>(k1) & mask1};
  bo[3] = v4u{dppu<0xAA>(k1) & mask0, dppu<0xAA>(k1) & mask1, dppu<0xFF>(k1) & mask0, dppu<0xFF>(k1) & mask1};
  asm volatile("" : "+v"(bo[0]), "+v"(bo[1]), "+v"(bo[2]), "+v"(bo[3]));
}
#define RD2(A, P, O0, O1) "ds_read2st64_b64 %[" A "], %[" P "] offset0:" O0 " offset1:" O1 "\n\t"
#define MF(N, C, A, B) "s_waitcnt lgkmcnt(" N ")\n\tv_mfma_f32_16x16x32_bf16 %[" C "], %[" A "], %[" B "], %[" C "]\n\t"
template <bool PAD> __device__ __forceinline__ void pv32(unsigned p, const v4u (&bo)[4], f32x4& acc0, f32x4& acc1) {
  v4u a0, a1, a2, a3, a4, a5, a6, a7;
  asm volatile(RD2("a0", "p0", "0", "2") RD2("a1", "p1", "0", "2") RD2("a2", "p0", "4", "6") RD2("a3", "p1", "4", "6")
               RD2("a4", "p0", "8", "10") RD2("a5", "p1", "8", "10") RD2("a6", "p0", "12", "14") RD2("a7", "p1", "12", "14")
               MF("7", "c0", "a0", "b0") MF("6", "c1", "a1", "b0") MF("5", "c0", "a2", "b1") MF("4", "c1", "a3", "b1")
               MF("3", "c0", "a4", "b2") MF("2", "c1", "a5", "b2") MF("1", "c0", "a6", "b3") MF("0", "c1", "a7", "b3") "s_nop %[pad]"
      : [c0] "+v"(acc0), [c1] "+v"(acc1), [a0] "=&v"(a0), [a1] "=&v"(a1), [a2] "=&v"(a2), [a3] "=&v"(a3), [a4] "=&v"(a4), [a5] "=&v"(a5), [a6] "=&v"(a6), [a7] "=&v"(a7)
      : [p0] "v"(p), [p1] "v"(p + 8), [b0] "v"(bo[0]), [b1] "v"(bo[1]), [b2] "v"(bo[2]), [b3] "v"(bo[3]), [pad] "i"(PAD ? 7 : 0) : "memory");
}
__device__ __forceinline__ v4u tag_bytes(unsigned tag) { return v4u{tag & 0xffu, (tag >> 8) & 0xffu, (tag >> 16) & 0xffu, tag >> 24}; }
__device__ __forceinline__ v4u tag4(f32x4 v, v4u tb) { return (__builtin_bit_cast(v4u, v) & 0xffffff00u) | tb; }
__device__ __forceinline__ unsigned or4(v4u u) { return (u[0] | u[1]) | (u[2] | u[3]); }
__device__ __forceinline__ unsigned load_tag(const unsigned char* ws) {
  unsigned e0;
  asm volatile("s_load_dword %0, %1, 0x0\n\ts_waitcnt lgkmcnt(0)" : "=&s"(e0) : "s"(ws) : "memory");
  return ((e0 & 0x7f7f7f7fu) + 0x51276b35u) ^ (e0 & 0x80808080u);
}

__global__ void __launch_bounds__(NTHR, 1) gqa_fused(const unsigned char* __restrict__ x, const unsigned char* __restrict__ kc, const unsigned char* __restrict__ vc,
                                                     const unsigned char* __restrict__ wq, const unsigned char* __restrict__ wo, unsigned char* __restrict__ out,
                                                     unsigned char* ws TUNE_ARG) {
  __shared__ __attribute__((aligned(16))) unsigned char smem[L_TOT];
  LAS unsigned char* L = (LAS unsigned char*)smem;
  unsigned long long t64;
  unsigned pf, pa;
  unsigned long long pc;
  unsigned clen;
  {
    unsigned long long ex;
    asm volatile("s_memrealtime %0\n\ts_getpc_b64 %1\nLgqa_pc:\n\t"
                 "s_mov_b32 %2, Lgqa_end_a-Lgqa_pc+128\n\ts_max_u32 %2, %2, Lgqa_end_b-Lgqa_pc+128\n\ts_max_u32 %2, %2, Lgqa_end_c-Lgqa_pc+128\n\t"
                 "v_lshlrev_b32 %5, 7, %6\n\tv_add_u32 %5, 0xffffc000, %5\n\tv_cmp_gt_u32 vcc, %2, %5\n\t"
                 "s_and_saveexec_b64 %3, vcc\n\ts_cbranch_execz Lgqa_nopf\n\tglobal_load_dword %4, %5, %1 offset:-32\nLgqa_nopf:\n\ts_mov_b64 exec, %3"
                 : "=&s"(t64), "=&s"(pc), "=&s"(clen), "=&s"(ex), "=&v"(pf), "=&v"(pa) : "v"(threadIdx.x) : "scc", "vcc", "memory");
  }
  const int lane = threadIdx.x & 63, wv = __builtin_amdgcn_readfirstlane(threadIdx.x >> 6);
  const int j = blockIdx.x, xg = j & 7, idx = j >> 3;
  const int yy = (xg - idx) & 7, pr = (XF & 16) ? (idx >> 2) * 8 + xg : (XF & 128) ? xg * 8 + (idx >> 2) : (XF & 256) ? (idx >> 2) * 8 + ((xg + (idx >> 2)) & 7) : 2 * idx + (yy >> 2), s = (XF & (16 | 128 | 256)) ? (idx & 3) : yy & 3;
  const int ch = 4 * xg + (idx >> 3), nb = idx & 7;
  const int g = lane >> 4;
  const unsigned so0 = (unsigned)(pr >> 1) * 65536u + s * 16384u;
  const unsigned vv = lane * 16;
  const unsigned ra0 = ((unsigned)(j * 37) & 31u) * PHASE >> 1;
  asm volatile("s_waitcnt lgkmcnt(0)" : "+s"(t64) : "s"(x), "s"(kc), "s"(vc), "s"(wq), "s"(wo), "s"(out), "s"(ws), "s"(so0), "v"(vv));
  const unsigned t0 = (unsigned)t64;
  const size_t pbase = (size_t)pr * (SK * DH * 2);
  Dbg dbg = {0u, 0u};

  const v4i rk = mkrsrc(kc + pbase, SK * DH * 2);
  if (wv == 0) {
    __builtin_amdgcn_s_setprio(3);
#pragma unroll
    for (int u = 0; u < NFIRST; u += 2) issue_two(rk, vv, (so0 + (u >> 1) * 65536u) & 0x1fffffu, u * SLOT);
    const unsigned keep = (unsigned)((lane & 7) - 4) >> 31;
    *(LAS v2u*)(L + L_CTL + (lane & 7) * 8) = v2u{(12u + (lane & 7)) * keep, (t0 + LINE0) * keep};
    STAMP(1);
  }
  BARRIER();
  if (wv == 0) stream_units(rk, vv, NFIRST, 64u, (so0 + (NFIRST >> 1) * 65536u) & 0x1fffffu, NFIRST * UC16 + ra0, UC16, HOLD, MINSP, -LATE, MINSP2, dbg, lane);
  const __amdgpu_buffer_rsrc_t rw = __builtin_amdgcn_make_buffer_rsrc((void*)ws, 0, W_END, 0x00020000);
  const unsigned j0 = lane & 3;
  const int h = (lane >> 2) & 3;
  const unsigned mask0 = j0 == 0 ? 0x0000ffffu : (j0 == 1 ? 0xffff0000u : 0u);
  const unsigned mask1 = j0 == 2 ? 0x0000ffffu : (j0 == 3 ? 0xffff0000u : 0u);

  if (wv == 0) {
    const v4i rv = mkrsrc(vc + pbase, SK * DH * 2);
    while (lds_word(L + C_FREE) < 64) __builtin_amdgcn_s_sleep(1);
    stream_units(rv, vv, 64u, UPREP, so0, 64u * UC16 + ra0, UC16, HOLD, MINSP, -LATE, MINSP2, dbg, lane);
    while (lds_word(L + C_FREE + 24) < 64 + 4 * 8 + 3 + 16) __builtin_amdgcn_s_sleep(1);
    v4u bo[NFIN][4], tb;
    unsigned vo = h * 512 + g * 64 + j0 * 16;
    float scale;
    {
      v2u pl[NFIN];
#pragma unroll
      for (int k = 0; k < NFIN; ++k) pl[k] = *(LAS v2u*)(L + L_PL + (4 - NFIN + k) * 512 + lane * 8);
      tb = tag_bytes(*(LAS unsigned*)(L + C_TAG));
      scale = *(LAS float*)(L + L_ML + 128 + h * 4);
      const int fr = *(LAS int*)(L + C_FREE + (UPREP & 3) * 8);
#pragma unroll
      for (int k = 0; k < NFIN; ++k) p_operands(pl[k][0], pl[k][1], mask0, mask1, bo[k]);
      asm volatile("" : "+v"(vo), "+v"(tb), "+v"(scale));
      if (__builtin_amdgcn_readfirstlane(fr) < UPREP) { while (lds_word(L + C_FREE + (UPREP & 3) * 8) < UPREP) __builtin_amdgcn_s_sleep(1); }
    }
    stream_units(rv, vv, UPREP, NUNIT, (so0 + ((UPREP - 64) >> 1) * 65536u) & 0x1fffffu, UPREP * UC16 + ra0, UC16, HOLD, MINSP, -LATE, MINSP2, dbg, lane);
    STAMP(2);
    const int so = W_PA + (pr * 4 + s) * PA_REC;
    f32x4 acc0 = {0.f, 0.f, 0.f, 0.f}, acc1 = {0.f, 0.f, 0.f, 0.f};
    static_assert(NFIN >= 2 && LAG <= 3, "the drain below starts at three units outstanding");
    f32x4 s0, s1;
#pragma unroll
    for (int k = 0; k < 3; ++k) {
      if (k == 0) WAITVM(24); else if (k == 1) WAITVM(16); else WAITVM(8);
      if (k < 2) *(LAS int*)(L + C_LAND) = NUNIT - 3 + k;
      if (k >= 4 - NFIN) pv32<false>((12 + k) * SLOT + vv, bo[k >= 4 - NFIN ? k - (4 - NFIN) : 0], acc0, acc1);
      if (k == MPOS) {
        while (lds_word(L + C_NA) < 4) {}
        f32x4 a[8];
#pragma unroll
        for (int u = 0; u < 8; ++u) a[u] = *(LAS f32x4*)(L + L_ACC + vv + u * 1024);
        __builtin_amdgcn_sched_barrier(0);
        s0 = ((a[0] + a[2]) + (a[4] + a[6])) * scale; s1 = ((a[1] + a[3]) + (a[5] + a[7])) * scale;
        asm volatile("" : "+v"(s0), "+v"(s1) : "v"(14 * SLOT + vv), "v"(14 * SLOT + vv + 8), "v"(15 * SLOT + vv), "v"(15 * SLOT + vv + 8), "s"(so), "s"(0xffffff00u));
      }
    }
    STAMP(4);
    WAITVM(0);
    STAMP(3);
    pv32<true>(15 * SLOT + vv, bo[NFIN - 1], acc0, acc1);
    const v4u o0 = tag4(acc0 * scale + s0, tb), o1 = tag4(acc1 * scale + s1, tb);
    __builtin_amdgcn_raw_buffer_store_b128(o0, rw, vo, so, AUX_SC1);
    __builtin_amdgcn_raw_buffer_store_b128(o1, rw, vo + 256, so, AUX_SC1);
    asm volatile("s_nop 1" :: "v"(o0), "v"(o1) : "memory");
    *(LAS int*)(L + C_LAND) = NUNIT;
    asm volatile("Lgqa_end_a:" ::: "memory");
    STAMP(5);
    STAMP_OUT();
    return;
  }

  if (wv >= 2) {
    const int w = wv - 2;
    const unsigned vk = (h * 16 + g * 4 + j0) * 16;
    asm volatile("s_waitcnt vmcnt(0)" :: "v"(pf), "v"(pa) : "memory");
    f32x32 sc;
    float m;
    {
      v4u P[NREG][8];
#pragma unroll
      for (int r = 0; r < NREG; ++r) {
        while (lds_word(L + C_LAND) <= 4 * r + w) __builtin_amdgcn_s_sleep(2);
#pragma unroll
        for (int u = 0; u < 8; ++u) P[r][u] = *(LAS v4u*)(L + ((4 * r + w) & 15) * SLOT + vk + u * 1024);
        asm volatile("" :: "v"(P[r][0]), "v"(P[r][1]), "v"(P[r][2]), "v"(P[r][3]), "v"(P[r][4]), "v"(P[r][5]), "v"(P[r][6]), "v"(P[r][7]) : "memory");
        *(LAS int*)(L + C_FREE + w * 8) = 4 * r + w + 16;
      }
      STAMP(1);
      while (lds_word(L + C_READY) == 0) __builtin_amdgcn_s_sleep(2);
      STAMP(2);
      const v4u qf = *(LAS v4u*)(L + L_Q + vk);
      m = -INFINITY;
#pragma unroll
      for (int r = 0; r < NREG; ++r) {
        const float s0 = score16(&P[r][0], qf, j0), s1 = score16(&P[r][4], qf, j0);
        sc[2 * r] = s0; sc[2 * r + 1] = s1; m = fmaxf(m, fmaxf(s0, s1));
      }
#pragma unroll 1
      for (int n = NREG; n < 16; ++n) {
        while (lds_word(L + C_LAND) <= 4 * n + w) __builtin_amdgcn_s_sleep(2);
        v4u K[8];
#pragma unroll
        for (int u = 0; u < 8; ++u) K[u] = *(LAS v4u*)(L + ((4 * n + w) & 15) * SLOT + vk + u * 1024);
        asm volatile("" :: "v"(K[0]), "v"(K[1]), "v"(K[2]), "v"(K[3]), "v"(K[4]), "v"(K[5]), "v"(K[6]), "v"(K[7]) : "memory");
        *(LAS int*)(L + C_FREE + w * 8) = 4 * n + w + 16;
        const float s0 = score16(&K[0], qf, j0), s1 = score16(&K[4], qf, j0);
        sc[2 * n] = s0; sc[2 * n + 1] = s1; m = fmaxf(m, fmaxf(s0, s1));
      }
    }
    STAMP(10);
    const v4u tb = tag_bytes((unsigned)lds_word(L + C_TAG));
    m = fmaxf(m, dpp<0xB1>(m)); m = fmaxf(m, dpp<0x4E>(m)); m = xrow_max(m);
    *(LAS float*)(L + L_ML + h * 16 + w * 4) = m;
    lds_inc(L + C_NM);
    while (lds_word(L + C_NM) < 4) __builtin_amdgcn_s_sleep(1);
    {
      const f32x4 mw = *(LAS f32x4*)(L + L_ML + h * 16);
      m = __uint_as_float(__float_as_uint(fmaxf(fmaxf(mw[0], mw[1]), fmaxf(mw[2], mw[3]))) & 0xffffff00u);
      const float mc = -m * C2;
      float l = 0.f;
#pragma unroll
      for (int n = 0; n < 32; ++n) { const float p = __builtin_amdgcn_exp2f(fmaf(sc[n], C2, mc)); l += p; sc[n] = __uint_as_float(cvtpk(p, p)); }
      l += dpp<0xB1>(l); l += dpp<0x4E>(l); l = xrow_sum(l);
      *(LAS float*)(L + L_ML + 64 + h * 16 + w * 4) = l;
      *(LAS v2u*)(L + L_PL + w * 512 + lane * 8) = v2u{__float_as_uint(sc[30]), __float_as_uint(sc[31])};
      lds_inc(L + C_NL);
    }
    STAMP(11);
    f32x4 acc0 = {0.f, 0.f, 0.f, 0.f}, acc1 = {0.f, 0.f, 0.f, 0.f};
#pragma unroll 1
    for (int n = 0; n < (w < 4 - NFIN ? 16 : 15); ++n) {
      if (w == 3 && n == 1) {
        while (lds_word(L + C_NL) < 4) __builtin_amdgcn_s_sleep(1);
        const f32x4 lw = *(LAS f32x4*)(L + L_ML + 64 + h * 16);
        if (lane < 16 && j0 == 0) __builtin_amdgcn_raw_buffer_store_b128(tag4(f32x4{m, (lw[0] + lw[1]) + (lw[2] + lw[3]), 0.f, 0.f}, tb), rw, h * 16, W_ML + (pr * 4 + s) * 128, AUX_SC1);
      }
      if ((XF & 512) && n == PF2ROW && pa < clen) { const unsigned pt = *(const GAS unsigned*)(pc - 32 + pa); asm volatile("" :: "v"(pt)); }
      if (w == 3 && n == 8) {
        v4u ml;
        for (unsigned spin = 0; spin < SPIN_CAP; ++spin) {
          RELOAD();
          ml = __builtin_amdgcn_raw_buffer_load_b128(rw, j0 * 128 + h * 16, W_ML + pr * 512, AUX_SC1) ^ tb;
          if (__all((or4(ml) & 0xffu) == 0u)) break;
        }
        const float ms = __uint_as_float(ml[0]);
        float M = fmaxf(ms, dpp<0xB1>(ms)); M = fmaxf(M, dpp<0x4E>(M));
        float ls = __uint_as_float(ml[1]) * __builtin_amdgcn_exp2f((ms - M) * C2);
        ls += dpp<0xB1>(ls); ls += dpp<0x4E>(ls);
        if (lane < 16 && j0 == 0) *(LAS float*)(L + L_ML + 128 + h * 4) = __builtin_amdgcn_exp2f((m - M) * C2) * __builtin_amdgcn_rcpf(ls);
      }
      v4u bo[4];
      p_operands(__float_as_uint(sc[2 * n]), __float_as_uint(sc[2 * n + 1]), mask0, mask1, bo);
      while (lds_word(L + C_LAND) <= 64 + 4 * n + w) __builtin_amdgcn_s_sleep(1);
      pv32<true>(((4 * n + w) & 15) * SLOT + vv, bo, acc0, acc1);
      *(LAS int*)(L + C_FREE + w * 8) = 64 + 4 * n + w + 16;
    }
    *(LAS f32x4*)(L + L_ACC + w * 2048 + vv) = acc0; *(LAS f32x4*)(L + L_ACC + w * 2048 + 1024 + vv) = acc1;
    lds_inc(L + C_NA);
    STAMP(3);
    const int bb = 2 * w + (lane >> 5), c = lane & 31;
    unsigned vg = bb * (HKV * 4 * PA_REC) + c * 16, vy = (lane & 7) * (32 * 2048) + g * 16;
    const int sg = W_PA + (ch >> 2) * (4 * PA_REC) + (ch & 3) * 512, sy = W_YB + (nb * 4 + w) * 2048 + ch * 64;
    LAS unsigned char* ap = L + L_W + (8 * g + ((lane & 15) >> 2)) * 128 + (16 * w + 4 * j0) * 2;
    LAS unsigned char* op = L + L_OT + bb * OT_ROW + c * 8;
    LAS unsigned char* bp = L + L_OT + (lane & 7) * OT_ROW + g * 16;
    if (lane & 8) vy = 0x40000000u;
    v4u tv = tb;
    asm volatile("" : "+v"(vg), "+v"(vy), "+v"(ap), "+v"(op), "+v"(bp), "+v"(tv) : "s"(sg), "s"(sy));
    while (lds_word(L + C_LAND) < GPOS) __builtin_amdgcn_s_sleep(1);
    STAMP(5);
    {
      const unsigned tp = now() + D1, tg = (unsigned)lds_word(L + C_FREE + 4) + (NUNIT * UC16 >> 4) + D1G;
      unsigned tc;
      do tc = now(); while ((int)(tc - tp) < 0 || ((int)(tc - tg) < 0 && (int)(tc - tp) < 200));
    }
    STAMP(4);
    v4u a[4], af[4];
#pragma unroll
    for (int u = 0; u < 4; ++u) a[u] = __builtin_amdgcn_raw_buffer_load_b128(rw, vg, sg + u * PA_REC, AUX_SC1);
    bool wo_in;
    { const unsigned tw = now(); do wo_in = lds_word(L + C_WO) != 0; while (!wo_in && (int)(now() - tw) < 30); }
    if (wo_in) {
#pragma unroll
      for (int ks = 0; ks < 4; ++ks) af[ks] = tr_frag(ap + ks * 4096, 512);
      asm volatile("" : "+v"(af[0]), "+v"(af[1]), "+v"(af[2]), "+v"(af[3]));
    }
#pragma unroll
    for (int u = 0; u < 4; ++u) a[u] ^= tv;
    if (!__all((or4((a[0] | a[1]) | (a[2] | a[3])) & 0xffu) == 0u)) {
      for (unsigned spin = 0; spin < SPIN_CAP; ++spin) {
        RELOAD();
#pragma unroll
        for (int u = 0; u < 4; ++u) a[u] = __builtin_amdgcn_raw_buffer_load_b128(rw, vg, sg + u * PA_REC, AUX_SC1) ^ tv;
        if (__all((or4((a[0] | a[1]) | (a[2] | a[3])) & 0xffu) == 0u)) break;
      }
    }
    STAMP(6);
    const f32x4 o = (__builtin_bit_cast(f32x4, a[0]) + __builtin_bit_cast(f32x4, a[1])) + (__builtin_bit_cast(f32x4, a[2]) + __builtin_bit_cast(f32x4, a[3]));
    *(LAS v2u*)op = v2u{cvtpk(o[0], o[1]), cvtpk(o[2], o[3])};
    BARRIER();
    STAMP(7);
    v4u bf[4];
    if (!wo_in) {
#pragma unroll
      for (int ks = 0; ks < 4; ++ks) af[ks] = tr_frag(ap + ks * 4096, 512);
    }
#pragma unroll
    for (int ks = 0; ks < 4; ++ks) bf[ks] = *(LAS v4u*)(bp + ks * 64);
    __builtin_amdgcn_sched_barrier(0);
    f32x4 y = {0.f, 0.f, 0.f, 0.f};
#pragma unroll
    for (int ks = 0; ks < 4; ++ks) y = mfma32(af[ks], bf[ks], y);
    __builtin_amdgcn_raw_buffer_store_b128(tag4(y, tv), rw, vy, sy, AUX_SC1);
    asm volatile("Lgqa_end_b:" ::: "memory");
    STAMP(8);
    STAMP_OUT();
    return;
  }

  __builtin_amdgcn_s_setprio(2);
  unsigned tag;
  {
    const int cb = j & 63, kq = j >> 6;
    const v4i rx = mkrsrc(x, NB * DM * 2), rq = mkrsrc(wq, (XF & 8) ? 0 : DM * HQ * DH * 2);
    const unsigned vx = (lane & 7) * 1024 + (lane >> 3) * 16, vq = (lane >> 3) * 8192 + (lane & 7) * 16;
    const bool hi = (lane & 8) != 0;
    const int vs = (lane & 7) * (HKV * 8192) + (hi ? 64 : 0) + g * 16, ss = W_QG + (cb >> 3) * 8192 + kq * 2048 + (cb & 7) * 256;
    if (WQT) while ((int)(now() - t0) < WQT) {}
    dma1(rx, vx, kq * 256, L_X);
    dma1(rx, vx, kq * 256 + 128, L_X + 1024);
#pragma unroll 1
    for (int u = 0; u < 16; ++u) dma1_nt(rq, vq, (kq * 16 + u) * 65536 + cb * 128, L_W + u * 1024);
    if (XF & 64) { unsigned pt = 0; if (lane < 16) pt = __builtin_amdgcn_raw_buffer_load_b32(rw, (lane & 7) * (HKV * 8192) + (lane >> 3) * 128, ss, 0); asm volatile("" :: "v"(pt)); }
    STAMP(5);
    tag = load_tag(ws);
    WAITVM(0);
    STAMP(10);
    f32x4 qa[4];
#pragma unroll
    for (int t = 0; t < 4; ++t) qa[t] = f32x4{0.f, 0.f, 0.f, 0.f};
    LAS unsigned char* ap = L + L_W + (8 * g + ((lane & 15) >> 2)) * 128 + j0 * 8;
    LAS unsigned char* bp = L + L_X + g * 128 + (lane & 7) * 16;
#pragma unroll
    for (int ks = 0; ks < 4; ++ks) {
      const v4u xb = *(LAS v4u*)(bp + ks * 512);
#pragma unroll
      for (int t = 0; t < 4; ++t) qa[t] = mfma32(tr_frag(ap + ks * 4096 + t * 32, 512), xb, qa[t]);
    }
    STAMP(1);
    const v4u tq = tag_bytes(tag);
    const v4u o0 = tag4(f32x4{hi ? qa[1][0] : qa[0][0], hi ? qa[1][1] : qa[0][1], hi ? qa[1][2] : qa[0][2], hi ? qa[1][3] : qa[0][3]}, tq);
    const v4u o1 = tag4(f32x4{hi ? qa[3][0] : qa[2][0], hi ? qa[3][1] : qa[2][1], hi ? qa[3][2] : qa[2][2], hi ? qa[3][3] : qa[2][3]}, tq);
    if (!(XF & 1)) {
      __builtin_amdgcn_raw_buffer_store_b128(o0, rw, vs, ss, AUX_SC1);
      __builtin_amdgcn_raw_buffer_store_b128(o1, rw, vs + 128, ss, AUX_SC1);
    }
    asm volatile("s_nop 1" :: "v"(o0), "v"(o1) : "memory");
    STAMP(2);
    while ((int)(now() - t0) < QPOLL) __builtin_amdgcn_s_sleep(2);
    const int so = W_QG + pr * 8192;
    v4u a[8];
    for (unsigned spin = 0; spin < SPIN_CAP; ++spin) {
      RELOAD();
#pragma unroll
      for (int u = 0; u < 8; ++u) a[u] = __builtin_amdgcn_raw_buffer_load_b128(rw, (XF & 2) ? 0x7000000 : vv + (u & 3) * 1024, so + (u >> 2) * 4096, AUX_SC1) ^ tq;
      if ((XF & 3) || __all((or4(((a[0] | a[1]) | (a[2] | a[3])) | ((a[4] | a[5]) | (a[6] | a[7]))) & 0xffu) == 0u)) break;
      STAMP(15);
    }
#pragma unroll
    for (int u = 0; u < 2; ++u) {
      const f32x4 v = (__builtin_bit_cast(f32x4, a[u]) + __builtin_bit_cast(f32x4, a[2 + u])) + (__builtin_bit_cast(f32x4, a[4 + u]) + __builtin_bit_cast(f32x4, a[6 + u]));
      *(LAS v2u*)(L + L_Q + u * 512 + lane * 8) = v2u{cvtpk(v[0], v[1]), cvtpk(v[2], v[3])};
    }
    RELOAD();
    *(LAS v2u*)(L + C_READY) = v2u{1u, tag};
  }
  STAMP(3);
  if ((XF & 32) && lane == 0) {
    const unsigned a = __builtin_nontemporal_load((const GAS unsigned*)(vc + pbase + so0));
    const unsigned b2 = __builtin_nontemporal_load((const GAS unsigned*)(wo + ch * 128 * 1024 + nb * 128));
    asm volatile("" :: "v"(a), "v"(b2));
  }
  {
    unsigned k = now() / FB_P + 1u;
    float af = 0.f, dr = 0.f;
    int nv = 0;
#pragma unroll 1
    for (int it = 0; it < 40; ++it, ++k) {
      const unsigned tk = k * FB_P;
      while ((int)(now() - tk) < 0) __builtin_amdgcn_s_sleep(2);
      RELOAD();
      const v2u pr2 = *(LAS v2u*)(L + C_LAND);
      if (__builtin_amdgcn_readfirstlane(pr2[0]) >= NUNIT - 12) break;
      const v2u fb = __builtin_amdgcn_raw_buffer_load_b64(rw, (lane & (FB_NREP - 1)) * 8, W_FB + ((k - 1u) & 63u) * FB_SLOT, AUX_SC1);
      if (j < FB_NREP && lane == 0) {
        const float p = (float)(8 * ((int)pr2[0] + LAG)) + fminf((float)(int)(tk - pr2[1]) * (1.f / 3.4f), 8.f);
        __builtin_amdgcn_raw_buffer_store_b64(v2u{__float_as_uint(p), tag ^ k}, rw, 0, W_FB + (k & 63u) * FB_SLOT + j * 8, AUX_SC1);
      }
      const bool ok = fb[1] == (tag ^ (k - 1u));
      float ps = ok ? __uint_as_float(fb[0]) : 0.f, pc = ok ? 1.f : 0.f;
      ps += dpp<0xB1>(ps); ps += dpp<0x4E>(ps); ps += dpp<0x124>(ps); ps += dpp<0x128>(ps); ps = xrow_sum(ps);
      pc += dpp<0xB1>(pc); pc += dpp<0x4E>(pc); pc += dpp<0x124>(pc); pc += dpp<0x128>(pc); pc = xrow_sum(pc);
      if (__builtin_amdgcn_readfirstlane(pc < 48.f)) continue;
      const float pbar = ps * __builtin_amdgcn_rcpf(pc);
      const float as = (float)(int)(tk - FB_P - t0) - pbar * TL0 - (float)(DELTA + PHASE / 2);
      if (nv == 0) af = as;
      else {
        const float er = as - af;
        if (nv >= FB_I0) dr = fminf(fmaxf(dr + FB_KI * fminf(er, 30.f), -6.f), 30.f);
        af += dr + FB_G * er;
      }
      ++nv;
      *(LAS unsigned*)(L + C_FREE + 4 + j0 * 8) = t0 + (unsigned)(int)af;
    }
  }
  v4u tb = tag_bytes(tag);
  GAS v2u* po = (GAS v2u*)(out + j * 32 + j0 * 8);
  {
    const v4i ro = mkrsrc(wo, HQ * DH * DM * 2);
    const unsigned vo = ((lane >> 5) + 8 * ((lane >> 3) & 3)) * 1024 + (lane & 7) * 16;
    while (lds_word(L + C_LAND) < WOPOS) __builtin_amdgcn_s_sleep(1);
#pragma unroll 1
    for (int u = 0; u < 16; ++u) dma1_nt(ro, vo, (ch * 128 + 32 * ((u >> 1) & 3) + 4 * (u >> 3) + 2 * (u & 1)) * 1024 + nb * 128, L_W + u * 1024);
    asm volatile("s_waitcnt vmcnt(0)" : "+v"(tb), "+v"(po) :: "memory");
    *(LAS int*)(L + C_WO) = 1;
    STAMP(11);
  }
  const unsigned tg2 = (unsigned)lds_word(L + C_FREE + 4) + (NUNIT * UC16 >> 4) + D1G + D2G;
  BARRIER();
  STAMP(7);
  {
    const unsigned tp = now() + D2;
    unsigned tc;
    do tc = now(); while ((int)(tc - tp) < 0 || ((int)(tc - tg2) < 0 && (int)(tc - tp) < 100));
  }
  {
    v4u a0, a1;
    for (unsigned spin = 0; spin < SPIN_CAP; ++spin) {
      RELOAD();
      a0 = __builtin_amdgcn_raw_buffer_load_b128(rw, vv, W_YB + j * 2048, AUX_SC1) ^ tb;
      a1 = __builtin_amdgcn_raw_buffer_load_b128(rw, vv + 1024, W_YB + j * 2048, AUX_SC1) ^ tb;
      if (__all((or4(a0 | a1) & 0xffu) == 0u)) break;
    }
    STAMP(8);
    f32x4 y = __builtin_bit_cast(f32x4, a0) + __builtin_bit_cast(f32x4, a1);
#pragma unroll
    for (int q = 0; q < 4; ++q) { float t = y[q]; t += dpp<0x124>(t); t += dpp<0x128>(t); y[q] = xrow_sum(t); }
    if (lane < 4) *po = v2u{cvtpk(y[0], y[1]), cvtpk(y[2], y[3])};
    if (j == 0 && lane == 0) __builtin_amdgcn_raw_buffer_store_b32(tag, rw, 0, W_E, AUX_SC1);
  }
  asm volatile("Lgqa_end_c:" ::: "memory");
  STAMP(9);
  STAMP_OUT();
}

extern "C" void kernel_launch(void* const* d_in, const int* in_sizes, int n_in, void* d_out, int out_size, void* d_ws, size_t ws_size,
                              hipStream_t stream) {
  gqa_fused<<<NWG, NTHR, 0, stream>>>((const unsigned char*)d_in[0], (const unsigned char*)d_in[1], (const unsigned char*)d_in[2], (const unsigned char*)d_in[3],
                                      (const unsigned char*)d_in[4], (unsigned char*)d_out, (unsigned char*)d_ws TUNE_PASS);
}
```

```cpp
#define LINE0 100
#define DIAG 109
#define DSIGN 0
#include <hip/hip_runtime.h>
#include <hip/hip_bf16.h>
#include <stdint.h>

#define GAS __attribute__((address_space(1)))
#define LAS __attribute__((address_space(3)))
typedef unsigned v4u __attribute__((ext_vector_type(4)));
typedef unsigned v2u __attribute__((ext_vector_type(2)));
typedef int v4i __attribute__((ext_vector_type(4)));
typedef float f32x4 __attribute__((ext_vector_type(4)));
typedef float f32x32 __attribute__((ext_vector_type(32)));
typedef short s16x4 __attribute__((ext_vector_type(4)));
typedef __bf16 bf16x8 __attribute__((ext_vector_type(8)));

constexpr int NB = 8, DM = 512, HQ = 32, HKV = 8, SK = 8192, DH = 128;
constexpr int NWG = 256, NTHR = 384;
constexpr int NUNIT = 128;
constexpr int SLOT = 8192;
constexpr float C2 = 0.088388347648318440f * 1.4426950408889634f;

#define NREG 4
#define NFIRST 4
#define WIN 24
#define UPREP 112
#define NFIN 3
#define MPOS 1
#define WOLAZY 1
#define PF2ROW 11
#define FB_NREP 32
#define QMODE 4
#define XF 256
#define TUNE_ARG
#define TUNE_PASS
#define TL0 3.35f
#define UC16 ((unsigned)(TL0 * 128.f))
#define DELTA 0
#define HOLD 30
#define MINSP 20
#define LATE 40
#define MINSP2 16
#define PHASE 28
#define FB_P 192u
#define FB_G 0.4f
#define FB_KI 0.f
#define FB_I0 3
#define WQT 0
#define WQSP 12
#define QPOLL 480
#define WOPOS 125
#define D1 30
#define D1G 138
#define GPOS 126
#define D2G 0
#define D2 40
constexpr unsigned SPIN_CAP = 1u << 16;
constexpr int LAG = (WIN + 7) / 8;

constexpr int L_Q = 16 * SLOT, L_ACC = L_Q + 1024, L_PL = L_ACC + 4 * 2048, L_DG = L_PL + 2048, L_OT = L_DG + 16, OT_ROW = 272, L_ML = L_OT + 8 * OT_ROW, L_CTL = (L_ML + 144 + 63) / 64 * 64;
constexpr int L_X = L_ACC, L_W = L_CTL + 64, L_TOT = L_W + 16384;
constexpr int C_FREE = L_CTL, C_LAND = L_CTL + 32, C_READY = L_CTL + 40, C_TAG = L_CTL + 44, C_NM = L_CTL + 48, C_NL = L_CTL + 52, C_NA = L_CTL + 56, C_WO = L_CTL + 60;
static_assert(L_CTL % 32 == 0 && L_TOT <= 160 * 1024, "lds map");
constexpr int W_E = 0, W_FB = 1024, FB_SLOT = 512, W_ML = W_FB + 64 * FB_SLOT + 1024, W_QG = W_ML + NWG * 128, W_PA = W_QG + NWG * 2048, PA_REC = 2048, W_YB = W_PA + NWG * PA_REC,
              W_END = W_YB + NWG * 2048;

#define STAMP(i) do { if (DIAG % 100 == (i) && DIAG / 100 == (wv > 2 ? 2 : wv) && lane == 0) (void)__hip_atomic_fetch_max((LAS int*)(L + L_DG), (int)(now() - t0), __ATOMIC_RELAXED, __HIP_MEMORY_SCOPE_WORKGROUP); } while (0)
#define STAMP_OUT() do { if (wv == 1) { while ((int)(now() - t0) < 9000) __builtin_amdgcn_s_sleep(8); const int x_ = lds_word(L + L_DG); const int tg_ = DSIGN >= 0 ? 10000 + DSIGN * 10 * x_ : 70000 - 10 * x_; while ((int)(now() - t0) < tg_) __builtin_amdgcn_s_sleep(4); } } while (0)

#define STR_(x) #x
#define STRX_(x) STR_(x)
#define WAITVM(n) asm volatile("s_waitcnt vmcnt(" STR_(n) ")" ::: "memory")
#define BARRIER() asm volatile("s_waitcnt lgkmcnt(0)\n\ts_barrier" ::: "memory")
#define RELOAD() asm volatile("" ::: "memory")
constexpr int AUX_SC1 = 16;

__device__ __forceinline__ unsigned now() { return (unsigned)__builtin_amdgcn_s_memrealtime(); }
__device__ __forceinline__ unsigned cvtpk(float lo, float hi) {
  typedef __bf16 bf16x2_t __attribute__((ext_vector_type(2)));
  typedef float f32x2_t __attribute__((ext_vector_type(2)));
  return __builtin_bit_cast(unsigned, __builtin_convertvector(f32x2_t{lo, hi}, bf16x2_t));
}
template <int CTRL> __device__ __forceinline__ unsigned dppu(unsigned x) { return (unsigned)__builtin_amdgcn_mov_dpp((int)x, CTRL, 0xf, 0xf, true); }
template <int CTRL> __device__ __forceinline__ float dpp(float x) { return __uint_as_float(dppu<CTRL>(__float_as_uint(x))); }
__device__ __forceinline__ float xrow_max(float x) {
  auto s = __builtin_amdgcn_permlane16_swap(__float_as_uint(x), __float_as_uint(x), false, false);
  x = fmaxf(__uint_as_float(s[0]), __uint_as_float(s[1]));
  auto t = __builtin_amdgcn_permlane32_swap(__float_as_uint(x), __float_as_uint(x), false, false);
  return fmaxf(__uint_as_float(t[0]), __uint_as_float(t[1]));
}
__device__ __forceinline__ float xrow_sum(float x) {
  auto s = __builtin_amdgcn_permlane16_swap(__float_as_uint(x), __float_as_uint(x), false, false);
  x = __uint_as_float(s[0]) + __uint_as_float(s[1]);
  auto t = __builtin_amdgcn_permlane32_swap(__float_as_uint(x), __float_as_uint(x), false, false);
  return __uint_as_float(t[0]) + __uint_as_float(t[1]);
}
__device__ __forceinline__ v4i mkrsrc(const void* p, unsigned bytes) {
  const unsigned long long a = (unsigned long long)p;
  v4i r; r[0] = (int)(unsigned)a; r[1] = (int)((unsigned)(a >> 32) & 0xffffu); r[2] = (int)bytes; r[3] = 0x00020000; return r;
}
__device__ __forceinline__ int lds_word(LAS unsigned char* p) { RELOAD(); const int v = __builtin_amdgcn_readfirstlane(*(LAS int*)p); RELOAD(); return v; }
__device__ __forceinline__ void lds_inc(LAS unsigned char* p) {
  unsigned long long ex;
  asm volatile("s_mov_b64 %0, exec\n\ts_mov_b64 exec, 1\n\tds_add_u32 %1, %2\n\ts_mov_b64 exec, %0" : "=&s"(ex) : "v"((unsigned)(size_t)p), "v"(1u) : "memory");
}
#define DMA4(V, R, S) "buffer_load_dwordx4 " V ", " R ", " S " offen nt lds\n\tbuffer_load_dwordx4 " V ", " R ", " S " offen offset:1024 nt lds\n\t" \
                      "buffer_load_dwordx4 " V ", " R ", " S " offen offset:2048 nt lds\n\tbuffer_load_dwordx4 " V ", " R ", " S " offen offset:3072 nt lds\n\t"
__device__ __forceinline__ void issue_two(v4i r, unsigned vv, unsigned so, unsigned lds) {
  unsigned t;
  asm volatile("s_mov_b32 m0, %4\n\ts_add_u32 %0, %3, 0x1000\n\t" DMA4("%2", "%1", "%3") "s_add_u32 m0, m0, 0x1000\n\ts_nop 0\n\t" DMA4("%2", "%1", "%0")
               "s_add_u32 m0, m0, 0x1000\n\ts_add_u32 %0, %0, 0x1000\n\t" DMA4("%2", "%1", "%0") "s_add_u32 m0, m0, 0x1000\n\ts_add_u32 %0, %0, 0x1000\n\t" DMA4("%2", "%1", "%0")
               : "=&s"(t) : "s"(r), "v"(vv), "s"(so), "s"(lds) : "memory", "scc");
}
__device__ __forceinline__ void dma1_nt(v4i r, unsigned voff, unsigned soff, unsigned lds) {
  asm volatile("s_mov_b32 m0, %3\n\ts_nop 0\n\tbuffer_load_dwordx4 %1, %0, %2 offen nt lds" :: "s"(r), "v"(voff), "s"(soff), "s"(lds) : "memory");
}
__device__ __forceinline__ void dma1(v4i r, unsigned voff, unsigned soff, unsigned lds) {
  asm volatile("s_mov_b32 m0, %3\n\ts_nop 0\n\tbuffer_load_dwordx4 %1, %0, %2 offen lds" :: "s"(r), "v"(voff), "s"(soff), "s"(lds) : "memory");
}
struct Dbg { unsigned d0, d1; };
__device__ __forceinline__ void stream_units(v4i rs, unsigned vv, unsigned u, unsigned ue, unsigned so, unsigned ra, unsigned uc16, int hold, int msp, int lat, int ms2, Dbg& dbg, int lane) {
  unsigned ta, tb, st = 8192u, fo = ((u + 1u) & 3u) * 8u + C_FREE;
#define STAMP_ASM
#define STAMP_OPS
#define STAMP_INS
  asm volatile(
      "s_mov_b32 m0, %[m0i]\n\t"
      "v_mov_b32 v2, %[ld0]\n"
      "LT%=:\n\t"
      "s_waitcnt vmcnt(" STRX_(WIN) ")\n\t"
      DMA4("%[v0]", "%[rs]", "%[so]")
      "s_add_u32 m0, m0, 0x1000\n\t"
      "s_memrealtime s[40:41]\n\t"
      DMA4("%[v1]", "%[rs]", "%[so]")
      "ds_read_b64 v[4:5], %[fo]\n\t"
      "s_add_u32 m0, m0, 0x1000\n\t"
      "s_add_u32 %[so], %[so], %[st]\n\t"
      "s_and_b32 m0, m0, 0x1ffff\n\t"
      "s_and_b32 %[so], %[so], 0x1fffff\n\t"
      "s_xor_b32 %[st], %[st], 0xc000\n\t"
      "s_add_u32 %[ra], %[ra], %[uc]\n\t"
      "v_add_u32 v2, 1, v2\n\t"
      "v_add_u32 %[fo], 8, %[fo]\n\t"
      "s_add_u32 %[u], %[u], 1\n\t"
      "v_and_b32 %[fo], %[fm], %[fo]\n\t"
      "s_waitcnt lgkmcnt(0)\n\t"
      "v_mov_b32 v3, s40\n\t"
      "v_readfirstlane_b32 %[a], v5\n\t"
      "s_lshr_b32 s41, %[ra], 4\n\t"
      "v_readfirstlane_b32 %[b], v4\n\t"
      "ds_write_b64 %[vl], v[2:3]\n\t"
      STAMP_ASM
      "s_cmp_eq_u32 %[u], %[ue]\n\t"
      "s_cbranch_scc1 LE%=\n\t"
      "s_add_u32 %[a], %[a], s41\n\t"
      "s_sub_i32 %[a], %[a], s40\n\t"
      "s_cmp_lt_i32 %[a], %[lat]\n\t"
      "s_cselect_b32 s41, %[ms2], %[msp]\n\t"
      "s_min_i32 %[a], %[a], %[hold]\n\t"
      "s_max_i32 %[a], %[a], s41\n\t"
      "s_add_u32 %[a], %[a], s40\n\t"
      "s_cmp_lt_u32 %[b], %[u]\n\t"
      "s_cbranch_scc1 LS%=\n"
      "LW%=:\n\t"
      "s_memrealtime s[40:41]\n\t"
      "s_waitcnt lgkmcnt(0)\n\t"
      "s_sub_i32 %[b], %[a], s40\n\t"
      "s_cmp_gt_i32 %[b], 0\n\t"
      "s_cbranch_scc0 LT%=\n\t"
      "s_branch LW%=\n"
      "LS%=:\n\t"
      "s_and_b32 %[b], %[u], 3\n\t"
      "s_lshl_b32 %[b], %[b], 3\n\t"
      "s_add_u32 %[b], %[b], %[fb]\n\t"
      "v_mov_b32 v5, %[b]\n"
      "LR%=:\n\t"
      "s_sleep 1\n\t"
      "ds_read_b32 v4, v5\n\t"
      "s_waitcnt lgkmcnt(0)\n\t"
      "v_readfirstlane_b32 %[b], v4\n\t"
      "s_cmp_lt_u32 %[b], %[u]\n\t"
      "s_cbranch_scc1 LR%=\n\t"
      "s_branch LW%=\n"
      "LE%=:"
      : [so] "+s"(so), [ra] "+s"(ra), [u] "+s"(u), [st] "+s"(st), [fo] "+v"(fo), [a] "=&s"(ta), [b] "=&s"(tb) STAMP_OPS
      : [rs] "s"(rs), [v0] "v"(vv), [v1] "v"(vv + 4096u), [vl] "v"((unsigned)C_LAND), [ue] "s"(ue), [uc] "s"(uc16), [hold] "s"(hold), [msp] "s"(msp), [lat] "s"(lat), [ms2] "s"(ms2),
        [m0i] "s"((u & 15u) * SLOT), [ld0] "s"(u - 1u - LAG), [fm] "s"(~32u), [fb] "s"((unsigned)C_FREE) STAMP_INS
      : "memory", "scc", "vcc", "v2", "v3", "v4", "v5", "s40", "s41");
}
static_assert((C_FREE & 32) == 0, "the record pointer wraps by clearing bit 5");

__device__ __forceinline__ f32x4 mfma32(v4u a, v4u b, f32x4 c) {
  return __builtin_amdgcn_mfma_f32_16x16x32_bf16(__builtin_bit_cast(bf16x8, a), __builtin_bit_cast(bf16x8, b), c, 0, 0, 0);
}
__device__ __forceinline__ v4u tr_frag(LAS unsigned char* p, int hi) {
  const v2u lo = __builtin_bit_cast(v2u, __builtin_amdgcn_ds_read_tr16_b64_v4i16((LAS s16x4*)p));
  const v2u up = __builtin_bit_cast(v2u, __builtin_amdgcn_ds_read_tr16_b64_v4i16((LAS s16x4*)(p + hi)));
  return v4u{lo[0], lo[1], up[0], up[1]};
}
__device__ __forceinline__ float score16(const v4u* K, v4u qf, unsigned j0) {
  float sc[4];
#pragma unroll
  for (int u = 0; u < 4; ++u) {
    const f32x4 d = mfma32(K[u], qf, f32x4{0.f, 0.f, 0.f, 0.f});
    sc[u] = ((dpp<0x00>(d[0]) + dpp<0x55>(d[1])) + dpp<0xAA>(d[2])) + dpp<0xFF>(d[3]);
  }
  return (j0 & 2) ? ((j0 & 1) ? sc[3] : sc[2]) : ((j0 & 1) ? sc[1] : sc[0]);
}
__device__ __forceinline__ void p_operands(unsigned k0, unsigned k1, unsigned mask0, unsigned mask1, v4u (&bo)[4]) {
  bo[0] = v4u{dppu<0x00>(k0) & mask0, dppu<0x00>(k0) & mask1, dppu<0x55>(k0) & mask0, dppu<0x55>(k0) & mask1};
  bo[1] = v4u{dppu<0xAA>(k0) & mask0, dppu<0xAA>(k0) & mask1, dppu<0xFF>(k0) & mask0, dppu<0xFF>(k0) & mask1};
  bo[2] = v4u{dppu<0x00>(k1) & mask0, dppu<0x00>(k1) & mask1, dppu<0x55>(k1) & mask0, dppu<0x55>(k1) & mask1};
  bo[3] = v4u{dppu<0xAA>(k1) & mask0, dppu<0xAA>(k1) & mask1, dppu<0xFF>(k1) & mask0, dppu<0xFF>(k1) & mask1};
  asm volatile("" : "+v"(bo[0]), "+v"(bo[1]), "+v"(bo[2]), "+v"(bo[3]));
}
#define RD2(A, P, O0, O1) "ds_read2st64_b64 %[" A "], %[" P "] offset0:" O0 " offset1:" O1 "\n\t"
#define MF(N, C, A, B) "s_waitcnt lgkmcnt(" N ")\n\tv_mfma_f32_16x16x32_bf16 %[" C "], %[" A "], %[" B "], %[" C "]\n\t"
template <bool PAD> __device__ __forceinline__ void pv32(unsigned p, const v4u (&bo)[4], f32x4& acc0, f32x4& acc1) {
  v4u a0, a1, a2, a3, a4, a5, a6, a7;
  asm volatile(RD2("a0", "p0", "0", "2") RD2("a1", "p1", "0", "2") RD2("a2", "p0", "4", "6") RD2("a3", "p1", "4", "6")
               RD2("a4", "p0", "8", "10") RD2("a5", "p1", "8", "10") RD2("a6", "p0", "12", "14") RD2("a7", "p1", "12", "14")
               MF("7", "c0", "a0", "b0") MF("6", "c1", "a1", "b0") MF("5", "c0", "a2", "b1") MF("4", "c1", "a3", "b1")
               MF("3", "c0", "a4", "b2") MF("2", "c1", "a5", "b2") MF("1", "c0", "a6", "b3") MF("0", "c1", "a7", "b3") "s_nop %[pad]"
      : [c0] "+v"(acc0), [c1] "+v"(acc1), [a0] "=&v"(a0), [a1] "=&v"(a1), [a2] "=&v"(a2), [a3] "=&v"(a3), [a4] "=&v"(a4), [a5] "=&v"(a5), [a6] "=&v"(a6), [a7] "=&v"(a7)
      : [p0] "v"(p), [p1] "v"(p + 8), [b0] "v"(bo[0]), [b1] "v"(bo[1]), [b2] "v"(bo[2]), [b3] "v"(bo[3]), [pad] "i"(PAD ? 7 : 0) : "memory");
}
__device__ __forceinline__ v4u tag_bytes(unsigned tag) { return v4u{tag & 0xffu, (tag >> 8) & 0xffu, (tag >> 16) & 0xffu, tag >> 24}; }
__device__ __forceinline__ v4u tag4(f32x4 v, v4u tb) { return (__builtin_bit_cast(v4u, v) & 0xffffff00u) | tb; }
__device__ __forceinline__ unsigned or4(v4u u) { return (u[0] | u[1]) | (u[2] | u[3]); }
__device__ __forceinline__ unsigned load_tag(const unsigned char* ws) {
  unsigned e0;
  asm volatile("s_load_dword %0, %1, 0x0\n\ts_waitcnt lgkmcnt(0)" : "=&s"(e0) : "s"(ws) : "memory");
  return ((e0 & 0x7f7f7f7fu) + 0x51276b35u) ^ (e0 & 0x80808080u);
}

__global__ void __launch_bounds__(NTHR, 1) gqa_fused(const unsigned char* __restrict__ x, const unsigned char* __restrict__ kc, const unsigned char* __restrict__ vc,
                                                     const unsigned char* __restrict__ wq, const unsigned char* __restrict__ wo, unsigned char* __restrict__ out,
                                                     unsigned char* ws TUNE_ARG) {
  __shared__ __attribute__((aligned(16))) unsigned char smem[L_TOT];
  LAS unsigned char* L = (LAS unsigned char*)smem;
  unsigned long long t64;
  unsigned pf, pa;
  unsigned long long pc;
  unsigned clen;
  {
    unsigned long long ex;
    asm volatile("s_memrealtime %0\n\ts_getpc_b64 %1\nLgqa_pc:\n\t"
                 "s_mov_b32 %2, Lgqa_end_a-Lgqa_pc+128\n\ts_max_u32 %2, %2, Lgqa_end_b-Lgqa_pc+128\n\ts_max_u32 %2, %2, Lgqa_end_c-Lgqa_pc+128\n\t"
                 "v_lshlrev_b32 %5, 7, %6\n\tv_add_u32 %5, 0xffffc000, %5\n\tv_cmp_gt_u32 vcc, %2, %5\n\t"
                 "s_and_saveexec_b64 %3, vcc\n\ts_cbranch_execz Lgqa_nopf\n\tglobal_load_dword %4, %5, %1 offset:-32\nLgqa_nopf:\n\ts_mov_b64 exec, %3"
                 : "=&s"(t64), "=&s"(pc), "=&s"(clen), "=&s"(ex), "=&v"(pf), "=&v"(pa) : "v"(threadIdx.x) : "scc", "vcc", "memory");
  }
  const int lane = threadIdx.x & 63, wv = __builtin_amdgcn_readfirstlane(threadIdx.x >> 6);
  const int j = blockIdx.x, xg = j & 7, idx = j >> 3;
  const int yy = (xg - idx) & 7, pr = (XF & 16) ? (idx >> 2) * 8 + xg : (XF & 128) ? xg * 8 + (idx >> 2) : (XF & 256) ? (idx >> 2) * 8 + ((xg + (idx >> 2)) & 7) : 2 * idx + (yy >> 2), s = (XF & (16 | 128 | 256)) ? (idx & 3) : yy & 3;
  const int ch = 4 * xg + (idx >> 3), nb = idx & 7;
  const int g = lane >> 4;
  const unsigned so0 = (unsigned)(pr >> 1) * 65536u + s * 16384u;
  const unsigned vv = lane * 16;
  const unsigned ra0 = ((unsigned)(j * 37) & 31u) * PHASE >> 1;
  asm volatile("s_waitcnt lgkmcnt(0)" : "+s"(t64) : "s"(x), "s"(kc), "s"(vc), "s"(wq), "s"(wo), "s"(out), "s"(ws), "s"(so0), "v"(vv));
  const unsigned t0 = (unsigned)t64;
  const size_t pbase = (size_t)pr * (SK * DH * 2);
  Dbg dbg = {0u, 0u};

  const v4i rk = mkrsrc(kc + pbase, SK * DH * 2);
  if (wv == 0) {
    __builtin_amdgcn_s_setprio(3);
#pragma unroll
    for (int u = 0; u < NFIRST; u += 2) issue_two(rk, vv, (so0 + (u >> 1) * 65536u) & 0x1fffffu, u * SLOT);
    const unsigned keep = (unsigned)((lane & 7) - 4) >> 31;
    *(LAS v2u*)(L + L_CTL + (lane & 7) * 8) = v2u{(12u + (lane & 7)) * keep, (t0 + LINE0) * keep};
    *(LAS int*)(L + L_DG) = 0;
    STAMP(1);
  }
  BARRIER();
  if (wv == 0) stream_units(rk, vv, NFIRST, 64u, (so0 + (NFIRST >> 1) * 65536u) & 0x1fffffu, NFIRST * UC16 + ra0, UC16, HOLD, MINSP, -LATE, MINSP2, dbg, lane);
  const __amdgpu_buffer_rsrc_t rw = __builtin_amdgcn_make_buffer_rsrc((void*)ws, 0, W_END, 0x00020000);
  const unsigned j0 = lane & 3;
  const int h = (lane >> 2) & 3;
  const unsigned mask0 = j0 == 0 ? 0x0000ffffu : (j0 == 1 ? 0xffff0000u : 0u);
  const unsigned mask1 = j0 == 2 ? 0x0000ffffu : (j0 == 3 ? 0xffff0000u : 0u);

  if (wv == 0) {
    const v4i rv = mkrsrc(vc + pbase, SK * DH * 2);
    while (lds_word(L + C_FREE) < 64) __builtin_amdgcn_s_sleep(1);
    stream_units(rv, vv, 64u, UPREP, so0, 64u * UC16 + ra0, UC16, HOLD, MINSP, -LATE, MINSP2, dbg, lane);
    while (lds_word(L + C_FREE + 24) < 64 + 4 * 8 + 3 + 16) __builtin_amdgcn_s_sleep(1);
    v4u bo[NFIN][4], tb;
    unsigned vo = h * 512 + g * 64 + j0 * 16;
    float scale;
    {
      v2u pl[NFIN];
#pragma unroll
      for (int k = 0; k < NFIN; ++k) pl[k] = *(LAS v2u*)(L + L_PL + (4 - NFIN + k) * 512 + lane * 8);
      tb = tag_bytes(*(LAS unsigned*)(L + C_TAG));
      scale = *(LAS float*)(L + L_ML + 128 + h * 4);
      const int fr = *(LAS int*)(L + C_FREE + (UPREP & 3) * 8);
#pragma unroll
      for (int k = 0; k < NFIN; ++k) p_operands(pl[k][0], pl[k][1], mask0, mask1, bo[k]);
      asm volatile("" : "+v"(vo), "+v"(tb), "+v"(scale));
      if (__builtin_amdgcn_readfirstlane(fr) < UPREP) { while (lds_word(L + C_FREE + (UPREP & 3) * 8) < UPREP) __builtin_amdgcn_s_sleep(1); }
    }
    stream_units(rv, vv, UPREP, NUNIT, (so0 + ((UPREP - 64) >> 1) * 65536u) & 0x1fffffu, UPREP * UC16 + ra0, UC16, HOLD, MINSP, -LATE, MINSP2, dbg, lane);
    STAMP(2);
    const int so = W_PA + (pr * 4 + s) * PA_REC;
    f32x4 acc0 = {0.f, 0.f, 0.f, 0.f}, acc1 = {0.f, 0.f, 0.f, 0.f};
    static_assert(NFIN >= 2 && LAG <= 3, "the drain below starts at three units outstanding");
    f32x4 s0, s1;
#pragma unroll
    for (int k = 0; k < 3; ++k) {
      if (k == 0) WAITVM(24); else if (k == 1) WAITVM(16); else WAITVM(8);
      if (k < 2) *(LAS int*)(L + C_LAND) = NUNIT - 3 + k;
      if (k >= 4 - NFIN) pv32<false>((12 + k) * SLOT + vv, bo[k >= 4 - NFIN ? k - (4 - NFIN) : 0], acc0, acc1);
      if (k == MPOS) {
        while (lds_word(L + C_NA) < 4) {}
        f32x4 a[8];
#pragma unroll
        for (int u = 0; u < 8; ++u) a[u] = *(LAS f32x4*)(L + L_ACC + vv + u * 1024);
        __builtin_amdgcn_sched_barrier(0);
        s0 = ((a[0] + a[2]) + (a[4] + a[6])) * scale; s1 = ((a[1] + a[3]) + (a[5] + a[7])) * scale;
        asm volatile("" : "+v"(s0), "+v"(s1) : "v"(14 * SLOT + vv), "v"(14 * SLOT + vv + 8), "v"(15 * SLOT + vv), "v"(15 * SLOT + vv + 8), "s"(so), "s"(0xffffff00u));
      }
    }
    STAMP(4);
    WAITVM(0);
    STAMP(3);
    pv32<true>(15 * SLOT + vv, bo[NFIN - 1], acc0, acc1);
    const v4u o0 = tag4(acc0 * scale + s0, tb), o1 = tag4(acc1 * scale + s1, tb);
    __builtin_amdgcn_raw_buffer_store_b128(o0, rw, vo, so, AUX_SC1);
    __builtin_amdgcn_raw_buffer_store_b128(o1, rw, vo + 256, so, AUX_SC1);
    asm volatile("s_nop 1" :: "v"(o0), "v"(o1) : "memory");
    *(LAS int*)(L + C_LAND) = NUNIT;
    asm volatile("Lgqa_end_a:" ::: "memory");
    STAMP(5);
    STAMP_OUT();
    return;
  }

  if (wv >= 2) {
    const int w = wv - 2;
    const unsigned vk = (h * 16 + g * 4 + j0) * 16;
    asm volatile("s_waitcnt vmcnt(0)" :: "v"(pf), "v"(pa) : "memory");
    f32x32 sc;
    float m;
    {
      v4u P[NREG][8];
#pragma unroll
      for (int r = 0; r < NREG; ++r) {
        while (lds_word(L + C_LAND) <= 4 * r + w) __builtin_amdgcn_s_sleep(2);
#pragma unroll
        for (int u = 0; u < 8; ++u) P[r][u] = *(LAS v4u*)(L + ((4 * r + w) & 15) * SLOT + vk + u * 1024);
        asm volatile("" :: "v"(P[r][0]), "v"(P[r][1]), "v"(P[r][2]), "v"(P[r][3]), "v"(P[r][4]), "v"(P[r][5]), "v"(P[r][6]), "v"(P[r][7]) : "memory");
        *(LAS int*)(L + C_FREE + w * 8) = 4 * r + w + 16;
      }
      STAMP(1);
      while (lds_word(L + C_READY) == 0) __builtin_amdgcn_s_sleep(2);
      STAMP(2);
      const v4u qf = *(LAS v4u*)(L + L_Q + vk);
      m = -INFINITY;
#pragma unroll
      for (int r = 0; r < NREG; ++r) {
        const float s0 = score16(&P[r][0], qf, j0), s1 = score16(&P[r][4], qf, j0);
        sc[2 * r] = s0; sc[2 * r + 1] = s1; m = fmaxf(m, fmaxf(s0, s1));
      }
#pragma unroll 1
      for (int n = NREG; n < 16; ++n) {
        while (lds_word(L + C_LAND) <= 4 * n + w) __builtin_amdgcn_s_sleep(2);
        v4u K[8];
#pragma unroll
        for (int u = 0; u < 8; ++u) K[u] = *(LAS v4u*)(L + ((4 * n + w) & 15) * SLOT + vk + u * 1024);
        asm volatile("" :: "v"(K[0]), "v"(K[1]), "v"(K[2]), "v"(K[3]), "v"(K[4]), "v"(K[5]), "v"(K[6]), "v"(K[7]) : "memory");
        *(LAS int*)(L + C_FREE + w * 8) = 4 * n + w + 16;
        const float s0 = score16(&K[0], qf, j0), s1 = score16(&K[4], qf, j0);
        sc[2 * n] = s0; sc[2 * n + 1] = s1; m = fmaxf(m, fmaxf(s0, s1));
      }
    }
    STAMP(10);
    const v4u tb = tag_bytes((unsigned)lds_word(L + C_TAG));
    m = fmaxf(m, dpp<0xB1>(m)); m = fmaxf(m, dpp<0x4E>(m)); m = xrow_max(m);
    *(LAS float*)(L + L_ML + h * 16 + w * 4) = m;
    lds_inc(L + C_NM);
    while (lds_word(L + C_NM) < 4) __builtin_amdgcn_s_sleep(1);
    {
      const f32x4 mw = *(LAS f32x4*)(L + L_ML + h * 16);
      m = __uint_as_float(__float_as_uint(fmaxf(fmaxf(mw[0], mw[1]), fmaxf(mw[2], mw[3]))) & 0xffffff00u);
      const float mc = -m * C2;
      float l = 0.f;
#pragma unroll
      for (int n = 0; n < 32; ++n) { const float p = __builtin_amdgcn_exp2f(fmaf(sc[n], C2, mc)); l += p; sc[n] = __uint_as_float(cvtpk(p, p)); }
      l += dpp<0xB1>(l); l += dpp<0x4E>(l); l = xrow_sum(l);
      *(LAS float*)(L + L_ML + 64 + h * 16 + w * 4) = l;
      *(LAS v2u*)(L + L_PL + w * 512 + lane * 8) = v2u{__float_as_uint(sc[30]), __float_as_uint(sc[31])};
      lds_inc(L + C_NL);
    }
    STAMP(11);
    f32x4 acc0 = {0.f, 0.f, 0.f, 0.f}, acc1 = {0.f, 0.f, 0.f, 0.f};
#pragma unroll 1
    for (int n = 0; n < (w < 4 - NFIN ? 16 : 15); ++n) {
      if (w == 3 && n == 1) {
        while (lds_word(L + C_NL) < 4) __builtin_amdgcn_s_sleep(1);
        const f32x4 lw = *(LAS f32x4*)(L + L_ML + 64 + h * 16);
        if (lane < 16 && j0 == 0) __builtin_amdgcn_raw_buffer_store_b128(tag4(f32x4{m, (lw[0] + lw[1]) + (lw[2] + lw[3]), 0.f, 0.f}, tb), rw, h * 16, W_ML + (pr * 4 + s) * 128, AUX_SC1);
      }
      if ((XF & 512) && n == PF2ROW && pa < clen) { const unsigned pt = *(const GAS unsigned*)(pc - 32 + pa); asm volatile("" :: "v"(pt)); }
      if (w == 3 && n == 8) {
        v4u ml;
        for (unsigned spin = 0; spin < SPIN_CAP; ++spin) {
          RELOAD();
          ml = __builtin_amdgcn_raw_buffer_load_b128(rw, j0 * 128 + h * 16, W_ML + pr * 512, AUX_SC1) ^ tb;
          if (__all((or4(ml) & 0xffu) == 0u)) break;
        }
        const float ms = __uint_as_float(ml[0]);
        float M = fmaxf(ms, dpp<0xB1>(ms)); M = fmaxf(M, dpp<0x4E>(M));
        float ls = __uint_as_float(ml[1]) * __builtin_amdgcn_exp2f((ms - M) * C2);
        ls += dpp<0xB1>(ls); ls += dpp<0x4E>(ls);
        if (lane < 16 && j0 == 0) *(LAS float*)(L + L_ML + 128 + h * 4) = __builtin_amdgcn_exp2f((m - M) * C2) * __builtin_amdgcn_rcpf(ls);
      }
      v4u bo[4];
      p_operands(__float_as_uint(sc[2 * n]), __float_as_uint(sc[2 * n + 1]), mask0, mask1, bo);
      while (lds_word(L + C_LAND) <= 64 + 4 * n + w) __builtin_amdgcn_s_sleep(1);
      pv32<true>(((4 * n + w) & 15) * SLOT + vv, bo, acc0, acc1);
      *(LAS int*)(L + C_FREE + w * 8) = 64 + 4 * n + w + 16;
    }
    *(LAS f32x4*)(L + L_ACC + w * 2048 + vv) = acc0; *(LAS f32x4*)(L + L_ACC + w * 2048 + 1024 + vv) = acc1;
    lds_inc(L + C_NA);
    STAMP(3);
    const int bb = 2 * w + (lane >> 5), c = lane & 31;
    unsigned vg = bb * (HKV * 4 * PA_REC) + c * 16, vy = (lane & 7) * (32 * 2048) + g * 16;
    const int sg = W_PA + (ch >> 2) * (4 * PA_REC) + (ch & 3) * 512, sy = W_YB + (nb * 4 + w) * 2048 + ch * 64;
    LAS unsigned char* ap = L + L_W + (8 * g + ((lane & 15) >> 2)) * 128 + (16 * w + 4 * j0) * 2;
    LAS unsigned char* op = L + L_OT + bb * OT_ROW + c * 8;
    LAS unsigned char* bp = L + L_OT + (lane & 7) * OT_ROW + g * 16;
    if (lane & 8) vy = 0x40000000u;
    v4u tv = tb;
    asm volatile("" : "+v"(vg), "+v"(vy), "+v"(ap), "+v"(op), "+v"(bp), "+v"(tv) : "s"(sg), "s"(sy));
    while (lds_word(L + C_LAND) < GPOS) __builtin_amdgcn_s_sleep(1);
    STAMP(5);
    {
      const unsigned tp = now() + D1, tg = (unsigned)lds_word(L + C_FREE + 4) + (NUNIT * UC16 >> 4) + D1G;
      unsigned tc;
      do tc = now(); while ((int)(tc - tp) < 0 || ((int)(tc - tg) < 0 && (int)(tc - tp) < 200));
    }
    STAMP(4);
    v4u a[4], af[4];
#pragma unroll
    for (int u = 0; u < 4; ++u) a[u] = __builtin_amdgcn_raw_buffer_load_b128(rw, vg, sg + u * PA_REC, AUX_SC1);
    bool wo_in;
    { const unsigned tw = now(); do wo_in = lds_word(L + C_WO) != 0; while (!wo_in && (int)(now() - tw) < 30); }
    if (wo_in) {
#pragma unroll
      for (int ks = 0; ks < 4; ++ks) af[ks] = tr_frag(ap + ks * 4096, 512);
      asm volatile("" : "+v"(af[0]), "+v"(af[1]), "+v"(af[2]), "+v"(af[3]));
    }
#pragma unroll
    for (int u = 0; u < 4; ++u) a[u] ^= tv;
    if (!__all((or4((a[0] | a[1]) | (a[2] | a[3])) & 0xffu) == 0u)) {
      for (unsigned spin = 0; spin < SPIN_CAP; ++spin) {
        RELOAD();
#pragma unroll
        for (int u = 0; u < 4; ++u) a[u] = __builtin_amdgcn_raw_buffer_load_b128(rw, vg, sg + u * PA_REC, AUX_SC1) ^ tv;
        if (__all((or4((a[0] | a[1]) | (a[2] | a[3])) & 0xffu) == 0u)) break;
      }
    }
    STAMP(6);
    const f32x4 o = (__builtin_bit_cast(f32x4, a[0]) + __builtin_bit_cast(f32x4, a[1])) + (__builtin_bit_cast(f32x4, a[2]) + __builtin_bit_cast(f32x4, a[3]));
    *(LAS v2u*)op = v2u{cvtpk(o[0], o[1]), cvtpk(o[2], o[3])};
    BARRIER();
    STAMP(7);
    v4u bf[4];
    if (!wo_in) {
#pragma unroll
      for (int ks = 0; ks < 4; ++ks) af[ks] = tr_frag(ap + ks * 4096, 512);
    }
#pragma unroll
    for (int ks = 0; ks < 4; ++ks) bf[ks] = *(LAS v4u*)(bp + ks * 64);
    __builtin_amdgcn_sched_barrier(0);
    f32x4 y = {0.f, 0.f, 0.f, 0.f};
#pragma unroll
    for (int ks = 0; ks < 4; ++ks) y = mfma32(af[ks], bf[ks], y);
    __builtin_amdgcn_raw_buffer_store_b128(tag4(y, tv), rw, vy, sy, AUX_SC1);
    asm volatile("Lgqa_end_b:" ::: "memory");
    STAMP(8);
    STAMP_OUT();
    return;
  }

  __builtin_amdgcn_s_setprio(2);
  unsigned tag;
  {
    const int cb = j & 63, kq = j >> 6;
    const v4i rx = mkrsrc(x, NB * DM * 2), rq = mkrsrc(wq, (XF & 8) ? 0 : DM * HQ * DH * 2);
    const unsigned vx = (lane & 7) * 1024 + (lane >> 3) * 16, vq = (lane >> 3) * 8192 + (lane & 7) * 16;
    const bool hi = (lane & 8) != 0;
    const int vs = (lane & 7) * (HKV * 8192) + (hi ? 64 : 0) + g * 16, ss = W_QG + (cb >> 3) * 8192 + kq * 2048 + (cb & 7) * 256;
    if (WQT) while ((int)(now() - t0) < WQT) {}
    dma1(rx, vx, kq * 256, L_X);
    dma1(rx, vx, kq * 256 + 128, L_X + 1024);
#pragma unroll 1
    for (int u = 0; u < 16; ++u) dma1_nt(rq, vq, (kq * 16 + u) * 65536 + cb * 128, L_W + u * 1024);
    if (XF & 64) { unsigned pt = 0; if (lane < 16) pt = __builtin_amdgcn_raw_buffer_load_b32(rw, (lane & 7) * (HKV * 8192) + (lane >> 3) * 128, ss, 0); asm volatile("" :: "v"(pt)); }
    STAMP(5);
    tag = load_tag(ws);
    WAITVM(0);
    STAMP(10);
    f32x4 qa[4];
#pragma unroll
    for (int t = 0; t < 4; ++t) qa[t] = f32x4{0.f, 0.f, 0.f, 0.f};
    LAS unsigned char* ap = L + L_W + (8 * g + ((lane & 15) >> 2)) * 128 + j0 * 8;
    LAS unsigned char* bp = L + L_X + g * 128 + (lane & 7) * 16;
#pragma unroll
    for (int ks = 0; ks < 4; ++ks) {
      const v4u xb = *(LAS v4u*)(bp + ks * 512);
#pragma unroll
      for (int t = 0; t < 4; ++t) qa[t] = mfma32(tr_frag(ap + ks * 4096 + t * 32, 512), xb, qa[t]);
    }
    STAMP(1);
    const v4u tq = tag_bytes(tag);
    const v4u o0 = tag4(f32x4{hi ? qa[1][0] : qa[0][0], hi ? qa[1][1] : qa[0][1], hi ? qa[1][2] : qa[0][2], hi ? qa[1][3] : qa[0][3]}, tq);
    const v4u o1 = tag4(f32x4{hi ? qa[3][0] : qa[2][0], hi ? qa[3][1] : qa[2][1], hi ? qa[3][2] : qa[2][2], hi ? qa[3][3] : qa[2][3]}, tq);
    if (!(XF & 1)) {
      __builtin_amdgcn_raw_buffer_store_b128(o0, rw, vs, ss, AUX_SC1);
      __builtin_amdgcn_raw_buffer_store_b128(o1, rw, vs + 128, ss, AUX_SC1);
    }
    asm volatile("s_nop 1" :: "v"(o0), "v"(o1) : "memory");
    STAMP(2);
    while ((int)(now() - t0) < QPOLL) __builtin_amdgcn_s_sleep(2);
    const int so = W_QG + pr * 8192;
    v4u a[8];
    for (unsigned spin = 0; spin < SPIN_CAP; ++spin) {
      RELOAD();
#pragma unroll
      for (int u = 0; u < 8; ++u) a[u] = __builtin_amdgcn_raw_buffer_load_b128(rw, (XF & 2) ? 0x7000000 : vv + (u & 3) * 1024, so + (u >> 2) * 4096, AUX_SC1) ^ tq;
      if ((XF & 3) || __all((or4(((a[0] | a[1]) | (a[2] | a[3])) | ((a[4] | a[5]) | (a[6] | a[7]))) & 0xffu) == 0u)) break;
      STAMP(15);
    }
#pragma unroll
    for (int u = 0; u < 2; ++u) {
      const f32x4 v = (__builtin_bit_cast(f32x4, a[u]) + __builtin_bit_cast(f32x4, a[2 + u])) + (__builtin_bit_cast(f32x4, a[4 + u]) + __builtin_bit_cast(f32x4, a[6 + u]));
      *(LAS v2u*)(L + L_Q + u * 512 + lane * 8) = v2u{cvtpk(v[0], v[1]), cvtpk(v[2], v[3])};
    }
    RELOAD();
    *(LAS v2u*)(L + C_READY) = v2u{1u, tag};
  }
  STAMP(3);
  if ((XF & 32) && lane == 0) {
    const unsigned a = __builtin_nontemporal_load((const GAS unsigned*)(vc + pbase + so0));
    const unsigned b2 = __builtin_nontemporal_load((const GAS unsigned*)(wo + ch * 128 * 1024 + nb * 128));
    asm volatile("" :: "v"(a), "v"(b2));
  }
  {
    unsigned k = now() / FB_P + 1u;
    float af = 0.f, dr = 0.f;
    int nv = 0;
#pragma unroll 1
    for (int it = 0; it < 40; ++it, ++k) {
      const unsigned tk = k * FB_P;
      while ((int)(now() - tk) < 0) __builtin_amdgcn_s_sleep(2);
      RELOAD();
      const v2u pr2 = *(LAS v2u*)(L + C_LAND);
      if (__builtin_amdgcn_readfirstlane(pr2[0]) >= NUNIT - 12) break;
      const v2u fb = __builtin_amdgcn_raw_buffer_load_b64(rw, (lane & (FB_NREP - 1)) * 8, W_FB + ((k - 1u) & 63u) * FB_SLOT, AUX_SC1);
      if (j < FB_NREP && lane == 0) {
        const float p = (float)(8 * ((int)pr2[0] + LAG)) + fminf((float)(int)(tk - pr2[1]) * (1.f / 3.4f), 8.f);
        __builtin_amdgcn_raw_buffer_store_b64(v2u{__float_as_uint(p), tag ^ k}, rw, 0, W_FB + (k & 63u) * FB_SLOT + j * 8, AUX_SC1);
      }
      const bool ok = fb[1] == (tag ^ (k - 1u));
      float ps = ok ? __uint_as_float(fb[0]) : 0.f, pc = ok ? 1.f : 0.f;
      ps += dpp<0xB1>(ps); ps += dpp<0x4E>(ps); ps += dpp<0x124>(ps); ps += dpp<0x128>(ps); ps = xrow_sum(ps);
      pc += dpp<0xB1>(pc); pc += dpp<0x4E>(pc); pc += dpp<0x124>(pc); pc += dpp<0x128>(pc); pc = xrow_sum(pc);
      if (__builtin_amdgcn_readfirstlane(pc < 48.f)) continue;
      const float pbar = ps * __builtin_amdgcn_rcpf(pc);
      const float as = (float)(int)(tk - FB_P - t0) - pbar * TL0 - (float)(DELTA + PHASE / 2);
      if (nv == 0) af = as;
      else {
        const float er = as - af;
        if (nv >= FB_I0) dr = fminf(fmaxf(dr + FB_KI * fminf(er, 30.f), -6.f), 30.f);
        af += dr + FB_G * er;
      }
      ++nv;
      *(LAS unsigned*)(L + C_FREE + 4 + j0 * 8) = t0 + (unsigned)(int)af;
    }
  }
  v4u tb = tag_bytes(tag);
  GAS v2u* po = (GAS v2u*)(out + j * 32 + j0 * 8);
  {
    const v4i ro = mkrsrc(wo, HQ * DH * DM * 2);
    const unsigned vo = ((lane >> 5) + 8 * ((lane >> 3) & 3)) * 1024 + (lane & 7) * 16;
    while (lds_word(L + C_LAND) < WOPOS) __builtin_amdgcn_s_sleep(1);
#pragma unroll 1
    for (int u = 0; u < 16; ++u) dma1_nt(ro, vo, (ch * 128 + 32 * ((u >> 1) & 3) + 4 * (u >> 3) + 2 * (u & 1)) * 1024 + nb * 128, L_W + u * 1024);
    asm volatile("s_waitcnt vmcnt(0)" : "+v"(tb), "+v"(po) :: "memory");
    *(LAS int*)(L + C_WO) = 1;
    STAMP(11);
  }
  const unsigned tg2 = (unsigned)lds_word(L + C_FREE + 4) + (NUNIT * UC16 >> 4) + D1G + D2G;
  BARRIER();
  STAMP(7);
  {
    const unsigned tp = now() + D2;
    unsigned tc;
    do tc = now(); while ((int)(tc - tp) < 0 || ((int)(tc - tg2) < 0 && (int)(tc - tp) < 100));
  }
  {
    v4u a0, a1;
    for (unsigned spin = 0; spin < SPIN_CAP; ++spin) {
      RELOAD();
      a0 = __builtin_amdgcn_raw_buffer_load_b128(rw, vv, W_YB + j * 2048, AUX_SC1) ^ tb;
      a1 = __builtin_amdgcn_raw_buffer_load_b128(rw, vv + 1024, W_YB + j * 2048, AUX_SC1) ^ tb;
      if (__all((or4(a0 | a1) & 0xffu) == 0u)) break;
    }
    STAMP(8);
    f32x4 y = __builtin_bit_cast(f32x4, a0) + __builtin_bit_cast(f32x4, a1);
#pragma unroll
    for (int q = 0; q < 4; ++q) { float t = y[q]; t += dpp<0x124>(t); t += dpp<0x128>(t); y[q] = xrow_sum(t); }
    if (lane < 4) *po = v2u{cvtpk(y[0], y[1]), cvtpk(y[2], y[3])};
    if (j == 0 && lane == 0) __builtin_amdgcn_raw_buffer_store_b32(tag, rw, 0, W_E, AUX_SC1);
  }
  asm volatile("Lgqa_end_c:" ::: "memory");
  STAMP(9);
  STAMP_OUT();
}

extern "C" void kernel_launch(void* const* d_in, const int* in_sizes, int n_in, void* d_out, int out_size, void* d_ws, size_t ws_size,
                              hipStream_t stream) {
  gqa_fused<<<NWG, NTHR, 0, stream>>>((const unsigned char*)d_in[0], (const unsigned char*)d_in[1], (const unsigned char*)d_in[2], (const unsigned char*)d_in[3],
                                      (const unsigned char*)d_in[4], (unsigned char*)d_out, (unsigned char*)d_ws TUNE_PASS);
}
```

```cpp
#include <hip/hip_runtime.h>
#include <hip/hip_bf16.h>
#include <stdint.h>

#define GAS __attribute__((address_space(1)))
#define LAS __attribute__((address_space(3)))
typedef unsigned v4u __attribute__((ext_vector_type(4)));
typedef unsigned v2u __attribute__((ext_vector_type(2)));
typedef int v4i __attribute__((ext_vector_type(4)));
typedef float f32x4 __attribute__((ext_vector_type(4)));
typedef float f32x32 __attribute__((ext_vector_type(32)));
typedef short s16x4 __attribute__((ext_vector_type(4)));
typedef __bf16 bf16x8 __attribute__((ext_vector_type(8)));

constexpr int NB = 8, DM = 512, HQ = 32, HKV = 8, SK = 8192, DH = 128;
constexpr int NWG = 256, NTHR = 384;
constexpr int NUNIT = 128;
constexpr int SLOT = 8192;
constexpr float C2 = 0.088388347648318440f * 1.4426950408889634f;

#define NREG 4
#define NFIRST 4
#define WIN 24
#define UPREP 112
#define NFIN 3
#define MPOS 1
#define ROLL2 0
#define DRY 0
#define OUTAUX 0
#define FB_NREP 32
#define XF 1312
#define TUNE_ARG
#define TUNE_PASS
#define TL0 3.39f
#define UC16 ((unsigned)(TL0 * 128.f))
#define DELTA 4
#define HOLD 30
#define MINSP 20
#define LINE0 100
#define LATE 40
#define MINSP2 16
#define PHASE 28
#define FB_P 192u
#define FB_G 0.4f
#define FB_KI 0.f
#define FB_I0 3
#define WQT 0
#define WQSP 12
#define QPOLL 480
#define WOPOS 125
#define D1 30
#define D1G 138
#define GPOS 126
#define D2 40
constexpr unsigned SPIN_CAP = 1u << 16;
constexpr int LAG = (WIN + 7) / 8;

constexpr int L_Q = 16 * SLOT, L_ACC = L_Q + 1024, L_PL = L_ACC + 4 * 2048, L_DG = L_PL + 2048, L_OT = L_DG + 16, OT_ROW = 272, L_ML = L_OT + 8 * OT_ROW, L_CTL = (L_ML + 144 + 63) / 64 * 64;
constexpr int L_X = L_ACC, L_W = L_CTL + 64, L_TOT = L_W + 16384;
constexpr int C_FREE = L_CTL, C_LAND = L_CTL + 32, C_READY = L_CTL + 40, C_TAG = L_CTL + 44, C_NM = L_CTL + 48, C_NL = L_CTL + 52, C_NA = L_CTL + 56, C_WO = L_CTL + 60;
static_assert(L_CTL % 32 == 0 && L_TOT <= 160 * 1024, "lds map");
constexpr int W_E = 0, W_FB = 1024, FB_SLOT = 512, W_ML = W_FB + 64 * FB_SLOT + 1024, W_QG = W_ML + NWG * 128, W_PA = W_QG + NWG * 2048, PA_REC = 2048, W_YB = W_PA + NWG * PA_REC,
              W_END = W_YB + NWG * 2048;

#define STAMP(i)
#define STAMP_OUT()

#define STR_(x) #x
#define STRX_(x) STR_(x)
#define WAITVM(n) asm volatile("s_waitcnt vmcnt(" STR_(n) ")" ::: "memory")
#define BARRIER() asm volatile("s_waitcnt lgkmcnt(0)\n\ts_barrier" ::: "memory")
#define RELOAD() asm volatile("" ::: "memory")
constexpr int AUX_SC1 = 16;

__device__ __forceinline__ unsigned now() { return (unsigned)__builtin_amdgcn_s_memrealtime(); }
__device__ __forceinline__ unsigned cvtpk(float lo, float hi) {
  typedef __bf16 bf16x2_t __attribute__((ext_vector_type(2)));
  typedef float f32x2_t __attribute__((ext_vector_type(2)));
  return __builtin_bit_cast(unsigned, __builtin_convertvector(f32x2_t{lo, hi}, bf16x2_t));
}
template <int CTRL> __device__ __forceinline__ unsigned dppu(unsigned x) { return (unsigned)__builtin_amdgcn_mov_dpp((int)x, CTRL, 0xf, 0xf, true); }
template <int CTRL> __device__ __forceinline__ float dpp(float x) { return __uint_as_float(dppu<CTRL>(__float_as_uint(x))); }
__device__ __forceinline__ float xrow_max(float x) {
  auto s = __builtin_amdgcn_permlane16_swap(__float_as_uint(x), __float_as_uint(x), false, false);
  x = fmaxf(__uint_as_float(s[0]), __uint_as_float(s[1]));
  auto t = __builtin_amdgcn_permlane32_swap(__float_as_uint(x), __float_as_uint(x), false, false);
  return fmaxf(__uint_as_float(t[0]), __uint_as_float(t[1]));
}
__device__ __forceinline__ float xrow_sum(float x) {
  auto s = __builtin_amdgcn_permlane16_swap(__float_as_uint(x), __float_as_uint(x), false, false);
  x = __uint_as_float(s[0]) + __uint_as_float(s[1]);
  auto t = __builtin_amdgcn_permlane32_swap(__float_as_uint(x), __float_as_uint(x), false, false);
  return __uint_as_float(t[0]) + __uint_as_float(t[1]);
}
__device__ __forceinline__ v4i mkrsrc(const void* p, unsigned bytes) {
  const unsigned long long a = (unsigned long long)p;
  v4i r; r[0] = (int)(unsigned)a; r[1] = (int)((unsigned)(a >> 32) & 0xffffu); r[2] = (int)bytes; r[3] = 0x00020000; return r;
}
__device__ __forceinline__ int lds_word(LAS unsigned char* p) { RELOAD(); const int v = __builtin_amdgcn_readfirstlane(*(LAS int*)p); RELOAD(); return v; }
__device__ __forceinline__ void lds_inc(LAS unsigned char* p) {
  unsigned long long ex;
  asm volatile("s_mov_b64 %0, exec\n\ts_mov_b64 exec, 1\n\tds_add_u32 %1, %2\n\ts_mov_b64 exec, %0" : "=&s"(ex) : "v"((unsigned)(size_t)p), "v"(1u) : "memory");
}
#define DMA4(V, R, S) "buffer_load_dwordx4 " V ", " R ", " S " offen nt lds\n\tbuffer_load_dwordx4 " V ", " R ", " S " offen offset:1024 nt lds\n\t" \
                      "buffer_load_dwordx4 " V ", " R ", " S " offen offset:2048 nt lds\n\tbuffer_load_dwordx4 " V ", " R ", " S " offen offset:3072 nt lds\n\t"
__device__ __forceinline__ void issue_two(v4i r, unsigned vv, unsigned so, unsigned lds) {
  unsigned t;
  asm volatile("s_mov_b32 m0, %4\n\ts_add_u32 %0, %3, 0x1000\n\t" DMA4("%2", "%1", "%3") "s_add_u32 m0, m0, 0x1000\n\ts_nop 0\n\t" DMA4("%2", "%1", "%0")
               "s_add_u32 m0, m0, 0x1000\n\ts_add_u32 %0, %0, 0x1000\n\t" DMA4("%2", "%1", "%0") "s_add_u32 m0, m0, 0x1000\n\ts_add_u32 %0, %0, 0x1000\n\t" DMA4("%2", "%1", "%0")
               : "=&s"(t) : "s"(r), "v"(vv), "s"(so), "s"(lds) : "memory", "scc");
}
__device__ __forceinline__ void dma1_nt(v4i r, unsigned voff, unsigned soff, unsigned lds) {
  asm volatile("s_mov_b32 m0, %3\n\ts_nop 0\n\tbuffer_load_dwordx4 %1, %0, %2 offen nt lds" :: "s"(r), "v"(voff), "s"(soff), "s"(lds) : "memory");
}
__device__ __forceinline__ void dma1(v4i r, unsigned voff, unsigned soff, unsigned lds) {
  asm volatile("s_mov_b32 m0, %3\n\ts_nop 0\n\tbuffer_load_dwordx4 %1, %0, %2 offen lds" :: "s"(r), "v"(voff), "s"(soff), "s"(lds) : "memory");
}
struct Dbg { unsigned d0, d1; };
__device__ __forceinline__ void stream_units(v4i rs, unsigned vv, unsigned u, unsigned ue, unsigned so, unsigned ra, unsigned uc16, int hold, int msp, int lat, int ms2, Dbg& dbg, int lane) {
  unsigned ta, tb, st = 8192u, fo = ((u + 1u) & 3u) * 8u + C_FREE;
#define STAMP_ASM
#define STAMP_OPS
#define STAMP_INS
  asm volatile(
      "s_mov_b32 m0, %[m0i]\n\t"
      "v_mov_b32 v2, %[ld0]\n"
      "LT%=:\n\t"
      "s_waitcnt vmcnt(" STRX_(WIN) ")\n\t"
      DMA4("%[v0]", "%[rs]", "%[so]")
      "s_add_u32 m0, m0, 0x1000\n\t"
      "s_memrealtime s[40:41]\n\t"
      DMA4("%[v1]", "%[rs]", "%[so]")
      "ds_read_b64 v[4:5], %[fo]\n\t"
      "s_add_u32 m0, m0, 0x1000\n\t"
      "s_add_u32 %[so], %[so], %[st]\n\t"
      "s_and_b32 m0, m0, 0x1ffff\n\t"
      "s_and_b32 %[so], %[so], 0x1fffff\n\t"
      "s_xor_b32 %[st], %[st], 0xc000\n\t"
      "s_add_u32 %[ra], %[ra], %[uc]\n\t"
      "v_add_u32 v2, 1, v2\n\t"
      "v_add_u32 %[fo], 8, %[fo]\n\t"
      "s_add_u32 %[u], %[u], 1\n\t"
      "v_and_b32 %[fo], %[fm], %[fo]\n\t"
      "s_waitcnt lgkmcnt(0)\n\t"
      "v_mov_b32 v3, s40\n\t"
      "v_readfirstlane_b32 %[a], v5\n\t"
      "s_lshr_b32 s41, %[ra], 4\n\t"
      "v_readfirstlane_b32 %[b], v4\n\t"
      "ds_write_b64 %[vl], v[2:3]\n\t"
      STAMP_ASM
      "s_cmp_eq_u32 %[u], %[ue]\n\t"
      "s_cbranch_scc1 LE%=\n\t"
      "s_add_u32 %[a], %[a], s41\n\t"
      "s_sub_i32 %[a], %[a], s40\n\t"
      "s_cmp_lt_i32 %[a], %[lat]\n\t"
      "s_cselect_b32 s41, %[ms2], %[msp]\n\t"
      "s_min_i32 %[a], %[a], %[hold]\n\t"
      "s_max_i32 %[a], %[a], s41\n\t"
      "s_add_u32 %[a], %[a], s40\n\t"
      "s_cmp_lt_u32 %[b], %[u]\n\t"
      "s_cbranch_scc1 LS%=\n"
      "LW%=:\n\t"
      "s_memrealtime s[40:41]\n\t"
      "s_waitcnt lgkmcnt(0)\n\t"
      "s_sub_i32 %[b], %[a], s40\n\t"
      "s_cmp_gt_i32 %[b], 0\n\t"
      "s_cbranch_scc0 LT%=\n\t"
      "s_branch LW%=\n"
      "LS%=:\n\t"
      "s_and_b32 %[b], %[u], 3\n\t"
      "s_lshl_b32 %[b], %[b], 3\n\t"
      "s_add_u32 %[b], %[b], %[fb]\n\t"
      "v_mov_b32 v5, %[b]\n"
      "LR%=:\n\t"
      "s_sleep 1\n\t"
      "ds_read_b32 v4, v5\n\t"
      "s_waitcnt lgkmcnt(0)\n\t"
      "v_readfirstlane_b32 %[b], v4\n\t"
      "s_cmp_lt_u32 %[b], %[u]\n\t"
      "s_cbranch_scc1 LR%=\n\t"
      "s_branch LW%=\n"
      "LE%=:"
      : [so] "+s"(so), [ra] "+s"(ra), [u] "+s"(u), [st] "+s"(st), [fo] "+v"(fo), [a] "=&s"(ta), [b] "=&s"(tb) STAMP_OPS
      : [rs] "s"(rs), [v0] "v"(vv), [v1] "v"(vv + 4096u), [vl] "v"((unsigned)C_LAND), [ue] "s"(ue), [uc] "s"(uc16), [hold] "s"(hold), [msp] "s"(msp), [lat] "s"(lat), [ms2] "s"(ms2),
        [m0i] "s"((u & 15u) * SLOT), [ld0] "s"(u - 1u - LAG), [fm] "s"(~32u), [fb] "s"((unsigned)C_FREE) STAMP_INS
      : "memory", "scc", "vcc", "v2", "v3", "v4", "v5", "s40", "s41");
}
static_assert((C_FREE & 32) == 0, "the record pointer wraps by clearing bit 5");

__device__ __forceinline__ f32x4 mfma32(v4u a, v4u b, f32x4 c) {
  return __builtin_amdgcn_mfma_f32_16x16x32_bf16(__builtin_bit_cast(bf16x8, a), __builtin_bit_cast(bf16x8, b), c, 0, 0, 0);
}
__device__ __forceinline__ v4u tr_frag(LAS unsigned char* p, int hi) {
  const v2u lo = __builtin_bit_cast(v2u, __builtin_amdgcn_ds_read_tr16_b64_v4i16((LAS s16x4*)p));
  const v2u up = __builtin_bit_cast(v2u, __builtin_amdgcn_ds_read_tr16_b64_v4i16((LAS s16x4*)(p + hi)));
  return v4u{lo[0], lo[1], up[0], up[1]};
}
__device__ __forceinline__ float score16(const v4u* K, v4u qf, unsigned j0) {
  float sc[4];
#pragma unroll
  for (int u = 0; u < 4; ++u) {
    const f32x4 d = mfma32(K[u], qf, f32x4{0.f, 0.f, 0.f, 0.f});
    sc[u] = ((dpp<0x00>(d[0]) + dpp<0x55>(d[1])) + dpp<0xAA>(d[2])) + dpp<0xFF>(d[3]);
  }
  return (j0 & 2) ? ((j0 & 1) ? sc[3] : sc[2]) : ((j0 & 1) ? sc[1] : sc[0]);
}
__device__ __forceinline__ void p_operands(unsigned k0, unsigned k1, unsigned mask0, unsigned mask1, v4u (&bo)[4]) {
  bo[0] = v4u{dppu<0x00>(k0) & mask0, dppu<0x00>(k0) & mask1, dppu<0x55>(k0) & mask0, dppu<0x55>(k0) & mask1};
  bo[1] = v4u{dppu<0xAA>(k0) & mask0, dppu<0xAA>(k0) & mask1, dppu<0xFF>(k0) & mask0, dppu<0xFF>(k0) & mask1};
  bo[2] = v4u{dppu<0x00>(k1) & mask0, dppu<0x00>(k1) & mask1, dppu<0x55>(k1) & mask0, dppu<0x55>(k1) & mask1};
  bo[3] = v4u{dppu<0xAA>(k1) & mask0, dppu<0xAA>(k1) & mask1, dppu<0xFF>(k1) & mask0, dppu<0xFF>(k1) & mask1};
  asm volatile("" : "+v"(bo[0]), "+v"(bo[1]), "+v"(bo[2]), "+v"(bo[3]));
}
#define RD2(A, P, O0, O1) "ds_read2st64_b64 %[" A "], %[" P "] offset0:" O0 " offset1:" O1 "\n\t"
#define MF(N, C, A, B) "s_waitcnt lgkmcnt(" N ")\n\tv_mfma_f32_16x16x32_bf16 %[" C "], %[" A "], %[" B "], %[" C "]\n\t"
template <bool PAD> __device__ __forceinline__ void pv32(unsigned p, const v4u (&bo)[4], f32x4& acc0, f32x4& acc1) {
  v4u a0, a1, a2, a3, a4, a5, a6, a7;
  asm volatile(RD2("a0", "p0", "0", "2") RD2("a1", "p1", "0", "2") RD2("a2", "p0", "4", "6") RD2("a3", "p1", "4", "6")
               RD2("a4", "p0", "8", "10") RD2("a5", "p1", "8", "10") RD2("a6", "p0", "12", "14") RD2("a7", "p1", "12", "14")
               MF("7", "c0", "a0", "b0") MF("6", "c1", "a1", "b0") MF("5", "c0", "a2", "b1") MF("4", "c1", "a3", "b1")
               MF("3", "c0", "a4", "b2") MF("2", "c1", "a5", "b2") MF("1", "c0", "a6", "b3") MF("0", "c1", "a7", "b3") "s_nop %[pad]"
      : [c0] "+v"(acc0), [c1] "+v"(acc1), [a0] "=&v"(a0), [a1] "=&v"(a1), [a2] "=&v"(a2), [a3] "=&v"(a3), [a4] "=&v"(a4), [a5] "=&v"(a5), [a6] "=&v"(a6), [a7] "=&v"(a7)
      : [p0] "v"(p), [p1] "v"(p + 8), [b0] "v"(bo[0]), [b1] "v"(bo[1]), [b2] "v"(bo[2]), [b3] "v"(bo[3]), [pad] "i"(PAD ? 7 : 0) : "memory");
}
__device__ __forceinline__ v4u tag_bytes(unsigned tag) { return v4u{tag & 0xffu, (tag >> 8) & 0xffu, (tag >> 16) & 0xffu, tag >> 24}; }
__device__ __forceinline__ v4u tag4(f32x4 v, v4u tb) { return (__builtin_bit_cast(v4u, v) & 0xffffff00u) | tb; }
__device__ __forceinline__ unsigned or4(v4u u) { return (u[0] | u[1]) | (u[2] | u[3]); }

__global__ void __launch_bounds__(NTHR, 1) gqa_fused(const unsigned char* __restrict__ x, const unsigned char* __restrict__ kc, const unsigned char* __restrict__ vc,
                                                     const unsigned char* __restrict__ wq, const unsigned char* __restrict__ wo, unsigned char* __restrict__ out,
                                                     unsigned char* ws TUNE_ARG) {
  __shared__ __attribute__((aligned(16))) unsigned char smem[L_TOT];
  LAS unsigned char* L = (LAS unsigned char*)smem;
  unsigned long long t64;
  unsigned pf, pa;
  unsigned long long pc;
  unsigned clen;
  {
    unsigned long long ex;
    asm volatile("s_memrealtime %0\n\ts_getpc_b64 %1\nLgqa_pc:\n\t"
                 "s_mov_b32 %2, Lgqa_end_a-Lgqa_pc+128\n\ts_max_u32 %2, %2, Lgqa_end_b-Lgqa_pc+128\n\ts_max_u32 %2, %2, Lgqa_end_c-Lgqa_pc+128\n\t"
                 "v_lshlrev_b32 %5, 7, %6\n\tv_add_u32 %5, 0xffffc000, %5\n\tv_cmp_gt_u32 vcc, %2, %5\n\t"
                 "s_and_saveexec_b64 %3, vcc\n\ts_cbranch_execz Lgqa_nopf\n\tglobal_load_dword %4, %5, %1 offset:-32\nLgqa_nopf:\n\ts_mov_b64 exec, %3"
                 : "=&s"(t64), "=&s"(pc), "=&s"(clen), "=&s"(ex), "=&v"(pf), "=&v"(pa) : "v"(threadIdx.x) : "scc", "vcc", "memory");
  }
  const int lane = threadIdx.x & 63, wv = __builtin_amdgcn_readfirstlane(threadIdx.x >> 6);
  const int j = blockIdx.x, xg = j & 7, idx = j >> 3;
  const int yy = (xg - idx) & 7, pr = (XF & 16) ? (idx >> 2) * 8 + xg : (XF & 128) ? xg * 8 + (idx >> 2) : (XF & 256) ? (idx >> 2) * 8 + ((xg + (idx >> 2)) & 7) : 2 * idx + (yy >> 2), s = (XF & (16 | 128 | 256)) ? (idx & 3) : yy & 3;
  const int ch = 4 * xg + (idx >> 3), nb = idx & 7;
  const int g = lane >> 4;
  const unsigned so0 = (unsigned)(pr >> 1) * 65536u + s * 16384u;
  const unsigned vv = lane * 16;
  const unsigned ra0 = ((unsigned)(j * 37) & 31u) * PHASE >> 1;
  asm volatile("s_waitcnt lgkmcnt(0)" : "+s"(t64) : "s"(x), "s"(kc), "s"(vc), "s"(wq), "s"(wo), "s"(out), "s"(ws), "s"(so0), "v"(vv));
  const unsigned t0 = (unsigned)t64;
  const size_t pbase = (size_t)pr * (SK * DH * 2);
  Dbg dbg = {0u, 0u};

  const v4i rk = mkrsrc(kc + pbase, SK * DH * 2);
  if (wv == 0) {
    __builtin_amdgcn_s_setprio(3);
#pragma unroll
    for (int u = 0; u < NFIRST; u += 2) issue_two(rk, vv, (so0 + (u >> 1) * 65536u) & 0x1fffffu, u * SLOT);
    __builtin_amdgcn_sched_barrier(0);
    const unsigned keep = (unsigned)((lane & 7) - 4) >> 31;
    *(LAS v2u*)(L + L_CTL + (lane & 7) * 8) = v2u{(12u + (lane & 7)) * keep, (t0 + LINE0) * keep};
    STAMP(1);
    asm volatile("s_waitcnt lgkmcnt(0)" ::: "memory");
  }
  unsigned e0, e1, e2;
  if (wv == 1) asm volatile("s_load_dword %0, %3, 0x0\n\ts_load_dword %1, %4, 0x0\n\ts_load_dword %2, %5, %6"
                            : "=&s"(e0), "=&s"(e1), "=&s"(e2) : "s"(ws), "s"((XF & 1024) ? x : ws), "s"((XF & 1024) ? wq : ws), "s"((XF & 1024) ? (j >> 6) * (128 * 8192) + (j & 63) * 128 : 0) : "memory");
  asm volatile("s_barrier" ::: "memory");
  if (wv == 0) stream_units(rk, vv, NFIRST, 64u, (so0 + (NFIRST >> 1) * 65536u) & 0x1fffffu, NFIRST * UC16 + ra0, UC16, HOLD, MINSP, -LATE, MINSP2, dbg, lane);
  const __amdgpu_buffer_rsrc_t rw = __builtin_amdgcn_make_buffer_rsrc((void*)ws, 0, W_END, 0x00020000);
  const unsigned j0 = lane & 3;
  const int h = (lane >> 2) & 3;
  const unsigned mask0 = j0 == 0 ? 0x0000ffffu : (j0 == 1 ? 0xffff0000u : 0u);
  const unsigned mask1 = j0 == 2 ? 0x0000ffffu : (j0 == 3 ? 0xffff0000u : 0u);

  if (wv == 0) {
    const v4i rv = mkrsrc(vc + pbase, SK * DH * 2);
    while (lds_word(L + C_FREE) < 64) __builtin_amdgcn_s_sleep(1);
    stream_units(rv, vv, 64u, UPREP, so0, 64u * UC16 + ra0, UC16, HOLD, MINSP, -LATE, MINSP2, dbg, lane);
    while (lds_word(L + C_FREE + 24) < 64 + 4 * 8 + 3 + 16) __builtin_amdgcn_s_sleep(1);
    v4u bo[NFIN][4], tb;
    unsigned vo = h * 512 + g * 64 + j0 * 16;
    float scale;
    {
      v2u pl[NFIN];
#pragma unroll
      for (int k = 0; k < NFIN; ++k) pl[k] = *(LAS v2u*)(L + L_PL + (4 - NFIN + k) * 512 + lane * 8);
      tb = tag_bytes(*(LAS unsigned*)(L + C_TAG));
      scale = *(LAS float*)(L + L_ML + 128 + h * 4);
      const int fr = *(LAS int*)(L + C_FREE + (UPREP & 3) * 8);
#pragma unroll
      for (int k = 0; k < NFIN; ++k) p_operands(pl[k][0], pl[k][1], mask0, mask1, bo[k]);
      asm volatile("" : "+v"(vo), "+v"(tb), "+v"(scale));
      if (__builtin_amdgcn_readfirstlane(fr) < UPREP) { while (lds_word(L + C_FREE + (UPREP & 3) * 8) < UPREP) __builtin_amdgcn_s_sleep(1); }
    }
    stream_units(rv, vv, UPREP, NUNIT, (so0 + ((UPREP - 64) >> 1) * 65536u) & 0x1fffffu, UPREP * UC16 + ra0, UC16, HOLD, MINSP, -LATE, MINSP2, dbg, lane);
    STAMP(2);
    const int so = W_PA + (pr * 4 + s) * PA_REC;
    f32x4 acc0 = {0.f, 0.f, 0.f, 0.f}, acc1 = {0.f, 0.f, 0.f, 0.f};
    static_assert(NFIN >= 2 && LAG <= 3, "the drain below starts at three units outstanding");
    f32x4 s0, s1;
#pragma unroll
    for (int k = 0; k < 3; ++k) {
      if (k == 0) WAITVM(24); else if (k == 1) WAITVM(16); else WAITVM(8);
      if (k < 2) *(LAS int*)(L + C_LAND) = NUNIT - 3 + k;
      if (k >= 4 - NFIN) pv32<false>((12 + k) * SLOT + vv, bo[k >= 4 - NFIN ? k - (4 - NFIN) : 0], acc0, acc1);
      if (k == MPOS) {
        while (lds_word(L + C_NA) < 4) {}
        f32x4 a[8];
#pragma unroll
        for (int u = 0; u < 8; ++u) a[u] = *(LAS f32x4*)(L + L_ACC + vv + u * 1024);
        __builtin_amdgcn_sched_barrier(0);
        s0 = ((a[0] + a[2]) + (a[4] + a[6])) * scale; s1 = ((a[1] + a[3]) + (a[5] + a[7])) * scale;
        asm volatile("" : "+v"(s0), "+v"(s1) : "v"(14 * SLOT + vv), "v"(14 * SLOT + vv + 8), "v"(15 * SLOT + vv), "v"(15 * SLOT + vv + 8), "s"(so), "s"(0xffffff00u));
      }
    }
    STAMP(4);
    WAITVM(0);
    STAMP(3);
    pv32<true>(15 * SLOT + vv, bo[NFIN - 1], acc0, acc1);
    const v4u o0 = tag4(acc0 * scale + s0, tb), o1 = tag4(acc1 * scale + s1, tb);
    __builtin_amdgcn_raw_buffer_store_b128(o0, rw, vo, so, AUX_SC1);
    __builtin_amdgcn_raw_buffer_store_b128(o1, rw, vo + 256, so, AUX_SC1);
    asm volatile("s_nop 1" :: "v"(o0), "v"(o1) : "memory");
    *(LAS int*)(L + C_LAND) = NUNIT;
    asm volatile("Lgqa_end_a:" ::: "memory");
    STAMP(5);
    STAMP_OUT();
    return;
  }

  if (wv >= 2) {
    const int w = wv - 2;
    const unsigned vk = (h * 16 + g * 4 + j0) * 16;
    asm volatile("s_waitcnt vmcnt(0)" :: "v"(pf), "v"(pa) : "memory");
    f32x32 sc;
    float m;
    {
      v4u P[NREG][8];
#pragma unroll
      for (int r = 0; r < NREG; ++r) {
        while (lds_word(L + C_LAND) <= 4 * r + w) __builtin_amdgcn_s_sleep(2);
#pragma unroll
        for (int u = 0; u < 8; ++u) P[r][u] = *(LAS v4u*)(L + ((4 * r + w) & 15) * SLOT + vk + u * 1024);
        asm volatile("" :: "v"(P[r][0]), "v"(P[r][1]), "v"(P[r][2]), "v"(P[r][3]), "v"(P[r][4]), "v"(P[r][5]), "v"(P[r][6]), "v"(P[r][7]) : "memory");
        *(LAS int*)(L + C_FREE + w * 8) = 4 * r + w + 16;
      }
      STAMP(1);
      while (lds_word(L + C_READY) == 0) __builtin_amdgcn_s_sleep(2);
      STAMP(2);
      const v4u qf = *(LAS v4u*)(L + L_Q + vk);
      m = -INFINITY;
#pragma unroll
      for (int r = 0; r < NREG; ++r) {
        const float s0 = score16(&P[r][0], qf, j0), s1 = score16(&P[r][4], qf, j0);
        sc[2 * r] = s0; sc[2 * r + 1] = s1; m = fmaxf(m, fmaxf(s0, s1));
      }
#pragma unroll 1
      for (int n = NREG; n < 16; ++n) {
        while (lds_word(L + C_LAND) <= 4 * n + w) __builtin_amdgcn_s_sleep(2);
        v4u K[8];
#pragma unroll
        for (int u = 0; u < 8; ++u) K[u] = *(LAS v4u*)(L + ((4 * n + w) & 15) * SLOT + vk + u * 1024);
        asm volatile("" :: "v"(K[0]), "v"(K[1]), "v"(K[2]), "v"(K[3]), "v"(K[4]), "v"(K[5]), "v"(K[6]), "v"(K[7]) : "memory");
        *(LAS int*)(L + C_FREE + w * 8) = 4 * n + w + 16;
        const float s0 = score16(&K[0], qf, j0), s1 = score16(&K[4], qf, j0);
        sc[2 * n] = s0; sc[2 * n + 1] = s1; m = fmaxf(m, fmaxf(s0, s1));
      }
    }
    STAMP(10);
    const v4u tb = tag_bytes((unsigned)lds_word(L + C_TAG));
    m = fmaxf(m, dpp<0xB1>(m)); m = fmaxf(m, dpp<0x4E>(m)); m = xrow_max(m);
    *(LAS float*)(L + L_ML + h * 16 + w * 4) = m;
    lds_inc(L + C_NM);
    while (lds_word(L + C_NM) < 4) __builtin_amdgcn_s_sleep(1);
    {
      const f32x4 mw = *(LAS f32x4*)(L + L_ML + h * 16);
      m = __uint_as_float(__float_as_uint(fmaxf(fmaxf(mw[0], mw[1]), fmaxf(mw[2], mw[3]))) & 0xffffff00u);
      const float mc = -m * C2;
      float l = 0.f;
#pragma unroll
      for (int n = 0; n < 32; ++n) { const float p = __builtin_amdgcn_exp2f(fmaf(sc[n], C2, mc)); l += p; sc[n] = __uint_as_float(cvtpk(p, p)); }
      l += dpp<0xB1>(l); l += dpp<0x4E>(l); l = xrow_sum(l);
      *(LAS float*)(L + L_ML + 64 + h * 16 + w * 4) = l;
      *(LAS v2u*)(L + L_PL + w * 512 + lane * 8) = v2u{__float_as_uint(sc[30]), __float_as_uint(sc[31])};
      lds_inc(L + C_NL);
    }
    STAMP(11);
    f32x4 acc0 = {0.f, 0.f, 0.f, 0.f}, acc1 = {0.f, 0.f, 0.f, 0.f};
#pragma unroll 1
    for (int n = 0; n < (w < 4 - NFIN ? 16 : 15); ++n) {
      if (w == 3 && n == 1) {
        while (lds_word(L + C_NL) < 4) __builtin_amdgcn_s_sleep(1);
        const f32x4 lw = *(LAS f32x4*)(L + L_ML + 64 + h * 16);
        if (lane < 16 && j0 == 0) __builtin_amdgcn_raw_buffer_store_b128(tag4(f32x4{m, (lw[0] + lw[1]) + (lw[2] + lw[3]), 0.f, 0.f}, tb), rw, h * 16, W_ML + (pr * 4 + s) * 128, AUX_SC1);
      }
      if (w == 3 && n == 8) {
        v4u ml;
        for (unsigned spin = 0; spin < SPIN_CAP; ++spin) {
          RELOAD();
          ml = __builtin_amdgcn_raw_buffer_load_b128(rw, j0 * 128 + h * 16, W_ML + pr * 512, AUX_SC1) ^ tb;
          if (__all((or4(ml) & 0xffu) == 0u)) break;
        }
        const float ms = __uint_as_float(ml[0]);
        float M = fmaxf(ms, dpp<0xB1>(ms)); M = fmaxf(M, dpp<0x4E>(M));
        float ls = __uint_as_float(ml[1]) * __builtin_amdgcn_exp2f((ms - M) * C2);
        ls += dpp<0xB1>(ls); ls += dpp<0x4E>(ls);
        if (lane < 16 && j0 == 0) *(LAS float*)(L + L_ML + 128 + h * 4) = __builtin_amdgcn_exp2f((m - M) * C2) * __builtin_amdgcn_rcpf(ls);
      }
      v4u bo[4];
      p_operands(__float_as_uint(sc[2 * n]), __float_as_uint(sc[2 * n + 1]), mask0, mask1, bo);
      while (lds_word(L + C_LAND) <= 64 + 4 * n + w) __builtin_amdgcn_s_sleep(1);
      pv32<true>(((4 * n + w) & 15) * SLOT + vv, bo, acc0, acc1);
      *(LAS int*)(L + C_FREE + w * 8) = 64 + 4 * n + w + 16;
    }
    *(LAS f32x4*)(L + L_ACC + w * 2048 + vv) = acc0; *(LAS f32x4*)(L + L_ACC + w * 2048 + 1024 + vv) = acc1;
    lds_inc(L + C_NA);
    STAMP(3);
    const int bb = 2 * w + (lane >> 5), c = lane & 31;
    unsigned vg = bb * (HKV * 4 * PA_REC) + c * 16, vy = (lane & 7) * (32 * 2048) + g * 16;
    const int sg = W_PA + (ch >> 2) * (4 * PA_REC) + (ch & 3) * 512, sy = W_YB + (nb * 4 + w) * 2048 + ch * 64;
    LAS unsigned char* ap = L + L_W + (8 * g + ((lane & 15) >> 2)) * 128 + (16 * w + 4 * j0) * 2;
    LAS unsigned char* op = L + L_OT + bb * OT_ROW + c * 8;
    LAS unsigned char* bp = L + L_OT + (lane & 7) * OT_ROW + g * 16;
    if (lane & 8) vy = 0x40000000u;
    v4u tv = tb;
    asm volatile("" : "+v"(vg), "+v"(vy), "+v"(ap), "+v"(op), "+v"(bp), "+v"(tv) : "s"(sg), "s"(sy));
#pragma unroll 1
    for (int pass = (DRY & 1) ? 0 : 1; pass < 2; ++pass) {
      unsigned cm = pass ? 0xffu : 0u;
      v4u a[4], af[4];
      asm volatile("" : "+s"(cm), "=v"(a[0]), "=v"(a[1]), "=v"(a[2]), "=v"(a[3]));
      if (pass) {
        while (lds_word(L + C_LAND) < GPOS) __builtin_amdgcn_s_sleep(1);
        STAMP(5);
        const unsigned tp = now() + D1;
        unsigned tg = (unsigned)lds_word(L + C_FREE + 4) + (NUNIT * UC16 >> 4) + D1G;
        tg = tp + (unsigned)min(max((int)(tg - tp), 0), 200);
        while ((int)(now() - tg) < 0) {}
        STAMP(4);
#pragma unroll
        for (int u = 0; u < 4; ++u) a[u] = __builtin_amdgcn_raw_buffer_load_b128(rw, vg, sg + u * PA_REC, AUX_SC1);
      }
      while (lds_word(L + C_WO) < pass) {}
#pragma unroll
      for (int ks = 0; ks < 4; ++ks) af[ks] = tr_frag(ap + ks * 4096, 512);
      asm volatile("" : "+v"(af[0]), "+v"(af[1]), "+v"(af[2]), "+v"(af[3]));
#pragma unroll
      for (int u = 0; u < 4; ++u) a[u] ^= tv;
      asm volatile("" : "+v"(a[0]), "+v"(a[1]), "+v"(a[2]), "+v"(a[3]));
      if (__builtin_expect(!__all((or4((a[0] | a[1]) | (a[2] | a[3])) & cm) == 0u), 0)) {
        for (unsigned spin = 0; spin < SPIN_CAP; ++spin) {
          RELOAD();
#pragma unroll
          for (int u = 0; u < 4; ++u) a[u] = __builtin_amdgcn_raw_buffer_load_b128(rw, vg, sg + u * PA_REC, AUX_SC1) ^ tv;
          if (__all((or4((a[0] | a[1]) | (a[2] | a[3])) & 0xffu) == 0u)) break;
        }
      }
      STAMP(6);
      const f32x4 o = (__builtin_bit_cast(f32x4, a[0]) + __builtin_bit_cast(f32x4, a[1])) + (__builtin_bit_cast(f32x4, a[2]) + __builtin_bit_cast(f32x4, a[3]));
      *(LAS v2u*)op = v2u{cvtpk(o[0], o[1]), cvtpk(o[2], o[3])};
      asm volatile("s_cmp_eq_u32 %0, 0\n\ts_waitcnt lgkmcnt(0)\n\ts_cbranch_scc1 LB%=\n\ts_barrier\nLB%=:" :: "s"(pass) : "memory", "scc");
      STAMP(7);
      v4u bf[4];
#pragma unroll
      for (int ks = 0; ks < 4; ++ks) bf[ks] = *(LAS v4u*)(bp + ks * 64);
      __builtin_amdgcn_sched_barrier(0);
      f32x4 y = {0.f, 0.f, 0.f, 0.f};
#pragma unroll
      for (int ks = 0; ks < 4; ++ks) y = mfma32(af[ks], bf[ks], y);
      const v4u yt = tag4(y, tv);
      asm volatile("" :: "v"(yt));
      if (pass) __builtin_amdgcn_raw_buffer_store_b128(yt, rw, vy, sy, AUX_SC1);
    }
    asm volatile("Lgqa_end_b:" ::: "memory");
    STAMP(8);
    STAMP_OUT();
    return;
  }

  __builtin_amdgcn_s_setprio(2);
  unsigned tag;
  {
    const int cb = j & 63, kq = j >> 6;
    const v4i rx = mkrsrc(x, NB * DM * 2), rq = mkrsrc(wq, (XF & 8) ? 0 : DM * HQ * DH * 2);
    const unsigned vx = (lane & 7) * 1024 + (lane >> 3) * 16, vq = (lane >> 3) * 8192 + (lane & 7) * 16;
    const bool hi = (lane & 8) != 0;
    const int vs = (lane & 7) * (HKV * 8192) + (hi ? 64 : 0) + g * 16, ss = W_QG + (cb >> 3) * 8192 + kq * 2048 + (cb & 7) * 256;
    if (WQT) while ((int)(now() - t0) < WQT) {}
    dma1(rx, vx, kq * 256, L_X);
    dma1(rx, vx, kq * 256 + 128, L_X + 1024);
#pragma unroll 1
    for (int u = 0; u < 16; ++u) dma1_nt(rq, vq, (kq * 16 + u) * 65536 + cb * 128, L_W + u * 1024);
    if (XF & 64) { unsigned pt = 0; if (lane < 16) pt = __builtin_amdgcn_raw_buffer_load_b32(rw, (lane & 7) * (HKV * 8192) + (lane >> 3) * 128, ss, 0); asm volatile("" :: "v"(pt)); }
    STAMP(5);
    asm volatile("s_waitcnt vmcnt(0) lgkmcnt(0)" : "+s"(e0) : "s"(e1), "s"(e2) : "memory");
    tag = ((e0 & 0x7f7f7f7fu) + 0x51276b35u) ^ (e0 & 0x80808080u);
    STAMP(10);
    f32x4 qa[4];
#pragma unroll
    for (int t = 0; t < 4; ++t) qa[t] = f32x4{0.f, 0.f, 0.f, 0.f};
    LAS unsigned char* ap = L + L_W + (8 * g + ((lane & 15) >> 2)) * 128 + j0 * 8;
    LAS unsigned char* bp = L + L_X + g * 128 + (lane & 7) * 16;
#pragma unroll
    for (int ks = 0; ks < 4; ++ks) {
      const v4u xb = *(LAS v4u*)(bp + ks * 512);
#pragma unroll
      for (int t = 0; t < 4; ++t) qa[t] = mfma32(tr_frag(ap + ks * 4096 + t * 32, 512), xb, qa[t]);
    }
    STAMP(1);
    const v4u tq = tag_bytes(tag);
    const v4u o0 = tag4(f32x4{hi ? qa[1][0] : qa[0][0], hi ? qa[1][1] : qa[0][1], hi ? qa[1][2] : qa[0][2], hi ? qa[1][3] : qa[0][3]}, tq);
    const v4u o1 = tag4(f32x4{hi ? qa[3][0] : qa[2][0], hi ? qa[3][1] : qa[2][1], hi ? qa[3][2] : qa[2][2], hi ? qa[3][3] : qa[2][3]}, tq);
    if (!(XF & 1)) {
      __builtin_amdgcn_raw_buffer_store_b128(o0, rw, vs, ss, AUX_SC1);
      __builtin_amdgcn_raw_buffer_store_b128(o1, rw, vs + 128, ss, AUX_SC1);
    }
    asm volatile("s_nop 1" :: "v"(o0), "v"(o1) : "memory");
    STAMP(2);
    while ((int)(now() - t0) < QPOLL) __builtin_amdgcn_s_sleep(2);
    const int so = W_QG + pr * 8192;
    v4u a[8];
    for (unsigned spin = 0; spin < SPIN_CAP; ++spin) {
      RELOAD();
#pragma unroll
      for (int u = 0; u < 8; ++u) a[u] = __builtin_amdgcn_raw_buffer_load_b128(rw, (XF & 2) ? 0x7000000 : vv + (u & 3) * 1024, so + (u >> 2) * 4096, AUX_SC1) ^ tq;
      if ((XF & 3) || __all((or4(((a[0] | a[1]) | (a[2] | a[3])) | ((a[4] | a[5]) | (a[6] | a[7]))) & 0xffu) == 0u)) break;
      STAMP(15);
    }
#pragma unroll
    for (int u = 0; u < 2; ++u) {
      const f32x4 v = (__builtin_bit_cast(f32x4, a[u]) + __builtin_bit_cast(f32x4, a[2 + u])) + (__builtin_bit_cast(f32x4, a[4 + u]) + __builtin_bit_cast(f32x4, a[6 + u]));
      *(LAS v2u*)(L + L_Q + u * 512 + lane * 8) = v2u{cvtpk(v[0], v[1]), cvtpk(v[2], v[3])};
    }
    RELOAD();
    *(LAS v2u*)(L + C_READY) = v2u{1u, tag};
  }
  STAMP(3);
  if ((XF & 32) && lane == 0) {
    const unsigned a = __builtin_nontemporal_load((const GAS unsigned*)(vc + pbase + so0));
    const unsigned b2 = __builtin_nontemporal_load((const GAS unsigned*)(wo + ch * 128 * 1024 + nb * 128));
    const unsigned c = (XF & 2048) ? __builtin_nontemporal_load((const GAS unsigned*)(out + j * 32)) : 0u;
    asm volatile("" :: "v"(a), "v"(b2), "v"(c));
  }
  {
    unsigned k = now() / FB_P + 1u;
    float af = 0.f, dr = 0.f;
    int nv = 0;
#pragma unroll 1
    for (int it = 0; it < 40; ++it, ++k) {
      const unsigned tk = k * FB_P;
      while ((int)(now() - tk) < 0) __builtin_amdgcn_s_sleep(2);
      RELOAD();
      const v2u pr2 = *(LAS v2u*)(L + C_LAND);
      if (__builtin_amdgcn_readfirstlane(pr2[0]) >= NUNIT - 12) break;
      const v2u fb = __builtin_amdgcn_raw_buffer_load_b64(rw, (lane & (FB_NREP - 1)) * 8, W_FB + ((k - 1u) & 63u) * FB_SLOT, AUX_SC1);
      if (j < FB_NREP && lane == 0) {
        const float p = (float)(8 * ((int)pr2[0] + LAG)) + fminf((float)(int)(tk - pr2[1]) * (1.f / 3.4f), 8.f);
        __builtin_amdgcn_raw_buffer_store_b64(v2u{__float_as_uint(p), tag ^ k}, rw, 0, W_FB + (k & 63u) * FB_SLOT + j * 8, AUX_SC1);
      }
      const bool ok = fb[1] == (tag ^ (k - 1u));
      float ps = ok ? __uint_as_float(fb[0]) : 0.f, pc = ok ? 1.f : 0.f;
      ps += dpp<0xB1>(ps); ps += dpp<0x4E>(ps); ps += dpp<0x124>(ps); ps += dpp<0x128>(ps); ps = xrow_sum(ps);
      pc += dpp<0xB1>(pc); pc += dpp<0x4E>(pc); pc += dpp<0x124>(pc); pc += dpp<0x128>(pc); pc = xrow_sum(pc);
      if (__builtin_amdgcn_readfirstlane(pc < 48.f)) continue;
      const float pbar = ps * __builtin_amdgcn_rcpf(pc);
      const float as = (float)(int)(tk - FB_P - t0) - pbar * TL0 - (float)(DELTA + PHASE / 2);
      if (nv == 0) af = as;
      else {
        const float er = as - af;
        if (nv >= FB_I0) dr = fminf(fmaxf(dr + FB_KI * fminf(er, 30.f), -6.f), 30.f);
        af += dr + FB_G * er;
      }
      ++nv;
      *(LAS unsigned*)(L + C_FREE + 4 + j0 * 8) = t0 + (unsigned)(int)af;
    }
  }
  v4u tb = tag_bytes(tag);
  const __amdgpu_buffer_rsrc_t rout = __builtin_amdgcn_make_buffer_rsrc((void*)out, 0, NB * DM * 2, 0x00020000);
#pragma unroll 1
  for (int pass = (DRY & 2) ? 0 : 1; pass < 2; ++pass) {
    unsigned vl = pass ? vv : 0x40000000u, vs = (pass && (lane & 28) == 0) ? j0 * 8 + (lane >> 5) * 4 : 0x40000000u;
    unsigned cm = pass ? 0xffu : 0u;
    asm volatile("" : "+v"(vl), "+v"(vs), "+v"(tb), "+s"(cm));
    if (pass) {
      const v4i ro = mkrsrc(wo, HQ * DH * DM * 2);
      const unsigned vo = ((lane >> 5) + 8 * ((lane >> 3) & 3)) * 1024 + (lane & 7) * 16;
      while (lds_word(L + C_LAND) < WOPOS) __builtin_amdgcn_s_sleep(1);
#pragma unroll 1
      for (int u = 0; u < 16; ++u) dma1_nt(ro, vo, (ch * 128 + 32 * ((u >> 1) & 3) + 4 * (u >> 3) + 2 * (u & 1)) * 1024 + nb * 128, L_W + u * 1024);
      WAITVM(0);
      *(LAS int*)(L + C_WO) = 1;
      STAMP(11);
      BARRIER();
      STAMP(7);
      const unsigned tp = now() + D2;
      while ((int)(now() - tp) < 0) {}
    }
    v4u a0, a1;
    for (unsigned spin = 0; spin < SPIN_CAP; ++spin) {
      RELOAD();
      a0 = __builtin_amdgcn_raw_buffer_load_b128(rw, vl, W_YB + j * 2048, AUX_SC1) ^ tb;
      a1 = __builtin_amdgcn_raw_buffer_load_b128(rw, vl + 1024, W_YB + j * 2048, AUX_SC1) ^ tb;
      if (__all((or4(a0 | a1) & cm) == 0u)) break;
    }
    STAMP(8);
    const f32x4 y = __builtin_bit_cast(f32x4, a0) + __builtin_bit_cast(f32x4, a1);
    const auto u02 = __builtin_amdgcn_permlane32_swap(__float_as_uint(y[0]), __float_as_uint(y[2]), false, false);
    const auto u13 = __builtin_amdgcn_permlane32_swap(__float_as_uint(y[1]), __float_as_uint(y[3]), false, false);
    float e = __uint_as_float(u02[0]) + __uint_as_float(u02[1]), f = __uint_as_float(u13[0]) + __uint_as_float(u13[1]);
    e += dpp<0x124>(e); f += dpp<0x124>(f); e += dpp<0x128>(e); f += dpp<0x128>(f);
    const auto ue = __builtin_amdgcn_permlane16_swap(__float_as_uint(e), __float_as_uint(e), false, false);
    const auto uf = __builtin_amdgcn_permlane16_swap(__float_as_uint(f), __float_as_uint(f), false, false);
    __builtin_amdgcn_raw_buffer_store_b32(cvtpk(__uint_as_float(ue[0]) + __uint_as_float(ue[1]), __uint_as_float(uf[0]) + __uint_as_float(uf[1])), rout, vs, j * 32, OUTAUX);
  }
  if (j == 0 && lane == 0) __builtin_amdgcn_raw_buffer_store_b32(tag, rw, 0, W_E, AUX_SC1);
  asm volatile("Lgqa_end_c:" ::: "memory");
  STAMP(9);
  STAMP_OUT();
}

extern "C" void kernel_launch(void* const* d_in, const int* in_sizes, int n_in, void* d_out, int out_size, void* d_ws, size_t ws_size,
                              hipStream_t stream) {
  gqa_fused<<<NWG, NTHR, 0, stream>>>((const unsigned char*)d_in[0], (const unsigned char*)d_in[1], (const unsigned char*)d_in[2], (const unsigned char*)d_in[3],
                                      (const unsigned char*)d_in[4], (unsigned char*)d_out, (unsigned char*)d_ws TUNE_PASS);
}
```

```cpp
#define DIAG 1
#define DSIGN 1
#include <hip/hip_runtime.h>
#include <hip/hip_bf16.h>
#include <stdint.h>

#define GAS __attribute__((address_space(1)))
#define LAS __attribute__((address_space(3)))
typedef unsigned v4u __attribute__((ext_vector_type(4)));
typedef unsigned v2u __attribute__((ext_vector_type(2)));
typedef int v4i __attribute__((ext_vector_type(4)));
typedef float f32x4 __attribute__((ext_vector_type(4)));
typedef float f32x32 __attribute__((ext_vector_type(32)));
typedef short s16x4 __attribute__((ext_vector_type(4)));
typedef __bf16 bf16x8 __attribute__((ext_vector_type(8)));

constexpr int NB = 8, DM = 512, HQ = 32, HKV = 8, SK = 8192, DH = 128;
constexpr int NWG = 256, NTHR = 384;
constexpr int NUNIT = 128;
constexpr int SLOT = 8192;
constexpr float C2 = 0.088388347648318440f * 1.4426950408889634f;

#define NREG 4
#define NFIRST 4
#define WIN 24
#define UPREP 112
#define NFIN 3
#define MPOS 1
#define ROLL2 0
#define DRY 0
#define OUTAUX 0
#define FB_NREP 32
#define XF 1312
#define TUNE_ARG
#define TUNE_PASS
#define TL0 3.39f
#define UC16 ((unsigned)(TL0 * 128.f))
#define DELTA 4
#define HOLD 30
#define MINSP 20
#define LINE0 100
#define LATE 40
#define MINSP2 16
#define PHASE 28
#define FB_P 192u
#define FB_G 0.4f
#define FB_KI 0.f
#define FB_I0 3
#define WQT 0
#define WQSP 12
#define QPOLL 480
#define WOPOS 125
#define D1 30
#define D1G 138
#define GPOS 126
#define D2 40
constexpr unsigned SPIN_CAP = 1u << 16;
constexpr int LAG = (WIN + 7) / 8;

constexpr int L_Q = 16 * SLOT, L_ACC = L_Q + 1024, L_PL = L_ACC + 4 * 2048, L_DG = L_PL + 2048, L_OT = L_DG + 16, OT_ROW = 272, L_ML = L_OT + 8 * OT_ROW, L_CTL = (L_ML + 144 + 63) / 64 * 64;
constexpr int L_X = L_ACC, L_W = L_CTL + 64, L_TOT = L_W + 16384;
constexpr int C_FREE = L_CTL, C_LAND = L_CTL + 32, C_READY = L_CTL + 40, C_TAG = L_CTL + 44, C_NM = L_CTL + 48, C_NL = L_CTL + 52, C_NA = L_CTL + 56, C_WO = L_CTL + 60;
static_assert(L_CTL % 32 == 0 && L_TOT <= 160 * 1024, "lds map");
constexpr int W_E = 0, W_FB = 1024, FB_SLOT = 512, W_ML = W_FB + 64 * FB_SLOT + 1024, W_QG = W_ML + NWG * 128, W_PA = W_QG + NWG * 2048, PA_REC = 2048, W_YB = W_PA + NWG * PA_REC,
              W_END = W_YB + NWG * 2048;

#define STAMP(i) do { if (DIAG % 100 == (i) && DIAG / 100 == (wv > 2 ? 2 : wv) && lane == 0) (void)__hip_atomic_fetch_max((LAS int*)(L + L_DG), (int)(now() - t0), __ATOMIC_RELAXED, __HIP_MEMORY_SCOPE_WORKGROUP); } while (0)
#define STAMP_OUT() do { if (wv == 1) { while ((int)(now() - t0) < 9000) __builtin_amdgcn_s_sleep(8); const int x_ = lds_word(L + L_DG); const int tg_ = DSIGN >= 0 ? 10000 + DSIGN * 10 * x_ : 70000 - 10 * x_; while ((int)(now() - t0) < tg_) __builtin_amdgcn_s_sleep(4); } } while (0)

#define STR_(x) #x
#define STRX_(x) STR_(x)
#define WAITVM(n) asm volatile("s_waitcnt vmcnt(" STR_(n) ")" ::: "memory")
#define BARRIER() asm volatile("s_waitcnt lgkmcnt(0)\n\ts_barrier" ::: "memory")
#define RELOAD() asm volatile("" ::: "memory")
constexpr int AUX_SC1 = 16;

__device__ __forceinline__ unsigned now() { return (unsigned)__builtin_amdgcn_s_memrealtime(); }
__device__ __forceinline__ unsigned cvtpk(float lo, float hi) {
  typedef __bf16 bf16x2_t __attribute__((ext_vector_type(2)));
  typedef float f32x2_t __attribute__((ext_vector_type(2)));
  return __builtin_bit_cast(unsigned, __builtin_convertvector(f32x2_t{lo, hi}, bf16x2_t));
}
template <int CTRL> __device__ __forceinline__ unsigned dppu(unsigned x) { return (unsigned)__builtin_amdgcn_mov_dpp((int)x, CTRL, 0xf, 0xf, true); }
template <int CTRL> __device__ __forceinline__ float dpp(float x) { return __uint_as_float(dppu<CTRL>(__float_as_uint(x))); }
__device__ __forceinline__ float xrow_max(float x) {
  auto s = __builtin_amdgcn_permlane16_swap(__float_as_uint(x), __float_as_uint(x), false, false);
  x = fmaxf(__uint_as_float(s[0]), __uint_as_float(s[1]));
  auto t = __builtin_amdgcn_permlane32_swap(__float_as_uint(x), __float_as_uint(x), false, false);
  return fmaxf(__uint_as_float(t[0]), __uint_as_float(t[1]));
}
__device__ __forceinline__ float xrow_sum(float x) {
  auto s = __builtin_amdgcn_permlane16_swap(__float_as_uint(x), __float_as_uint(x), false, false);
  x = __uint_as_float(s[0]) + __uint_as_float(s[1]);
  auto t = __builtin_amdgcn_permlane32_swap(__float_as_uint(x), __float_as_uint(x), false, false);
  return __uint_as_float(t[0]) + __uint_as_float(t[1]);
}
__device__ __forceinline__ v4i mkrsrc(const void* p, unsigned bytes) {
  const unsigned long long a = (unsigned long long)p;
  v4i r; r[0] = (int)(unsigned)a; r[1] = (int)((unsigned)(a >> 32) & 0xffffu); r[2] = (int)bytes; r[3] = 0x00020000; return r;
}
__device__ __forceinline__ int lds_word(LAS unsigned char* p) { RELOAD(); const int v = __builtin_amdgcn_readfirstlane(*(LAS int*)p); RELOAD(); return v; }
__device__ __forceinline__ void lds_inc(LAS unsigned char* p) {
  unsigned long long ex;
  asm volatile("s_mov_b64 %0, exec\n\ts_mov_b64 exec, 1\n\tds_add_u32 %1, %2\n\ts_mov_b64 exec, %0" : "=&s"(ex) : "v"((unsigned)(size_t)p), "v"(1u) : "memory");
}
#define DMA4(V, R, S) "buffer_load_dwordx4 " V ", " R ", " S " offen nt lds\n\tbuffer_load_dwordx4 " V ", " R ", " S " offen offset:1024 nt lds\n\t" \
                      "buffer_load_dwordx4 " V ", " R ", " S " offen offset:2048 nt lds\n\tbuffer_load_dwordx4 " V ", " R ", " S " offen offset:3072 nt lds\n\t"
__device__ __forceinline__ void issue_two(v4i r, unsigned vv, unsigned so, unsigned lds) {
  unsigned t;
  asm volatile("s_mov_b32 m0, %4\n\ts_add_u32 %0, %3, 0x1000\n\t" DMA4("%2", "%1", "%3") "s_add_u32 m0, m0, 0x1000\n\ts_nop 0\n\t" DMA4("%2", "%1", "%0")
               "s_add_u32 m0, m0, 0x1000\n\ts_add_u32 %0, %0, 0x1000\n\t" DMA4("%2", "%1", "%0") "s_add_u32 m0, m0, 0x1000\n\ts_add_u32 %0, %0, 0x1000\n\t" DMA4("%2", "%1", "%0")
               : "=&s"(t) : "s"(r), "v"(vv), "s"(so), "s"(lds) : "memory", "scc");
}
__device__ __forceinline__ void dma1_nt(v4i r, unsigned voff, unsigned soff, unsigned lds) {
  asm volatile("s_mov_b32 m0, %3\n\ts_nop 0\n\tbuffer_load_dwordx4 %1, %0, %2 offen nt lds" :: "s"(r), "v"(voff), "s"(soff), "s"(lds) : "memory");
}
__device__ __forceinline__ void dma1(v4i r, unsigned voff, unsigned soff, unsigned lds) {
  asm volatile("s_mov_b32 m0, %3\n\ts_nop 0\n\tbuffer_load_dwordx4 %1, %0, %2 offen lds" :: "s"(r), "v"(voff), "s"(soff), "s"(lds) : "memory");
}
struct Dbg { unsigned d0, d1; };
__device__ __forceinline__ void stream_units(v4i rs, unsigned vv, unsigned u, unsigned ue, unsigned so, unsigned ra, unsigned uc16, int hold, int msp, int lat, int ms2, Dbg& dbg, int lane) {
  unsigned ta, tb, st = 8192u, fo = ((u + 1u) & 3u) * 8u + C_FREE;
#define STAMP_ASM
#define STAMP_OPS
#define STAMP_INS
  asm volatile(
      "s_mov_b32 m0, %[m0i]\n\t"
      "v_mov_b32 v2, %[ld0]\n"
      "LT%=:\n\t"
      "s_waitcnt vmcnt(" STRX_(WIN) ")\n\t"
      DMA4("%[v0]", "%[rs]", "%[so]")
      "s_add_u32 m0, m0, 0x1000\n\t"
      "s_memrealtime s[40:41]\n\t"
      DMA4("%[v1]", "%[rs]", "%[so]")
      "ds_read_b64 v[4:5], %[fo]\n\t"
      "s_add_u32 m0, m0, 0x1000\n\t"
      "s_add_u32 %[so], %[so], %[st]\n\t"
      "s_and_b32 m0, m0, 0x1ffff\n\t"
      "s_and_b32 %[so], %[so], 0x1fffff\n\t"
      "s_xor_b32 %[st], %[st], 0xc000\n\t"
      "s_add_u32 %[ra], %[ra], %[uc]\n\t"
      "v_add_u32 v2, 1, v2\n\t"
      "v_add_u32 %[fo], 8, %[fo]\n\t"
      "s_add_u32 %[u], %[u], 1\n\t"
      "v_and_b32 %[fo], %[fm], %[fo]\n\t"
      "s_waitcnt lgkmcnt(0)\n\t"
      "v_mov_b32 v3, s40\n\t"
      "v_readfirstlane_b32 %[a], v5\n\t"
      "s_lshr_b32 s41, %[ra], 4\n\t"
      "v_readfirstlane_b32 %[b], v4\n\t"
      "ds_write_b64 %[vl], v[2:3]\n\t"
      STAMP_ASM
      "s_cmp_eq_u32 %[u], %[ue]\n\t"
      "s_cbranch_scc1 LE%=\n\t"
      "s_add_u32 %[a], %[a], s41\n\t"
      "s_sub_i32 %[a], %[a], s40\n\t"
      "s_cmp_lt_i32 %[a], %[lat]\n\t"
      "s_cselect_b32 s41, %[ms2], %[msp]\n\t"
      "s_min_i32 %[a], %[a], %[hold]\n\t"
      "s_max_i32 %[a], %[a], s41\n\t"
      "s_add_u32 %[a], %[a], s40\n\t"
      "s_cmp_lt_u32 %[b], %[u]\n\t"
      "s_cbranch_scc1 LS%=\n"
      "LW%=:\n\t"
      "s_memrealtime s[40:41]\n\t"
      "s_waitcnt lgkmcnt(0)\n\t"
      "s_sub_i32 %[b], %[a], s40\n\t"
      "s_cmp_gt_i32 %[b], 0\n\t"
      "s_cbranch_scc0 LT%=\n\t"
      "s_branch LW%=\n"
      "LS%=:\n\t"
      "s_and_b32 %[b], %[u], 3\n\t"
      "s_lshl_b32 %[b], %[b], 3\n\t"
      "s_add_u32 %[b], %[b], %[fb]\n\t"
      "v_mov_b32 v5, %[b]\n"
      "LR%=:\n\t"
      "s_sleep 1\n\t"
      "ds_read_b32 v4, v5\n\t"
      "s_waitcnt lgkmcnt(0)\n\t"
      "v_readfirstlane_b32 %[b], v4\n\t"
      "s_cmp_lt_u32 %[b], %[u]\n\t"
      "s_cbranch_scc1 LR%=\n\t"
      "s_branch LW%=\n"
      "LE%=:"
      : [so] "+s"(so), [ra] "+s"(ra), [u] "+s"(u), [st] "+s"(st), [fo] "+v"(fo), [a] "=&s"(ta), [b] "=&s"(tb) STAMP_OPS
      : [rs] "s"(rs), [v0] "v"(vv), [v1] "v"(vv + 4096u), [vl] "v"((unsigned)C_LAND), [ue] "s"(ue), [uc] "s"(uc16), [hold] "s"(hold), [msp] "s"(msp), [lat] "s"(lat), [ms2] "s"(ms2),
        [m0i] "s"((u & 15u) * SLOT), [ld0] "s"(u - 1u - LAG), [fm] "s"(~32u), [fb] "s"((unsigned)C_FREE) STAMP_INS
      : "memory", "scc", "vcc", "v2", "v3", "v4", "v5", "s40", "s41");
}
static_assert((C_FREE & 32) == 0, "the record pointer wraps by clearing bit 5");

__device__ __forceinline__ f32x4 mfma32(v4u a, v4u b, f32x4 c) {
  return __builtin_amdgcn_mfma_f32_16x16x32_bf16(__builtin_bit_cast(bf16x8, a), __builtin_bit_cast(bf16x8, b), c, 0, 0, 0);
}
__device__ __forceinline__ v4u tr_frag(LAS unsigned char* p, int hi) {
  const v2u lo = __builtin_bit_cast(v2u, __builtin_amdgcn_ds_read_tr16_b64_v4i16((LAS s16x4*)p));
  const v2u up = __builtin_bit_cast(v2u, __builtin_amdgcn_ds_read_tr16_b64_v4i16((LAS s16x4*)(p + hi)));
  return v4u{lo[0], lo[1], up[0], up[1]};
}
__device__ __forceinline__ float score16(const v4u* K, v4u qf, unsigned j0) {
  float sc[4];
#pragma unroll
  for (int u = 0; u < 4; ++u) {
    const f32x4 d = mfma32(K[u], qf, f32x4{0.f, 0.f, 0.f, 0.f});
    sc[u] = ((dpp<0x00>(d[0]) + dpp<0x55>(d[1])) + dpp<0xAA>(d[2])) + dpp<0xFF>(d[3]);
  }
  return (j0 & 2) ? ((j0 & 1) ? sc[3] : sc[2]) : ((j0 & 1) ? sc[1] : sc[0]);
}
__device__ __forceinline__ void p_operands(unsigned k0, unsigned k1, unsigned mask0, unsigned mask1, v4u (&bo)[4]) {
  bo[0] = v4u{dppu<0x00>(k0) & mask0, dppu<0x00>(k0) & mask1, dppu<0x55>(k0) & mask0, dppu<0x55>(k0) & mask1};
  bo[1] = v4u{dppu<0xAA>(k0) & mask0, dppu<0xAA>(k0) & mask1, dppu<0xFF>(k0) & mask0, dppu<0xFF>(k0) & mask1};
  bo[2] = v4u{dppu<0x00>(k1) & mask0, dppu<0x00>(k1) & mask1, dppu<0x55>(k1) & mask0, dppu<0x55>(k1) & mask1};
  bo[3] = v4u{dppu<0xAA>(k1) & mask0, dppu<0xAA>(k1) & mask1, dppu<0xFF>(k1) & mask0, dppu<0xFF>(k1) & mask1};
  asm volatile("" : "+v"(bo[0]), "+v"(bo[1]), "+v"(bo[2]), "+v"(bo[3]));
}
#define RD2(A, P, O0, O1) "ds_read2st64_b64 %[" A "], %[" P "] offset0:" O0 " offset1:" O1 "\n\t"
#define MF(N, C, A, B) "s_waitcnt lgkmcnt(" N ")\n\tv_mfma_f32_16x16x32_bf16 %[" C "], %[" A "], %[" B "], %[" C "]\n\t"
template <bool PAD> __device__ __forceinline__ void pv32(unsigned p, const v4u (&bo)[4], f32x4& acc0, f32x4& acc1) {
  v4u a0, a1, a2, a3, a4, a5, a6, a7;
  asm volatile(RD2("a0", "p0", "0", "2") RD2("a1", "p1", "0", "2") RD2("a2", "p0", "4", "6") RD2("a3", "p1", "4", "6")
               RD2("a4", "p0", "8", "10") RD2("a5", "p1", "8", "10") RD2("a6", "p0", "12", "14") RD2("a7", "p1", "12", "14")
               MF("7", "c0", "a0", "b0") MF("6", "c1", "a1", "b0") MF("5", "c0", "a2", "b1") MF("4", "c1", "a3", "b1")
               MF("3", "c0", "a4", "b2") MF("2", "c1", "a5", "b2") MF("1", "c0", "a6", "b3") MF("0", "c1", "a7", "b3") "s_nop %[pad]"
      : [c0] "+v"(acc0), [c1] "+v"(acc1), [a0] "=&v"(a0), [a1] "=&v"(a1), [a2] "=&v"(a2), [a3] "=&v"(a3), [a4] "=&v"(a4), [a5] "=&v"(a5), [a6] "=&v"(a6), [a7] "=&v"(a7)
      : [p0] "v"(p), [p1] "v"(p + 8), [b0] "v"(bo[0]), [b1] "v"(bo[1]), [b2] "v"(bo[2]), [b3] "v"(bo[3]), [pad] "i"(PAD ? 7 : 0) : "memory");
}
__device__ __forceinline__ v4u tag_bytes(unsigned tag) { return v4u{tag & 0xffu, (tag >> 8) & 0xffu, (tag >> 16) & 0xffu, tag >> 24}; }
__device__ __forceinline__ v4u tag4(f32x4 v, v4u tb) { return (__builtin_bit_cast(v4u, v) & 0xffffff00u) | tb; }
__device__ __forceinline__ unsigned or4(v4u u) { return (u[0] | u[1]) | (u[2] | u[3]); }

__global__ void __launch_bounds__(NTHR, 1) gqa_fused(const unsigned char* __restrict__ x, const unsigned char* __restrict__ kc, const unsigned char* __restrict__ vc,
                                                     const unsigned char* __restrict__ wq, const unsigned char* __restrict__ wo, unsigned char* __restrict__ out,
                                                     unsigned char* ws TUNE_ARG) {
  __shared__ __attribute__((aligned(16))) unsigned char smem[L_TOT];
  LAS unsigned char* L = (LAS unsigned char*)smem;
  unsigned long long t64;
  unsigned pf, pa;
  unsigned long long pc;
  unsigned clen;
  {
    unsigned long long ex;
    asm volatile("s_memrealtime %0\n\ts_getpc_b64 %1\nLgqa_pc:\n\t"
                 "s_mov_b32 %2, Lgqa_end_a-Lgqa_pc+128\n\ts_max_u32 %2, %2, Lgqa_end_b-Lgqa_pc+128\n\ts_max_u32 %2, %2, Lgqa_end_c-Lgqa_pc+128\n\t"
                 "v_lshlrev_b32 %5, 7, %6\n\tv_add_u32 %5, 0xffffc000, %5\n\tv_cmp_gt_u32 vcc, %2, %5\n\t"
                 "s_and_saveexec_b64 %3, vcc\n\ts_cbranch_execz Lgqa_nopf\n\tglobal_load_dword %4, %5, %1 offset:-32\nLgqa_nopf:\n\ts_mov_b64 exec, %3"
                 : "=&s"(t64), "=&s"(pc), "=&s"(clen), "=&s"(ex), "=&v"(pf), "=&v"(pa) : "v"(threadIdx.x) : "scc", "vcc", "memory");
  }
  const int lane = threadIdx.x & 63, wv = __builtin_amdgcn_readfirstlane(threadIdx.x >> 6);
  const int j = blockIdx.x, xg = j & 7, idx = j >> 3;
  const int yy = (xg - idx) & 7, pr = (XF & 16) ? (idx >> 2) * 8 + xg : (XF & 128) ? xg * 8 + (idx >> 2) : (XF & 256) ? (idx >> 2) * 8 + ((xg + (idx >> 2)) & 7) : 2 * idx + (yy >> 2), s = (XF & (16 | 128 | 256)) ? (idx & 3) : yy & 3;
  const int ch = 4 * xg + (idx >> 3), nb = idx & 7;
  const int g = lane >> 4;
  const unsigned so0 = (unsigned)(pr >> 1) * 65536u + s * 16384u;
  const unsigned vv = lane * 16;
  const unsigned ra0 = ((unsigned)(j * 37) & 31u) * PHASE >> 1;
  asm volatile("s_waitcnt lgkmcnt(0)" : "+s"(t64) : "s"(x), "s"(kc), "s"(vc), "s"(wq), "s"(wo), "s"(out), "s"(ws), "s"(so0), "v"(vv));
  const unsigned t0 = (unsigned)t64;
  const size_t pbase = (size_t)pr * (SK * DH * 2);
  Dbg dbg = {0u, 0u};

  const v4i rk = mkrsrc(kc + pbase, SK * DH * 2);
  if (wv == 0) {
    __builtin_amdgcn_s_setprio(3);
#pragma unroll
    for (int u = 0; u < NFIRST; u += 2) issue_two(rk, vv, (so0 + (u >> 1) * 65536u) & 0x1fffffu, u * SLOT);
    __builtin_amdgcn_sched_barrier(0);
    const unsigned keep = (unsigned)((lane & 7) - 4) >> 31;
    *(LAS v2u*)(L + L_CTL + (lane & 7) * 8) = v2u{(12u + (lane & 7)) * keep, (t0 + LINE0) * keep};
    *(LAS int*)(L + L_DG) = 0;
    STAMP(1);
    asm volatile("s_waitcnt lgkmcnt(0)" ::: "memory");
  }
  unsigned e0, e1, e2;
  if (wv == 1) asm volatile("s_load_dword %0, %3, 0x0\n\ts_load_dword %1, %4, 0x0\n\ts_load_dword %2, %5, %6"
                            : "=&s"(e0), "=&s"(e1), "=&s"(e2) : "s"(ws), "s"((XF & 1024) ? x : ws), "s"((XF & 1024) ? wq : ws), "s"((XF & 1024) ? (j >> 6) * (128 * 8192) + (j & 63) * 128 : 0) : "memory");
  asm volatile("s_barrier" ::: "memory");
  if (wv == 0) stream_units(rk, vv, NFIRST, 64u, (so0 + (NFIRST >> 1) * 65536u) & 0x1fffffu, NFIRST * UC16 + ra0, UC16, HOLD, MINSP, -LATE, MINSP2, dbg, lane);
  const __amdgpu_buffer_rsrc_t rw = __builtin_amdgcn_make_buffer_rsrc((void*)ws, 0, W_END, 0x00020000);
  const unsigned j0 = lane & 3;
  const int h = (lane >> 2) & 3;
  const unsigned mask0 = j0 == 0 ? 0x0000ffffu : (j0 == 1 ? 0xffff0000u : 0u);
  const unsigned mask1 = j0 == 2 ? 0x0000ffffu : (j0 == 3 ? 0xffff0000u : 0u);

  if (wv == 0) {
    const v4i rv = mkrsrc(vc + pbase, SK * DH * 2);
    while (lds_word(L + C_FREE) < 64) __builtin_amdgcn_s_sleep(1);
    stream_units(rv, vv, 64u, UPREP, so0, 64u * UC16 + ra0, UC16, HOLD, MINSP, -LATE, MINSP2, dbg, lane);
    while (lds_word(L + C_FREE + 24) < 64 + 4 * 8 + 3 + 16) __builtin_amdgcn_s_sleep(1);
    v4u bo[NFIN][4], tb;
    unsigned vo = h * 512 + g * 64 + j0 * 16;
    float scale;
    {
      v2u pl[NFIN];
#pragma unroll
      for (int k = 0; k < NFIN; ++k) pl[k] = *(LAS v2u*)(L + L_PL + (4 - NFIN + k) * 512 + lane * 8);
      tb = tag_bytes(*(LAS unsigned*)(L + C_TAG));
      scale = *(LAS float*)(L + L_ML + 128 + h * 4);
      const int fr = *(LAS int*)(L + C_FREE + (UPREP & 3) * 8);
#pragma unroll
      for (int k = 0; k < NFIN; ++k) p_operands(pl[k][0], pl[k][1], mask0, mask1, bo[k]);
      asm volatile("" : "+v"(vo), "+v"(tb), "+v"(scale));
      if (__builtin_amdgcn_readfirstlane(fr) < UPREP) { while (lds_word(L + C_FREE + (UPREP & 3) * 8) < UPREP) __builtin_amdgcn_s_sleep(1); }
    }
    stream_units(rv, vv, UPREP, NUNIT, (so0 + ((UPREP - 64) >> 1) * 65536u) & 0x1fffffu, UPREP * UC16 + ra0, UC16, HOLD, MINSP, -LATE, MINSP2, dbg, lane);
    STAMP(2);
    const int so = W_PA + (pr * 4 + s) * PA_REC;
    f32x4 acc0 = {0.f, 0.f, 0.f, 0.f}, acc1 = {0.f, 0.f, 0.f, 0.f};
    static_assert(NFIN >= 2 && LAG <= 3, "the drain below starts at three units outstanding");
    f32x4 s0, s1;
#pragma unroll
    for (int k = 0; k < 3; ++k) {
      if (k == 0) WAITVM(24); else if (k == 1) WAITVM(16); else WAITVM(8);
      if (k < 2) *(LAS int*)(L + C_LAND) = NUNIT - 3 + k;
      if (k >= 4 - NFIN) pv32<false>((12 + k) * SLOT + vv, bo[k >= 4 - NFIN ? k - (4 - NFIN) : 0], acc0, acc1);
      if (k == MPOS) {
        while (lds_word(L + C_NA) < 4) {}
        f32x4 a[8];
#pragma unroll
        for (int u = 0; u < 8; ++u) a[u] = *(LAS f32x4*)(L + L_ACC + vv + u * 1024);
        __builtin_amdgcn_sched_barrier(0);
        s0 = ((a[0] + a[2]) + (a[4] + a[6])) * scale; s1 = ((a[1] + a[3]) + (a[5] + a[7])) * scale;
        asm volatile("" : "+v"(s0), "+v"(s1) : "v"(14 * SLOT + vv), "v"(14 * SLOT + vv + 8), "v"(15 * SLOT + vv), "v"(15 * SLOT + vv + 8), "s"(so), "s"(0xffffff00u));
      }
    }
    STAMP(4);
    WAITVM(0);
    STAMP(3);
    pv32<true>(15 * SLOT + vv, bo[NFIN - 1], acc0, acc1);
    const v4u o0 = tag4(acc0 * scale + s0, tb), o1 = tag4(acc1 * scale + s1, tb);
    __builtin_amdgcn_raw_buffer_store_b128(o0, rw, vo, so, AUX_SC1);
    __builtin_amdgcn_raw_buffer_store_b128(o1, rw, vo + 256, so, AUX_SC1);
    asm volatile("s_nop 1" :: "v"(o0), "v"(o1) : "memory");
    *(LAS int*)(L + C_LAND) = NUNIT;
    asm volatile("Lgqa_end_a:" ::: "memory");
    STAMP(5);
    STAMP_OUT();
    return;
  }

  if (wv >= 2) {
    const int w = wv - 2;
    const unsigned vk = (h * 16 + g * 4 + j0) * 16;
    asm volatile("s_waitcnt vmcnt(0)" :: "v"(pf), "v"(pa) : "memory");
    f32x32 sc;
    float m;
    {
      v4u P[NREG][8];
#pragma unroll
      for (int r = 0; r < NREG; ++r) {
        while (lds_word(L + C_LAND) <= 4 * r + w) __builtin_amdgcn_s_sleep(2);
#pragma unroll
        for (int u = 0; u < 8; ++u) P[r][u] = *(LAS v4u*)(L + ((4 * r + w) & 15) * SLOT + vk + u * 1024);
        asm volatile("" :: "v"(P[r][0]), "v"(P[r][1]), "v"(P[r][2]), "v"(P[r][3]), "v"(P[r][4]), "v"(P[r][5]), "v"(P[r][6]), "v"(P[r][7]) : "memory");
        *(LAS int*)(L + C_FREE + w * 8) = 4 * r + w + 16;
      }
      STAMP(1);
      while (lds_word(L + C_READY) == 0) __builtin_amdgcn_s_sleep(2);
      STAMP(2);
      const v4u qf = *(LAS v4u*)(L + L_Q + vk);
      m = -INFINITY;
#pragma unroll
      for (int r = 0; r < NREG; ++r) {
        const float s0 = score16(&P[r][0], qf, j0), s1 = score16(&P[r][4], qf, j0);
        sc[2 * r] = s0; sc[2 * r + 1] = s1; m = fmaxf(m, fmaxf(s0, s1));
      }
#pragma unroll 1
      for (int n = NREG; n < 16; ++n) {
        while (lds_word(L + C_LAND) <= 4 * n + w) __builtin_amdgcn_s_sleep(2);
        v4u K[8];
#pragma unroll
        for (int u = 0; u < 8; ++u) K[u] = *(LAS v4u*)(L + ((4 * n + w) & 15) * SLOT + vk + u * 1024);
        asm volatile("" :: "v"(K[0]), "v"(K[1]), "v"(K[2]), "v"(K[3]), "v"(K[4]), "v"(K[5]), "v"(K[6]), "v"(K[7]) : "memory");
        *(LAS int*)(L + C_FREE + w * 8) = 4 * n + w + 16;
        const float s0 = score16(&K[0], qf, j0), s1 = score16(&K[4], qf, j0);
        sc[2 * n] = s0; sc[2 * n + 1] = s1; m = fmaxf(m, fmaxf(s0, s1));
      }
    }
    STAMP(10);
    const v4u tb = tag_bytes((unsigned)lds_word(L + C_TAG));
    m = fmaxf(m, dpp<0xB1>(m)); m = fmaxf(m, dpp<0x4E>(m)); m = xrow_max(m);
    *(LAS float*)(L + L_ML + h * 16 + w * 4) = m;
    lds_inc(L + C_NM);
    while (lds_word(L + C_NM) < 4) __builtin_amdgcn_s_sleep(1);
    {
      const f32x4 mw = *(LAS f32x4*)(L + L_ML + h * 16);
      m = __uint_as_float(__float_as_uint(fmaxf(fmaxf(mw[0], mw[1]), fmaxf(mw[2], mw[3]))) & 0xffffff00u);
      const float mc = -m * C2;
      float l = 0.f;
#pragma unroll
      for (int n = 0; n < 32; ++n) { const float p = __builtin_amdgcn_exp2f(fmaf(sc[n], C2, mc)); l += p; sc[n] = __uint_as_float(cvtpk(p, p)); }
      l += dpp<0xB1>(l); l += dpp<0x4E>(l); l = xrow_sum(l);
      *(LAS float*)(L + L_ML + 64 + h * 16 + w * 4) = l;
      *(LAS v2u*)(L + L_PL + w * 512 + lane * 8) = v2u{__float_as_uint(sc[30]), __float_as_uint(sc[31])};
      lds_inc(L + C_NL);
    }
    STAMP(11);
    f32x4 acc0 = {0.f, 0.f, 0.f, 0.f}, acc1 = {0.f, 0.f, 0.f, 0.f};
#pragma unroll 1
    for (int n = 0; n < (w < 4 - NFIN ? 16 : 15); ++n) {
      if (w == 3 && n == 1) {
        while (lds_word(L + C_NL) < 4) __builtin_amdgcn_s_sleep(1);
        const f32x4 lw = *(LAS f32x4*)(L + L_ML + 64 + h * 16);
        if (lane < 16 && j0 == 0) __builtin_amdgcn_raw_buffer_store_b128(tag4(f32x4{m, (lw[0] + lw[1]) + (lw[2] + lw[3]), 0.f, 0.f}, tb), rw, h * 16, W_ML + (pr * 4 + s) * 128, AUX_SC1);
      }
      if (w == 3 && n == 8) {
        v4u ml;
        for (unsigned spin = 0; spin < SPIN_CAP; ++spin) {
          RELOAD();
          ml = __builtin_amdgcn_raw_buffer_load_b128(rw, j0 * 128 + h * 16, W_ML + pr * 512, AUX_SC1) ^ tb;
          if (__all((or4(ml) & 0xffu) == 0u)) break;
        }
        const float ms = __uint_as_float(ml[0]);
        float M = fmaxf(ms, dpp<0xB1>(ms)); M = fmaxf(M, dpp<0x4E>(M));
        float ls = __uint_as_float(ml[1]) * __builtin_amdgcn_exp2f((ms - M) * C2);
        ls += dpp<0xB1>(ls); ls += dpp<0x4E>(ls);
        if (lane < 16 && j0 == 0) *(LAS float*)(L + L_ML + 128 + h * 4) = __builtin_amdgcn_exp2f((m - M) * C2) * __builtin_amdgcn_rcpf(ls);
      }
      v4u bo[4];
      p_operands(__float_as_uint(sc[2 * n]), __float_as_uint(sc[2 * n + 1]), mask0, mask1, bo);
      while (lds_word(L + C_LAND) <= 64 + 4 * n + w) __builtin_amdgcn_s_sleep(1);
      pv32<true>(((4 * n + w) & 15) * SLOT + vv, bo, acc0, acc1);
      *(LAS int*)(L + C_FREE + w * 8) = 64 + 4 * n + w + 16;
    }
    *(LAS f32x4*)(L + L_ACC + w * 2048 + vv) = acc0; *(LAS f32x4*)(L + L_ACC + w * 2048 + 1024 + vv) = acc1;
    lds_inc(L + C_NA);
    STAMP(3);
    const int bb = 2 * w + (lane >> 5), c = lane & 31;
    unsigned vg = bb * (HKV * 4 * PA_REC) + c * 16, vy = (lane & 7) * (32 * 2048) + g * 16;
    const int sg = W_PA + (ch >> 2) * (4 * PA_REC) + (ch & 3) * 512, sy = W_YB + (nb * 4 + w) * 2048 + ch * 64;
    LAS unsigned char* ap = L + L_W + (8 * g + ((lane & 15) >> 2)) * 128 + (16 * w + 4 * j0) * 2;
    LAS unsigned char* op = L + L_OT + bb * OT_ROW + c * 8;
    LAS unsigned char* bp = L + L_OT + (lane & 7) * OT_ROW + g * 16;
    if (lane & 8) vy = 0x40000000u;
    v4u tv = tb;
    asm volatile("" : "+v"(vg), "+v"(vy), "+v"(ap), "+v"(op), "+v"(bp), "+v"(tv) : "s"(sg), "s"(sy));
#pragma unroll 1
    for (int pass = (DRY & 1) ? 0 : 1; pass < 2; ++pass) {
      unsigned cm = pass ? 0xffu : 0u;
      v4u a[4], af[4];
      asm volatile("" : "+s"(cm), "=v"(a[0]), "=v"(a[1]), "=v"(a[2]), "=v"(a[3]));
      if (pass) {
        while (lds_word(L + C_LAND) < GPOS) __builtin_amdgcn_s_sleep(1);
        STAMP(5);
        const unsigned tp = now() + D1;
        unsigned tg = (unsigned)lds_word(L + C_FREE + 4) + (NUNIT * UC16 >> 4) + D1G;
        tg = tp + (unsigned)min(max((int)(tg - tp), 0), 200);
        while ((int)(now() - tg) < 0) {}
        STAMP(4);
#pragma unroll
        for (int u = 0; u < 4; ++u) a[u] = __builtin_amdgcn_raw_buffer_load_b128(rw, vg, sg + u * PA_REC, AUX_SC1);
      }
      while (lds_word(L + C_WO) < pass) {}
#pragma unroll
      for (int ks = 0; ks < 4; ++ks) af[ks] = tr_frag(ap + ks * 4096, 512);
      asm volatile("" : "+v"(af[0]), "+v"(af[1]), "+v"(af[2]), "+v"(af[3]));
#pragma unroll
      for (int u = 0; u < 4; ++u) a[u] ^= tv;
      asm volatile("" : "+v"(a[0]), "+v"(a[1]), "+v"(a[2]), "+v"(a[3]));
      if (__builtin_expect(!__all((or4((a[0] | a[1]) | (a[2] | a[3])) & cm) == 0u), 0)) {
        for (unsigned spin = 0; spin < SPIN_CAP; ++spin) {
          RELOAD();
#pragma unroll
          for (int u = 0; u < 4; ++u) a[u] = __builtin_amdgcn_raw_buffer_load_b128(rw, vg, sg + u * PA_REC, AUX_SC1) ^ tv;
          if (__all((or4((a[0] | a[1]) | (a[2] | a[3])) & 0xffu) == 0u)) break;
        }
      }
      STAMP(6);
      const f32x4 o = (__builtin_bit_cast(f32x4, a[0]) + __builtin_bit_cast(f32x4, a[1])) + (__builtin_bit_cast(f32x4, a[2]) + __builtin_bit_cast(f32x4, a[3]));
      *(LAS v2u*)op = v2u{cvtpk(o[0], o[1]), cvtpk(o[2], o[3])};
      asm volatile("s_cmp_eq_u32 %0, 0\n\ts_waitcnt lgkmcnt(0)\n\ts_cbranch_scc1 LB%=\n\ts_barrier\nLB%=:" :: "s"(pass) : "memory", "scc");
      STAMP(7);
      v4u bf[4];
#pragma unroll
      for (int ks = 0; ks < 4; ++ks) bf[ks] = *(LAS v4u*)(bp + ks * 64);
      __builtin_amdgcn_sched_barrier(0);
      f32x4 y = {0.f, 0.f, 0.f, 0.f};
#pragma unroll
      for (int ks = 0; ks < 4; ++ks) y = mfma32(af[ks], bf[ks], y);
      const v4u yt = tag4(y, tv);
      asm volatile("" :: "v"(yt));
      if (pass) __builtin_amdgcn_raw_buffer_store_b128(yt, rw, vy, sy, AUX_SC1);
    }
    asm volatile("Lgqa_end_b:" ::: "memory");
    STAMP(8);
    STAMP_OUT();
    return;
  }

  __builtin_amdgcn_s_setprio(2);
  unsigned tag;
  {
    const int cb = j & 63, kq = j >> 6;
    const v4i rx = mkrsrc(x, NB * DM * 2), rq = mkrsrc(wq, (XF & 8) ? 0 : DM * HQ * DH * 2);
    const unsigned vx = (lane & 7) * 1024 + (lane >> 3) * 16, vq = (lane >> 3) * 8192 + (lane & 7) * 16;
    const bool hi = (lane & 8) != 0;
    const int vs = (lane & 7) * (HKV * 8192) + (hi ? 64 : 0) + g * 16, ss = W_QG + (cb >> 3) * 8192 + kq * 2048 + (cb & 7) * 256;
    if (WQT) while ((int)(now() - t0) < WQT) {}
    dma1(rx, vx, kq * 256, L_X);
    dma1(rx, vx, kq * 256 + 128, L_X + 1024);
#pragma unroll 1
    for (int u = 0; u < 16; ++u) dma1_nt(rq, vq, (kq * 16 + u) * 65536 + cb * 128, L_W + u * 1024);
    if (XF & 64) { unsigned pt = 0; if (lane < 16) pt = __builtin_amdgcn_raw_buffer_load_b32(rw, (lane & 7) * (HKV * 8192) + (lane >> 3) * 128, ss, 0); asm volatile("" :: "v"(pt)); }
    STAMP(5);
    asm volatile("s_waitcnt vmcnt(0) lgkmcnt(0)" : "+s"(e0) : "s"(e1), "s"(e2) : "memory");
    tag = ((e0 & 0x7f7f7f7fu) + 0x51276b35u) ^ (e0 & 0x80808080u);
    STAMP(10);
    f32x4 qa[4];
#pragma unroll
    for (int t = 0; t < 4; ++t) qa[t] = f32x4{0.f, 0.f, 0.f, 0.f};
    LAS unsigned char* ap = L + L_W + (8 * g + ((lane & 15) >> 2)) * 128 + j0 * 8;
    LAS unsigned char* bp = L + L_X + g * 128 + (lane & 7) * 16;
#pragma unroll
    for (int ks = 0; ks < 4; ++ks) {
      const v4u xb = *(LAS v4u*)(bp + ks * 512);
#pragma unroll
      for (int t = 0; t < 4; ++t) qa[t] = mfma32(tr_frag(ap + ks * 4096 + t * 32, 512), xb, qa[t]);
    }
    STAMP(1);
    const v4u tq = tag_bytes(tag);
    const v4u o0 = tag4(f32x4{hi ? qa[1][0] : qa[0][0], hi ? qa[1][1] : qa[0][1], hi ? qa[1][2] : qa[0][2], hi ? qa[1][3] : qa[0][3]}, tq);
    const v4u o1 = tag4(f32x4{hi ? qa[3][0] : qa[2][0], hi ? qa[3][1] : qa[2][1], hi ? qa[3][2] : qa[2][2], hi ? qa[3][3] : qa[2][3]}, tq);
    if (!(XF & 1)) {
      __builtin_amdgcn_raw_buffer_store_b128(o0, rw, vs, ss, AUX_SC1);
      __builtin_amdgcn_raw_buffer_store_b128(o1, rw, vs + 128, ss, AUX_SC1);
    }
    asm volatile("s_nop 1" :: "v"(o0), "v"(o1) : "memory");
    STAMP(2);
    while ((int)(now() - t0) < QPOLL) __builtin_amdgcn_s_sleep(2);
    const int so = W_QG + pr * 8192;
    v4u a[8];
    for (unsigned spin = 0; spin < SPIN_CAP; ++spin) {
      RELOAD();
#pragma unroll
      for (int u = 0; u < 8; ++u) a[u] = __builtin_amdgcn_raw_buffer_load_b128(rw, (XF & 2) ? 0x7000000 : vv + (u & 3) * 1024, so + (u >> 2) * 4096, AUX_SC1) ^ tq;
      if ((XF & 3) || __all((or4(((a[0] | a[1]) | (a[2] | a[3])) | ((a[4] | a[5]) | (a[6] | a[7]))) & 0xffu) == 0u)) break;
      STAMP(15);
    }
#pragma unroll
    for (int u = 0; u < 2; ++u) {
      const f32x4 v = (__builtin_bit_cast(f32x4, a[u]) + __builtin_bit_cast(f32x4, a[2 + u])) + (__builtin_bit_cast(f32x4, a[4 + u]) + __builtin_bit_cast(f32x4, a[6 + u]));
      *(LAS v2u*)(L + L_Q + u * 512 + lane * 8) = v2u{cvtpk(v[0], v[1]), cvtpk(v[2], v[3])};
    }
    RELOAD();
    *(LAS v2u*)(L + C_READY) = v2u{1u, tag};
  }
  STAMP(3);
  if ((XF & 32) && lane == 0) {
    const unsigned a = __builtin_nontemporal_load((const GAS unsigned*)(vc + pbase + so0));
    const unsigned b2 = __builtin_nontemporal_load((const GAS unsigned*)(wo + ch * 128 * 1024 + nb * 128));
    const unsigned c = (XF & 2048) ? __builtin_nontemporal_load((const GAS unsigned*)(out + j * 32)) : 0u;
    asm volatile("" :: "v"(a), "v"(b2), "v"(c));
  }
  {
    unsigned k = now() / FB_P + 1u;
    float af = 0.f, dr = 0.f;
    int nv = 0;
#pragma unroll 1
    for (int it = 0; it < 40; ++it, ++k) {
      const unsigned tk = k * FB_P;
      while ((int)(now() - tk) < 0) __builtin_amdgcn_s_sleep(2);
      RELOAD();
      const v2u pr2 = *(LAS v2u*)(L + C_LAND);
      if (__builtin_amdgcn_readfirstlane(pr2[0]) >= NUNIT - 12) break;
      const v2u fb = __builtin_amdgcn_raw_buffer_load_b64(rw, (lane & (FB_NREP - 1)) * 8, W_FB + ((k - 1u) & 63u) * FB_SLOT, AUX_SC1);
      if (j < FB_NREP && lane == 0) {
        const float p = (float)(8 * ((int)pr2[0] + LAG)) + fminf((float)(int)(tk - pr2[1]) * (1.f / 3.4f), 8.f);
        __builtin_amdgcn_raw_buffer_store_b64(v2u{__float_as_uint(p), tag ^ k}, rw, 0, W_FB + (k & 63u) * FB_SLOT + j * 8, AUX_SC1);
      }
      const bool ok = fb[1] == (tag ^ (k - 1u));
      float ps = ok ? __uint_as_float(fb[0]) : 0.f, pc = ok ? 1.f : 0.f;
      ps += dpp<0xB1>(ps); ps += dpp<0x4E>(ps); ps += dpp<0x124>(ps); ps += dpp<0x128>(ps); ps = xrow_sum(ps);
      pc += dpp<0xB1>(pc); pc += dpp<0x4E>(pc); pc += dpp<0x124>(pc); pc += dpp<0x128>(pc); pc = xrow_sum(pc);
      if (__builtin_amdgcn_readfirstlane(pc < 48.f)) continue;
      const float pbar = ps * __builtin_amdgcn_rcpf(pc);
      const float as = (float)(int)(tk - FB_P - t0) - pbar * TL0 - (float)(DELTA + PHASE / 2);
      if (nv == 0) af = as;
      else {
        const float er = as - af;
        if (nv >= FB_I0) dr = fminf(fmaxf(dr + FB_KI * fminf(er, 30.f), -6.f), 30.f);
        af += dr + FB_G * er;
      }
      ++nv;
      *(LAS unsigned*)(L + C_FREE + 4 + j0 * 8) = t0 + (unsigned)(int)af;
    }
  }
  v4u tb = tag_bytes(tag);
  const __amdgpu_buffer_rsrc_t rout = __builtin_amdgcn_make_buffer_rsrc((void*)out, 0, NB * DM * 2, 0x00020000);
#pragma unroll 1
  for (int pass = (DRY & 2) ? 0 : 1; pass < 2; ++pass) {
    unsigned vl = pass ? vv : 0x40000000u, vs = (pass && (lane & 28) == 0) ? j0 * 8 + (lane >> 5) * 4 : 0x40000000u;
    unsigned cm = pass ? 0xffu : 0u;
    asm volatile("" : "+v"(vl), "+v"(vs), "+v"(tb), "+s"(cm));
    if (pass) {
      const v4i ro = mkrsrc(wo, HQ * DH * DM * 2);
      const unsigned vo = ((lane >> 5) + 8 * ((lane >> 3) & 3)) * 1024 + (lane & 7) * 16;
      while (lds_word(L + C_LAND) < WOPOS) __builtin_amdgcn_s_sleep(1);
#pragma unroll 1
      for (int u = 0; u < 16; ++u) dma1_nt(ro, vo, (ch * 128 + 32 * ((u >> 1) & 3) + 4 * (u >> 3) + 2 * (u & 1)) * 1024 + nb * 128, L_W + u * 1024);
      WAITVM(0);
      *(LAS int*)(L + C_WO) = 1;
      STAMP(11);
      BARRIER();
      STAMP(7);
      const unsigned tp = now() + D2;
      while ((int)(now() - tp) < 0) {}
    }
    v4u a0, a1;
    for (unsigned spin = 0; spin < SPIN_CAP; ++spin) {
      RELOAD();
      a0 = __builtin_amdgcn_raw_buffer_load_b128(rw, vl, W_YB + j * 2048, AUX_SC1) ^ tb;
      a1 = __builtin_amdgcn_raw_buffer_load_b128(rw, vl + 1024, W_YB + j * 2048, AUX_SC1) ^ tb;
      if (__all((or4(a0 | a1) & cm) == 0u)) break;
    }
    STAMP(8);
    const f32x4 y = __builtin_bit_cast(f32x4, a0) + __builtin_bit_cast(f32x4, a1);
    const auto u02 = __builtin_amdgcn_permlane32_swap(__float_as_uint(y[0]), __float_as_uint(y[2]), false, false);
    const auto u13 = __builtin_amdgcn_permlane32_swap(__float_as_uint(y[1]), __float_as_uint(y[3]), false, false);
    float e = __uint_as_float(u02[0]) + __uint_as_float(u02[1]), f = __uint_as_float(u13[0]) + __uint_as_float(u13[1]);
    e += dpp<0x124>(e); f += dpp<0x124>(f); e += dpp<0x128>(e); f += dpp<0x128>(f);
    const auto ue = __builtin_amdgcn_permlane16_swap(__float_as_uint(e), __float_as_uint(e), false, false);
    const auto uf = __builtin_amdgcn_permlane16_swap(__float_as_uint(f), __float_as_uint(f), false, false);
    __builtin_amdgcn_raw_buffer_store_b32(cvtpk(__uint_as_float(ue[0]) + __uint_as_float(ue[1]), __uint_as_float(uf[0]) + __uint_as_float(uf[1])), rout, vs, j * 32, OUTAUX);
  }
  if (j == 0 && lane == 0) __builtin_amdgcn_raw_buffer_store_b32(tag, rw, 0, W_E, AUX_SC1);
  asm volatile("Lgqa_end_c:" ::: "memory");
  STAMP(9);
  STAMP_OUT();
}

extern "C" void kernel_launch(void* const* d_in, const int* in_sizes, int n_in, void* d_out, int out_size, void* d_ws, size_t ws_size,
                              hipStream_t stream) {
  gqa_fused<<<NWG, NTHR, 0, stream>>>((const unsigned char*)d_in[0], (const unsigned char*)d_in[1], (const unsigned char*)d_in[2], (const unsigned char*)d_in[3],
                                      (const unsigned char*)d_in[4], (unsigned char*)d_out, (unsigned char*)d_ws TUNE_PASS);
}
```

```cpp
#include <hip/hip_runtime.h>
#include <hip/hip_bf16.h>
#include <stdint.h>

#define GAS __attribute__((address_space(1)))
#define LAS __attribute__((address_space(3)))
typedef unsigned v4u __attribute__((ext_vector_type(4)));
typedef unsigned v2u __attribute__((ext_vector_type(2)));
typedef int v4i __attribute__((ext_vector_type(4)));
typedef float f32x4 __attribute__((ext_vector_type(4)));
typedef float f32x32 __attribute__((ext_vector_type(32)));
typedef short s16x4 __attribute__((ext_vector_type(4)));
typedef __bf16 bf16x8 __attribute__((ext_vector_type(8)));

constexpr int NB = 8, DM = 512, HQ = 32, HKV = 8, SK = 8192, DH = 128;
constexpr int NWG = 256, NTHR = 384;
constexpr int NUNIT = 128;
constexpr int SLOT = 8192;
constexpr float C2 = 0.088388347648318440f * 1.4426950408889634f;

#define NREG 4
#define NFIRST 4
#define WIN 24
#define UPREP 112
#define NFIN 3
#define MPOS 1
#define MLROW 8
#define NCH 2
#define WIN0 16
#define FB_NREP 32
#define XF 1312
#define TUNE_ARG
#define TUNE_PASS
#define TL0 3.39f
#define UC16 ((unsigned)(TL0 * 128.f))
#define DELTA 10
#define DELTA2 2
#define DSW 650
#define FBEND 104
#define UW0 32
#define HOLD0 30
#define HOLD 30
#define MINSP 20
#define LINE0 65
#define LATE 40
#define MINSP2 16
#define PHASE 28
#define FB_P 192u
#define FB_G 0.55f
#define FB_KI 0.f
#define FB_I0 3
#define WQT 0
#define WQSP 12
#define QPOLL 560
#define WOPOS 125
#define D1 30
#define D1G 134
#define GPOS 126
#define D2 40
constexpr unsigned SPIN_CAP = 1u << 20;
constexpr int LAG = (WIN + 7) / 8;

constexpr int L_Q = 16 * SLOT, L_ACC = L_Q + 1024, L_PL = L_ACC + 4 * 2048, L_DG = L_PL + 2048, L_OT = L_DG + 16, OT_ROW = 272, L_ML = L_OT + 8 * OT_ROW, L_CTL = (L_ML + 144 + 63) / 64 * 64;
constexpr int L_X = L_ACC, L_W = L_CTL + 64, L_TOT = L_W + 16384;
constexpr int C_FREE = L_CTL, C_LAND = L_CTL + 32, C_READY = L_CTL + 40, C_TAG = L_CTL + 44, C_NM = L_CTL + 48, C_NL = L_CTL + 52, C_NA = L_CTL + 56, C_WO = L_CTL + 60;
static_assert(L_CTL % 32 == 0 && L_TOT <= 160 * 1024, "lds map");
constexpr int W_E = 0, W_FB = 1024, FB_SLOT = 512, W_ML = W_FB + 64 * FB_SLOT + 1024, W_QG = W_ML + NWG * 128, W_PA = W_QG + NWG * 2048, PA_REC = 2048, W_YB = W_PA + NWG * PA_REC,
              W_END = W_YB + NWG * 2048;

#define STAMP(i)
#define STAMP_OUT()

#define STR_(x) #x
#define STRX_(x) STR_(x)
#define WAITVM(n) asm volatile("s_waitcnt vmcnt(" STR_(n) ")" ::: "memory")
#define BARRIER() asm volatile("s_waitcnt lgkmcnt(0)\n\ts_barrier" ::: "memory")
#define RELOAD() asm volatile("" ::: "memory")
constexpr int AUX_SC1 = 16;

__device__ __forceinline__ unsigned now() { return (unsigned)__builtin_amdgcn_s_memrealtime(); }
__device__ __forceinline__ unsigned cvtpk(float lo, float hi) {
  typedef __bf16 bf16x2_t __attribute__((ext_vector_type(2)));
  typedef float f32x2_t __attribute__((ext_vector_type(2)));
  return __builtin_bit_cast(unsigned, __builtin_convertvector(f32x2_t{lo, hi}, bf16x2_t));
}
template <int CTRL> __device__ __forceinline__ unsigned dppu(unsigned x) { return (unsigned)__builtin_amdgcn_mov_dpp((int)x, CTRL, 0xf, 0xf, true); }
template <int CTRL> __device__ __forceinline__ float dpp(float x) { return __uint_as_float(dppu<CTRL>(__float_as_uint(x))); }
__device__ __forceinline__ float xrow_max(float x) {
  auto s = __builtin_amdgcn_permlane16_swap(__float_as_uint(x), __float_as_uint(x), false, false);
  x = fmaxf(__uint_as_float(s[0]), __uint_as_float(s[1]));
  auto t = __builtin_amdgcn_permlane32_swap(__float_as_uint(x), __float_as_uint(x), false, false);
  return fmaxf(__uint_as_float(t[0]), __uint_as_float(t[1]));
}
__device__ __forceinline__ float xrow_sum(float x) {
  auto s = __builtin_amdgcn_permlane16_swap(__float_as_uint(x), __float_as_uint(x), false, false);
  x = __uint_as_float(s[0]) + __uint_as_float(s[1]);
  auto t = __builtin_amdgcn_permlane32_swap(__float_as_uint(x), __float_as_uint(x), false, false);
  return __uint_as_float(t[0]) + __uint_as_float(t[1]);
}
__device__ __forceinline__ v4i mkrsrc(const void* p, unsigned bytes) {
  const unsigned long long a = (unsigned long long)p;
  v4i r; r[0] = (int)(unsigned)a; r[1] = (int)((unsigned)(a >> 32) & 0xffffu); r[2] = (int)bytes; r[3] = 0x00020000; return r;
}
__device__ __forceinline__ int lds_word(LAS unsigned char* p) { RELOAD(); const int v = __builtin_amdgcn_readfirstlane(*(LAS int*)p); RELOAD(); return v; }
__device__ __forceinline__ void lds_inc(LAS unsigned char* p) {
  unsigned long long ex;
  asm volatile("s_mov_b64 %0, exec\n\ts_mov_b64 exec, 1\n\tds_add_u32 %1, %2\n\ts_mov_b64 exec, %0" : "=&s"(ex) : "v"((unsigned)(size_t)p), "v"(1u) : "memory");
}
#define DMA4(V, R, S) "buffer_load_dwordx4 " V ", " R ", " S " offen nt lds\n\tbuffer_load_dwordx4 " V ", " R ", " S " offen offset:1024 nt lds\n\t" \
                      "buffer_load_dwordx4 " V ", " R ", " S " offen offset:2048 nt lds\n\tbuffer_load_dwordx4 " V ", " R ", " S " offen offset:3072 nt lds\n\t"
__device__ __forceinline__ void issue_two(v4i r, unsigned vv, unsigned so, unsigned lds) {
  unsigned t;
  asm volatile("s_mov_b32 m0, %4\n\ts_add_u32 %0, %3, 0x1000\n\t" DMA4("%2", "%1", "%3") "s_add_u32 m0, m0, 0x1000\n\ts_nop 0\n\t" DMA4("%2", "%1", "%0")
               "s_add_u32 m0, m0, 0x1000\n\ts_add_u32 %0, %0, 0x1000\n\t" DMA4("%2", "%1", "%0") "s_add_u32 m0, m0, 0x1000\n\ts_add_u32 %0, %0, 0x1000\n\t" DMA4("%2", "%1", "%0")
               : "=&s"(t) : "s"(r), "v"(vv), "s"(so), "s"(lds) : "memory", "scc");
}
__device__ __forceinline__ void dma1_nt(v4i r, unsigned voff, unsigned soff, unsigned lds) {
  asm volatile("s_mov_b32 m0, %3\n\ts_nop 0\n\tbuffer_load_dwordx4 %1, %0, %2 offen nt lds" :: "s"(r), "v"(voff), "s"(soff), "s"(lds) : "memory");
}
__device__ __forceinline__ void dma1(v4i r, unsigned voff, unsigned soff, unsigned lds) {
  asm volatile("s_mov_b32 m0, %3\n\ts_nop 0\n\tbuffer_load_dwordx4 %1, %0, %2 offen lds" :: "s"(r), "v"(voff), "s"(soff), "s"(lds) : "memory");
}
struct Dbg { unsigned d0, d1; };
template <int W> __device__ __forceinline__ void stream_units(v4i rs, unsigned vv, unsigned u, unsigned ue, unsigned so, unsigned ra, unsigned uc16, int hold, int msp, int lat, int ms2, Dbg& dbg, int lane) {
  unsigned ta, tb, st = 8192u, fo = ((u + 1u) & 3u) * 8u + C_FREE;
#define STAMP_ASM
#define STAMP_OPS
#define STAMP_INS
  asm volatile(
      "s_mov_b32 m0, %[m0i]\n\t"
      "v_mov_b32 v2, %[ld0]\n"
      "LT%=:\n\t"
      "s_waitcnt vmcnt(%[win])\n\t"
      DMA4("%[v0]", "%[rs]", "%[so]")
      "s_add_u32 m0, m0, 0x1000\n\t"
      "s_memrealtime s[40:41]\n\t"
      DMA4("%[v1]", "%[rs]", "%[so]")
      "ds_read_b64 v[4:5], %[fo]\n\t"
      "s_add_u32 m0, m0, 0x1000\n\t"
      "s_add_u32 %[so], %[so], %[st]\n\t"
      "s_and_b32 m0, m0, 0x1ffff\n\t"
      "s_and_b32 %[so], %[so], 0x1fffff\n\t"
      "s_xor_b32 %[st], %[st], 0xc000\n\t"
      "s_add_u32 %[ra], %[ra], %[uc]\n\t"
      "v_add_u32 v2, 1, v2\n\t"
      "v_add_u32 %[fo], 8, %[fo]\n\t"
      "s_add_u32 %[u], %[u], 1\n\t"
      "v_and_b32 %[fo], %[fm], %[fo]\n\t"
      "s_waitcnt lgkmcnt(0)\n\t"
      "v_mov_b32 v3, s40\n\t"
      "v_readfirstlane_b32 %[a], v5\n\t"
      "s_lshr_b32 s41, %[ra], 4\n\t"
      "v_readfirstlane_b32 %[b], v4\n\t"
      "ds_write_b64 %[vl], v[2:3]\n\t"
      STAMP_ASM
      "s_cmp_eq_u32 %[u], %[ue]\n\t"
      "s_cbranch_scc1 LE%=\n\t"
      "s_add_u32 %[a], %[a], s41\n\t"
      "s_sub_i32 %[a], %[a], s40\n\t"
      "s_cmp_lt_i32 %[a], %[lat]\n\t"
      "s_cselect_b32 s41, %[ms2], %[msp]\n\t"
      "s_min_i32 %[a], %[a], %[hold]\n\t"
      "s_max_i32 %[a], %[a], s41\n\t"
      "s_add_u32 %[a], %[a], s40\n\t"
      "s_cmp_lt_u32 %[b], %[u]\n\t"
      "s_cbranch_scc1 LS%=\n"
      "LW%=:\n\t"
      "s_memrealtime s[40:41]\n\t"
      "s_waitcnt lgkmcnt(0)\n\t"
      "s_sub_i32 %[b], %[a], s40\n\t"
      "s_cmp_gt_i32 %[b], 0\n\t"
      "s_cbranch_scc0 LT%=\n\t"
      "s_branch LW%=\n"
      "LS%=:\n\t"
      "s_and_b32 %[b], %[u], 3\n\t"
      "s_lshl_b32 %[b], %[b], 3\n\t"
      "s_add_u32 %[b], %[b], %[fb]\n\t"
      "v_mov_b32 v5, %[b]\n"
      "LR%=:\n\t"
      "s_sleep 1\n\t"
      "ds_read_b32 v4, v5\n\t"
      "s_waitcnt lgkmcnt(0)\n\t"
      "v_readfirstlane_b32 %[b], v4\n\t"
      "s_cmp_lt_u32 %[b], %[u]\n\t"
      "s_cbranch_scc1 LR%=\n\t"
      "s_branch LW%=\n"
      "LE%=:"
      : [so] "+s"(so), [ra] "+s"(ra), [u] "+s"(u), [st] "+s"(st), [fo] "+v"(fo), [a] "=&s"(ta), [b] "=&s"(tb) STAMP_OPS
      : [rs] "s"(rs), [v0] "v"(vv), [v1] "v"(vv + 4096u), [vl] "v"((unsigned)C_LAND), [ue] "s"(ue), [uc] "s"(uc16), [hold] "s"(hold), [msp] "s"(msp), [lat] "s"(lat), [ms2] "s"(ms2),
        [m0i] "s"((u & 15u) * SLOT), [ld0] "s"(u - 1u - (W + 7) / 8), [win] "n"(W), [fm] "s"(~32u), [fb] "s"((unsigned)C_FREE) STAMP_INS
      : "memory", "scc", "vcc", "v2", "v3", "v4", "v5", "s40", "s41");
}
static_assert((C_FREE & 32) == 0, "the record pointer wraps by clearing bit 5");

__device__ __forceinline__ f32x4 mfma32(v4u a, v4u b, f32x4 c) {
  return __builtin_amdgcn_mfma_f32_16x16x32_bf16(__builtin_bit_cast(bf16x8, a), __builtin_bit_cast(bf16x8, b), c, 0, 0, 0);
}
__device__ __forceinline__ v4u tr_frag(LAS unsigned char* p, int hi) {
  const v2u lo = __builtin_bit_cast(v2u, __builtin_amdgcn_ds_read_tr16_b64_v4i16((LAS s16x4*)p));
  const v2u up = __builtin_bit_cast(v2u, __builtin_amdgcn_ds_read_tr16_b64_v4i16((LAS s16x4*)(p + hi)));
  return v4u{lo[0], lo[1], up[0], up[1]};
}
__device__ __forceinline__ float score16(const v4u* K, v4u qf, unsigned j0) {
  float sc[4];
#pragma unroll
  for (int u = 0; u < 4; ++u) {
    const f32x4 d = mfma32(K[u], qf, f32x4{0.f, 0.f, 0.f, 0.f});
    sc[u] = ((dpp<0x00>(d[0]) + dpp<0x55>(d[1])) + dpp<0xAA>(d[2])) + dpp<0xFF>(d[3]);
  }
  return (j0 & 2) ? ((j0 & 1) ? sc[3] : sc[2]) : ((j0 & 1) ? sc[1] : sc[0]);
}
__device__ __forceinline__ void p_operands(unsigned k0, unsigned k1, unsigned mask0, unsigned mask1, v4u (&bo)[4]) {
  bo[0] = v4u{dppu<0x00>(k0) & mask0, dppu<0x00>(k0) & mask1, dppu<0x55>(k0) & mask0, dppu<0x55>(k0) & mask1};
  bo[1] = v4u{dppu<0xAA>(k0) & mask0, dppu<0xAA>(k0) & mask1, dppu<0xFF>(k0) & mask0, dppu<0xFF>(k0) & mask1};
  bo[2] = v4u{dppu<0x00>(k1) & mask0, dppu<0x00>(k1) & mask1, dppu<0x55>(k1) & mask0, dppu<0x55>(k1) & mask1};
  bo[3] = v4u{dppu<0xAA>(k1) & mask0, dppu<0xAA>(k1) & mask1, dppu<0xFF>(k1) & mask0, dppu<0xFF>(k1) & mask1};
  asm volatile("" : "+v"(bo[0]), "+v"(bo[1]), "+v"(bo[2]), "+v"(bo[3]));
}
#define RD2(A, P, O0, O1) "ds_read2st64_b64 %[" A "], %[" P "] offset0:" O0 " offset1:" O1 "\n\t"
#define MF(N, C, A, B) "s_waitcnt lgkmcnt(" N ")\n\tv_mfma_f32_16x16x32_bf16 %[" C "], %[" A "], %[" B "], %[" C "]\n\t"
__device__ __forceinline__ void pv32(unsigned p, const v4u (&bo)[4], f32x4& acc0, f32x4& acc1) {
  v4u a0, a1, a2, a3, a4, a5, a6, a7;
  asm volatile(RD2("a0", "p0", "0", "2") RD2("a1", "p1", "0", "2") RD2("a2", "p0", "4", "6") RD2("a3", "p1", "4", "6")
               RD2("a4", "p0", "8", "10") RD2("a5", "p1", "8", "10") RD2("a6", "p0", "12", "14") RD2("a7", "p1", "12", "14")
               MF("7", "c0", "a0", "b0") MF("6", "c1", "a1", "b0") MF("5", "c0", "a2", "b1") MF("4", "c1", "a3", "b1")
               MF("3", "c0", "a4", "b2") MF("2", "c1", "a5", "b2") MF("1", "c0", "a6", "b3") MF("0", "c1", "a7", "b3") "s_nop 7"
      : [c0] "+v"(acc0), [c1] "+v"(acc1), [a0] "=&v"(a0), [a1] "=&v"(a1), [a2] "=&v"(a2), [a3] "=&v"(a3), [a4] "=&v"(a4), [a5] "=&v"(a5), [a6] "=&v"(a6), [a7] "=&v"(a7)
      : [p0] "v"(p), [p1] "v"(p + 8), [b0] "v"(bo[0]), [b1] "v"(bo[1]), [b2] "v"(bo[2]), [b3] "v"(bo[3]) : "memory");
}
__device__ __forceinline__ v4u tag_bytes(unsigned tag) { return v4u{tag & 0xffu, (tag >> 8) & 0xffu, (tag >> 16) & 0xffu, tag >> 24}; }
__device__ __forceinline__ v4u tag4(f32x4 v, v4u tb) { return (__builtin_bit_cast(v4u, v) & 0xffffff00u) | tb; }
__device__ __forceinline__ unsigned or4(v4u u) { return (u[0] | u[1]) | (u[2] | u[3]); }

__global__ void __launch_bounds__(NTHR, 2) gqa_fused(const unsigned char* __restrict__ x, const unsigned char* __restrict__ kc, const unsigned char* __restrict__ vc,
                                                     const unsigned char* __restrict__ wq, const unsigned char* __restrict__ wo, unsigned char* __restrict__ out,
                                                     unsigned char* ws TUNE_ARG) {
  __shared__ __attribute__((aligned(16))) unsigned char smem[L_TOT];
  LAS unsigned char* L = (LAS unsigned char*)smem;
  unsigned long long t64;
  unsigned pf, pa;
  unsigned long long pc;
  unsigned clen;
  {
    unsigned long long ex;
    asm volatile("s_memrealtime %0\n\ts_getpc_b64 %1\nLgqa_pc:\n\t"
                 "s_mov_b32 %2, Lgqa_end_a-Lgqa_pc+160\n\ts_max_u32 %2, %2, Lgqa_end_b-Lgqa_pc+160\n\ts_max_u32 %2, %2, Lgqa_end_c-Lgqa_pc+160\n\t"
                 "s_max_u32 %2, %2, Lgqa_end_d-Lgqa_pc+160\n\ts_max_u32 %2, %2, Lgqa_end_e-Lgqa_pc+160\n\t"
                 "v_lshlrev_b32 %5, 7, %6\n\tv_add_u32 %5, 0xffffc000, %5\n\tv_cmp_gt_u32 vcc, %2, %5\n\t"
                 "s_and_saveexec_b64 %3, vcc\n\ts_cbranch_execz Lgqa_nopf\n\tglobal_load_dword %4, %5, %1 offset:-32\nLgqa_nopf:\n\ts_mov_b64 exec, %3"
                 : "=&s"(t64), "=&s"(pc), "=&s"(clen), "=&s"(ex), "=&v"(pf), "=&v"(pa) : "v"(threadIdx.x) : "scc", "vcc", "memory");
  }
  const int lane = threadIdx.x & 63, wv = __builtin_amdgcn_readfirstlane(threadIdx.x >> 6);
  const int j = blockIdx.x, xg = j & 7, idx = j >> 3;
  const int yy = (xg - idx) & 7, pr = (XF & 16) ? (idx >> 2) * 8 + xg : (XF & 128) ? xg * 8 + (idx >> 2) : (XF & 256) ? (idx >> 2) * 8 + ((xg + (idx >> 2)) & 7) : 2 * idx + (yy >> 2), s = (XF & (16 | 128 | 256)) ? (idx & 3) : yy & 3;
  const int ch = 4 * xg + (idx >> 3), nb = idx & 7;
  const int g = lane >> 4;
  const unsigned so0 = (unsigned)(pr >> 1) * 65536u + s * 16384u;
  const unsigned vv = lane * 16;
  const unsigned ra0 = ((unsigned)(j * 37) & 31u) * PHASE >> 1;
  asm volatile("s_waitcnt lgkmcnt(0)" : "+s"(t64) : "s"(x), "s"(kc), "s"(vc), "s"(wq), "s"(wo), "s"(out), "s"(ws), "s"(so0), "v"(vv));
  const unsigned t0 = (unsigned)t64;
  const size_t pbase = (size_t)pr * (SK * DH * 2);
  Dbg dbg = {0u, 0u};

  const v4i rk = mkrsrc(kc + pbase, SK * DH * 2);
  if (wv == 0) {
    __builtin_amdgcn_s_setprio(3);
#pragma unroll
    for (int u = 0; u < NFIRST; u += 2) issue_two(rk, vv, (so0 + (u >> 1) * 65536u) & 0x1fffffu, u * SLOT);
    __builtin_amdgcn_sched_barrier(0);
    const unsigned keep = (unsigned)((lane & 7) - 4) >> 31;
    *(LAS v2u*)(L + L_CTL + (lane & 7) * 8) = v2u{(12u + (lane & 7)) * keep, (t0 + LINE0) * keep};
    STAMP(1);
    asm volatile("s_waitcnt lgkmcnt(0)" ::: "memory");
  }
  unsigned e0, e1, e2;
  if (wv == 1) asm volatile("s_load_dword %0, %3, 0x0\n\ts_load_dword %1, %4, 0x0\n\ts_load_dword %2, %5, %6"
                            : "=&s"(e0), "=&s"(e1), "=&s"(e2) : "s"(ws), "s"((XF & 1024) ? x : ws), "s"((XF & 1024) ? wq : ws), "s"((XF & 1024) ? (j >> 6) * (128 * 8192) + (j & 63) * 128 : 0) : "memory");
  asm volatile("s_barrier" ::: "memory");
  if (wv == 0) stream_units<WIN0>(rk, vv, NFIRST, UW0, (so0 + (NFIRST >> 1) * 65536u) & 0x1fffffu, NFIRST * UC16 + ra0, UC16, HOLD0, MINSP, -LATE, MINSP2, dbg, lane);
  const __amdgpu_buffer_rsrc_t rw = __builtin_amdgcn_make_buffer_rsrc((void*)ws, 0, W_END, 0x00020000);
  const unsigned j0 = lane & 3;
  const int h = (lane >> 2) & 3;
  const unsigned mask0 = j0 == 0 ? 0x0000ffffu : (j0 == 1 ? 0xffff0000u : 0u);
  const unsigned mask1 = j0 == 2 ? 0x0000ffffu : (j0 == 3 ? 0xffff0000u : 0u);

  if (wv == 0) {
    const v4i rv = mkrsrc(vc + pbase, SK * DH * 2);
#pragma unroll 1
    for (int pass = 0; pass < 2; ++pass) {
      const unsigned ub = pass ? 64u : (unsigned)UW0;
      while ((unsigned)lds_word(L + C_FREE + (ub & 3u) * 8) < ub) __builtin_amdgcn_s_sleep(1);
      const v4i rs = {pass ? rv[0] : rk[0], pass ? rv[1] : rk[1], rk[2], rk[3]};
      stream_units<WIN>(rs, vv, ub, pass ? (unsigned)UPREP : 64u, (so0 + (ub >> 1) * 65536u) & 0x1fffffu, ub * UC16 + ra0, UC16, HOLD, MINSP, -LATE, MINSP2, dbg, lane);
    }
    while (lds_word(L + C_FREE + 24) < 64 + 4 * MLROW + 3 + 16) __builtin_amdgcn_s_sleep(1);
    v4u bo[NFIN][4], tb;
    unsigned vo = h * 512 + g * 64 + j0 * 16;
    float scale;
    {
      v2u pl[NFIN];
#pragma unroll
      for (int k = 0; k < NFIN; ++k) pl[k] = *(LAS v2u*)(L + L_PL + (4 - NFIN + k) * 512 + lane * 8);
      tb = tag_bytes(*(LAS unsigned*)(L + C_TAG));
      scale = *(LAS float*)(L + L_ML + 128 + h * 4);
      const int fr = *(LAS int*)(L + C_FREE + (UPREP & 3) * 8);
#pragma unroll
      for (int k = 0; k < NFIN; ++k) p_operands(pl[k][0], pl[k][1], mask0, mask1, bo[k]);
      asm volatile("" : "+v"(vo), "+v"(tb), "+v"(scale));
      if (__builtin_amdgcn_readfirstlane(fr) < UPREP) { while (lds_word(L + C_FREE + (UPREP & 3) * 8) < UPREP) __builtin_amdgcn_s_sleep(1); }
    }
    stream_units<WIN>(rv, vv, UPREP, NUNIT, (so0 + ((UPREP - 64) >> 1) * 65536u) & 0x1fffffu, UPREP * UC16 + ra0, UC16, HOLD, MINSP, -LATE, MINSP2, dbg, lane);
    STAMP(2);
    const int so = W_PA + (pr * 4 + s) * PA_REC;
    f32x4 acc0 = {0.f, 0.f, 0.f, 0.f}, acc1 = {0.f, 0.f, 0.f, 0.f};
    static_assert(NFIN >= 2 && LAG <= 3, "the drain below starts at three units outstanding");
    f32x4 s0, s1;
#pragma unroll
    for (int k = 0; k < 3; ++k) {
      if (k == 0) WAITVM(24); else if (k == 1) WAITVM(16); else WAITVM(8);
      if (k < 2) *(LAS int*)(L + C_LAND) = NUNIT - 3 + k;
      if (k >= 4 - NFIN) pv32((12 + k) * SLOT + vv, bo[k >= 4 - NFIN ? k - (4 - NFIN) : 0], acc0, acc1);
      if (k == MPOS) {
        while (lds_word(L + C_NA) < 4) {}
        f32x4 a[8];
#pragma unroll
        for (int u = 0; u < 8; ++u) a[u] = *(LAS f32x4*)(L + L_ACC + vv + u * 1024);
        __builtin_amdgcn_sched_barrier(0);
        s0 = ((a[0] + a[2]) + (a[4] + a[6])) * scale; s1 = ((a[1] + a[3]) + (a[5] + a[7])) * scale;
        asm volatile("" : "+v"(s0), "+v"(s1) : "v"(14 * SLOT + vv), "v"(14 * SLOT + vv + 8), "v"(15 * SLOT + vv), "v"(15 * SLOT + vv + 8), "s"(so), "s"(0xffffff00u));
      }
    }
    STAMP(4);
    WAITVM(0);
    STAMP(3);
    pv32(15 * SLOT + vv, bo[NFIN - 1], acc0, acc1);
    const v4u o0 = tag4(acc0 * scale + s0, tb), o1 = tag4(acc1 * scale + s1, tb);
    __builtin_amdgcn_raw_buffer_store_b128(o0, rw, vo, so, AUX_SC1);
    __builtin_amdgcn_raw_buffer_store_b128(o1, rw, vo + 256, so, AUX_SC1);
    asm volatile("s_nop 1" :: "v"(o0), "v"(o1) : "memory");
    *(LAS int*)(L + C_LAND) = NUNIT;
    asm volatile("Lgqa_end_a:" ::: "memory");
    STAMP(5);
    STAMP_OUT();
    return;
  }

  if (wv >= 2) {
    const int w = wv - 2;
    const unsigned vk = (h * 16 + g * 4 + j0) * 16;
    asm volatile("s_waitcnt vmcnt(0)" :: "v"(pf), "v"(pa) : "memory");
    f32x32 sc;
    float m;
    {
      v4u P[NREG][8];
#pragma unroll
      for (int r = 0; r < NREG; ++r) {
        while (lds_word(L + C_LAND) <= 4 * r + w) __builtin_amdgcn_s_sleep(2);
#pragma unroll
        for (int u = 0; u < 8; ++u) P[r][u] = *(LAS v4u*)(L + ((4 * r + w) & 15) * SLOT + vk + u * 1024);
        asm volatile("" :: "v"(P[r][0]), "v"(P[r][1]), "v"(P[r][2]), "v"(P[r][3]), "v"(P[r][4]), "v"(P[r][5]), "v"(P[r][6]), "v"(P[r][7]) : "memory");
        *(LAS int*)(L + C_FREE + w * 8) = 4 * r + w + 16;
      }
      STAMP(1);
      while (lds_word(L + C_READY) == 0) __builtin_amdgcn_s_sleep(2);
      STAMP(2);
      const v4u qf = *(LAS v4u*)(L + L_Q + vk);
      m = -INFINITY;
#pragma unroll
      for (int r = 0; r < NREG; ++r) {
        const float s0 = score16(&P[r][0], qf, j0), s1 = score16(&P[r][4], qf, j0);
        sc[2 * r] = s0; sc[2 * r + 1] = s1; m = fmaxf(m, fmaxf(s0, s1));
      }
#pragma unroll 1
      for (int n = NREG; n < 16; ++n) {
        while (lds_word(L + C_LAND) <= 4 * n + w) __builtin_amdgcn_s_sleep(2);
        v4u K[8];
#pragma unroll
        for (int u = 0; u < 8; ++u) K[u] = *(LAS v4u*)(L + ((4 * n + w) & 15) * SLOT + vk + u * 1024);
        asm volatile("" :: "v"(K[0]), "v"(K[1]), "v"(K[2]), "v"(K[3]), "v"(K[4]), "v"(K[5]), "v"(K[6]), "v"(K[7]) : "memory");
        *(LAS int*)(L + C_FREE + w * 8) = 4 * n + w + 16;
        const float s0 = score16(&K[0], qf, j0), s1 = score16(&K[4], qf, j0);
        sc[2 * n] = s0; sc[2 * n + 1] = s1; m = fmaxf(m, fmaxf(s0, s1));
      }
    }
    STAMP(10);
    const v4u tb = tag_bytes((unsigned)lds_word(L + C_TAG));
    m = fmaxf(m, dpp<0xB1>(m)); m = fmaxf(m, dpp<0x4E>(m)); m = xrow_max(m);
    *(LAS float*)(L + L_ML + h * 16 + w * 4) = m;
    lds_inc(L + C_NM);
    while (lds_word(L + C_NM) < 4) __builtin_amdgcn_s_sleep(1);
    {
      const f32x4 mw = *(LAS f32x4*)(L + L_ML + h * 16);
      m = fmaxf(fmaxf(mw[0], mw[1]), fmaxf(mw[2], mw[3]));
      float l = 0.f;
#pragma unroll
      for (int n = 0; n < 32; ++n) { const float p = __builtin_amdgcn_exp2f((sc[n] - m) * C2); l += p; sc[n] = __uint_as_float(cvtpk(p, p)); }
      l += dpp<0xB1>(l); l += dpp<0x4E>(l); l = xrow_sum(l);
      *(LAS float*)(L + L_ML + 64 + h * 16 + w * 4) = l;
      *(LAS v2u*)(L + L_PL + w * 512 + lane * 8) = v2u{__float_as_uint(sc[30]), __float_as_uint(sc[31])};
      lds_inc(L + C_NL);
    }
    STAMP(11);
    f32x4 acc0 = {0.f, 0.f, 0.f, 0.f}, acc1 = {0.f, 0.f, 0.f, 0.f};
#pragma unroll 1
    for (int n = 0; n < (w < 4 - NFIN ? 16 : 15); ++n) {
      if (w == 3 && n == 1) {
        while (lds_word(L + C_NL) < 4) __builtin_amdgcn_s_sleep(1);
        const f32x4 lw = *(LAS f32x4*)(L + L_ML + 64 + h * 16);
        if (lane < 16 && j0 == 0) __builtin_amdgcn_raw_buffer_store_b128(tag4(f32x4{m, (lw[0] + lw[1]) + (lw[2] + lw[3]), __uint_as_float(__float_as_uint(m) << 24), 0.f}, tb), rw, h * 16, W_ML + (pr * 4 + s) * 128, AUX_SC1);
      }
      if (w == 3 && n == MLROW) {
        v4u ml;
        for (unsigned spin = 0; spin < SPIN_CAP; ++spin) {
          RELOAD();
          ml = __builtin_amdgcn_raw_buffer_load_b128(rw, j0 * 128 + h * 16, W_ML + pr * 512, AUX_SC1) ^ tb;
          if (__all((or4(ml) & 0xffu) == 0u)) break;
        }
        const float ms = __uint_as_float(ml[0] | (ml[2] >> 24));
        float M = fmaxf(ms, dpp<0xB1>(ms)); M = fmaxf(M, dpp<0x4E>(M));
        float ls = __uint_as_float(ml[1]) * __builtin_amdgcn_exp2f((ms - M) * C2);
        ls += dpp<0xB1>(ls); ls += dpp<0x4E>(ls);
        if (lane < 16 && j0 == 0) *(LAS float*)(L + L_ML + 128 + h * 4) = __builtin_amdgcn_exp2f((m - M) * C2) * __builtin_amdgcn_rcpf(ls);
      }
      v4u bo[4];
      p_operands(__float_as_uint(sc[2 * n]), __float_as_uint(sc[2 * n + 1]), mask0, mask1, bo);
      while (lds_word(L + C_LAND) <= 64 + 4 * n + w) __builtin_amdgcn_s_sleep(1);
      pv32(((4 * n + w) & 15) * SLOT + vv, bo, acc0, acc1);
      *(LAS int*)(L + C_FREE + w * 8) = 64 + 4 * n + w + 16;
    }
    *(LAS f32x4*)(L + L_ACC + w * 2048 + vv) = acc0; *(LAS f32x4*)(L + L_ACC + w * 2048 + 1024 + vv) = acc1;
    lds_inc(L + C_NA);
    STAMP(3);
    const int bb = 2 * w + (lane >> 5), c = lane & 31;
    unsigned vg = bb * (HKV * 4 * PA_REC) + c * 16, vy = (lane & 7) * (32 * 2048) + g * 16;
    const int sg = W_PA + (ch >> 2) * (4 * PA_REC) + (ch & 3) * 512, sy = W_YB + (nb * 4 + w) * 2048 + ch * 64;
    LAS unsigned char* ap = L + L_W + (8 * g + ((lane & 15) >> 2)) * 128 + (16 * w + 4 * j0) * 2;
    LAS unsigned char* op = L + L_OT + bb * OT_ROW + c * 8;
    LAS unsigned char* bp = L + L_OT + (lane & 7) * OT_ROW + g * 16;
    if (lane & 8) vy = 0x40000000u;
    v4u tv = tb;
    asm volatile("" : "+v"(vg), "+v"(vy), "+v"(ap), "+v"(op), "+v"(bp), "+v"(tv) : "s"(sg), "s"(sy));
    while (lds_word(L + C_LAND) < GPOS) __builtin_amdgcn_s_sleep(1);
    STAMP(5);
    {
      const unsigned tp = now() + D1;
      unsigned tg = (unsigned)lds_word(L + C_FREE + 4) + (NUNIT * UC16 >> 4) + D1G;
      tg = tp + (unsigned)min(max((int)(tg - tp), 0), 200);
      while ((int)(now() - tg) < 0) {}
    }
    STAMP(4);
    v4u a[4], af[4], bf[4];
#pragma unroll
    for (int u = 0; u < 4; ++u) a[u] = __builtin_amdgcn_raw_buffer_load_b128(rw, vg, sg + u * PA_REC, AUX_SC1);
    while (lds_word(L + C_WO) == 0) {}
#pragma unroll
    for (int ks = 0; ks < 4; ++ks) af[ks] = tr_frag(ap + ks * 4096, 512);
    asm volatile("" : "+v"(af[0]), "+v"(af[1]), "+v"(af[2]), "+v"(af[3]));
#pragma unroll
    for (int u = 0; u < 4; ++u) a[u] ^= tv;
    asm volatile("" : "+v"(a[0]), "+v"(a[1]), "+v"(a[2]), "+v"(a[3]));
    if (__builtin_expect(!__all((or4((a[0] | a[1]) | (a[2] | a[3])) & 0xffu) == 0u), 0)) {
      for (unsigned spin = 0; spin < SPIN_CAP; ++spin) {
        RELOAD();
#pragma unroll
        for (int u = 0; u < 4; ++u) a[u] = __builtin_amdgcn_raw_buffer_load_b128(rw, vg, sg + u * PA_REC, AUX_SC1) ^ tv;
        if (__all((or4((a[0] | a[1]) | (a[2] | a[3])) & 0xffu) == 0u)) break;
      }
      asm volatile("Lgqa_end_d:" ::: "memory");
    }
    STAMP(6);
    const f32x4 o = (__builtin_bit_cast(f32x4, a[0]) + __builtin_bit_cast(f32x4, a[1])) + (__builtin_bit_cast(f32x4, a[2]) + __builtin_bit_cast(f32x4, a[3]));
    *(LAS v2u*)op = v2u{cvtpk(o[0], o[1]), cvtpk(o[2], o[3])};
    BARRIER();
    STAMP(7);
#pragma unroll
    for (int ks = 0; ks < 4; ++ks) bf[ks] = *(LAS v4u*)(bp + ks * 64);
    __builtin_amdgcn_sched_barrier(0);
    f32x4 y = {0.f, 0.f, 0.f, 0.f};
#pragma unroll
    for (int ks = 0; ks < 4; ++ks) y = mfma32(af[ks], bf[ks], y);
    __builtin_amdgcn_raw_buffer_store_b128(tag4(y, tv), rw, vy, sy, AUX_SC1);
    asm volatile("Lgqa_end_b:" ::: "memory");
    STAMP(8);
    STAMP_OUT();
    return;
  }

  __builtin_amdgcn_s_setprio(2);
  unsigned tag;
  {
    constexpr int NKS = 4 / NCH;
    const bool prod = (idx & (NCH - 1)) == NCH - 1;
    const int pid = (idx / NCH) * 8 + xg, cb = pid & 63, kq = pid >> 6;
    asm volatile("s_waitcnt lgkmcnt(0)" : "+s"(e0) : "s"(e1), "s"(e2) : "memory");
    v4u tq;
    if (prod) {
      const v4i rx = mkrsrc(x, NB * DM * 2), rq = mkrsrc(wq, (XF & 8) ? 0 : DM * HQ * DH * 2);
      const unsigned vx = (lane & 7) * 1024 + (lane >> 3) * 16, vq = (lane >> 3) * 8192 + (lane & 7) * 16;
      const bool hi = (lane & 8) != 0;
      const int vs = (lane & 7) * (HKV * NKS * 2048) + (hi ? 64 : 0) + g * 16, ss = W_QG + (cb >> 3) * (NKS * 2048) + kq * 2048 + (cb & 7) * 256;
      f32x4 qa[4];
#pragma unroll
      for (int t = 0; t < 4; ++t) qa[t] = f32x4{0.f, 0.f, 0.f, 0.f};
      LAS unsigned char* ap = L + L_W + (8 * g + ((lane & 15) >> 2)) * 128 + j0 * 8;
      LAS unsigned char* bp = L + L_X + g * 128 + (lane & 7) * 16;
#pragma unroll 1
      for (int c = kq * NCH; c < kq * NCH + NCH; ++c) {
        dma1(rx, vx, c * 256, L_X);
        dma1(rx, vx, c * 256 + 128, L_X + 1024);
#pragma unroll 1
        for (int u = 0; u < 16; ++u) dma1_nt(rq, vq, (c * 16 + u) * 65536 + cb * 128, L_W + u * 1024);
        STAMP(5);
        WAITVM(0);
        STAMP(10);
#pragma unroll
        for (int ks = 0; ks < 4; ++ks) {
          const v4u xb = *(LAS v4u*)(bp + ks * 512);
#pragma unroll
          for (int t = 0; t < 4; ++t) qa[t] = mfma32(tr_frag(ap + ks * 4096 + t * 32, 512), xb, qa[t]);
        }
        asm volatile("" : "+v"(qa[0]), "+v"(qa[1]), "+v"(qa[2]), "+v"(qa[3]) :: "memory");
      }
      STAMP(1);
      tag = ((e0 & 0x7f7f7f7fu) + 0x51276b35u) ^ (e0 & 0x80808080u);
      tq = tag_bytes(tag);
      const v4u o0 = tag4(f32x4{hi ? qa[1][0] : qa[0][0], hi ? qa[1][1] : qa[0][1], hi ? qa[1][2] : qa[0][2], hi ? qa[1][3] : qa[0][3]}, tq);
      const v4u o1 = tag4(f32x4{hi ? qa[3][0] : qa[2][0], hi ? qa[3][1] : qa[2][1], hi ? qa[3][2] : qa[2][2], hi ? qa[3][3] : qa[2][3]}, tq);
      if (!(XF & 1)) {
        __builtin_amdgcn_raw_buffer_store_b128(o0, rw, vs, ss, AUX_SC1);
        __builtin_amdgcn_raw_buffer_store_b128(o1, rw, vs + 128, ss, AUX_SC1);
      }
      asm volatile("s_nop 1" :: "v"(o0), "v"(o1) : "memory");
      STAMP(2);
    }
    tag = ((e0 & 0x7f7f7f7fu) + 0x51276b35u) ^ (e0 & 0x80808080u);
    tq = tag_bytes(tag);
    while ((int)(now() - t0) < QPOLL) __builtin_amdgcn_s_sleep(2);
    const int so = W_QG + pr * (NKS * 2048);
    v4u a[2 * NKS];
    for (unsigned spin = 0; spin < SPIN_CAP; ++spin) {
      RELOAD();
      v4u bad = {0u, 0u, 0u, 0u};
#pragma unroll
      for (int u = 0; u < 2 * NKS; ++u) { a[u] = __builtin_amdgcn_raw_buffer_load_b128(rw, vv + (u & 3) * 1024, so + (u >> 2) * 4096, AUX_SC1) ^ tq; bad |= a[u]; }
      if ((XF & 3) || __all((or4(bad) & 0xffu) == 0u)) break;
      STAMP(15);
    }
#pragma unroll
    for (int u = 0; u < 2; ++u) {
      f32x4 v = __builtin_bit_cast(f32x4, a[u]);
#pragma unroll
      for (int p = 1; p < NKS; ++p) v += __builtin_bit_cast(f32x4, a[2 * p + u]);
      *(LAS v2u*)(L + L_Q + u * 512 + lane * 8) = v2u{cvtpk(v[0], v[1]), cvtpk(v[2], v[3])};
    }
    RELOAD();
    *(LAS v2u*)(L + C_READY) = v2u{1u, tag};
  }
  STAMP(3);
  if ((XF & 32) && lane == 0) {
    const unsigned a = __builtin_nontemporal_load((const GAS unsigned*)(vc + pbase + so0));
    const unsigned b2 = __builtin_nontemporal_load((const GAS unsigned*)(wo + ch * 128 * 1024 + nb * 128));
    const unsigned c = (XF & 2048) ? __builtin_nontemporal_load((const GAS unsigned*)(out + j * 32)) : 0u;
    asm volatile("" :: "v"(a), "v"(b2), "v"(c));
  }
  {
    unsigned k = now() / FB_P + 1u;
    float af = 0.f, dr = 0.f;
    int nv = 0;
#pragma unroll 1
    for (int it = 0; it < 40; ++it, ++k) {
      const unsigned tk = k * FB_P;
      while ((int)(now() - tk) < 0) __builtin_amdgcn_s_sleep(2);
      RELOAD();
      const v2u pr2 = *(LAS v2u*)(L + C_LAND);
      if (__builtin_amdgcn_readfirstlane(pr2[0]) >= FBEND) break;
      const v2u fb = __builtin_amdgcn_raw_buffer_load_b64(rw, (lane & (FB_NREP - 1)) * 8, W_FB + ((k - 1u) & 63u) * FB_SLOT, AUX_SC1);
      if (j < FB_NREP && lane == 0) {
        const float p = (float)(8 * ((int)pr2[0] + LAG)) + fminf((float)(int)(tk - pr2[1]) * (1.f / 3.4f), 8.f);
        __builtin_amdgcn_raw_buffer_store_b64(v2u{__float_as_uint(p), tag ^ k}, rw, 0, W_FB + (k & 63u) * FB_SLOT + j * 8, AUX_SC1);
      }
      const bool ok = fb[1] == (tag ^ (k - 1u));
      float ps = ok ? __uint_as_float(fb[0]) : 0.f, pc = ok ? 1.f : 0.f;
      ps += dpp<0xB1>(ps); ps += dpp<0x4E>(ps); ps += dpp<0x124>(ps); ps += dpp<0x128>(ps); ps = xrow_sum(ps);
      pc += dpp<0xB1>(pc); pc += dpp<0x4E>(pc); pc += dpp<0x124>(pc); pc += dpp<0x128>(pc); pc = xrow_sum(pc);
      if (__builtin_amdgcn_readfirstlane(pc < 48.f)) continue;
      const float pbar = ps * __builtin_amdgcn_rcpf(pc);
      const float as = (float)(int)(tk - FB_P - t0) - pbar * TL0 - (float)((pbar < (float)DSW ? DELTA : DELTA2) + PHASE / 2);
      if (nv == 0) af = as;
      else {
        const float er = as - af;
        if (nv >= FB_I0) dr = fminf(fmaxf(dr + FB_KI * fminf(er, 30.f), -6.f), 30.f);
        af += dr + FB_G * er;
      }
      ++nv;
      *(LAS unsigned*)(L + C_FREE + 4 + j0 * 8) = t0 + (unsigned)(int)af;
    }
  }
  {
    const v4i ro = mkrsrc(wo, HQ * DH * DM * 2);
    const unsigned vo = ((lane >> 5) + 8 * ((lane >> 3) & 3)) * 1024 + (lane & 7) * 16;
    while (lds_word(L + C_LAND) < WOPOS) __builtin_amdgcn_s_sleep(1);
#pragma unroll 1
    for (int u = 0; u < 16; ++u) dma1_nt(ro, vo, (ch * 128 + 32 * ((u >> 1) & 3) + 4 * (u >> 3) + 2 * (u & 1)) * 1024 + nb * 128, L_W + u * 1024);
    const __amdgpu_buffer_rsrc_t rout = __builtin_amdgcn_make_buffer_rsrc((void*)out, 0, NB * DM * 2, 0x00020000);
    v4u tb = tag_bytes(tag);
    unsigned vs = (lane & 28) == 0 ? j0 * 8 + (lane >> 5) * 4 : 0x40000000u;
    asm volatile("s_waitcnt vmcnt(0)" : "+v"(vs), "+v"(tb) :: "memory");
    *(LAS int*)(L + C_WO) = 1;
    STAMP(11);
    BARRIER();
    STAMP(7);
    const unsigned tp = now() + D2;
    while ((int)(now() - tp) < 0) {}
    v4u a0 = __builtin_amdgcn_raw_buffer_load_b128(rw, vv, W_YB + j * 2048, AUX_SC1) ^ tb;
    v4u a1 = __builtin_amdgcn_raw_buffer_load_b128(rw, vv + 1024, W_YB + j * 2048, AUX_SC1) ^ tb;
    asm volatile("" : "+v"(a0), "+v"(a1));
    if (__builtin_expect(!__all((or4(a0 | a1) & 0xffu) == 0u), 0)) {
      for (unsigned spin = 0; spin < SPIN_CAP; ++spin) {
        RELOAD();
        a0 = __builtin_amdgcn_raw_buffer_load_b128(rw, vv, W_YB + j * 2048, AUX_SC1) ^ tb;
        a1 = __builtin_amdgcn_raw_buffer_load_b128(rw, vv + 1024, W_YB + j * 2048, AUX_SC1) ^ tb;
        if (__all((or4(a0 | a1) & 0xffu) == 0u)) break;
      }
      asm volatile("Lgqa_end_e:" ::: "memory");
    }
    STAMP(8);
    const f32x4 y = __builtin_bit_cast(f32x4, a0) + __builtin_bit_cast(f32x4, a1);
    const auto u02 = __builtin_amdgcn_permlane32_swap(__float_as_uint(y[0]), __float_as_uint(y[2]), false, false);
    const auto u13 = __builtin_amdgcn_permlane32_swap(__float_as_uint(y[1]), __float_as_uint(y[3]), false, false);
    float e = __uint_as_float(u02[0]) + __uint_as_float(u02[1]), f = __uint_as_float(u13[0]) + __uint_as_float(u13[1]);
    e += dpp<0x124>(e); f += dpp<0x124>(f); e += dpp<0x128>(e); f += dpp<0x128>(f);
    const auto ue = __builtin_amdgcn_permlane16_swap(__float_as_uint(e), __float_as_uint(e), false, false);
    const auto uf = __builtin_amdgcn_permlane16_swap(__float_as_uint(f), __float_as_uint(f), false, false);
    __builtin_amdgcn_raw_buffer_store_b32(cvtpk(__uint_as_float(ue[0]) + __uint_as_float(ue[1]), __uint_as_float(uf[0]) + __uint_as_float(uf[1])), rout, vs, j * 32, 0);
  }
  if (j == 0 && lane == 0) __builtin_amdgcn_raw_buffer_store_b32(tag, rw, 0, W_E, AUX_SC1);
  asm volatile("Lgqa_end_c:" ::: "memory");
  STAMP(9);
  STAMP_OUT();
}

extern "C" void kernel_launch(void* const* d_in, const int* in_sizes, int n_in, void* d_out, int out_size, void* d_ws, size_t ws_size,
                              hipStream_t stream) {
  gqa_fused<<<NWG, NTHR, 0, stream>>>((const unsigned char*)d_in[0], (const unsigned char*)d_in[1], (const unsigned char*)d_in[2], (const unsigned char*)d_in[3],
                                      (const unsigned char*)d_in[4], (unsigned char*)d_out, (unsigned char*)d_ws TUNE_PASS);
}
```

```cpp
#define DIAG 901
#define DSIGN 1
#include <hip/hip_runtime.h>
#include <hip/hip_bf16.h>
#include <stdint.h>

#define GAS __attribute__((address_space(1)))
#define LAS __attribute__((address_space(3)))
typedef unsigned v4u __attribute__((ext_vector_type(4)));
typedef unsigned v2u __attribute__((ext_vector_type(2)));
typedef int v4i __attribute__((ext_vector_type(4)));
typedef float f32x4 __attribute__((ext_vector_type(4)));
typedef float f32x32 __attribute__((ext_vector_type(32)));
typedef short s16x4 __attribute__((ext_vector_type(4)));
typedef __bf16 bf16x8 __attribute__((ext_vector_type(8)));

constexpr int NB = 8, DM = 512, HQ = 32, HKV = 8, SK = 8192, DH = 128;
constexpr int NWG = 256, NTHR = 384;
constexpr int NUNIT = 128;
constexpr int SLOT = 8192;
constexpr float C2 = 0.088388347648318440f * 1.4426950408889634f;

#define NREG 4
#define NFIRST 4
#define WIN 24
#define UPREP 112
#define NFIN 3
#define MPOS 1
#define MLROW 8
#define NCH 2
#define WIN0 16
#define FB_NREP 32
#define XF 1312
#define TUNE_ARG
#define TUNE_PASS
#define TL0 3.39f
#define UC16 ((unsigned)(TL0 * 128.f))
#define DELTA 10
#define DELTA2 2
#define DSW 650
#define FBEND 104
#define UW0 32
#define HOLD0 30
#define HOLD 30
#define MINSP 20
#define LINE0 65
#define LATE 40
#define MINSP2 16
#define PHASE 28
#define FB_P 192u
#define FB_G 0.55f
#define FB_KI 0.f
#define FB_I0 3
#define WQT 0
#define WQSP 12
#define QPOLL 560
#define WOPOS 125
#define D1 30
#define D1G 134
#define GPOS 126
#define D2 40
constexpr unsigned SPIN_CAP = 1u << 20;
constexpr int LAG = (WIN + 7) / 8;

constexpr int L_Q = 16 * SLOT, L_ACC = L_Q + 1024, L_PL = L_ACC + 4 * 2048, L_DG = L_PL + 2048, L_OT = L_DG + 16, OT_ROW = 272, L_ML = L_OT + 8 * OT_ROW, L_CTL = (L_ML + 144 + 63) / 64 * 64;
constexpr int L_X = L_ACC, L_W = L_CTL + 64, L_TOT = L_W + 16384;
constexpr int C_FREE = L_CTL, C_LAND = L_CTL + 32, C_READY = L_CTL + 40, C_TAG = L_CTL + 44, C_NM = L_CTL + 48, C_NL = L_CTL + 52, C_NA = L_CTL + 56, C_WO = L_CTL + 60;
static_assert(L_CTL % 32 == 0 && L_TOT <= 160 * 1024, "lds map");
constexpr int W_E = 0, W_FB = 1024, FB_SLOT = 512, W_ML = W_FB + 64 * FB_SLOT + 1024, W_QG = W_ML + NWG * 128, W_PA = W_QG + NWG * 2048, PA_REC = 2048, W_YB = W_PA + NWG * PA_REC,
              W_END = W_YB + NWG * 2048;

#define STAMP(i) do { if (DIAG % 100 == (i) && DIAG / 100 == (wv > 2 ? 2 : wv) && lane == 0) (void)__hip_atomic_fetch_max((LAS int*)(L + L_DG), (int)(now() - t0), __ATOMIC_RELAXED, __HIP_MEMORY_SCOPE_WORKGROUP); } while (0)
#define STAMP_OUT() do { if (wv == 1) { while ((int)(now() - t0) < 9000) __builtin_amdgcn_s_sleep(8); const int x_ = lds_word(L + L_DG); const int tg_ = DSIGN >= 0 ? 10000 + DSIGN * 10 * x_ : 70000 - 10 * x_; while ((int)(now() - t0) < tg_) __builtin_amdgcn_s_sleep(4); } } while (0)

#define STR_(x) #x
#define STRX_(x) STR_(x)
#define WAITVM(n) asm volatile("s_waitcnt vmcnt(" STR_(n) ")" ::: "memory")
#define BARRIER() asm volatile("s_waitcnt lgkmcnt(0)\n\ts_barrier" ::: "memory")
#define RELOAD() asm volatile("" ::: "memory")
constexpr int AUX_SC1 = 16;

__device__ __forceinline__ unsigned now() { return (unsigned)__builtin_amdgcn_s_memrealtime(); }
__device__ __forceinline__ unsigned cvtpk(float lo, float hi) {
  typedef __bf16 bf16x2_t __attribute__((ext_vector_type(2)));
  typedef float f32x2_t __attribute__((ext_vector_type(2)));
  return __builtin_bit_cast(unsigned, __builtin_convertvector(f32x2_t{lo, hi}, bf16x2_t));
}
template <int CTRL> __device__ __forceinline__ unsigned dppu(unsigned x) { return (unsigned)__builtin_amdgcn_mov_dpp((int)x, CTRL, 0xf, 0xf, true); }
template <int CTRL> __device__ __forceinline__ float dpp(float x) { return __uint_as_float(dppu<CTRL>(__float_as_uint(x))); }
__device__ __forceinline__ float xrow_max(float x) {
  auto s = __builtin_amdgcn_permlane16_swap(__float_as_uint(x), __float_as_uint(x), false, false);
  x = fmaxf(__uint_as_float(s[0]), __uint_as_float(s[1]));
  auto t = __builtin_amdgcn_permlane32_swap(__float_as_uint(x), __float_as_uint(x), false, false);
  return fmaxf(__uint_as_float(t[0]), __uint_as_float(t[1]));
}
__device__ __forceinline__ float xrow_sum(float x) {
  auto s = __builtin_amdgcn_permlane16_swap(__float_as_uint(x), __float_as_uint(x), false, false);
  x = __uint_as_float(s[0]) + __uint_as_float(s[1]);
  auto t = __builtin_amdgcn_permlane32_swap(__float_as_uint(x), __float_as_uint(x), false, false);
  return __uint_as_float(t[0]) + __uint_as_float(t[1]);
}
__device__ __forceinline__ v4i mkrsrc(const void* p, unsigned bytes) {
  const unsigned long long a = (unsigned long long)p;
  v4i r; r[0] = (int)(unsigned)a; r[1] = (int)((unsigned)(a >> 32) & 0xffffu); r[2] = (int)bytes; r[3] = 0x00020000; return r;
}
__device__ __forceinline__ int lds_word(LAS unsigned char* p) { RELOAD(); const int v = __builtin_amdgcn_readfirstlane(*(LAS int*)p); RELOAD(); return v; }
__device__ __forceinline__ void lds_inc(LAS unsigned char* p) {
  unsigned long long ex;
  asm volatile("s_mov_b64 %0, exec\n\ts_mov_b64 exec, 1\n\tds_add_u32 %1, %2\n\ts_mov_b64 exec, %0" : "=&s"(ex) : "v"((unsigned)(size_t)p), "v"(1u) : "memory");
}
#define DMA4(V, R, S) "buffer_load_dwordx4 " V ", " R ", " S " offen nt lds\n\tbuffer_load_dwordx4 " V ", " R ", " S " offen offset:1024 nt lds\n\t" \
                      "buffer_load_dwordx4 " V ", " R ", " S " offen offset:2048 nt lds\n\tbuffer_load_dwordx4 " V ", " R ", " S " offen offset:3072 nt lds\n\t"
__device__ __forceinline__ void issue_two(v4i r, unsigned vv, unsigned so, unsigned lds) {
  unsigned t;
  asm volatile("s_mov_b32 m0, %4\n\ts_add_u32 %0, %3, 0x1000\n\t" DMA4("%2", "%1", "%3") "s_add_u32 m0, m0, 0x1000\n\ts_nop 0\n\t" DMA4("%2", "%1", "%0")
               "s_add_u32 m0, m0, 0x1000\n\ts_add_u32 %0, %0, 0x1000\n\t" DMA4("%2", "%1", "%0") "s_add_u32 m0, m0, 0x1000\n\ts_add_u32 %0, %0, 0x1000\n\t" DMA4("%2", "%1", "%0")
               : "=&s"(t) : "s"(r), "v"(vv), "s"(so), "s"(lds) : "memory", "scc");
}
__device__ __forceinline__ void dma1_nt(v4i r, unsigned voff, unsigned soff, unsigned lds) {
  asm volatile("s_mov_b32 m0, %3\n\ts_nop 0\n\tbuffer_load_dwordx4 %1, %0, %2 offen nt lds" :: "s"(r), "v"(voff), "s"(soff), "s"(lds) : "memory");
}
__device__ __forceinline__ void dma1(v4i r, unsigned voff, unsigned soff, unsigned lds) {
  asm volatile("s_mov_b32 m0, %3\n\ts_nop 0\n\tbuffer_load_dwordx4 %1, %0, %2 offen lds" :: "s"(r), "v"(voff), "s"(soff), "s"(lds) : "memory");
}
struct Dbg { unsigned d0, d1; };
template <int W> __device__ __forceinline__ void stream_units(v4i rs, unsigned vv, unsigned u, unsigned ue, unsigned so, unsigned ra, unsigned uc16, int hold, int msp, int lat, int ms2, Dbg& dbg, int lane) {
  unsigned ta, tb, st = 8192u, fo = ((u + 1u) & 3u) * 8u + C_FREE;
#define STAMP_ASM
#define STAMP_OPS
#define STAMP_INS
  asm volatile(
      "s_mov_b32 m0, %[m0i]\n\t"
      "v_mov_b32 v2, %[ld0]\n"
      "LT%=:\n\t"
      "s_waitcnt vmcnt(%[win])\n\t"
      DMA4("%[v0]", "%[rs]", "%[so]")
      "s_add_u32 m0, m0, 0x1000\n\t"
      "s_memrealtime s[40:41]\n\t"
      DMA4("%[v1]", "%[rs]", "%[so]")
      "ds_read_b64 v[4:5], %[fo]\n\t"
      "s_add_u32 m0, m0, 0x1000\n\t"
      "s_add_u32 %[so], %[so], %[st]\n\t"
      "s_and_b32 m0, m0, 0x1ffff\n\t"
      "s_and_b32 %[so], %[so], 0x1fffff\n\t"
      "s_xor_b32 %[st], %[st], 0xc000\n\t"
      "s_add_u32 %[ra], %[ra], %[uc]\n\t"
      "v_add_u32 v2, 1, v2\n\t"
      "v_add_u32 %[fo], 8, %[fo]\n\t"
      "s_add_u32 %[u], %[u], 1\n\t"
      "v_and_b32 %[fo], %[fm], %[fo]\n\t"
      "s_waitcnt lgkmcnt(0)\n\t"
      "v_mov_b32 v3, s40\n\t"
      "v_readfirstlane_b32 %[a], v5\n\t"
      "s_lshr_b32 s41, %[ra], 4\n\t"
      "v_readfirstlane_b32 %[b], v4\n\t"
      "ds_write_b64 %[vl], v[2:3]\n\t"
      STAMP_ASM
      "s_cmp_eq_u32 %[u], %[ue]\n\t"
      "s_cbranch_scc1 LE%=\n\t"
      "s_add_u32 %[a], %[a], s41\n\t"
      "s_sub_i32 %[a], %[a], s40\n\t"
      "s_cmp_lt_i32 %[a], %[lat]\n\t"
      "s_cselect_b32 s41, %[ms2], %[msp]\n\t"
      "s_min_i32 %[a], %[a], %[hold]\n\t"
      "s_max_i32 %[a], %[a], s41\n\t"
      "s_add_u32 %[a], %[a], s40\n\t"
      "s_cmp_lt_u32 %[b], %[u]\n\t"
      "s_cbranch_scc1 LS%=\n"
      "LW%=:\n\t"
      "s_memrealtime s[40:41]\n\t"
      "s_waitcnt lgkmcnt(0)\n\t"
      "s_sub_i32 %[b], %[a], s40\n\t"
      "s_cmp_gt_i32 %[b], 0\n\t"
      "s_cbranch_scc0 LT%=\n\t"
      "s_branch LW%=\n"
      "LS%=:\n\t"
      "s_and_b32 %[b], %[u], 3\n\t"
      "s_lshl_b32 %[b], %[b], 3\n\t"
      "s_add_u32 %[b], %[b], %[fb]\n\t"
      "v_mov_b32 v5, %[b]\n"
      "LR%=:\n\t"
      "s_sleep 1\n\t"
      "ds_read_b32 v4, v5\n\t"
      "s_waitcnt lgkmcnt(0)\n\t"
      "v_readfirstlane_b32 %[b], v4\n\t"
      "s_cmp_lt_u32 %[b], %[u]\n\t"
      "s_cbranch_scc1 LR%=\n\t"
      "s_branch LW%=\n"
      "LE%=:"
      : [so] "+s"(so), [ra] "+s"(ra), [u] "+s"(u), [st] "+s"(st), [fo] "+v"(fo), [a] "=&s"(ta), [b] "=&s"(tb) STAMP_OPS
      : [rs] "s"(rs), [v0] "v"(vv), [v1] "v"(vv + 4096u), [vl] "v"((unsigned)C_LAND), [ue] "s"(ue), [uc] "s"(uc16), [hold] "s"(hold), [msp] "s"(msp), [lat] "s"(lat), [ms2] "s"(ms2),
        [m0i] "s"((u & 15u) * SLOT), [ld0] "s"(u - 1u - (W + 7) / 8), [win] "n"(W), [fm] "s"(~32u), [fb] "s"((unsigned)C_FREE) STAMP_INS
      : "memory", "scc", "vcc", "v2", "v3", "v4", "v5", "s40", "s41");
}
static_assert((C_FREE & 32) == 0, "the record pointer wraps by clearing bit 5");

__device__ __forceinline__ f32x4 mfma32(v4u a, v4u b, f32x4 c) {
  return __builtin_amdgcn_mfma_f32_16x16x32_bf16(__builtin_bit_cast(bf16x8, a), __builtin_bit_cast(bf16x8, b), c, 0, 0, 0);
}
__device__ __forceinline__ v4u tr_frag(LAS unsigned char* p, int hi) {
  const v2u lo = __builtin_bit_cast(v2u, __builtin_amdgcn_ds_read_tr16_b64_v4i16((LAS s16x4*)p));
  const v2u up = __builtin_bit_cast(v2u, __builtin_amdgcn_ds_read_tr16_b64_v4i16((LAS s16x4*)(p + hi)));
  return v4u{lo[0], lo[1], up[0], up[1]};
}
__device__ __forceinline__ float score16(const v4u* K, v4u qf, unsigned j0) {
  float sc[4];
#pragma unroll
  for (int u = 0; u < 4; ++u) {
    const f32x4 d = mfma32(K[u], qf, f32x4{0.f, 0.f, 0.f, 0.f});
    sc[u] = ((dpp<0x00>(d[0]) + dpp<0x55>(d[1])) + dpp<0xAA>(d[2])) + dpp<0xFF>(d[3]);
  }
  return (j0 & 2) ? ((j0 & 1) ? sc[3] : sc[2]) : ((j0 & 1) ? sc[1] : sc[0]);
}
__device__ __forceinline__ void p_operands(unsigned k0, unsigned k1, unsigned mask0, unsigned mask1, v4u (&bo)[4]) {
  bo[0] = v4u{dppu<0x00>(k0) & mask0, dppu<0x00>(k0) & mask1, dppu<0x55>(k0) & mask0, dppu<0x55>(k0) & mask1};
  bo[1] = v4u{dppu<0xAA>(k0) & mask0, dppu<0xAA>(k0) & mask1, dppu<0xFF>(k0) & mask0, dppu<0xFF>(k0) & mask1};
  bo[2] = v4u{dppu<0x00>(k1) & mask0, dppu<0x00>(k1) & mask1, dppu<0x55>(k1) & mask0, dppu<0x55>(k1) & mask1};
  bo[3] = v4u{dppu<0xAA>(k1) & mask0, dppu<0xAA>(k1) & mask1, dppu<0xFF>(k1) & mask0, dppu<0xFF>(k1) & mask1};
  asm volatile("" : "+v"(bo[0]), "+v"(bo[1]), "+v"(bo[2]), "+v"(bo[3]));
}
#define RD2(A, P, O0, O1) "ds_read2st64_b64 %[" A "], %[" P "] offset0:" O0 " offset1:" O1 "\n\t"
#define MF(N, C, A, B) "s_waitcnt lgkmcnt(" N ")\n\tv_mfma_f32_16x16x32_bf16 %[" C "], %[" A "], %[" B "], %[" C "]\n\t"
__device__ __forceinline__ void pv32(unsigned p, const v4u (&bo)[4], f32x4& acc0, f32x4& acc1) {
  v4u a0, a1, a2, a3, a4, a5, a6, a7;
  asm volatile(RD2("a0", "p0", "0", "2") RD2("a1", "p1", "0", "2") RD2("a2", "p0", "4", "6") RD2("a3", "p1", "4", "6")
               RD2("a4", "p0", "8", "10") RD2("a5", "p1", "8", "10") RD2("a6", "p0", "12", "14") RD2("a7", "p1", "12", "14")
               MF("7", "c0", "a0", "b0") MF("6", "c1", "a1", "b0") MF("5", "c0", "a2", "b1") MF("4", "c1", "a3", "b1")
               MF("3", "c0", "a4", "b2") MF("2", "c1", "a5", "b2") MF("1", "c0", "a6", "b3") MF("0", "c1", "a7", "b3") "s_nop 7"
      : [c0] "+v"(acc0), [c1] "+v"(acc1), [a0] "=&v"(a0), [a1] "=&v"(a1), [a2] "=&v"(a2), [a3] "=&v"(a3), [a4] "=&v"(a4), [a5] "=&v"(a5), [a6] "=&v"(a6), [a7] "=&v"(a7)
      : [p0] "v"(p), [p1] "v"(p + 8), [b0] "v"(bo[0]), [b1] "v"(bo[1]), [b2] "v"(bo[2]), [b3] "v"(bo[3]) : "memory");
}
__device__ __forceinline__ v4u tag_bytes(unsigned tag) { return v4u{tag & 0xffu, (tag >> 8) & 0xffu, (tag >> 16) & 0xffu, tag >> 24}; }
__device__ __forceinline__ v4u tag4(f32x4 v, v4u tb) { return (__builtin_bit_cast(v4u, v) & 0xffffff00u) | tb; }
__device__ __forceinline__ unsigned or4(v4u u) { return (u[0] | u[1]) | (u[2] | u[3]); }

__global__ void __launch_bounds__(NTHR, 2) gqa_fused(const unsigned char* __restrict__ x, const unsigned char* __restrict__ kc, const unsigned char* __restrict__ vc,
                                                     const unsigned char* __restrict__ wq, const unsigned char* __restrict__ wo, unsigned char* __restrict__ out,
                                                     unsigned char* ws TUNE_ARG) {
  __shared__ __attribute__((aligned(16))) unsigned char smem[L_TOT];
  LAS unsigned char* L = (LAS unsigned char*)smem;
  unsigned long long t64;
  unsigned pf, pa;
  unsigned long long pc;
  unsigned clen;
  {
    unsigned long long ex;
    asm volatile("s_memrealtime %0\n\ts_getpc_b64 %1\nLgqa_pc:\n\t"
                 "s_mov_b32 %2, Lgqa_end_a-Lgqa_pc+160\n\ts_max_u32 %2, %2, Lgqa_end_b-Lgqa_pc+160\n\ts_max_u32 %2, %2, Lgqa_end_c-Lgqa_pc+160\n\t"
                 "s_max_u32 %2, %2, Lgqa_end_d-Lgqa_pc+160\n\ts_max_u32 %2, %2, Lgqa_end_e-Lgqa_pc+160\n\t"
                 "v_lshlrev_b32 %5, 7, %6\n\tv_add_u32 %5, 0xffffc000, %5\n\tv_cmp_gt_u32 vcc, %2, %5\n\t"
                 "s_and_saveexec_b64 %3, vcc\n\ts_cbranch_execz Lgqa_nopf\n\tglobal_load_dword %4, %5, %1 offset:-32\nLgqa_nopf:\n\ts_mov_b64 exec, %3"
                 : "=&s"(t64), "=&s"(pc), "=&s"(clen), "=&s"(ex), "=&v"(pf), "=&v"(pa) : "v"(threadIdx.x) : "scc", "vcc", "memory");
  }
  const int lane = threadIdx.x & 63, wv = __builtin_amdgcn_readfirstlane(threadIdx.x >> 6);
  const int j = blockIdx.x, xg = j & 7, idx = j >> 3;
  const int yy = (xg - idx) & 7, pr = (XF & 16) ? (idx >> 2) * 8 + xg : (XF & 128) ? xg * 8 + (idx >> 2) : (XF & 256) ? (idx >> 2) * 8 + ((xg + (idx >> 2)) & 7) : 2 * idx + (yy >> 2), s = (XF & (16 | 128 | 256)) ? (idx & 3) : yy & 3;
  const int ch = 4 * xg + (idx >> 3), nb = idx & 7;
  const int g = lane >> 4;
  const unsigned so0 = (unsigned)(pr >> 1) * 65536u + s * 16384u;
  const unsigned vv = lane * 16;
  const unsigned ra0 = ((unsigned)(j * 37) & 31u) * PHASE >> 1;
  asm volatile("s_waitcnt lgkmcnt(0)" : "+s"(t64) : "s"(x), "s"(kc), "s"(vc), "s"(wq), "s"(wo), "s"(out), "s"(ws), "s"(so0), "v"(vv));
  const unsigned t0 = (unsigned)t64;
  const size_t pbase = (size_t)pr * (SK * DH * 2);
  Dbg dbg = {0u, 0u};

  const v4i rk = mkrsrc(kc + pbase, SK * DH * 2);
  if (wv == 0) {
    __builtin_amdgcn_s_setprio(3);
#pragma unroll
    for (int u = 0; u < NFIRST; u += 2) issue_two(rk, vv, (so0 + (u >> 1) * 65536u) & 0x1fffffu, u * SLOT);
    __builtin_amdgcn_sched_barrier(0);
    const unsigned keep = (unsigned)((lane & 7) - 4) >> 31;
    *(LAS v2u*)(L + L_CTL + (lane & 7) * 8) = v2u{(12u + (lane & 7)) * keep, (t0 + LINE0) * keep};
    *(LAS int*)(L + L_DG) = 0;
    STAMP(1);
    asm volatile("s_waitcnt lgkmcnt(0)" ::: "memory");
  }
  unsigned e0, e1, e2;
  if (wv == 1) asm volatile("s_load_dword %0, %3, 0x0\n\ts_load_dword %1, %4, 0x0\n\ts_load_dword %2, %5, %6"
                            : "=&s"(e0), "=&s"(e1), "=&s"(e2) : "s"(ws), "s"((XF & 1024) ? x : ws), "s"((XF & 1024) ? wq : ws), "s"((XF & 1024) ? (j >> 6) * (128 * 8192) + (j & 63) * 128 : 0) : "memory");
  asm volatile("s_barrier" ::: "memory");
  if (wv == 0) stream_units<WIN0>(rk, vv, NFIRST, UW0, (so0 + (NFIRST >> 1) * 65536u) & 0x1fffffu, NFIRST * UC16 + ra0, UC16, HOLD0, MINSP, -LATE, MINSP2, dbg, lane);
  const __amdgpu_buffer_rsrc_t rw = __builtin_amdgcn_make_buffer_rsrc((void*)ws, 0, W_END, 0x00020000);
  const unsigned j0 = lane & 3;
  const int h = (lane >> 2) & 3;
  const unsigned mask0 = j0 == 0 ? 0x0000ffffu : (j0 == 1 ? 0xffff0000u : 0u);
  const unsigned mask1 = j0 == 2 ? 0x0000ffffu : (j0 == 3 ? 0xffff0000u : 0u);

  if (wv == 0) {
    const v4i rv = mkrsrc(vc + pbase, SK * DH * 2);
#pragma unroll 1
    for (int pass = 0; pass < 2; ++pass) {
      const unsigned ub = pass ? 64u : (unsigned)UW0;
      while ((unsigned)lds_word(L + C_FREE + (ub & 3u) * 8) < ub) __builtin_amdgcn_s_sleep(1);
      const v4i rs = {pass ? rv[0] : rk[0], pass ? rv[1] : rk[1], rk[2], rk[3]};
      stream_units<WIN>(rs, vv, ub, pass ? (unsigned)UPREP : 64u, (so0 + (ub >> 1) * 65536u) & 0x1fffffu, ub * UC16 + ra0, UC16, HOLD, MINSP, -LATE, MINSP2, dbg, lane);
    }
    while (lds_word(L + C_FREE + 24) < 64 + 4 * MLROW + 3 + 16) __builtin_amdgcn_s_sleep(1);
    v4u bo[NFIN][4], tb;
    unsigned vo = h * 512 + g * 64 + j0 * 16;
    float scale;
    {
      v2u pl[NFIN];
#pragma unroll
      for (int k = 0; k < NFIN; ++k) pl[k] = *(LAS v2u*)(L + L_PL + (4 - NFIN + k) * 512 + lane * 8);
      tb = tag_bytes(*(LAS unsigned*)(L + C_TAG));
      scale = *(LAS float*)(L + L_ML + 128 + h * 4);
      const int fr = *(LAS int*)(L + C_FREE + (UPREP & 3) * 8);
#pragma unroll
      for (int k = 0; k < NFIN; ++k) p_operands(pl[k][0], pl[k][1], mask0, mask1, bo[k]);
      asm volatile("" : "+v"(vo), "+v"(tb), "+v"(scale));
      if (__builtin_amdgcn_readfirstlane(fr) < UPREP) { while (lds_word(L + C_FREE + (UPREP & 3) * 8) < UPREP) __builtin_amdgcn_s_sleep(1); }
    }
    stream_units<WIN>(rv, vv, UPREP, NUNIT, (so0 + ((UPREP - 64) >> 1) * 65536u) & 0x1fffffu, UPREP * UC16 + ra0, UC16, HOLD, MINSP, -LATE, MINSP2, dbg, lane);
    STAMP(2);
    const int so = W_PA + (pr * 4 + s) * PA_REC;
    f32x4 acc0 = {0.f, 0.f, 0.f, 0.f}, acc1 = {0.f, 0.f, 0.f, 0.f};
    static_assert(NFIN >= 2 && LAG <= 3, "the drain below starts at three units outstanding");
    f32x4 s0, s1;
#pragma unroll
    for (int k = 0; k < 3; ++k) {
      if (k == 0) WAITVM(24); else if (k == 1) WAITVM(16); else WAITVM(8);
      if (k < 2) *(LAS int*)(L + C_LAND) = NUNIT - 3 + k;
      if (k >= 4 - NFIN) pv32((12 + k) * SLOT + vv, bo[k >= 4 - NFIN ? k - (4 - NFIN) : 0], acc0, acc1);
      if (k == MPOS) {
        while (lds_word(L + C_NA) < 4) {}
        f32x4 a[8];
#pragma unroll
        for (int u = 0; u < 8; ++u) a[u] = *(LAS f32x4*)(L + L_ACC + vv + u * 1024);
        __builtin_amdgcn_sched_barrier(0);
        s0 = ((a[0] + a[2]) + (a[4] + a[6])) * scale; s1 = ((a[1] + a[3]) + (a[5] + a[7])) * scale;
        asm volatile("" : "+v"(s0), "+v"(s1) : "v"(14 * SLOT + vv), "v"(14 * SLOT + vv + 8), "v"(15 * SLOT + vv), "v"(15 * SLOT + vv + 8), "s"(so), "s"(0xffffff00u));
      }
    }
    STAMP(4);
    WAITVM(0);
    STAMP(3);
    pv32(15 * SLOT + vv, bo[NFIN - 1], acc0, acc1);
    const v4u o0 = tag4(acc0 * scale + s0, tb), o1 = tag4(acc1 * scale + s1, tb);
    __builtin_amdgcn_raw_buffer_store_b128(o0, rw, vo, so, AUX_SC1);
    __builtin_amdgcn_raw_buffer_store_b128(o1, rw, vo + 256, so, AUX_SC1);
    asm volatile("s_nop 1" :: "v"(o0), "v"(o1) : "memory");
    *(LAS int*)(L + C_LAND) = NUNIT;
    asm volatile("Lgqa_end_a:" ::: "memory");
    STAMP(5);
    STAMP_OUT();
    return;
  }

  if (wv >= 2) {
    const int w = wv - 2;
    const unsigned vk = (h * 16 + g * 4 + j0) * 16;
    asm volatile("s_waitcnt vmcnt(0)" :: "v"(pf), "v"(pa) : "memory");
    f32x32 sc;
    float m;
    {
      v4u P[NREG][8];
#pragma unroll
      for (int r = 0; r < NREG; ++r) {
        while (lds_word(L + C_LAND) <= 4 * r + w) __builtin_amdgcn_s_sleep(2);
#pragma unroll
        for (int u = 0; u < 8; ++u) P[r][u] = *(LAS v4u*)(L + ((4 * r + w) & 15) * SLOT + vk + u * 1024);
        asm volatile("" :: "v"(P[r][0]), "v"(P[r][1]), "v"(P[r][2]), "v"(P[r][3]), "v"(P[r][4]), "v"(P[r][5]), "v"(P[r][6]), "v"(P[r][7]) : "memory");
        *(LAS int*)(L + C_FREE + w * 8) = 4 * r + w + 16;
      }
      STAMP(1);
      while (lds_word(L + C_READY) == 0) __builtin_amdgcn_s_sleep(2);
      STAMP(2);
      const v4u qf = *(LAS v4u*)(L + L_Q + vk);
      m = -INFINITY;
#pragma unroll
      for (int r = 0; r < NREG; ++r) {
        const float s0 = score16(&P[r][0], qf, j0), s1 = score16(&P[r][4], qf, j0);
        sc[2 * r] = s0; sc[2 * r + 1] = s1; m = fmaxf(m, fmaxf(s0, s1));
      }
#pragma unroll 1
      for (int n = NREG; n < 16; ++n) {
        while (lds_word(L + C_LAND) <= 4 * n + w) __builtin_amdgcn_s_sleep(2);
        v4u K[8];
#pragma unroll
        for (int u = 0; u < 8; ++u) K[u] = *(LAS v4u*)(L + ((4 * n + w) & 15) * SLOT + vk + u * 1024);
        asm volatile("" :: "v"(K[0]), "v"(K[1]), "v"(K[2]), "v"(K[3]), "v"(K[4]), "v"(K[5]), "v"(K[6]), "v"(K[7]) : "memory");
        *(LAS int*)(L + C_FREE + w * 8) = 4 * n + w + 16;
        const float s0 = score16(&K[0], qf, j0), s1 = score16(&K[4], qf, j0);
        sc[2 * n] = s0; sc[2 * n + 1] = s1; m = fmaxf(m, fmaxf(s0, s1));
      }
    }
    STAMP(10);
    const v4u tb = tag_bytes((unsigned)lds_word(L + C_TAG));
    m = fmaxf(m, dpp<0xB1>(m)); m = fmaxf(m, dpp<0x4E>(m)); m = xrow_max(m);
    *(LAS float*)(L + L_ML + h * 16 + w * 4) = m;
    lds_inc(L + C_NM);
    while (lds_word(L + C_NM) < 4) __builtin_amdgcn_s_sleep(1);
    {
      const f32x4 mw = *(LAS f32x4*)(L + L_ML + h * 16);
      m = fmaxf(fmaxf(mw[0], mw[1]), fmaxf(mw[2], mw[3]));
      float l = 0.f;
#pragma unroll
      for (int n = 0; n < 32; ++n) { const float p = __builtin_amdgcn_exp2f((sc[n] - m) * C2); l += p; sc[n] = __uint_as_float(cvtpk(p, p)); }
      l += dpp<0xB1>(l); l += dpp<0x4E>(l); l = xrow_sum(l);
      *(LAS float*)(L + L_ML + 64 + h * 16 + w * 4) = l;
      *(LAS v2u*)(L + L_PL + w * 512 + lane * 8) = v2u{__float_as_uint(sc[30]), __float_as_uint(sc[31])};
      lds_inc(L + C_NL);
    }
    STAMP(11);
    f32x4 acc0 = {0.f, 0.f, 0.f, 0.f}, acc1 = {0.f, 0.f, 0.f, 0.f};
#pragma unroll 1
    for (int n = 0; n < (w < 4 - NFIN ? 16 : 15); ++n) {
      if (w == 3 && n == 1) {
        while (lds_word(L + C_NL) < 4) __builtin_amdgcn_s_sleep(1);
        const f32x4 lw = *(LAS f32x4*)(L + L_ML + 64 + h * 16);
        if (lane < 16 && j0 == 0) __builtin_amdgcn_raw_buffer_store_b128(tag4(f32x4{m, (lw[0] + lw[1]) + (lw[2] + lw[3]), __uint_as_float(__float_as_uint(m) << 24), 0.f}, tb), rw, h * 16, W_ML + (pr * 4 + s) * 128, AUX_SC1);
      }
      if (w == 3 && n == MLROW) {
        v4u ml;
        for (unsigned spin = 0; spin < SPIN_CAP; ++spin) {
          RELOAD();
          ml = __builtin_amdgcn_raw_buffer_load_b128(rw, j0 * 128 + h * 16, W_ML + pr * 512, AUX_SC1) ^ tb;
          if (__all((or4(ml) & 0xffu) == 0u)) break;
        }
        const float ms = __uint_as_float(ml[0] | (ml[2] >> 24));
        float M = fmaxf(ms, dpp<0xB1>(ms)); M = fmaxf(M, dpp<0x4E>(M));
        float ls = __uint_as_float(ml[1]) * __builtin_amdgcn_exp2f((ms - M) * C2);
        ls += dpp<0xB1>(ls); ls += dpp<0x4E>(ls);
        if (lane < 16 && j0 == 0) *(LAS float*)(L + L_ML + 128 + h * 4) = __builtin_amdgcn_exp2f((m - M) * C2) * __builtin_amdgcn_rcpf(ls);
      }
      v4u bo[4];
      p_operands(__float_as_uint(sc[2 * n]), __float_as_uint(sc[2 * n + 1]), mask0, mask1, bo);
      while (lds_word(L + C_LAND) <= 64 + 4 * n + w) __builtin_amdgcn_s_sleep(1);
      pv32(((4 * n + w) & 15) * SLOT + vv, bo, acc0, acc1);
      *(LAS int*)(L + C_FREE + w * 8) = 64 + 4 * n + w + 16;
    }
    *(LAS f32x4*)(L + L_ACC + w * 2048 + vv) = acc0; *(LAS f32x4*)(L + L_ACC + w * 2048 + 1024 + vv) = acc1;
    lds_inc(L + C_NA);
    STAMP(3);
    const int bb = 2 * w + (lane >> 5), c = lane & 31;
    unsigned vg = bb * (HKV * 4 * PA_REC) + c * 16, vy = (lane & 7) * (32 * 2048) + g * 16;
    const int sg = W_PA + (ch >> 2) * (4 * PA_REC) + (ch & 3) * 512, sy = W_YB + (nb * 4 + w) * 2048 + ch * 64;
    LAS unsigned char* ap = L + L_W + (8 * g + ((lane & 15) >> 2)) * 128 + (16 * w + 4 * j0) * 2;
    LAS unsigned char* op = L + L_OT + bb * OT_ROW + c * 8;
    LAS unsigned char* bp = L + L_OT + (lane & 7) * OT_ROW + g * 16;
    if (lane & 8) vy = 0x40000000u;
    v4u tv = tb;
    asm volatile("" : "+v"(vg), "+v"(vy), "+v"(ap), "+v"(op), "+v"(bp), "+v"(tv) : "s"(sg), "s"(sy));
    while (lds_word(L + C_LAND) < GPOS) __builtin_amdgcn_s_sleep(1);
    STAMP(5);
    {
      const unsigned tp = now() + D1;
      unsigned tg = (unsigned)lds_word(L + C_FREE + 4) + (NUNIT * UC16 >> 4) + D1G;
      tg = tp + (unsigned)min(max((int)(tg - tp), 0), 200);
      while ((int)(now() - tg) < 0) {}
    }
    STAMP(4);
    v4u a[4], af[4], bf[4];
#pragma unroll
    for (int u = 0; u < 4; ++u) a[u] = __builtin_amdgcn_raw_buffer_load_b128(rw, vg, sg + u * PA_REC, AUX_SC1);
    while (lds_word(L + C_WO) == 0) {}
#pragma unroll
    for (int ks = 0; ks < 4; ++ks) af[ks] = tr_frag(ap + ks * 4096, 512);
    asm volatile("" : "+v"(af[0]), "+v"(af[1]), "+v"(af[2]), "+v"(af[3]));
#pragma unroll
    for (int u = 0; u < 4; ++u) a[u] ^= tv;
    asm volatile("" : "+v"(a[0]), "+v"(a[1]), "+v"(a[2]), "+v"(a[3]));
    if (__builtin_expect(!__all((or4((a[0] | a[1]) | (a[2] | a[3])) & 0xffu) == 0u), 0)) {
      for (unsigned spin = 0; spin < SPIN_CAP; ++spin) {
        RELOAD();
#pragma unroll
        for (int u = 0; u < 4; ++u) a[u] = __builtin_amdgcn_raw_buffer_load_b128(rw, vg, sg + u * PA_REC, AUX_SC1) ^ tv;
        if (__all((or4((a[0] | a[1]) | (a[2] | a[3])) & 0xffu) == 0u)) break;
      }
      asm volatile("Lgqa_end_d:" ::: "memory");
    }
    STAMP(6);
    const f32x4 o = (__builtin_bit_cast(f32x4, a[0]) + __builtin_bit_cast(f32x4, a[1])) + (__builtin_bit_cast(f32x4, a[2]) + __builtin_bit_cast(f32x4, a[3]));
    *(LAS v2u*)op = v2u{cvtpk(o[0], o[1]), cvtpk(o[2], o[3])};
    BARRIER();
    STAMP(7);
#pragma unroll
    for (int ks = 0; ks < 4; ++ks) bf[ks] = *(LAS v4u*)(bp + ks * 64);
    __builtin_amdgcn_sched_barrier(0);
    f32x4 y = {0.f, 0.f, 0.f, 0.f};
#pragma unroll
    for (int ks = 0; ks < 4; ++ks) y = mfma32(af[ks], bf[ks], y);
    __builtin_amdgcn_raw_buffer_store_b128(tag4(y, tv), rw, vy, sy, AUX_SC1);
    asm volatile("Lgqa_end_b:" ::: "memory");
    STAMP(8);
    STAMP_OUT();
    return;
  }

  __builtin_amdgcn_s_setprio(2);
  unsigned tag;
  {
    constexpr int NKS = 4 / NCH;
    const bool prod = (idx & (NCH - 1)) == NCH - 1;
    const int pid = (idx / NCH) * 8 + xg, cb = pid & 63, kq = pid >> 6;
    asm volatile("s_waitcnt lgkmcnt(0)" : "+s"(e0) : "s"(e1), "s"(e2) : "memory");
    v4u tq;
    if (prod) {
      const v4i rx = mkrsrc(x, NB * DM * 2), rq = mkrsrc(wq, (XF & 8) ? 0 : DM * HQ * DH * 2);
      const unsigned vx = (lane & 7) * 1024 + (lane >> 3) * 16, vq = (lane >> 3) * 8192 + (lane & 7) * 16;
      const bool hi = (lane & 8) != 0;
      const int vs = (lane & 7) * (HKV * NKS * 2048) + (hi ? 64 : 0) + g * 16, ss = W_QG + (cb >> 3) * (NKS * 2048) + kq * 2048 + (cb & 7) * 256;
      f32x4 qa[4];
#pragma unroll
      for (int t = 0; t < 4; ++t) qa[t] = f32x4{0.f, 0.f, 0.f, 0.f};
      LAS unsigned char* ap = L + L_W + (8 * g + ((lane & 15) >> 2)) * 128 + j0 * 8;
      LAS unsigned char* bp = L + L_X + g * 128 + (lane & 7) * 16;
#pragma unroll 1
      for (int c = kq * NCH; c < kq * NCH + NCH; ++c) {
        dma1(rx, vx, c * 256, L_X);
        dma1(rx, vx, c * 256 + 128, L_X + 1024);
#pragma unroll 1
        for (int u = 0; u < 16; ++u) dma1_nt(rq, vq, (c * 16 + u) * 65536 + cb * 128, L_W + u * 1024);
        STAMP(5);
        WAITVM(0);
        STAMP(10);
#pragma unroll
        for (int ks = 0; ks < 4; ++ks) {
          const v4u xb = *(LAS v4u*)(bp + ks * 512);
#pragma unroll
          for (int t = 0; t < 4; ++t) qa[t] = mfma32(tr_frag(ap + ks * 4096 + t * 32, 512), xb, qa[t]);
        }
        asm volatile("" : "+v"(qa[0]), "+v"(qa[1]), "+v"(qa[2]), "+v"(qa[3]) :: "memory");
      }
      STAMP(1);
      tag = ((e0 & 0x7f7f7f7fu) + 0x51276b35u) ^ (e0 & 0x80808080u);
      tq = tag_bytes(tag);
      const v4u o0 = tag4(f32x4{hi ? qa[1][0] : qa[0][0], hi ? qa[1][1] : qa[0][1], hi ? qa[1][2] : qa[0][2], hi ? qa[1][3] : qa[0][3]}, tq);
      const v4u o1 = tag4(f32x4{hi ? qa[3][0] : qa[2][0], hi ? qa[3][1] : qa[2][1], hi ? qa[3][2] : qa[2][2], hi ? qa[3][3] : qa[2][3]}, tq);
      if (!(XF & 1)) {
        __builtin_amdgcn_raw_buffer_store_b128(o0, rw, vs, ss, AUX_SC1);
        __builtin_amdgcn_raw_buffer_store_b128(o1, rw, vs + 128, ss, AUX_SC1);
      }
      asm volatile("s_nop 1" :: "v"(o0), "v"(o1) : "memory");
      STAMP(2);
    }
    tag = ((e0 & 0x7f7f7f7fu) + 0x51276b35u) ^ (e0 & 0x80808080u);
    tq = tag_bytes(tag);
    while ((int)(now() - t0) < QPOLL) __builtin_amdgcn_s_sleep(2);
    const int so = W_QG + pr * (NKS * 2048);
    v4u a[2 * NKS];
    for (unsigned spin = 0; spin < SPIN_CAP; ++spin) {
      RELOAD();
      v4u bad = {0u, 0u, 0u, 0u};
#pragma unroll
      for (int u = 0; u < 2 * NKS; ++u) { a[u] = __builtin_amdgcn_raw_buffer_load_b128(rw, vv + (u & 3) * 1024, so + (u >> 2) * 4096, AUX_SC1) ^ tq; bad |= a[u]; }
      if ((XF & 3) || __all((or4(bad) & 0xffu) == 0u)) break;
      STAMP(15);
    }
#pragma unroll
    for (int u = 0; u < 2; ++u) {
      f32x4 v = __builtin_bit_cast(f32x4, a[u]);
#pragma unroll
      for (int p = 1; p < NKS; ++p) v += __builtin_bit_cast(f32x4, a[2 * p + u]);
      *(LAS v2u*)(L + L_Q + u * 512 + lane * 8) = v2u{cvtpk(v[0], v[1]), cvtpk(v[2], v[3])};
    }
    RELOAD();
    *(LAS v2u*)(L + C_READY) = v2u{1u, tag};
  }
  STAMP(3);
  if ((XF & 32) && lane == 0) {
    const unsigned a = __builtin_nontemporal_load((const GAS unsigned*)(vc + pbase + so0));
    const unsigned b2 = __builtin_nontemporal_load((const GAS unsigned*)(wo + ch * 128 * 1024 + nb * 128));
    const unsigned c = (XF & 2048) ? __builtin_nontemporal_load((const GAS unsigned*)(out + j * 32)) : 0u;
    asm volatile("" :: "v"(a), "v"(b2), "v"(c));
  }
  {
    unsigned k = now() / FB_P + 1u;
    float af = 0.f, dr = 0.f;
    int nv = 0;
    float asp = 0.f;
    if (DIAG == 900 && lane == 0) __hip_atomic_store((GAS unsigned*)(ws + (3 << 20)) + j, t0, __ATOMIC_RELAXED, __HIP_MEMORY_SCOPE_AGENT);
#pragma unroll 1
    for (int it = 0; it < 40; ++it, ++k) {
      const unsigned tk = k * FB_P;
      while ((int)(now() - tk) < 0) __builtin_amdgcn_s_sleep(2);
      RELOAD();
      const v2u pr2 = *(LAS v2u*)(L + C_LAND);
      if (__builtin_amdgcn_readfirstlane(pr2[0]) >= FBEND) break;
      const v2u fb = __builtin_amdgcn_raw_buffer_load_b64(rw, (lane & (FB_NREP - 1)) * 8, W_FB + ((k - 1u) & 63u) * FB_SLOT, AUX_SC1);
      if (j < FB_NREP && lane == 0) {
        const float p = (float)(8 * ((int)pr2[0] + LAG)) + fminf((float)(int)(tk - pr2[1]) * (1.f / 3.4f), 8.f);
        __builtin_amdgcn_raw_buffer_store_b64(v2u{__float_as_uint(p), tag ^ k}, rw, 0, W_FB + (k & 63u) * FB_SLOT + j * 8, AUX_SC1);
      }
      const bool ok = fb[1] == (tag ^ (k - 1u));
      float ps = ok ? __uint_as_float(fb[0]) : 0.f, pc = ok ? 1.f : 0.f;
      ps += dpp<0xB1>(ps); ps += dpp<0x4E>(ps); ps += dpp<0x124>(ps); ps += dpp<0x128>(ps); ps = xrow_sum(ps);
      pc += dpp<0xB1>(pc); pc += dpp<0x4E>(pc); pc += dpp<0x124>(pc); pc += dpp<0x128>(pc); pc = xrow_sum(pc);
      if (__builtin_amdgcn_readfirstlane(pc < 48.f)) continue;
      const float pbar = ps * __builtin_amdgcn_rcpf(pc);
      const float as = (float)(int)(tk - FB_P - t0) - pbar * TL0 - (float)((pbar < (float)DSW ? DELTA : DELTA2) + PHASE / 2);
      if (nv == 0) af = as;
      else {
        const float er = as - af;
        if (nv >= FB_I0) dr = fminf(fmaxf(dr + FB_KI * fminf(er, 30.f), -6.f), 30.f);
        af += dr + FB_G * er;
      }
      { static_assert(true, ""); float xv = DIAG == 901 ? (nv ? as - asp : 0.f) : DIAG == 902 ? (nv == 0 ? as : 0.f) : af;
        if (DIAG == 903) *(LAS int*)(L + L_DG) = (int)xv; else if (lane == 0) (void)__hip_atomic_fetch_max((LAS int*)(L + L_DG), (int)xv, __ATOMIC_RELAXED, __HIP_MEMORY_SCOPE_WORKGROUP);
        asp = as; }
      ++nv;
      *(LAS unsigned*)(L + C_FREE + 4 + j0 * 8) = t0 + (unsigned)(int)af;
    }
  }
  {
    const v4i ro = mkrsrc(wo, HQ * DH * DM * 2);
    const unsigned vo = ((lane >> 5) + 8 * ((lane >> 3) & 3)) * 1024 + (lane & 7) * 16;
    while (lds_word(L + C_LAND) < WOPOS) __builtin_amdgcn_s_sleep(1);
#pragma unroll 1
    for (int u = 0; u < 16; ++u) dma1_nt(ro, vo, (ch * 128 + 32 * ((u >> 1) & 3) + 4 * (u >> 3) + 2 * (u & 1)) * 1024 + nb * 128, L_W + u * 1024);
    const __amdgpu_buffer_rsrc_t rout = __builtin_amdgcn_make_buffer_rsrc((void*)out, 0, NB * DM * 2, 0x00020000);
    v4u tb = tag_bytes(tag);
    unsigned vs = (lane & 28) == 0 ? j0 * 8 + (lane >> 5) * 4 : 0x40000000u;
    asm volatile("s_waitcnt vmcnt(0)" : "+v"(vs), "+v"(tb) :: "memory");
    *(LAS int*)(L + C_WO) = 1;
    STAMP(11);
    BARRIER();
    STAMP(7);
    const unsigned tp = now() + D2;
    while ((int)(now() - tp) < 0) {}
    v4u a0 = __builtin_amdgcn_raw_buffer_load_b128(rw, vv, W_YB + j * 2048, AUX_SC1) ^ tb;
    v4u a1 = __builtin_amdgcn_raw_buffer_load_b128(rw, vv + 1024, W_YB + j * 2048, AUX_SC1) ^ tb;
    asm volatile("" : "+v"(a0), "+v"(a1));
    if (__builtin_expect(!__all((or4(a0 | a1) & 0xffu) == 0u), 0)) {
      for (unsigned spin = 0; spin < SPIN_CAP; ++spin) {
        RELOAD();
        a0 = __builtin_amdgcn_raw_buffer_load_b128(rw, vv, W_YB + j * 2048, AUX_SC1) ^ tb;
        a1 = __builtin_amdgcn_raw_buffer_load_b128(rw, vv + 1024, W_YB + j * 2048, AUX_SC1) ^ tb;
        if (__all((or4(a0 | a1) & 0xffu) == 0u)) break;
      }
      asm volatile("Lgqa_end_e:" ::: "memory");
    }
    STAMP(8);
    const f32x4 y = __builtin_bit_cast(f32x4, a0) + __builtin_bit_cast(f32x4, a1);
    const auto u02 = __builtin_amdgcn_permlane32_swap(__float_as_uint(y[0]), __float_as_uint(y[2]), false, false);
    const auto u13 = __builtin_amdgcn_permlane32_swap(__float_as_uint(y[1]), __float_as_uint(y[3]), false, false);
    float e = __uint_as_float(u02[0]) + __uint_as_float(u02[1]), f = __uint_as_float(u13[0]) + __uint_as_float(u13[1]);
    e += dpp<0x124>(e); f += dpp<0x124>(f); e += dpp<0x128>(e); f += dpp<0x128>(f);
    const auto ue = __builtin_amdgcn_permlane16_swap(__float_as_uint(e), __float_as_uint(e), false, false);
    const auto uf = __builtin_amdgcn_permlane16_swap(__float_as_uint(f), __float_as_uint(f), false, false);
    __builtin_amdgcn_raw_buffer_store_b32(cvtpk(__uint_as_float(ue[0]) + __uint_as_float(ue[1]), __uint_as_float(uf[0]) + __uint_as_float(uf[1])), rout, vs, j * 32, 0);
  }
  if (j == 0 && lane == 0) __builtin_amdgcn_raw_buffer_store_b32(tag, rw, 0, W_E, AUX_SC1);
  asm volatile("Lgqa_end_c:" ::: "memory");
  STAMP(9);
  STAMP_OUT();
}

extern "C" void kernel_launch(void* const* d_in, const int* in_sizes, int n_in, void* d_out, int out_size, void* d_ws, size_t ws_size,
                              hipStream_t stream) {
  gqa_fused<<<NWG, NTHR, 0, stream>>>((const unsigned char*)d_in[0], (const unsigned char*)d_in[1], (const unsigned char*)d_in[2], (const unsigned char*)d_in[3],
                                      (const unsigned char*)d_in[4], (unsigned char*)d_out, (unsigned char*)d_ws TUNE_PASS);
}
```

```cpp
#include <hip/hip_runtime.h>
#include <hip/hip_bf16.h>
#include <stdint.h>

#define GAS __attribute__((address_space(1)))
#define LAS __attribute__((address_space(3)))
typedef unsigned v4u __attribute__((ext_vector_type(4)));
typedef unsigned v2u __attribute__((ext_vector_type(2)));
typedef int v4i __attribute__((ext_vector_type(4)));
typedef float f32x4 __attribute__((ext_vector_type(4)));
typedef float f32x32 __attribute__((ext_vector_type(32)));
typedef short s16x4 __attribute__((ext_vector_type(4)));
typedef __bf16 bf16x8 __attribute__((ext_vector_type(8)));

constexpr int NB = 8, DM = 512, HQ = 32, HKV = 8, SK = 8192, DH = 128;
constexpr int NWG = 256, NTHR = 384;
constexpr int NUNIT = 128;
constexpr int SLOT = 8192;
constexpr float C2 = 0.088388347648318440f * 1.4426950408889634f;

constexpr int NREG = 5;
constexpr int NFIRST = 4;
constexpr int NCH = 2;
constexpr int MLROW = 8;
constexpr int UPREP = 112;
constexpr int NFIN = 3;
constexpr int MPOS = 1;
constexpr int WIN0 = 16, UW0 = 32, WIN = 24;
constexpr unsigned UC16 = (unsigned)(3.37f * 128.f);
constexpr int LINE0 = 65;
constexpr unsigned PHASE = 28;
constexpr int HOLD = 30, MINSP = 20;
constexpr int LATE = 40, MINSP2 = 16;
constexpr int FB_NREP = 32, FBEND = 110;
constexpr unsigned FB_P = 192u;
constexpr int DELTA = 12, DELTA2 = 3, DSW = 81;
constexpr float FB_G = 0.5f, FB_KI = 0.15f, FB_Z = 5.f, FB_FIN = 3.f;
constexpr int FB_I0 = 2;
constexpr int QPOLL = 550;
constexpr int WOPOS = 125;
constexpr int GPOS = 126, D1 = 30, D1G = 134;
constexpr int D2 = 40;
constexpr unsigned SPIN_CAP = 1u << 20;
constexpr int LAG = (WIN + 7) / 8;

constexpr int L_Q = 16 * SLOT, L_ACC = L_Q + 1024, L_PL = L_ACC + 4 * 2048, L_OT = L_PL + 2048, OT_ROW = 272, L_ML = L_OT + 8 * OT_ROW, L_CTL = (L_ML + 144 + 63) / 64 * 64;
constexpr int L_X = L_ACC, L_W = L_CTL + 64, L_TOT = L_W + 16384;
constexpr int C_FREE = L_CTL, C_LAND = L_CTL + 32, C_READY = L_CTL + 40, C_TAG = L_CTL + 44, C_NM = L_CTL + 48, C_NL = L_CTL + 52, C_NA = L_CTL + 56, C_WO = L_CTL + 60;
static_assert(L_CTL % 32 == 0 && L_TOT <= 160 * 1024, "lds map");
constexpr int W_E = 0, W_FB = 1024, FB_SLOT = 512, W_ML = W_FB + 64 * FB_SLOT + 1024, W_QG = W_ML + NWG * 128, W_PA = W_QG + NWG * 2048, PA_REC = 2048, W_YB = W_PA + NWG * PA_REC,
              W_END = W_YB + NWG * 2048;


#define STR_(x) #x
#define WAITVM(n) asm volatile("s_waitcnt vmcnt(" STR_(n) ")" ::: "memory")
#define BARRIER() asm volatile("s_waitcnt lgkmcnt(0)\n\ts_barrier" ::: "memory")
#define RELOAD() asm volatile("" ::: "memory")
constexpr int AUX_SC1 = 16;

__device__ __forceinline__ unsigned now() { return (unsigned)__builtin_amdgcn_s_memrealtime(); }
__device__ __forceinline__ unsigned cvtpk(float lo, float hi) {
  typedef __bf16 bf16x2_t __attribute__((ext_vector_type(2)));
  typedef float f32x2_t __attribute__((ext_vector_type(2)));
  return __builtin_bit_cast(unsigned, __builtin_convertvector(f32x2_t{lo, hi}, bf16x2_t));
}
template <int CTRL> __device__ __forceinline__ unsigned dppu(unsigned x) { return (unsigned)__builtin_amdgcn_mov_dpp((int)x, CTRL, 0xf, 0xf, true); }
template <int CTRL> __device__ __forceinline__ float dpp(float x) { return __uint_as_float(dppu<CTRL>(__float_as_uint(x))); }
__device__ __forceinline__ float xrow_max(float x) {
  auto s = __builtin_amdgcn_permlane16_swap(__float_as_uint(x), __float_as_uint(x), false, false);
  x = fmaxf(__uint_as_float(s[0]), __uint_as_float(s[1]));
  auto t = __builtin_amdgcn_permlane32_swap(__float_as_uint(x), __float_as_uint(x), false, false);
  return fmaxf(__uint_as_float(t[0]), __uint_as_float(t[1]));
}
__device__ __forceinline__ float xrow_sum(float x) {
  auto s = __builtin_amdgcn_permlane16_swap(__float_as_uint(x), __float_as_uint(x), false, false);
  x = __uint_as_float(s[0]) + __uint_as_float(s[1]);
  auto t = __builtin_amdgcn_permlane32_swap(__float_as_uint(x), __float_as_uint(x), false, false);
  return __uint_as_float(t[0]) + __uint_as_float(t[1]);
}
__device__ __forceinline__ v4i mkrsrc(const void* p, unsigned bytes) {
  const unsigned long long a = (unsigned long long)p;
  v4i r; r[0] = (int)(unsigned)a; r[1] = (int)((unsigned)(a >> 32) & 0xffffu); r[2] = (int)bytes; r[3] = 0x00020000; return r;
}
__device__ __forceinline__ int lds_word(LAS unsigned char* p) { RELOAD(); const int v = __builtin_amdgcn_readfirstlane(*(LAS int*)p); RELOAD(); return v; }
__device__ __forceinline__ void lds_inc(LAS unsigned char* p) {
  unsigned long long ex;
  asm volatile("s_mov_b64 %0, exec\n\ts_mov_b64 exec, 1\n\tds_add_u32 %1, %2\n\ts_mov_b64 exec, %0" : "=&s"(ex) : "v"((unsigned)(size_t)p), "v"(1u) : "memory");
}
#define DMA4(V, R, S) "buffer_load_dwordx4 " V ", " R ", " S " offen nt lds\n\tbuffer_load_dwordx4 " V ", " R ", " S " offen offset:1024 nt lds\n\t" \
                      "buffer_load_dwordx4 " V ", " R ", " S " offen offset:2048 nt lds\n\tbuffer_load_dwordx4 " V ", " R ", " S " offen offset:3072 nt lds\n\t"
__device__ __forceinline__ void issue_two(v4i r, unsigned vv, unsigned so, unsigned lds) {
  unsigned t;
  asm volatile("s_mov_b32 m0, %4\n\ts_add_u32 %0, %3, 0x1000\n\t" DMA4("%2", "%1", "%3") "s_add_u32 m0, m0, 0x1000\n\ts_nop 0\n\t" DMA4("%2", "%1", "%0")
               "s_add_u32 m0, m0, 0x1000\n\ts_add_u32 %0, %0, 0x1000\n\t" DMA4("%2", "%1", "%0") "s_add_u32 m0, m0, 0x1000\n\ts_add_u32 %0, %0, 0x1000\n\t" DMA4("%2", "%1", "%0")
               : "=&s"(t) : "s"(r), "v"(vv), "s"(so), "s"(lds) : "memory", "scc");
}
__device__ __forceinline__ void dma1_nt(v4i r, unsigned voff, unsigned soff, unsigned lds) {
  asm volatile("s_mov_b32 m0, %3\n\ts_nop 0\n\tbuffer_load_dwordx4 %1, %0, %2 offen nt lds" :: "s"(r), "v"(voff), "s"(soff), "s"(lds) : "memory");
}
__device__ __forceinline__ void dma1(v4i r, unsigned voff, unsigned soff, unsigned lds) {
  asm volatile("s_mov_b32 m0, %3\n\ts_nop 0\n\tbuffer_load_dwordx4 %1, %0, %2 offen lds" :: "s"(r), "v"(voff), "s"(soff), "s"(lds) : "memory");
}
template <int W> __device__ __forceinline__ void stream_units(v4i rs, unsigned vv, unsigned u, unsigned ue, unsigned so, unsigned ra, unsigned uc16, int hold, int msp, int lat, int ms2) {
  unsigned ta, tb, st = 8192u, fo = ((u + 1u) & 3u) * 8u + C_FREE;
  asm volatile(
      "s_mov_b32 m0, %[m0i]\n\t"
      "v_mov_b32 v2, %[ld0]\n"
      "LT%=:\n\t"
      "s_waitcnt vmcnt(%[win])\n\t"
      DMA4("%[v0]", "%[rs]", "%[so]")
      "s_add_u32 m0, m0, 0x1000\n\t"
      "s_memrealtime s[40:41]\n\t"
      DMA4("%[v1]", "%[rs]", "%[so]")
      "ds_read_b64 v[4:5], %[fo]\n\t"
      "s_add_u32 m0, m0, 0x1000\n\t"
      "s_add_u32 %[so], %[so], %[st]\n\t"
      "s_and_b32 m0, m0, 0x1ffff\n\t"
      "s_and_b32 %[so], %[so], 0x1fffff\n\t"
      "s_xor_b32 %[st], %[st], 0xc000\n\t"
      "s_add_u32 %[ra], %[ra], %[uc]\n\t"
      "v_add_u32 v2, 1, v2\n\t"
      "v_add_u32 %[fo], 8, %[fo]\n\t"
      "s_add_u32 %[u], %[u], 1\n\t"
      "v_and_b32 %[fo], %[fm], %[fo]\n\t"
      "s_waitcnt lgkmcnt(0)\n\t"
      "v_mov_b32 v3, s40\n\t"
      "v_readfirstlane_b32 %[a], v5\n\t"
      "s_lshr_b32 s41, %[ra], 4\n\t"
      "v_readfirstlane_b32 %[b], v4\n\t"
      "ds_write_b64 %[vl], v[2:3]\n\t"
      "s_cmp_eq_u32 %[u], %[ue]\n\t"
      "s_cbranch_scc1 LE%=\n\t"
      "s_add_u32 %[a], %[a], s41\n\t"
      "s_sub_i32 %[a], %[a], s40\n\t"
      "s_cmp_lt_i32 %[a], %[lat]\n\t"
      "s_cselect_b32 s41, %[ms2], %[msp]\n\t"
      "s_min_i32 %[a], %[a], %[hold]\n\t"
      "s_max_i32 %[a], %[a], s41\n\t"
      "s_add_u32 %[a], %[a], s40\n\t"
      "s_cmp_lt_u32 %[b], %[u]\n\t"
      "s_cbranch_scc1 LS%=\n"
      "LW%=:\n\t"
      "s_memrealtime s[40:41]\n\t"
      "s_waitcnt lgkmcnt(0)\n\t"
      "s_sub_i32 %[b], %[a], s40\n\t"
      "s_cmp_gt_i32 %[b], 0\n\t"
      "s_cbranch_scc0 LT%=\n\t"
      "s_branch LW%=\n"
      "LS%=:\n\t"
      "s_and_b32 %[b], %[u], 3\n\t"
      "s_lshl_b32 %[b], %[b], 3\n\t"
      "s_add_u32 %[b], %[b], %[fb]\n\t"
      "v_mov_b32 v5, %[b]\n"
      "LR%=:\n\t"
      "s_sleep 1\n\t"
      "ds_read_b32 v4, v5\n\t"
      "s_waitcnt lgkmcnt(0)\n\t"
      "v_readfirstlane_b32 %[b], v4\n\t"
      "s_cmp_lt_u32 %[b], %[u]\n\t"
      "s_cbranch_scc1 LR%=\n\t"
      "s_branch LW%=\n"
      "LE%=:"
      : [so] "+s"(so), [ra] "+s"(ra), [u] "+s"(u), [st] "+s"(st), [fo] "+v"(fo), [a] "=&s"(ta), [b] "=&s"(tb)
      : [rs] "s"(rs), [v0] "v"(vv), [v1] "v"(vv + 4096u), [vl] "v"((unsigned)C_LAND), [ue] "s"(ue), [uc] "s"(uc16), [hold] "s"(hold), [msp] "s"(msp), [lat] "s"(lat), [ms2] "s"(ms2),
        [m0i] "s"((u & 15u) * SLOT), [ld0] "s"(u - 1u - LAG), [win] "n"(W), [fm] "s"(~32u), [fb] "s"((unsigned)C_FREE)
      : "memory", "scc", "vcc", "v2", "v3", "v4", "v5", "s40", "s41");
}
static_assert((C_FREE & 32) == 0, "the record pointer wraps by clearing bit 5");

__device__ __forceinline__ f32x4 mfma32(v4u a, v4u b, f32x4 c) {
  return __builtin_amdgcn_mfma_f32_16x16x32_bf16(__builtin_bit_cast(bf16x8, a), __builtin_bit_cast(bf16x8, b), c, 0, 0, 0);
}
__device__ __forceinline__ v4u tr_frag(LAS unsigned char* p, int hi) {
  const v2u lo = __builtin_bit_cast(v2u, __builtin_amdgcn_ds_read_tr16_b64_v4i16((LAS s16x4*)p));
  const v2u up = __builtin_bit_cast(v2u, __builtin_amdgcn_ds_read_tr16_b64_v4i16((LAS s16x4*)(p + hi)));
  return v4u{lo[0], lo[1], up[0], up[1]};
}
__device__ __forceinline__ float score16(const v4u* K, v4u qf, unsigned j0) {
  float sc[4];
#pragma unroll
  for (int u = 0; u < 4; ++u) {
    const f32x4 d = mfma32(K[u], qf, f32x4{0.f, 0.f, 0.f, 0.f});
    sc[u] = ((dpp<0x00>(d[0]) + dpp<0x55>(d[1])) + dpp<0xAA>(d[2])) + dpp<0xFF>(d[3]);
  }
  return (j0 & 2) ? ((j0 & 1) ? sc[3] : sc[2]) : ((j0 & 1) ? sc[1] : sc[0]);
}
__device__ __forceinline__ void p_operands(unsigned k0, unsigned k1, unsigned mask0, unsigned mask1, v4u (&bo)[4]) {
  bo[0] = v4u{dppu<0x00>(k0) & mask0, dppu<0x00>(k0) & mask1, dppu<0x55>(k0) & mask0, dppu<0x55>(k0) & mask1};
  bo[1] = v4u{dppu<0xAA>(k0) & mask0, dppu<0xAA>(k0) & mask1, dppu<0xFF>(k0) & mask0, dppu<0xFF>(k0) & mask1};
  bo[2] = v4u{dppu<0x00>(k1) & mask0, dppu<0x00>(k1) & mask1, dppu<0x55>(k1) & mask0, dppu<0x55>(k1) & mask1};
  bo[3] = v4u{dppu<0xAA>(k1) & mask0, dppu<0xAA>(k1) & mask1, dppu<0xFF>(k1) & mask0, dppu<0xFF>(k1) & mask1};
  asm volatile("" : "+v"(bo[0]), "+v"(bo[1]), "+v"(bo[2]), "+v"(bo[3]));
}
#define RD2(A, P, O0, O1) "ds_read2st64_b64 %[" A "], %[" P "] offset0:" O0 " offset1:" O1 "\n\t"
#define MF(N, C, A, B) "s_waitcnt lgkmcnt(" N ")\n\tv_mfma_f32_16x16x32_bf16 %[" C "], %[" A "], %[" B "], %[" C "]\n\t"
__device__ __forceinline__ void pv32(unsigned p, const v4u (&bo)[4], f32x4& acc0, f32x4& acc1) {
  v4u a0, a1, a2, a3, a4, a5, a6, a7;
  asm volatile(RD2("a0", "p0", "0", "2") RD2("a1", "p1", "0", "2") RD2("a2", "p0", "4", "6") RD2("a3", "p1", "4", "6")
               RD2("a4", "p0", "8", "10") RD2("a5", "p1", "8", "10") RD2("a6", "p0", "12", "14") RD2("a7", "p1", "12", "14")
               MF("7", "c0", "a0", "b0") MF("6", "c1", "a1", "b0") MF("5", "c0", "a2", "b1") MF("4", "c1", "a3", "b1")
               MF("3", "c0", "a4", "b2") MF("2", "c1", "a5", "b2") MF("1", "c0", "a6", "b3") MF("0", "c1", "a7", "b3") "s_nop 7"
      : [c0] "+v"(acc0), [c1] "+v"(acc1), [a0] "=&v"(a0), [a1] "=&v"(a1), [a2] "=&v"(a2), [a3] "=&v"(a3), [a4] "=&v"(a4), [a5] "=&v"(a5), [a6] "=&v"(a6), [a7] "=&v"(a7)
      : [p0] "v"(p), [p1] "v"(p + 8), [b0] "v"(bo[0]), [b1] "v"(bo[1]), [b2] "v"(bo[2]), [b3] "v"(bo[3]) : "memory");
}
__device__ __forceinline__ v4u tag_bytes(unsigned tag) { return v4u{tag & 0xffu, (tag >> 8) & 0xffu, (tag >> 16) & 0xffu, tag >> 24}; }
__device__ __forceinline__ v4u tag4(f32x4 v, v4u tb) { return (__builtin_bit_cast(v4u, v) & 0xffffff00u) | tb; }
__device__ __forceinline__ unsigned next_tag(unsigned e) { return ((e & 0x7f7f7f7fu) + 0x51276b35u) ^ (e & 0x80808080u); }
__device__ __forceinline__ unsigned or4(v4u u) { return (u[0] | u[1]) | (u[2] | u[3]); }

__global__ void __launch_bounds__(NTHR, 2) gqa_fused(const unsigned char* __restrict__ x, const unsigned char* __restrict__ kc, const unsigned char* __restrict__ vc,
                                                     const unsigned char* __restrict__ wq, const unsigned char* __restrict__ wo, unsigned char* __restrict__ out,
                                                     unsigned char* ws) {
  __shared__ __attribute__((aligned(16))) unsigned char smem[L_TOT];
  LAS unsigned char* L = (LAS unsigned char*)smem;
  unsigned long long t64;
  unsigned pf, pa;
  {
    unsigned long long pc, ex;
    unsigned clen;
    asm volatile("s_memrealtime %0\n\ts_getpc_b64 %1\nLgqa_pc:\n\t"
                 "s_mov_b32 %2, Lgqa_end_a-Lgqa_pc+160\n\ts_max_u32 %2, %2, Lgqa_end_b-Lgqa_pc+160\n\ts_max_u32 %2, %2, Lgqa_end_c-Lgqa_pc+160\n\t"
                 "s_max_u32 %2, %2, Lgqa_end_d-Lgqa_pc+160\n\ts_max_u32 %2, %2, Lgqa_end_e-Lgqa_pc+160\n\t"
                 "v_lshlrev_b32 %5, 7, %6\n\tv_add_u32 %5, 0xffffc000, %5\n\tv_cmp_gt_u32 vcc, %2, %5\n\t"
                 "s_and_saveexec_b64 %3, vcc\n\ts_cbranch_execz Lgqa_nopf\n\tglobal_load_dword %4, %5, %1 offset:-32\nLgqa_nopf:\n\ts_mov_b64 exec, %3"
                 : "=&s"(t64), "=&s"(pc), "=&s"(clen), "=&s"(ex), "=&v"(pf), "=&v"(pa) : "v"(threadIdx.x) : "scc", "vcc", "memory");
  }
  const int lane = threadIdx.x & 63, wv = __builtin_amdgcn_readfirstlane(threadIdx.x >> 6);
  const int j = blockIdx.x, xg = j & 7, idx = j >> 3;
  const int pr = (idx >> 2) * 8 + ((xg + (idx >> 2)) & 7), s = idx & 3;
  const int ch = 4 * xg + (idx >> 3), nb = idx & 7;
  const int g = lane >> 4;
  const unsigned so0 = (unsigned)(pr >> 1) * 65536u + s * 16384u;
  const unsigned vv = lane * 16;
  const unsigned ra0 = ((unsigned)(j * 37) & 31u) * PHASE >> 1;
  asm volatile("s_waitcnt lgkmcnt(0)" : "+s"(t64) : "s"(x), "s"(kc), "s"(vc), "s"(wq), "s"(wo), "s"(out), "s"(ws), "s"(so0), "v"(vv));
  const unsigned t0 = (unsigned)t64;
  const size_t pbase = (size_t)pr * (SK * DH * 2);

  const v4i rk = mkrsrc(kc + pbase, SK * DH * 2);
  if (wv == 0) {
    __builtin_amdgcn_s_setprio(3);
#pragma unroll
    for (int u = 0; u < NFIRST; u += 2) issue_two(rk, vv, (so0 + (u >> 1) * 65536u) & 0x1fffffu, u * SLOT);
    __builtin_amdgcn_sched_barrier(0);
    const unsigned keep = (unsigned)((lane & 7) - 4) >> 31;
    *(LAS v2u*)(L + L_CTL + (lane & 7) * 8) = v2u{(12u + (lane & 7)) * keep, (t0 + LINE0) * keep};
    asm volatile("s_waitcnt lgkmcnt(0)" ::: "memory");
  }
  unsigned e0, e1, e2;
  if (wv == 1) asm volatile("s_load_dword %0, %3, 0x0\n\ts_load_dword %1, %4, 0x0\n\ts_load_dword %2, %5, %6"
                            : "=&s"(e0), "=&s"(e1), "=&s"(e2) : "s"(ws), "s"(x), "s"(wq), "s"((j >> 6) * (128 * 8192) + (j & 63) * 128) : "memory");
  asm volatile("s_barrier" ::: "memory");
  if (wv == 0) stream_units<WIN0>(rk, vv, NFIRST, UW0, (so0 + (NFIRST >> 1) * 65536u) & 0x1fffffu, NFIRST * UC16 + ra0, UC16, HOLD, MINSP, -LATE, MINSP2);
  const __amdgpu_buffer_rsrc_t rw = __builtin_amdgcn_make_buffer_rsrc((void*)ws, 0, W_END, 0x00020000);
  const unsigned j0 = lane & 3;
  const int h = (lane >> 2) & 3;
  const unsigned mask0 = j0 == 0 ? 0x0000ffffu : (j0 == 1 ? 0xffff0000u : 0u);
  const unsigned mask1 = j0 == 2 ? 0x0000ffffu : (j0 == 3 ? 0xffff0000u : 0u);

  if (wv == 0) {
    const v4i rv = mkrsrc(vc + pbase, SK * DH * 2);
#pragma unroll 1
    for (int pass = 0; pass < 2; ++pass) {
      const unsigned ub = pass ? 64u : (unsigned)UW0;
      while ((unsigned)lds_word(L + C_FREE + (ub & 3u) * 8) < ub) __builtin_amdgcn_s_sleep(1);
      const v4i rs = {pass ? rv[0] : rk[0], pass ? rv[1] : rk[1], rk[2], rk[3]};
      stream_units<WIN>(rs, vv, ub, pass ? (unsigned)UPREP : 64u, (so0 + (ub >> 1) * 65536u) & 0x1fffffu, ub * UC16 + ra0, UC16, HOLD, MINSP, -LATE, MINSP2);
    }
    while (lds_word(L + C_FREE + 24) < 64 + 4 * MLROW + 3 + 16) __builtin_amdgcn_s_sleep(1);
    v4u bo[NFIN][4], tb;
    unsigned vo = h * 512 + g * 64 + j0 * 16;
    float scale;
    {
      v2u pl[NFIN];
#pragma unroll
      for (int k = 0; k < NFIN; ++k) pl[k] = *(LAS v2u*)(L + L_PL + (4 - NFIN + k) * 512 + lane * 8);
      tb = tag_bytes(*(LAS unsigned*)(L + C_TAG));
      scale = *(LAS float*)(L + L_ML + 128 + h * 4);
      const int fr = *(LAS int*)(L + C_FREE + (UPREP & 3) * 8);
#pragma unroll
      for (int k = 0; k < NFIN; ++k) p_operands(pl[k][0], pl[k][1], mask0, mask1, bo[k]);
      asm volatile("" : "+v"(vo), "+v"(tb), "+v"(scale));
      if (__builtin_amdgcn_readfirstlane(fr) < UPREP) { while (lds_word(L + C_FREE + (UPREP & 3) * 8) < UPREP) __builtin_amdgcn_s_sleep(1); }
    }
    stream_units<WIN>(rv, vv, UPREP, NUNIT, (so0 + ((UPREP - 64) >> 1) * 65536u) & 0x1fffffu, UPREP * UC16 + ra0, UC16, HOLD, MINSP, -LATE, MINSP2);
    const int so = W_PA + (pr * 4 + s) * PA_REC;
    f32x4 acc0 = {0.f, 0.f, 0.f, 0.f}, acc1 = {0.f, 0.f, 0.f, 0.f};
    static_assert(NFIN >= 2 && LAG <= 3, "the drain below starts at three units outstanding");
    f32x4 s0, s1;
#pragma unroll
    for (int k = 0; k < 3; ++k) {
      if (k == 0) WAITVM(24); else if (k == 1) WAITVM(16); else WAITVM(8);
      if (k < 2) *(LAS int*)(L + C_LAND) = NUNIT - 3 + k;
      if (k >= 4 - NFIN) pv32((12 + k) * SLOT + vv, bo[k >= 4 - NFIN ? k - (4 - NFIN) : 0], acc0, acc1);
      if (k == MPOS) {
        while (lds_word(L + C_NA) < 4) {}
        f32x4 a[8];
#pragma unroll
        for (int u = 0; u < 8; ++u) a[u] = *(LAS f32x4*)(L + L_ACC + vv + u * 1024);
        __builtin_amdgcn_sched_barrier(0);
        s0 = ((a[0] + a[2]) + (a[4] + a[6])) * scale; s1 = ((a[1] + a[3]) + (a[5] + a[7])) * scale;
        asm volatile("" : "+v"(s0), "+v"(s1) : "v"(14 * SLOT + vv), "v"(14 * SLOT + vv + 8), "v"(15 * SLOT + vv), "v"(15 * SLOT + vv + 8), "s"(so), "s"(0xffffff00u));
      }
    }
    WAITVM(0);
    pv32(15 * SLOT + vv, bo[NFIN - 1], acc0, acc1);
    const v4u o0 = tag4(acc0 * scale + s0, tb), o1 = tag4(acc1 * scale + s1, tb);
    __builtin_amdgcn_raw_buffer_store_b128(o0, rw, vo, so, AUX_SC1);
    __builtin_amdgcn_raw_buffer_store_b128(o1, rw, vo + 256, so, AUX_SC1);
    asm volatile("s_nop 1" :: "v"(o0), "v"(o1) : "memory");
    *(LAS int*)(L + C_LAND) = NUNIT;
    asm volatile("Lgqa_end_a:" ::: "memory");
    return;
  }

  if (wv >= 2) {
    const int w = wv - 2;
    const unsigned vk = (h * 16 + g * 4 + j0) * 16;
    asm volatile("s_waitcnt vmcnt(0)" :: "v"(pf), "v"(pa) : "memory");
    f32x32 sc;
    float m;
    {
      v4u P[NREG][8];
#pragma unroll
      for (int r = 0; r < NREG; ++r) {
        while (lds_word(L + C_LAND) <= 4 * r + w) __builtin_amdgcn_s_sleep(2);
#pragma unroll
        for (int u = 0; u < 8; ++u) P[r][u] = *(LAS v4u*)(L + ((4 * r + w) & 15) * SLOT + vk + u * 1024);
        asm volatile("" :: "v"(P[r][0]), "v"(P[r][1]), "v"(P[r][2]), "v"(P[r][3]), "v"(P[r][4]), "v"(P[r][5]), "v"(P[r][6]), "v"(P[r][7]) : "memory");
        *(LAS int*)(L + C_FREE + w * 8) = 4 * r + w + 16;
      }
      while (lds_word(L + C_READY) == 0) __builtin_amdgcn_s_sleep(2);
      const v4u qf = *(LAS v4u*)(L + L_Q + vk);
      m = -INFINITY;
#pragma unroll
      for (int r = 0; r < NREG; ++r) {
        const float s0 = score16(&P[r][0], qf, j0), s1 = score16(&P[r][4], qf, j0);
        sc[2 * r] = s0; sc[2 * r + 1] = s1; m = fmaxf(m, fmaxf(s0, s1));
      }
#pragma unroll 1
      for (int n = NREG; n < 16; ++n) {
        while (lds_word(L + C_LAND) <= 4 * n + w) __builtin_amdgcn_s_sleep(2);
        v4u K[8];
#pragma unroll
        for (int u = 0; u < 8; ++u) K[u] = *(LAS v4u*)(L + ((4 * n + w) & 15) * SLOT + vk + u * 1024);
        asm volatile("" :: "v"(K[0]), "v"(K[1]), "v"(K[2]), "v"(K[3]), "v"(K[4]), "v"(K[5]), "v"(K[6]), "v"(K[7]) : "memory");
        *(LAS int*)(L + C_FREE + w * 8) = 4 * n + w + 16;
        const float s0 = score16(&K[0], qf, j0), s1 = score16(&K[4], qf, j0);
        sc[2 * n] = s0; sc[2 * n + 1] = s1; m = fmaxf(m, fmaxf(s0, s1));
      }
    }
    const v4u tb = tag_bytes((unsigned)lds_word(L + C_TAG));
    m = fmaxf(m, dpp<0xB1>(m)); m = fmaxf(m, dpp<0x4E>(m)); m = xrow_max(m);
    *(LAS float*)(L + L_ML + h * 16 + w * 4) = m;
    lds_inc(L + C_NM);
    while (lds_word(L + C_NM) < 4) __builtin_amdgcn_s_sleep(1);
    {
      const f32x4 mw = *(LAS f32x4*)(L + L_ML + h * 16);
      m = fmaxf(fmaxf(mw[0], mw[1]), fmaxf(mw[2], mw[3]));
      float l = 0.f;
#pragma unroll
      for (int n = 0; n < 32; ++n) { const float p = __builtin_amdgcn_exp2f((sc[n] - m) * C2); l += p; sc[n] = __uint_as_float(cvtpk(p, p)); }
      l += dpp<0xB1>(l); l += dpp<0x4E>(l); l = xrow_sum(l);
      *(LAS float*)(L + L_ML + 64 + h * 16 + w * 4) = l;
      *(LAS v2u*)(L + L_PL + w * 512 + lane * 8) = v2u{__float_as_uint(sc[30]), __float_as_uint(sc[31])};
      lds_inc(L + C_NL);
    }
    f32x4 acc0 = {0.f, 0.f, 0.f, 0.f}, acc1 = {0.f, 0.f, 0.f, 0.f};
#pragma unroll 1
    for (int n = 0; n < (w < 4 - NFIN ? 16 : 15); ++n) {
      if (w == 3 && n == 1) {
        while (lds_word(L + C_NL) < 4) __builtin_amdgcn_s_sleep(1);
        const f32x4 lw = *(LAS f32x4*)(L + L_ML + 64 + h * 16);
        if (lane < 16 && j0 == 0) __builtin_amdgcn_raw_buffer_store_b128(tag4(f32x4{m, (lw[0] + lw[1]) + (lw[2] + lw[3]), __uint_as_float(__float_as_uint(m) << 24), 0.f}, tb), rw, h * 16, W_ML + (pr * 4 + s) * 128, AUX_SC1);
      }
      if (w == 3 && n == MLROW) {
        v4u ml;
        for (unsigned spin = 0; spin < SPIN_CAP; ++spin) {
          RELOAD();
          ml = __builtin_amdgcn_raw_buffer_load_b128(rw, j0 * 128 + h * 16, W_ML + pr * 512, AUX_SC1) ^ tb;
          if (__all((or4(ml) & 0xffu) == 0u)) break;
        }
        const float ms = __uint_as_float(ml[0] | (ml[2] >> 24));
        float M = fmaxf(ms, dpp<0xB1>(ms)); M = fmaxf(M, dpp<0x4E>(M));
        float ls = __uint_as_float(ml[1]) * __builtin_amdgcn_exp2f((ms - M) * C2);
        ls += dpp<0xB1>(ls); ls += dpp<0x4E>(ls);
        if (lane < 16 && j0 == 0) *(LAS float*)(L + L_ML + 128 + h * 4) = __builtin_amdgcn_exp2f((m - M) * C2) * __builtin_amdgcn_rcpf(ls);
      }
      v4u bo[4];
      p_operands(__float_as_uint(sc[2 * n]), __float_as_uint(sc[2 * n + 1]), mask0, mask1, bo);
      while (lds_word(L + C_LAND) <= 64 + 4 * n + w) __builtin_amdgcn_s_sleep(1);
      pv32(((4 * n + w) & 15) * SLOT + vv, bo, acc0, acc1);
      *(LAS int*)(L + C_FREE + w * 8) = 64 + 4 * n + w + 16;
    }
    *(LAS f32x4*)(L + L_ACC + w * 2048 + vv) = acc0; *(LAS f32x4*)(L + L_ACC + w * 2048 + 1024 + vv) = acc1;
    lds_inc(L + C_NA);
    const int bb = 2 * w + (lane >> 5), c = lane & 31;
    unsigned vg = bb * (HKV * 4 * PA_REC) + c * 16, vy = (lane & 7) * (32 * 2048) + g * 16;
    const int sg = W_PA + (ch >> 2) * (4 * PA_REC) + (ch & 3) * 512, sy = W_YB + (nb * 4 + w) * 2048 + ch * 64;
    LAS unsigned char* ap = L + L_W + (8 * g + ((lane & 15) >> 2)) * 128 + (16 * w + 4 * j0) * 2;
    LAS unsigned char* op = L + L_OT + bb * OT_ROW + c * 8;
    LAS unsigned char* bp = L + L_OT + (lane & 7) * OT_ROW + g * 16;
    if (lane & 8) vy = 0x40000000u;
    v4u tv = tb;
    asm volatile("" : "+v"(vg), "+v"(vy), "+v"(ap), "+v"(op), "+v"(bp), "+v"(tv) : "s"(sg), "s"(sy));
    while (lds_word(L + C_LAND) < GPOS) __builtin_amdgcn_s_sleep(1);
    {
      const unsigned tp = now() + D1;
      unsigned tg = (unsigned)lds_word(L + C_FREE + 4) + (NUNIT * UC16 >> 4) + D1G;
      tg = tp + (unsigned)min(max((int)(tg - tp), 0), 200);
      while ((int)(now() - tg) < 0) {}
    }
    v4u a[4], af[4], bf[4];
#pragma unroll
    for (int u = 0; u < 4; ++u) a[u] = __builtin_amdgcn_raw_buffer_load_b128(rw, vg, sg + u * PA_REC, AUX_SC1);
    while (lds_word(L + C_WO) == 0) {}
#pragma unroll
    for (int ks = 0; ks < 4; ++ks) af[ks] = tr_frag(ap + ks * 4096, 512);
    asm volatile("" : "+v"(af[0]), "+v"(af[1]), "+v"(af[2]), "+v"(af[3]));
#pragma unroll
    for (int u = 0; u < 4; ++u) a[u] ^= tv;
    asm volatile("" : "+v"(a[0]), "+v"(a[1]), "+v"(a[2]), "+v"(a[3]));
    if (__builtin_expect(!__all((or4((a[0] | a[1]) | (a[2] | a[3])) & 0xffu) == 0u), 0)) {
      for (unsigned spin = 0; spin < SPIN_CAP; ++spin) {
        RELOAD();
#pragma unroll
        for (int u = 0; u < 4; ++u) a[u] = __builtin_amdgcn_raw_buffer_load_b128(rw, vg, sg + u * PA_REC, AUX_SC1) ^ tv;
        if (__all((or4((a[0] | a[1]) | (a[2] | a[3])) & 0xffu) == 0u)) break;
      }
      asm volatile("Lgqa_end_d:" ::: "memory");
    }
    const f32x4 o = (__builtin_bit_cast(f32x4, a[0]) + __builtin_bit_cast(f32x4, a[1])) + (__builtin_bit_cast(f32x4, a[2]) + __builtin_bit_cast(f32x4, a[3]));
    *(LAS v2u*)op = v2u{cvtpk(o[0], o[1]), cvtpk(o[2], o[3])};
    BARRIER();
#pragma unroll
    for (int ks = 0; ks < 4; ++ks) bf[ks] = *(LAS v4u*)(bp + ks * 64);
    __builtin_amdgcn_sched_barrier(0);
    f32x4 y = {0.f, 0.f, 0.f, 0.f};
#pragma unroll
    for (int ks = 0; ks < 4; ++ks) y = mfma32(af[ks], bf[ks], y);
    __builtin_amdgcn_raw_buffer_store_b128(tag4(y, tv), rw, vy, sy, AUX_SC1);
    asm volatile("Lgqa_end_b:" ::: "memory");
    return;
  }

  __builtin_amdgcn_s_setprio(2);
  unsigned tag;
  {
    constexpr int NKS = 4 / NCH;
    const bool prod = (idx & (NCH - 1)) == NCH - 1;
    const int pid = (idx / NCH) * 8 + xg, cb = pid & 63, kq = pid >> 6;
    v4u tq;
    if (prod) {
      const v4i rx = mkrsrc(x, NB * DM * 2), rq = mkrsrc(wq, DM * HQ * DH * 2);
      const unsigned vx = (lane & 7) * 1024 + (lane >> 3) * 16, vq = (lane >> 3) * 8192 + (lane & 7) * 16;
      const bool hi = (lane & 8) != 0;
      const int vs = (lane & 7) * (HKV * NKS * 2048) + (hi ? 64 : 0) + g * 16, ss = W_QG + (cb >> 3) * (NKS * 2048) + kq * 2048 + (cb & 7) * 256;
      f32x4 qa[4];
#pragma unroll
      for (int t = 0; t < 4; ++t) qa[t] = f32x4{0.f, 0.f, 0.f, 0.f};
      LAS unsigned char* ap = L + L_W + (8 * g + ((lane & 15) >> 2)) * 128 + j0 * 8;
      LAS unsigned char* bp = L + L_X + g * 128 + (lane & 7) * 16;
#pragma unroll 1
      for (int c = kq * NCH; c < kq * NCH + NCH; ++c) {
        dma1(rx, vx, c * 256, L_X);
        dma1(rx, vx, c * 256 + 128, L_X + 1024);
#pragma unroll 1
        for (int u = 0; u < 16; ++u) dma1_nt(rq, vq, (c * 16 + u) * 65536 + cb * 128, L_W + u * 1024);
        WAITVM(0);
#pragma unroll
        for (int ks = 0; ks < 4; ++ks) {
          const v4u xb = *(LAS v4u*)(bp + ks * 512);
#pragma unroll
          for (int t = 0; t < 4; ++t) qa[t] = mfma32(tr_frag(ap + ks * 4096 + t * 32, 512), xb, qa[t]);
        }
        asm volatile("" : "+v"(qa[0]), "+v"(qa[1]), "+v"(qa[2]), "+v"(qa[3]) :: "memory");
      }
      asm volatile("s_waitcnt lgkmcnt(0)" : "+s"(e0) :: "memory");
      tag = next_tag(e0);
      tq = tag_bytes(tag);
      const v4u o0 = tag4(f32x4{hi ? qa[1][0] : qa[0][0], hi ? qa[1][1] : qa[0][1], hi ? qa[1][2] : qa[0][2], hi ? qa[1][3] : qa[0][3]}, tq);
      const v4u o1 = tag4(f32x4{hi ? qa[3][0] : qa[2][0], hi ? qa[3][1] : qa[2][1], hi ? qa[3][2] : qa[2][2], hi ? qa[3][3] : qa[2][3]}, tq);
      __builtin_amdgcn_raw_buffer_store_b128(o0, rw, vs, ss, AUX_SC1);
      __builtin_amdgcn_raw_buffer_store_b128(o1, rw, vs + 128, ss, AUX_SC1);
      asm volatile("s_nop 1" :: "v"(o0), "v"(o1) : "memory");
    }
    asm volatile("s_waitcnt lgkmcnt(0)" : "+s"(e0) : "s"(e1), "s"(e2) : "memory");
    tag = next_tag(e0);
    tq = tag_bytes(tag);
    while ((int)(now() - t0) < QPOLL) __builtin_amdgcn_s_sleep(2);
    const int so = W_QG + pr * (NKS * 2048);
    v4u a[2 * NKS];
    for (unsigned spin = 0; spin < SPIN_CAP; ++spin) {
      RELOAD();
      v4u bad = {0u, 0u, 0u, 0u};
#pragma unroll
      for (int u = 0; u < 2 * NKS; ++u) { a[u] = __builtin_amdgcn_raw_buffer_load_b128(rw, vv + (u & 3) * 1024, so + (u >> 2) * 4096, AUX_SC1) ^ tq; bad |= a[u]; }
      if (__all((or4(bad) & 0xffu) == 0u)) break;
      __builtin_amdgcn_s_sleep(12);
    }
#pragma unroll
    for (int u = 0; u < 2; ++u) {
      f32x4 v = __builtin_bit_cast(f32x4, a[u]);
#pragma unroll
      for (int p = 1; p < NKS; ++p) v += __builtin_bit_cast(f32x4, a[2 * p + u]);
      *(LAS v2u*)(L + L_Q + u * 512 + lane * 8) = v2u{cvtpk(v[0], v[1]), cvtpk(v[2], v[3])};
    }
    RELOAD();
    *(LAS v2u*)(L + C_READY) = v2u{1u, tag};
  }
  if (lane == 0) {
    const unsigned a = __builtin_nontemporal_load((const GAS unsigned*)(vc + pbase + so0));
    const unsigned b2 = __builtin_nontemporal_load((const GAS unsigned*)(wo + ch * 128 * 1024 + nb * 128));
    asm volatile("" :: "v"(a), "v"(b2));
  }
  {
    unsigned k = now() / FB_P + 1u;
    float af = 0.f, dr = 0.f;
    int nv = 0;
#pragma unroll 1
    for (int it = 0; it < 40; ++it, ++k) {
      const unsigned tk = k * FB_P;
      while ((int)(now() - tk) < 0) __builtin_amdgcn_s_sleep(2);
      RELOAD();
      const v2u pr2 = *(LAS v2u*)(L + C_LAND);
      const int landed = __builtin_amdgcn_readfirstlane(pr2[0]);
      if (landed >= FBEND) break;
      const v2u fb = __builtin_amdgcn_raw_buffer_load_b64(rw, (lane & (FB_NREP - 1)) * 8, W_FB + ((k - 1u) & 63u) * FB_SLOT, AUX_SC1);
      if (j < FB_NREP && lane == 0) {
        const unsigned a = pr2[1] - (((pr2[0] + LAG) * UC16 + ra0) >> 4) + (unsigned)max((int)(tk - pr2[1]) - (int)(UC16 >> 4), 0);
        __builtin_amdgcn_raw_buffer_store_b64(v2u{a, tag ^ k}, rw, 0, W_FB + (k & 63u) * FB_SLOT + j * 8, AUX_SC1);
      }
      const bool ok = fb[1] == (tag ^ (k - 1u));
      float ps = ok ? (float)(int)(fb[0] - t0) : 0.f, pc = ok ? 1.f : 0.f;
      ps += dpp<0xB1>(ps); ps += dpp<0x4E>(ps); ps += dpp<0x124>(ps); ps += dpp<0x128>(ps); ps = xrow_sum(ps);
      pc += dpp<0xB1>(pc); pc += dpp<0x4E>(pc); pc += dpp<0x124>(pc); pc += dpp<0x128>(pc); pc = xrow_sum(pc);
      if (__builtin_amdgcn_readfirstlane(pc < 48.f)) continue;
      const float as = ps * __builtin_amdgcn_rcpf(pc) - (float)(landed < DSW ? DELTA : DELTA2);
      const float er = as - af;
      if (nv >= FB_I0) dr = fminf(fmaxf(dr + FB_KI * fminf(er - FB_Z, 30.f), 0.f), 16.f);
      af = nv ? af + dr + FB_G * er : as;
      ++nv;
      *(LAS unsigned*)(L + C_FREE + 4 + j0 * 8) = t0 + (unsigned)(int)af;
    }
    if (nv) *(LAS unsigned*)(L + C_FREE + 4 + j0 * 8) = t0 + (unsigned)(int)(af + FB_FIN * fmaxf(dr, 0.f));
  }
  {
    const v4i ro = mkrsrc(wo, HQ * DH * DM * 2);
    const unsigned vo = ((lane >> 5) + 8 * ((lane >> 3) & 3)) * 1024 + (lane & 7) * 16;
    while (lds_word(L + C_LAND) < WOPOS) __builtin_amdgcn_s_sleep(1);
#pragma unroll 1
    for (int u = 0; u < 16; ++u) dma1_nt(ro, vo, (ch * 128 + 32 * ((u >> 1) & 3) + 4 * (u >> 3) + 2 * (u & 1)) * 1024 + nb * 128, L_W + u * 1024);
    const __amdgpu_buffer_rsrc_t rout = __builtin_amdgcn_make_buffer_rsrc((void*)out, 0, NB * DM * 2, 0x00020000);
    v4u tb = tag_bytes(tag);
    unsigned vs = (lane & 28) == 0 ? j0 * 8 + (lane >> 5) * 4 : 0x40000000u;
    asm volatile("s_waitcnt vmcnt(0)" : "+v"(vs), "+v"(tb) :: "memory");
    *(LAS int*)(L + C_WO) = 1;
    BARRIER();
    const unsigned tp = now() + D2;
    while ((int)(now() - tp) < 0) {}
    v4u a0 = __builtin_amdgcn_raw_buffer_load_b128(rw, vv, W_YB + j * 2048, AUX_SC1) ^ tb;
    v4u a1 = __builtin_amdgcn_raw_buffer_load_b128(rw, vv + 1024, W_YB + j * 2048, AUX_SC1) ^ tb;
    asm volatile("" : "+v"(a0), "+v"(a1));
    if (__builtin_expect(!__all((or4(a0 | a1) & 0xffu) == 0u), 0)) {
      for (unsigned spin = 0; spin < SPIN_CAP; ++spin) {
        RELOAD();
        a0 = __builtin_amdgcn_raw_buffer_load_b128(rw, vv, W_YB + j * 2048, AUX_SC1) ^ tb;
        a1 = __builtin_amdgcn_raw_buffer_load_b128(rw, vv + 1024, W_YB + j * 2048, AUX_SC1) ^ tb;
        if (__all((or4(a0 | a1) & 0xffu) == 0u)) break;
      }
      asm volatile("Lgqa_end_e:" ::: "memory");
    }
    const f32x4 y = __builtin_bit_cast(f32x4, a0) + __builtin_bit_cast(f32x4, a1);
    const auto u02 = __builtin_amdgcn_permlane32_swap(__float_as_uint(y[0]), __float_as_uint(y[2]), false, false);
    const auto u13 = __builtin_amdgcn_permlane32_swap(__float_as_uint(y[1]), __float_as_uint(y[3]), false, false);
    float e = __uint_as_float(u02[0]) + __uint_as_float(u02[1]), f = __uint_as_float(u13[0]) + __uint_as_float(u13[1]);
    e += dpp<0x124>(e); f += dpp<0x124>(f); e += dpp<0x128>(e); f += dpp<0x128>(f);
    const auto ue = __builtin_amdgcn_permlane16_swap(__float_as_uint(e), __float_as_uint(e), false, false);
    const auto uf = __builtin_amdgcn_permlane16_swap(__float_as_uint(f), __float_as_uint(f), false, false);
    __builtin_amdgcn_raw_buffer_store_b32(cvtpk(__uint_as_float(ue[0]) + __uint_as_float(ue[1]), __uint_as_float(uf[0]) + __uint_as_float(uf[1])), rout, vs, j * 32, 0);
  }
  if (j == 0 && lane == 0) __builtin_amdgcn_raw_buffer_store_b32(tag, rw, 0, W_E, AUX_SC1);
  asm volatile("Lgqa_end_c:" ::: "memory");
}

extern "C" void kernel_launch(void* const* d_in, const int* in_sizes, int n_in, void* d_out, int out_size, void* d_ws, size_t ws_size,
                              hipStream_t stream) {
  gqa_fused<<<NWG, NTHR, 0, stream>>>((const unsigned char*)d_in[0], (const unsigned char*)d_in[1], (const unsigned char*)d_in[2], (const unsigned char*)d_in[3],
                                      (const unsigned char*)d_in[4], (unsigned char*)d_out, (unsigned char*)d_ws);
}
```

```cpp
#define DG 0
#include <hip/hip_runtime.h>
#include <hip/hip_bf16.h>
#include <stdint.h>

#define GAS __attribute__((address_space(1)))
#define LAS __attribute__((address_space(3)))
typedef unsigned v4u __attribute__((ext_vector_type(4)));
typedef unsigned v2u __attribute__((ext_vector_type(2)));
typedef int v4i __attribute__((ext_vector_type(4)));
typedef float f32x4 __attribute__((ext_vector_type(4)));
typedef float f32x32 __attribute__((ext_vector_type(32)));
typedef short s16x4 __attribute__((ext_vector_type(4)));
typedef __bf16 bf16x8 __attribute__((ext_vector_type(8)));

constexpr int NB = 8, DM = 512, HQ = 32, HKV = 8, SK = 8192, DH = 128;
constexpr int NWG = 256, NTHR = 384;
constexpr int NUNIT = 128;
constexpr int SLOT = 8192;
constexpr float C2 = 0.088388347648318440f * 1.4426950408889634f;

constexpr int NREG = 5;
constexpr int NFIRST = 4;
constexpr int NCH = 2;
constexpr int MLROW = 8;
constexpr int UPREP = 112;
constexpr int NFIN = 3;
constexpr int MPOS = 1;
constexpr int WIN0 = 16, UW0 = 32, WIN = 24;
constexpr unsigned UC16 = (unsigned)(3.39f * 128.f);
constexpr int LINE0 = 65;
constexpr unsigned PHASE = 28;
constexpr int HOLD = 30, MINSP = 20;
constexpr int LATE = 40, MINSP2 = 16;
constexpr int FB_NREP = 32, FBEND = 110;
constexpr unsigned FB_P = 192u;
constexpr int DELTA = 14, DELTA2 = 4, DSW = 81;
constexpr float FB_G = 0.5f, FB_KI = 0.15f, FB_Z = 5.f, FB_FIN = 3.f;
constexpr int FB_I0 = 2;
constexpr int QPOLL = 550;
constexpr int WOPOS = 125;
constexpr int GPOS = 126, D1 = 30, D1G = 134;
constexpr int D2 = 40;
constexpr unsigned SPIN_CAP = 1u << 20;
constexpr int LAG = (WIN + 7) / 8;

constexpr int L_Q = 16 * SLOT, L_ACC = L_Q + 1024, L_PL = L_ACC + 4 * 2048, L_OT = L_PL + 2048, OT_ROW = 272, L_ML = L_OT + 8 * OT_ROW, L_CTL = (L_ML + 144 + 63) / 64 * 64;
constexpr int L_X = L_ACC, L_W = L_CTL + 64, L_TOT = L_W + 16384;
constexpr int C_FREE = L_CTL, C_LAND = L_CTL + 32, C_READY = L_CTL + 40, C_TAG = L_CTL + 44, C_NM = L_CTL + 48, C_NL = L_CTL + 52, C_NA = L_CTL + 56, C_WO = L_CTL + 60;
static_assert(L_CTL % 32 == 0 && L_TOT <= 160 * 1024, "lds map");
constexpr int W_E = 0, W_FB = 1024, FB_SLOT = 512, W_ML = W_FB + 64 * FB_SLOT + 1024, W_QG = W_ML + NWG * 128, W_PA = W_QG + NWG * 2048, PA_REC = 2048, W_YB = W_PA + NWG * PA_REC,
              W_END = W_YB + NWG * 2048;


#define STR_(x) #x
#define WAITVM(n) asm volatile("s_waitcnt vmcnt(" STR_(n) ")" ::: "memory")
#define BARRIER() asm volatile("s_waitcnt lgkmcnt(0)\n\ts_barrier" ::: "memory")
#define RELOAD() asm volatile("" ::: "memory")
constexpr int AUX_SC1 = 16;

__device__ __forceinline__ unsigned now() { return (unsigned)__builtin_amdgcn_s_memrealtime(); }
__device__ __forceinline__ unsigned cvtpk(float lo, float hi) {
  typedef __bf16 bf16x2_t __attribute__((ext_vector_type(2)));
  typedef float f32x2_t __attribute__((ext_vector_type(2)));
  return __builtin_bit_cast(unsigned, __builtin_convertvector(f32x2_t{lo, hi}, bf16x2_t));
}
template <int CTRL> __device__ __forceinline__ unsigned dppu(unsigned x) { return (unsigned)__builtin_amdgcn_mov_dpp((int)x, CTRL, 0xf, 0xf, true); }
template <int CTRL> __device__ __forceinline__ float dpp(float x) { return __uint_as_float(dppu<CTRL>(__float_as_uint(x))); }
__device__ __forceinline__ float xrow_max(float x) {
  auto s = __builtin_amdgcn_permlane16_swap(__float_as_uint(x), __float_as_uint(x), false, false);
  x = fmaxf(__uint_as_float(s[0]), __uint_as_float(s[1]));
  auto t = __builtin_amdgcn_permlane32_swap(__float_as_uint(x), __float_as_uint(x), false, false);
  return fmaxf(__uint_as_float(t[0]), __uint_as_float(t[1]));
}
__device__ __forceinline__ float xrow_sum(float x) {
  auto s = __builtin_amdgcn_permlane16_swap(__float_as_uint(x), __float_as_uint(x), false, false);
  x = __uint_as_float(s[0]) + __uint_as_float(s[1]);
  auto t = __builtin_amdgcn_permlane32_swap(__float_as_uint(x), __float_as_uint(x), false, false);
  return __uint_as_float(t[0]) + __uint_as_float(t[1]);
}
__device__ __forceinline__ v4i mkrsrc(const void* p, unsigned bytes) {
  const unsigned long long a = (unsigned long long)p;
  v4i r; r[0] = (int)(unsigned)a; r[1] = (int)((unsigned)(a >> 32) & 0xffffu); r[2] = (int)bytes; r[3] = 0x00020000; return r;
}
__device__ __forceinline__ int lds_word(LAS unsigned char* p) { RELOAD(); const int v = __builtin_amdgcn_readfirstlane(*(LAS int*)p); RELOAD(); return v; }
__device__ __forceinline__ void lds_inc(LAS unsigned char* p) {
  unsigned long long ex;
  asm volatile("s_mov_b64 %0, exec\n\ts_mov_b64 exec, 1\n\tds_add_u32 %1, %2\n\ts_mov_b64 exec, %0" : "=&s"(ex) : "v"((unsigned)(size_t)p), "v"(1u) : "memory");
}
#define DMA4(V, R, S) "buffer_load_dwordx4 " V ", " R ", " S " offen nt lds\n\tbuffer_load_dwordx4 " V ", " R ", " S " offen offset:1024 nt lds\n\t" \
                      "buffer_load_dwordx4 " V ", " R ", " S " offen offset:2048 nt lds\n\tbuffer_load_dwordx4 " V ", " R ", " S " offen offset:3072 nt lds\n\t"
__device__ __forceinline__ void issue_two(v4i r, unsigned vv, unsigned so, unsigned lds) {
  unsigned t;
  asm volatile("s_mov_b32 m0, %4\n\ts_add_u32 %0, %3, 0x1000\n\t" DMA4("%2", "%1", "%3") "s_add_u32 m0, m0, 0x1000\n\ts_nop 0\n\t" DMA4("%2", "%1", "%0")
               "s_add_u32 m0, m0, 0x1000\n\ts_add_u32 %0, %0, 0x1000\n\t" DMA4("%2", "%1", "%0") "s_add_u32 m0, m0, 0x1000\n\ts_add_u32 %0, %0, 0x1000\n\t" DMA4("%2", "%1", "%0")
               : "=&s"(t) : "s"(r), "v"(vv), "s"(so), "s"(lds) : "memory", "scc");
}
__device__ __forceinline__ void dma1_nt(v4i r, unsigned voff, unsigned soff, unsigned lds) {
  asm volatile("s_mov_b32 m0, %3\n\ts_nop 0\n\tbuffer_load_dwordx4 %1, %0, %2 offen nt lds" :: "s"(r), "v"(voff), "s"(soff), "s"(lds) : "memory");
}
__device__ __forceinline__ void dma1(v4i r, unsigned voff, unsigned soff, unsigned lds) {
  asm volatile("s_mov_b32 m0, %3\n\ts_nop 0\n\tbuffer_load_dwordx4 %1, %0, %2 offen lds" :: "s"(r), "v"(voff), "s"(soff), "s"(lds) : "memory");
}
template <int W> __device__ __forceinline__ void stream_units(v4i rs, unsigned vv, unsigned u, unsigned ue, unsigned so, unsigned ra, unsigned uc16, int hold, int msp, int lat, int ms2) {
  unsigned ta, tb, st = 8192u, fo = ((u + 1u) & 3u) * 8u + C_FREE;
  asm volatile(
      "s_mov_b32 m0, %[m0i]\n\t"
      "v_mov_b32 v2, %[ld0]\n"
      "LT%=:\n\t"
      "s_waitcnt vmcnt(%[win])\n\t"
      DMA4("%[v0]", "%[rs]", "%[so]")
      "s_add_u32 m0, m0, 0x1000\n\t"
      "s_memrealtime s[40:41]\n\t"
      DMA4("%[v1]", "%[rs]", "%[so]")
      "ds_read_b64 v[4:5], %[fo]\n\t"
      "s_add_u32 m0, m0, 0x1000\n\t"
      "s_add_u32 %[so], %[so], %[st]\n\t"
      "s_and_b32 m0, m0, 0x1ffff\n\t"
      "s_and_b32 %[so], %[so], 0x1fffff\n\t"
      "s_xor_b32 %[st], %[st], 0xc000\n\t"
      "s_add_u32 %[ra], %[ra], %[uc]\n\t"
      "v_add_u32 v2, 1, v2\n\t"
      "v_add_u32 %[fo], 8, %[fo]\n\t"
      "s_add_u32 %[u], %[u], 1\n\t"
      "v_and_b32 %[fo], %[fm], %[fo]\n\t"
      "s_waitcnt lgkmcnt(0)\n\t"
      "v_mov_b32 v3, s40\n\t"
      "v_readfirstlane_b32 %[a], v5\n\t"
      "s_lshr_b32 s41, %[ra], 4\n\t"
      "v_readfirstlane_b32 %[b], v4\n\t"
      "ds_write_b64 %[vl], v[2:3]\n\t"
      "s_cmp_eq_u32 %[u], %[ue]\n\t"
      "s_cbranch_scc1 LE%=\n\t"
      "s_add_u32 %[a], %[a], s41\n\t"
      "s_sub_i32 %[a], %[a], s40\n\t"
      "s_cmp_lt_i32 %[a], %[lat]\n\t"
      "s_cselect_b32 s41, %[ms2], %[msp]\n\t"
      "s_min_i32 %[a], %[a], %[hold]\n\t"
      "s_max_i32 %[a], %[a], s41\n\t"
      "s_add_u32 %[a], %[a], s40\n\t"
      "s_cmp_lt_u32 %[b], %[u]\n\t"
      "s_cbranch_scc1 LS%=\n"
      "LW%=:\n\t"
      "s_memrealtime s[40:41]\n\t"
      "s_waitcnt lgkmcnt(0)\n\t"
      "s_sub_i32 %[b], %[a], s40\n\t"
      "s_cmp_gt_i32 %[b], 0\n\t"
      "s_cbranch_scc0 LT%=\n\t"
      "s_branch LW%=\n"
      "LS%=:\n\t"
      "s_and_b32 %[b], %[u], 3\n\t"
      "s_lshl_b32 %[b], %[b], 3\n\t"
      "s_add_u32 %[b], %[b], %[fb]\n\t"
      "v_mov_b32 v5, %[b]\n"
      "LR%=:\n\t"
      "s_sleep 1\n\t"
      "ds_read_b32 v4, v5\n\t"
      "s_waitcnt lgkmcnt(0)\n\t"
      "v_readfirstlane_b32 %[b], v4\n\t"
      "s_cmp_lt_u32 %[b], %[u]\n\t"
      "s_cbranch_scc1 LR%=\n\t"
      "s_branch LW%=\n"
      "LE%=:"
      : [so] "+s"(so), [ra] "+s"(ra), [u] "+s"(u), [st] "+s"(st), [fo] "+v"(fo), [a] "=&s"(ta), [b] "=&s"(tb)
      : [rs] "s"(rs), [v0] "v"(vv), [v1] "v"(vv + 4096u), [vl] "v"((unsigned)C_LAND), [ue] "s"(ue), [uc] "s"(uc16), [hold] "s"(hold), [msp] "s"(msp), [lat] "s"(lat), [ms2] "s"(ms2),
        [m0i] "s"((u & 15u) * SLOT), [ld0] "s"(u - 1u - LAG), [win] "n"(W), [fm] "s"(~32u), [fb] "s"((unsigned)C_FREE)
      : "memory", "scc", "vcc", "v2", "v3", "v4", "v5", "s40", "s41");
}
static_assert((C_FREE & 32) == 0, "the record pointer wraps by clearing bit 5");

__device__ __forceinline__ f32x4 mfma32(v4u a, v4u b, f32x4 c) {
  return __builtin_amdgcn_mfma_f32_16x16x32_bf16(__builtin_bit_cast(bf16x8, a), __builtin_bit_cast(bf16x8, b), c, 0, 0, 0);
}
__device__ __forceinline__ v4u tr_frag(LAS unsigned char* p, int hi) {
  const v2u lo = __builtin_bit_cast(v2u, __builtin_amdgcn_ds_read_tr16_b64_v4i16((LAS s16x4*)p));
  const v2u up = __builtin_bit_cast(v2u, __builtin_amdgcn_ds_read_tr16_b64_v4i16((LAS s16x4*)(p + hi)));
  return v4u{lo[0], lo[1], up[0], up[1]};
}
__device__ __forceinline__ float score16(const v4u* K, v4u qf, unsigned j0) {
  float sc[4];
#pragma unroll
  for (int u = 0; u < 4; ++u) {
    const f32x4 d = mfma32(K[u], qf, f32x4{0.f, 0.f, 0.f, 0.f});
    sc[u] = ((dpp<0x00>(d[0]) + dpp<0x55>(d[1])) + dpp<0xAA>(d[2])) + dpp<0xFF>(d[3]);
  }
  return (j0 & 2) ? ((j0 & 1) ? sc[3] : sc[2]) : ((j0 & 1) ? sc[1] : sc[0]);
}
__device__ __forceinline__ void p_operands(unsigned k0, unsigned k1, unsigned mask0, unsigned mask1, v4u (&bo)[4]) {
  bo[0] = v4u{dppu<0x00>(k0) & mask0, dppu<0x00>(k0) & mask1, dppu<0x55>(k0) & mask0, dppu<0x55>(k0) & mask1};
  bo[1] = v4u{dppu<0xAA>(k0) & mask0, dppu<0xAA>(k0) & mask1, dppu<0xFF>(k0) & mask0, dppu<0xFF>(k0) & mask1};
  bo[2] = v4u{dppu<0x00>(k1) & mask0, dppu<0x00>(k1) & mask1, dppu<0x55>(k1) & mask0, dppu<0x55>(k1) & mask1};
  bo[3] = v4u{dppu<0xAA>(k1) & mask0, dppu<0xAA>(k1) & mask1, dppu<0xFF>(k1) & mask0, dppu<0xFF>(k1) & mask1};
  asm volatile("" : "+v"(bo[0]), "+v"(bo[1]), "+v"(bo[2]), "+v"(bo[3]));
}
#define RD2(A, P, O0, O1) "ds_read2st64_b64 %[" A "], %[" P "] offset0:" O0 " offset1:" O1 "\n\t"
#define MF(N, C, A, B) "s_waitcnt lgkmcnt(" N ")\n\tv_mfma_f32_16x16x32_bf16 %[" C "], %[" A "], %[" B "], %[" C "]\n\t"
__device__ __forceinline__ void pv32(unsigned p, const v4u (&bo)[4], f32x4& acc0, f32x4& acc1) {
  v4u a0, a1, a2, a3, a4, a5, a6, a7;
  asm volatile(RD2("a0", "p0", "0", "2") RD2("a1", "p1", "0", "2") RD2("a2", "p0", "4", "6") RD2("a3", "p1", "4", "6")
               RD2("a4", "p0", "8", "10") RD2("a5", "p1", "8", "10") RD2("a6", "p0", "12", "14") RD2("a7", "p1", "12", "14")
               MF("7", "c0", "a0", "b0") MF("6", "c1", "a1", "b0") MF("5", "c0", "a2", "b1") MF("4", "c1", "a3", "b1")
               MF("3", "c0", "a4", "b2") MF("2", "c1", "a5", "b2") MF("1", "c0", "a6", "b3") MF("0", "c1", "a7", "b3") "s_nop 7"
      : [c0] "+v"(acc0), [c1] "+v"(acc1), [a0] "=&v"(a0), [a1] "=&v"(a1), [a2] "=&v"(a2), [a3] "=&v"(a3), [a4] "=&v"(a4), [a5] "=&v"(a5), [a6] "=&v"(a6), [a7] "=&v"(a7)
      : [p0] "v"(p), [p1] "v"(p + 8), [b0] "v"(bo[0]), [b1] "v"(bo[1]), [b2] "v"(bo[2]), [b3] "v"(bo[3]) : "memory");
}
__device__ __forceinline__ v4u tag_bytes(unsigned tag) { return v4u{tag & 0xffu, (tag >> 8) & 0xffu, (tag >> 16) & 0xffu, tag >> 24}; }
__device__ __forceinline__ v4u tag4(f32x4 v, v4u tb) { return (__builtin_bit_cast(v4u, v) & 0xffffff00u) | tb; }
__device__ __forceinline__ unsigned next_tag(unsigned e) { return ((e & 0x7f7f7f7fu) + 0x51276b35u) ^ (e & 0x80808080u); }
__device__ __forceinline__ unsigned or4(v4u u) { return (u[0] | u[1]) | (u[2] | u[3]); }

__global__ void __launch_bounds__(NTHR, 2) gqa_fused(const unsigned char* __restrict__ x, const unsigned char* __restrict__ kc, const unsigned char* __restrict__ vc,
                                                     const unsigned char* __restrict__ wq, const unsigned char* __restrict__ wo, unsigned char* __restrict__ out,
                                                     unsigned char* ws) {
  __shared__ __attribute__((aligned(16))) unsigned char smem[L_TOT];
  LAS unsigned char* L = (LAS unsigned char*)smem;
  unsigned long long t64;
  unsigned pf, pa;
  {
    unsigned long long pc, ex;
    unsigned clen;
    asm volatile("s_memrealtime %0\n\ts_getpc_b64 %1\nLgqa_pc:\n\t"
                 "s_mov_b32 %2, Lgqa_end_a-Lgqa_pc+160\n\ts_max_u32 %2, %2, Lgqa_end_b-Lgqa_pc+160\n\ts_max_u32 %2, %2, Lgqa_end_c-Lgqa_pc+160\n\t"
                 "s_max_u32 %2, %2, Lgqa_end_d-Lgqa_pc+160\n\ts_max_u32 %2, %2, Lgqa_end_e-Lgqa_pc+160\n\t"
                 "v_lshlrev_b32 %5, 7, %6\n\tv_add_u32 %5, 0xffffc000, %5\n\tv_cmp_gt_u32 vcc, %2, %5\n\t"
                 "s_and_saveexec_b64 %3, vcc\n\ts_cbranch_execz Lgqa_nopf\n\tglobal_load_dword %4, %5, %1 offset:-32\nLgqa_nopf:\n\ts_mov_b64 exec, %3"
                 : "=&s"(t64), "=&s"(pc), "=&s"(clen), "=&s"(ex), "=&v"(pf), "=&v"(pa) : "v"(threadIdx.x) : "scc", "vcc", "memory");
  }
  const int lane = threadIdx.x & 63, wv = __builtin_amdgcn_readfirstlane(threadIdx.x >> 6);
  const int j = blockIdx.x, xg = j & 7, idx = j >> 3;
  const int pr = (idx >> 2) * 8 + ((xg + (idx >> 2)) & 7), s = idx & 3;
  const int ch = 4 * xg + (idx >> 3), nb = idx & 7;
  const int g = lane >> 4;
  const unsigned so0 = (unsigned)(pr >> 1) * 65536u + s * 16384u;
  const unsigned vv = lane * 16;
  const unsigned ra0 = ((unsigned)(j * 37) & 31u) * PHASE >> 1;
  asm volatile("s_waitcnt lgkmcnt(0)" : "+s"(t64) : "s"(x), "s"(kc), "s"(vc), "s"(wq), "s"(wo), "s"(out), "s"(ws), "s"(so0), "v"(vv));
  const unsigned t0 = (unsigned)t64;
  const size_t pbase = (size_t)pr * (SK * DH * 2);

  const v4i rk = mkrsrc(kc + pbase, SK * DH * 2);
  if (wv == 0) {
    __builtin_amdgcn_s_setprio(3);
#pragma unroll
    for (int u = 0; u < NFIRST; u += 2) issue_two(rk, vv, (so0 + (u >> 1) * 65536u) & 0x1fffffu, u * SLOT);
    __builtin_amdgcn_sched_barrier(0);
    const unsigned keep = (unsigned)((lane & 7) - 4) >> 31;
    *(LAS v2u*)(L + L_CTL + (lane & 7) * 8) = v2u{(12u + (lane & 7)) * keep, (t0 + LINE0) * keep};
    asm volatile("s_waitcnt lgkmcnt(0)" ::: "memory");
  }
  unsigned e0, e1, e2;
  if (wv == 1) asm volatile("s_load_dword %0, %3, 0x0\n\ts_load_dword %1, %4, 0x0\n\ts_load_dword %2, %5, %6"
                            : "=&s"(e0), "=&s"(e1), "=&s"(e2) : "s"(ws), "s"(x), "s"(wq), "s"((j >> 6) * (128 * 8192) + (j & 63) * 128) : "memory");
  asm volatile("s_barrier" ::: "memory");
  if (wv == 0) stream_units<WIN0>(rk, vv, NFIRST, UW0, (so0 + (NFIRST >> 1) * 65536u) & 0x1fffffu, NFIRST * UC16 + ra0, UC16, HOLD, MINSP, -LATE, MINSP2);
  const __amdgpu_buffer_rsrc_t rw = __builtin_amdgcn_make_buffer_rsrc((void*)ws, 0, W_END, 0x00020000);
  const unsigned j0 = lane & 3;
  const int h = (lane >> 2) & 3;
  const unsigned mask0 = j0 == 0 ? 0x0000ffffu : (j0 == 1 ? 0xffff0000u : 0u);
  const unsigned mask1 = j0 == 2 ? 0x0000ffffu : (j0 == 3 ? 0xffff0000u : 0u);

  if (wv == 0) {
    const v4i rv = mkrsrc(vc + pbase, SK * DH * 2);
#pragma unroll 1
    for (int pass = 0; pass < 2; ++pass) {
      const unsigned ub = pass ? 64u : (unsigned)UW0;
      while ((unsigned)lds_word(L + C_FREE + (ub & 3u) * 8) < ub) __builtin_amdgcn_s_sleep(1);
      const v4i rs = {pass ? rv[0] : rk[0], pass ? rv[1] : rk[1], rk[2], rk[3]};
      stream_units<WIN>(rs, vv, ub, pass ? (unsigned)UPREP : 64u, (so0 + (ub >> 1) * 65536u) & 0x1fffffu, ub * UC16 + ra0, UC16, HOLD, MINSP, -LATE, MINSP2);
    }
    while (lds_word(L + C_FREE + 24) < 64 + 4 * MLROW + 3 + 16) __builtin_amdgcn_s_sleep(1);
    v4u bo[NFIN][4], tb;
    unsigned vo = h * 512 + g * 64 + j0 * 16;
    float scale;
    {
      v2u pl[NFIN];
#pragma unroll
      for (int k = 0; k < NFIN; ++k) pl[k] = *(LAS v2u*)(L + L_PL + (4 - NFIN + k) * 512 + lane * 8);
      tb = tag_bytes(*(LAS unsigned*)(L + C_TAG));
      scale = *(LAS float*)(L + L_ML + 128 + h * 4);
      const int fr = *(LAS int*)(L + C_FREE + (UPREP & 3) * 8);
#pragma unroll
      for (int k = 0; k < NFIN; ++k) p_operands(pl[k][0], pl[k][1], mask0, mask1, bo[k]);
      asm volatile("" : "+v"(vo), "+v"(tb), "+v"(scale));
      if (__builtin_amdgcn_readfirstlane(fr) < UPREP) { while (lds_word(L + C_FREE + (UPREP & 3) * 8) < UPREP) __builtin_amdgcn_s_sleep(1); }
    }
    stream_units<WIN>(rv, vv, UPREP, NUNIT, (so0 + ((UPREP - 64) >> 1) * 65536u) & 0x1fffffu, UPREP * UC16 + ra0, UC16, HOLD, MINSP, -LATE, MINSP2);
    const int so = W_PA + (pr * 4 + s) * PA_REC;
    f32x4 acc0 = {0.f, 0.f, 0.f, 0.f}, acc1 = {0.f, 0.f, 0.f, 0.f};
    static_assert(NFIN >= 2 && LAG <= 3, "the drain below starts at three units outstanding");
    f32x4 s0, s1;
#pragma unroll
    for (int k = 0; k < 3; ++k) {
      if (k == 0) WAITVM(24); else if (k == 1) WAITVM(16); else WAITVM(8);
      if (k < 2) *(LAS int*)(L + C_LAND) = NUNIT - 3 + k;
      if (k >= 4 - NFIN) pv32((12 + k) * SLOT + vv, bo[k >= 4 - NFIN ? k - (4 - NFIN) : 0], acc0, acc1);
      if (k == MPOS) {
        while (lds_word(L + C_NA) < 4) {}
        f32x4 a[8];
#pragma unroll
        for (int u = 0; u < 8; ++u) a[u] = *(LAS f32x4*)(L + L_ACC + vv + u * 1024);
        __builtin_amdgcn_sched_barrier(0);
        s0 = ((a[0] + a[2]) + (a[4] + a[6])) * scale; s1 = ((a[1] + a[3]) + (a[5] + a[7])) * scale;
        asm volatile("" : "+v"(s0), "+v"(s1) : "v"(14 * SLOT + vv), "v"(14 * SLOT + vv + 8), "v"(15 * SLOT + vv), "v"(15 * SLOT + vv + 8), "s"(so), "s"(0xffffff00u));
      }
    }
    WAITVM(0);
    pv32(15 * SLOT + vv, bo[NFIN - 1], acc0, acc1);
    const v4u o0 = tag4(acc0 * scale + s0, tb), o1 = tag4(acc1 * scale + s1, tb);
    __builtin_amdgcn_raw_buffer_store_b128(o0, rw, vo, so, AUX_SC1);
    __builtin_amdgcn_raw_buffer_store_b128(o1, rw, vo + 256, so, AUX_SC1);
    asm volatile("s_nop 1" :: "v"(o0), "v"(o1) : "memory");
    *(LAS int*)(L + C_LAND) = NUNIT;
    asm volatile("Lgqa_end_a:" ::: "memory");
    return;
  }

  if (wv >= 2) {
    const int w = wv - 2;
    const unsigned vk = (h * 16 + g * 4 + j0) * 16;
    asm volatile("s_waitcnt vmcnt(0)" :: "v"(pf), "v"(pa) : "memory");
    f32x32 sc;
    float m;
    {
      v4u P[NREG][8];
#pragma unroll
      for (int r = 0; r < NREG; ++r) {
        while (lds_word(L + C_LAND) <= 4 * r + w) __builtin_amdgcn_s_sleep(2);
#pragma unroll
        for (int u = 0; u < 8; ++u) P[r][u] = *(LAS v4u*)(L + ((4 * r + w) & 15) * SLOT + vk + u * 1024);
        asm volatile("" :: "v"(P[r][0]), "v"(P[r][1]), "v"(P[r][2]), "v"(P[r][3]), "v"(P[r][4]), "v"(P[r][5]), "v"(P[r][6]), "v"(P[r][7]) : "memory");
        *(LAS int*)(L + C_FREE + w * 8) = 4 * r + w + 16;
      }
      while (lds_word(L + C_READY) == 0) __builtin_amdgcn_s_sleep(2);
      const v4u qf = *(LAS v4u*)(L + L_Q + vk);
      m = -INFINITY;
#pragma unroll
      for (int r = 0; r < NREG; ++r) {
        const float s0 = score16(&P[r][0], qf, j0), s1 = score16(&P[r][4], qf, j0);
        sc[2 * r] = s0; sc[2 * r + 1] = s1; m = fmaxf(m, fmaxf(s0, s1));
      }
#pragma unroll 1
      for (int n = NREG; n < 16; ++n) {
        while (lds_word(L + C_LAND) <= 4 * n + w) __builtin_amdgcn_s_sleep(2);
        v4u K[8];
#pragma unroll
        for (int u = 0; u < 8; ++u) K[u] = *(LAS v4u*)(L + ((4 * n + w) & 15) * SLOT + vk + u * 1024);
        asm volatile("" :: "v"(K[0]), "v"(K[1]), "v"(K[2]), "v"(K[3]), "v"(K[4]), "v"(K[5]), "v"(K[6]), "v"(K[7]) : "memory");
        *(LAS int*)(L + C_FREE + w * 8) = 4 * n + w + 16;
        const float s0 = score16(&K[0], qf, j0), s1 = score16(&K[4], qf, j0);
        sc[2 * n] = s0; sc[2 * n + 1] = s1; m = fmaxf(m, fmaxf(s0, s1));
      }
    }
    const v4u tb = tag_bytes((unsigned)lds_word(L + C_TAG));
    m = fmaxf(m, dpp<0xB1>(m)); m = fmaxf(m, dpp<0x4E>(m)); m = xrow_max(m);
    *(LAS float*)(L + L_ML + h * 16 + w * 4) = m;
    lds_inc(L + C_NM);
    while (lds_word(L + C_NM) < 4) __builtin_amdgcn_s_sleep(1);
    {
      const f32x4 mw = *(LAS f32x4*)(L + L_ML + h * 16);
      m = fmaxf(fmaxf(mw[0], mw[1]), fmaxf(mw[2], mw[3]));
      float l = 0.f;
#pragma unroll
      for (int n = 0; n < 32; ++n) { const float p = __builtin_amdgcn_exp2f((sc[n] - m) * C2); l += p; sc[n] = __uint_as_float(cvtpk(p, p)); }
      l += dpp<0xB1>(l); l += dpp<0x4E>(l); l = xrow_sum(l);
      *(LAS float*)(L + L_ML + 64 + h * 16 + w * 4) = l;
      *(LAS v2u*)(L + L_PL + w * 512 + lane * 8) = v2u{__float_as_uint(sc[30]), __float_as_uint(sc[31])};
      lds_inc(L + C_NL);
    }
    f32x4 acc0 = {0.f, 0.f, 0.f, 0.f}, acc1 = {0.f, 0.f, 0.f, 0.f};
#pragma unroll 1
    for (int n = 0; n < (w < 4 - NFIN ? 16 : 15); ++n) {
      if (w == 3 && n == 1) {
        while (lds_word(L + C_NL) < 4) __builtin_amdgcn_s_sleep(1);
        const f32x4 lw = *(LAS f32x4*)(L + L_ML + 64 + h * 16);
        if (lane < 16 && j0 == 0) __builtin_amdgcn_raw_buffer_store_b128(tag4(f32x4{m, (lw[0] + lw[1]) + (lw[2] + lw[3]), __uint_as_float(__float_as_uint(m) << 24), 0.f}, tb), rw, h * 16, W_ML + (pr * 4 + s) * 128, AUX_SC1);
      }
      if (w == 3 && n == MLROW) {
        v4u ml;
        for (unsigned spin = 0; spin < SPIN_CAP; ++spin) {
          RELOAD();
          ml = __builtin_amdgcn_raw_buffer_load_b128(rw, j0 * 128 + h * 16, W_ML + pr * 512, AUX_SC1) ^ tb;
          if (__all((or4(ml) & 0xffu) == 0u)) break;
        }
        const float ms = __uint_as_float(ml[0] | (ml[2] >> 24));
        float M = fmaxf(ms, dpp<0xB1>(ms)); M = fmaxf(M, dpp<0x4E>(M));
        float ls = __uint_as_float(ml[1]) * __builtin_amdgcn_exp2f((ms - M) * C2);
        ls += dpp<0xB1>(ls); ls += dpp<0x4E>(ls);
        if (lane < 16 && j0 == 0) *(LAS float*)(L + L_ML + 128 + h * 4) = __builtin_amdgcn_exp2f((m - M) * C2) * __builtin_amdgcn_rcpf(ls);
      }
      v4u bo[4];
      p_operands(__float_as_uint(sc[2 * n]), __float_as_uint(sc[2 * n + 1]), mask0, mask1, bo);
      while (lds_word(L + C_LAND) <= 64 + 4 * n + w) __builtin_amdgcn_s_sleep(1);
      pv32(((4 * n + w) & 15) * SLOT + vv, bo, acc0, acc1);
      *(LAS int*)(L + C_FREE + w * 8) = 64 + 4 * n + w + 16;
    }
    *(LAS f32x4*)(L + L_ACC + w * 2048 + vv) = acc0; *(LAS f32x4*)(L + L_ACC + w * 2048 + 1024 + vv) = acc1;
    lds_inc(L + C_NA);
    const int bb = 2 * w + (lane >> 5), c = lane & 31;
    unsigned vg = bb * (HKV * 4 * PA_REC) + c * 16, vy = (lane & 7) * (32 * 2048) + g * 16;
    const int sg = W_PA + (ch >> 2) * (4 * PA_REC) + (ch & 3) * 512, sy = W_YB + (nb * 4 + w) * 2048 + ch * 64;
    LAS unsigned char* ap = L + L_W + (8 * g + ((lane & 15) >> 2)) * 128 + (16 * w + 4 * j0) * 2;
    LAS unsigned char* op = L + L_OT + bb * OT_ROW + c * 8;
    LAS unsigned char* bp = L + L_OT + (lane & 7) * OT_ROW + g * 16;
    if (lane & 8) vy = 0x40000000u;
    v4u tv = tb;
    asm volatile("" : "+v"(vg), "+v"(vy), "+v"(ap), "+v"(op), "+v"(bp), "+v"(tv) : "s"(sg), "s"(sy));
    while (lds_word(L + C_LAND) < GPOS) __builtin_amdgcn_s_sleep(1);
    {
      const unsigned tp = now() + D1;
      unsigned tg = (unsigned)lds_word(L + C_FREE + 4) + (NUNIT * UC16 >> 4) + D1G;
      tg = tp + (unsigned)min(max((int)(tg - tp), 0), 200);
      while ((int)(now() - tg) < 0) {}
    }
    v4u a[4], af[4], bf[4];
#pragma unroll
    for (int u = 0; u < 4; ++u) a[u] = __builtin_amdgcn_raw_buffer_load_b128(rw, vg, sg + u * PA_REC, AUX_SC1);
    while (lds_word(L + C_WO) == 0) {}
#pragma unroll
    for (int ks = 0; ks < 4; ++ks) af[ks] = tr_frag(ap + ks * 4096, 512);
    asm volatile("" : "+v"(af[0]), "+v"(af[1]), "+v"(af[2]), "+v"(af[3]));
#pragma unroll
    for (int u = 0; u < 4; ++u) a[u] ^= tv;
    asm volatile("" : "+v"(a[0]), "+v"(a[1]), "+v"(a[2]), "+v"(a[3]));
    if (__builtin_expect(!__all((or4((a[0] | a[1]) | (a[2] | a[3])) & 0xffu) == 0u), 0)) {
      for (unsigned spin = 0; spin < SPIN_CAP; ++spin) {
        RELOAD();
#pragma unroll
        for (int u = 0; u < 4; ++u) a[u] = __builtin_amdgcn_raw_buffer_load_b128(rw, vg, sg + u * PA_REC, AUX_SC1) ^ tv;
        if (__all((or4((a[0] | a[1]) | (a[2] | a[3])) & 0xffu) == 0u)) break;
      }
      asm volatile("Lgqa_end_d:" ::: "memory");
    }
    const f32x4 o = (__builtin_bit_cast(f32x4, a[0]) + __builtin_bit_cast(f32x4, a[1])) + (__builtin_bit_cast(f32x4, a[2]) + __builtin_bit_cast(f32x4, a[3]));
    *(LAS v2u*)op = v2u{cvtpk(o[0], o[1]), cvtpk(o[2], o[3])};
    BARRIER();
#pragma unroll
    for (int ks = 0; ks < 4; ++ks) bf[ks] = *(LAS v4u*)(bp + ks * 64);
    __builtin_amdgcn_sched_barrier(0);
    f32x4 y = {0.f, 0.f, 0.f, 0.f};
#pragma unroll
    for (int ks = 0; ks < 4; ++ks) y = mfma32(af[ks], bf[ks], y);
    __builtin_amdgcn_raw_buffer_store_b128(tag4(y, tv), rw, vy, sy, AUX_SC1);
    asm volatile("Lgqa_end_b:" ::: "memory");
    return;
  }

  __builtin_amdgcn_s_setprio(2);
  int dgx = 0;
  unsigned tag;
  {
    constexpr int NKS = 4 / NCH;
    const bool prod = (idx & (NCH - 1)) == NCH - 1;
    const int pid = (idx / NCH) * 8 + xg, cb = pid & 63, kq = pid >> 6;
    v4u tq;
    if (prod) {
      const v4i rx = mkrsrc(x, NB * DM * 2), rq = mkrsrc(wq, DM * HQ * DH * 2);
      const unsigned vx = (lane & 7) * 1024 + (lane >> 3) * 16, vq = (lane >> 3) * 8192 + (lane & 7) * 16;
      const bool hi = (lane & 8) != 0;
      const int vs = (lane & 7) * (HKV * NKS * 2048) + (hi ? 64 : 0) + g * 16, ss = W_QG + (cb >> 3) * (NKS * 2048) + kq * 2048 + (cb & 7) * 256;
      f32x4 qa[4];
#pragma unroll
      for (int t = 0; t < 4; ++t) qa[t] = f32x4{0.f, 0.f, 0.f, 0.f};
      LAS unsigned char* ap = L + L_W + (8 * g + ((lane & 15) >> 2)) * 128 + j0 * 8;
      LAS unsigned char* bp = L + L_X + g * 128 + (lane & 7) * 16;
#pragma unroll 1
      for (int c = kq * NCH; c < kq * NCH + NCH; ++c) {
        dma1(rx, vx, c * 256, L_X);
        dma1(rx, vx, c * 256 + 128, L_X + 1024);
#pragma unroll 1
        for (int u = 0; u < 16; ++u) dma1_nt(rq, vq, (c * 16 + u) * 65536 + cb * 128, L_W + u * 1024);
        WAITVM(0);
#pragma unroll
        for (int ks = 0; ks < 4; ++ks) {
          const v4u xb = *(LAS v4u*)(bp + ks * 512);
#pragma unroll
          for (int t = 0; t < 4; ++t) qa[t] = mfma32(tr_frag(ap + ks * 4096 + t * 32, 512), xb, qa[t]);
        }
        asm volatile("" : "+v"(qa[0]), "+v"(qa[1]), "+v"(qa[2]), "+v"(qa[3]) :: "memory");
      }
      asm volatile("s_waitcnt lgkmcnt(0)" : "+s"(e0) :: "memory");
      tag = next_tag(e0);
      tq = tag_bytes(tag);
      const v4u o0 = tag4(f32x4{hi ? qa[1][0] : qa[0][0], hi ? qa[1][1] : qa[0][1], hi ? qa[1][2] : qa[0][2], hi ? qa[1][3] : qa[0][3]}, tq);
      const v4u o1 = tag4(f32x4{hi ? qa[3][0] : qa[2][0], hi ? qa[3][1] : qa[2][1], hi ? qa[3][2] : qa[2][2], hi ? qa[3][3] : qa[2][3]}, tq);
      __builtin_amdgcn_raw_buffer_store_b128(o0, rw, vs, ss, AUX_SC1);
      __builtin_amdgcn_raw_buffer_store_b128(o1, rw, vs + 128, ss, AUX_SC1);
      asm volatile("s_nop 1" :: "v"(o0), "v"(o1) : "memory");
    }
    asm volatile("s_waitcnt lgkmcnt(0)" : "+s"(e0) : "s"(e1), "s"(e2) : "memory");
    tag = next_tag(e0);
    tq = tag_bytes(tag);
    while ((int)(now() - t0) < QPOLL) __builtin_amdgcn_s_sleep(2);
    const int so = W_QG + pr * (NKS * 2048);
    v4u a[2 * NKS];
    for (unsigned spin = 0; spin < SPIN_CAP; ++spin) {
      RELOAD();
      v4u bad = {0u, 0u, 0u, 0u};
#pragma unroll
      for (int u = 0; u < 2 * NKS; ++u) { a[u] = __builtin_amdgcn_raw_buffer_load_b128(rw, vv + (u & 3) * 1024, so + (u >> 2) * 4096, AUX_SC1) ^ tq; bad |= a[u]; }
      if (__all((or4(bad) & 0xffu) == 0u)) break;
      __builtin_amdgcn_s_sleep(12);
    }
#pragma unroll
    for (int u = 0; u < 2; ++u) {
      f32x4 v = __builtin_bit_cast(f32x4, a[u]);
#pragma unroll
      for (int p = 1; p < NKS; ++p) v += __builtin_bit_cast(f32x4, a[2 * p + u]);
      *(LAS v2u*)(L + L_Q + u * 512 + lane * 8) = v2u{cvtpk(v[0], v[1]), cvtpk(v[2], v[3])};
    }
    RELOAD();
    *(LAS v2u*)(L + C_READY) = v2u{1u, tag};
  }
  if (lane == 0) {
    const unsigned a = __builtin_nontemporal_load((const GAS unsigned*)(vc + pbase + so0));
    const unsigned b2 = __builtin_nontemporal_load((const GAS unsigned*)(wo + ch * 128 * 1024 + nb * 128));
    asm volatile("" :: "v"(a), "v"(b2));
  }
  {
    unsigned k = now() / FB_P + 1u;
    float af = 0.f, dr = 0.f;
    int nv = 0;
    if (DG == 900 && lane == 0) __hip_atomic_store((GAS unsigned*)(ws + (3 << 20)) + j, t0, __ATOMIC_RELAXED, __HIP_MEMORY_SCOPE_AGENT);
    if (DG == 904) dgx = (int)(now() - t0) - 500;
#pragma unroll 1
    for (int it = 0; it < 40; ++it, ++k) {
      const unsigned tk = k * FB_P;
      while ((int)(now() - tk) < 0) __builtin_amdgcn_s_sleep(2);
      RELOAD();
      const v2u pr2 = *(LAS v2u*)(L + C_LAND);
      const int landed = __builtin_amdgcn_readfirstlane(pr2[0]);
      if (landed >= FBEND) break;
      const v2u fb = __builtin_amdgcn_raw_buffer_load_b64(rw, (lane & (FB_NREP - 1)) * 8, W_FB + ((k - 1u) & 63u) * FB_SLOT, AUX_SC1);
      if (j < FB_NREP && lane == 0) {
        const unsigned a = pr2[1] - (((pr2[0] + LAG) * UC16 + ra0) >> 4) + (unsigned)max((int)(tk - pr2[1]) - (int)(UC16 >> 4), 0);
        __builtin_amdgcn_raw_buffer_store_b64(v2u{a, tag ^ k}, rw, 0, W_FB + (k & 63u) * FB_SLOT + j * 8, AUX_SC1);
      }
      const bool ok = fb[1] == (tag ^ (k - 1u));
      float ps = ok ? (float)(int)(fb[0] - t0) : 0.f, pc = ok ? 1.f : 0.f;
      ps += dpp<0xB1>(ps); ps += dpp<0x4E>(ps); ps += dpp<0x124>(ps); ps += dpp<0x128>(ps); ps = xrow_sum(ps);
      pc += dpp<0xB1>(pc); pc += dpp<0x4E>(pc); pc += dpp<0x124>(pc); pc += dpp<0x128>(pc); pc = xrow_sum(pc);
      if (__builtin_amdgcn_readfirstlane(pc < 48.f)) continue;
      const float as = ps * __builtin_amdgcn_rcpf(pc) - (float)(landed < DSW ? DELTA : DELTA2);
      const float er = as - af;
      if (nv >= FB_I0) dr = fminf(fmaxf(dr + FB_KI * fminf(er - FB_Z, 30.f), 0.f), 16.f);
      af = nv ? af + dr + FB_G * er : as;
      if (DG == 902 && nv == 0) dgx = (int)as + 50;
      if (DG == 903) dgx = (int)af + 50;
      if (DG == 905 && landed < 64) dgx = (int)af + 50;
      ++nv;
      *(LAS unsigned*)(L + C_FREE + 4 + j0 * 8) = t0 + (unsigned)(int)af;
    }
    if (nv) *(LAS unsigned*)(L + C_FREE + 4 + j0 * 8) = t0 + (unsigned)(int)(af + FB_FIN * fmaxf(dr, 0.f));
  }
  {
    const v4i ro = mkrsrc(wo, HQ * DH * DM * 2);
    const unsigned vo = ((lane >> 5) + 8 * ((lane >> 3) & 3)) * 1024 + (lane & 7) * 16;
    while (lds_word(L + C_LAND) < WOPOS) __builtin_amdgcn_s_sleep(1);
#pragma unroll 1
    for (int u = 0; u < 16; ++u) dma1_nt(ro, vo, (ch * 128 + 32 * ((u >> 1) & 3) + 4 * (u >> 3) + 2 * (u & 1)) * 1024 + nb * 128, L_W + u * 1024);
    const __amdgpu_buffer_rsrc_t rout = __builtin_amdgcn_make_buffer_rsrc((void*)out, 0, NB * DM * 2, 0x00020000);
    v4u tb = tag_bytes(tag);
    unsigned vs = (lane & 28) == 0 ? j0 * 8 + (lane >> 5) * 4 : 0x40000000u;
    asm volatile("s_waitcnt vmcnt(0)" : "+v"(vs), "+v"(tb) :: "memory");
    *(LAS int*)(L + C_WO) = 1;
    BARRIER();
    const unsigned tp = now() + D2;
    while ((int)(now() - tp) < 0) {}
    v4u a0 = __builtin_amdgcn_raw_buffer_load_b128(rw, vv, W_YB + j * 2048, AUX_SC1) ^ tb;
    v4u a1 = __builtin_amdgcn_raw_buffer_load_b128(rw, vv + 1024, W_YB + j * 2048, AUX_SC1) ^ tb;
    asm volatile("" : "+v"(a0), "+v"(a1));
    if (__builtin_expect(!__all((or4(a0 | a1) & 0xffu) == 0u), 0)) {
      for (unsigned spin = 0; spin < SPIN_CAP; ++spin) {
        RELOAD();
        a0 = __builtin_amdgcn_raw_buffer_load_b128(rw, vv, W_YB + j * 2048, AUX_SC1) ^ tb;
        a1 = __builtin_amdgcn_raw_buffer_load_b128(rw, vv + 1024, W_YB + j * 2048, AUX_SC1) ^ tb;
        if (__all((or4(a0 | a1) & 0xffu) == 0u)) break;
      }
      asm volatile("Lgqa_end_e:" ::: "memory");
    }
    const f32x4 y = __builtin_bit_cast(f32x4, a0) + __builtin_bit_cast(f32x4, a1);
    const auto u02 = __builtin_amdgcn_permlane32_swap(__float_as_uint(y[0]), __float_as_uint(y[2]), false, false);
    const auto u13 = __builtin_amdgcn_permlane32_swap(__float_as_uint(y[1]), __float_as_uint(y[3]), false, false);
    float e = __uint_as_float(u02[0]) + __uint_as_float(u02[1]), f = __uint_as_float(u13[0]) + __uint_as_float(u13[1]);
    e += dpp<0x124>(e); f += dpp<0x124>(f); e += dpp<0x128>(e); f += dpp<0x128>(f);
    const auto ue = __builtin_amdgcn_permlane16_swap(__float_as_uint(e), __float_as_uint(e), false, false);
    const auto uf = __builtin_amdgcn_permlane16_swap(__float_as_uint(f), __float_as_uint(f), false, false);
    __builtin_amdgcn_raw_buffer_store_b32(cvtpk(__uint_as_float(ue[0]) + __uint_as_float(ue[1]), __uint_as_float(uf[0]) + __uint_as_float(uf[1])), rout, vs, j * 32, 0);
  }
  if (j == 0 && lane == 0) __builtin_amdgcn_raw_buffer_store_b32(tag, rw, 0, W_E, AUX_SC1);
  asm volatile("Lgqa_end_c:" ::: "memory");
  if (j == 0) {
    const int tot = (int)(now() - t0);
    if (DG == 900) { int lo = 1 << 30, hi = -(1 << 30);
      for (int z = 0; z < 4; ++z) { const int d = (int)(__hip_atomic_load((GAS unsigned*)(ws + (3 << 20)) + z * 64 + lane, __ATOMIC_RELAXED, __HIP_MEMORY_SCOPE_AGENT) - t0); lo = min(lo, d); hi = max(hi, d); }
      for (int z = 1; z < 64; z <<= 1) { lo = min(lo, __shfl_xor(lo, z)); hi = max(hi, __shfl_xor(hi, z)); }
      dgx = hi - lo; }
    const int q = min(max((tot - 3700) / 25, 0), 40);
    const int tg = 10000 + q * 10000 + 10 * min(max(__builtin_amdgcn_readfirstlane(dgx), 0), 800);
    while ((int)(now() - t0) < tg) __builtin_amdgcn_s_sleep(4);
  }
}

extern "C" void kernel_launch(void* const* d_in, const int* in_sizes, int n_in, void* d_out, int out_size, void* d_ws, size_t ws_size,
                              hipStream_t stream) {
  gqa_fused<<<NWG, NTHR, 0, stream>>>((const unsigned char*)d_in[0], (const unsigned char*)d_in[1], (const unsigned char*)d_in[2], (const unsigned char*)d_in[3],
                                      (const unsigned char*)d_in[4], (unsigned char*)d_out, (unsigned char*)d_ws);
}
```

```cpp
#include <hip/hip_runtime.h>
#include <hip/hip_bf16.h>
#include <stdint.h>

#define GAS __attribute__((address_space(1)))
#define LAS __attribute__((address_space(3)))
typedef unsigned v4u __attribute__((ext_vector_type(4)));
typedef unsigned v2u __attribute__((ext_vector_type(2)));
typedef int v4i __attribute__((ext_vector_type(4)));
typedef float f32x4 __attribute__((ext_vector_type(4)));
typedef float f32x32 __attribute__((ext_vector_type(32)));
typedef short s16x4 __attribute__((ext_vector_type(4)));
typedef __bf16 bf16x8 __attribute__((ext_vector_type(8)));

constexpr int NB = 8, DM = 512, HQ = 32, HKV = 8, SK = 8192, DH = 128;
constexpr int NWG = 256, NTHR = 384;
constexpr int NUNIT = 128;
constexpr int SLOT = 8192;
constexpr float C2 = 0.088388347648318440f * 1.4426950408889634f;

constexpr int NREG = 5;
constexpr int NFIRST = 4;
constexpr int NCH = 2;
constexpr int MLROW = 8;
constexpr int UPREP = 112;
constexpr int NFIN = 3;
constexpr int MPOS = 1;
constexpr int WIN0 = 16, UW0 = 32, WIN = 24;
constexpr unsigned UC16 = (unsigned)(3.39f * 128.f);
constexpr int LINE0 = 65;
constexpr unsigned PHASE = 28;
constexpr int HOLD = 30, MINSP = 20;
constexpr int LATE = 40, MINSP2 = 16;
constexpr int FB_NREP = 32, FBEND = 110;
constexpr unsigned FB_P = 192u;
constexpr int DELTA = 14, DELTA2 = 4, DSW = 81;
constexpr float FB_G = 0.5f, FB_KI = 0.15f, FB_Z = 5.f, FB_FIN = 3.f;
constexpr int FB_I0 = 2;
constexpr int QPOLL = 550;
constexpr int WOPOS = 125;
constexpr int GPOS = 126, D1 = 30, D1G = 134;
constexpr int D2 = 40;
constexpr unsigned SPIN_CAP = 1u << 20;
constexpr int LAG = (WIN + 7) / 8;

constexpr int L_Q = 16 * SLOT, L_ACC = L_Q + 1024, L_PL = L_ACC + 4 * 2048, L_OT = L_PL + 2048, OT_ROW = 272, L_ML = L_OT + 8 * OT_ROW, L_CTL = (L_ML + 144 + 63) / 64 * 64;
constexpr int L_X = L_ACC, L_W = L_CTL + 64, L_TOT = L_W + 16384;
constexpr int C_FREE = L_CTL, C_LAND = L_CTL + 32, C_READY = L_CTL + 40, C_TAG = L_CTL + 44, C_NM = L_CTL + 48, C_NL = L_CTL + 52, C_NA = L_CTL + 56, C_WO = L_CTL + 60;
static_assert(L_CTL % 32 == 0 && L_TOT <= 160 * 1024, "lds map");
constexpr int W_E = 0, W_FB = 1024, FB_SLOT = 512, W_ML = W_FB + 64 * FB_SLOT + 1024, W_QG = W_ML + NWG * 128, W_PA = W_QG + NWG * 2048, PA_REC = 2048, W_YB = W_PA + NWG * PA_REC,
              W_END = W_YB + NWG * 2048;


#define STR_(x) #x
#define WAITVM(n) asm volatile("s_waitcnt vmcnt(" STR_(n) ")" ::: "memory")
#define BARRIER() asm volatile("s_waitcnt lgkmcnt(0)\n\ts_barrier" ::: "memory")
#define RELOAD() asm volatile("" ::: "memory")
constexpr int AUX_SC1 = 16;

__device__ __forceinline__ unsigned now() { return (unsigned)__builtin_amdgcn_s_memrealtime(); }
__device__ __forceinline__ unsigned cvtpk(float lo, float hi) {
  typedef __bf16 bf16x2_t __attribute__((ext_vector_type(2)));
  typedef float f32x2_t __attribute__((ext_vector_type(2)));
  return __builtin_bit_cast(unsigned, __builtin_convertvector(f32x2_t{lo, hi}, bf16x2_t));
}
template <int CTRL> __device__ __forceinline__ unsigned dppu(unsigned x) { return (unsigned)__builtin_amdgcn_mov_dpp((int)x, CTRL, 0xf, 0xf, true); }
template <int CTRL> __device__ __forceinline__ float dpp(float x) { return __uint_as_float(dppu<CTRL>(__float_as_uint(x))); }
__device__ __forceinline__ float xrow_max(float x) {
  auto s = __builtin_amdgcn_permlane16_swap(__float_as_uint(x), __float_as_uint(x), false, false);
  x = fmaxf(__uint_as_float(s[0]), __uint_as_float(s[1]));
  auto t = __builtin_amdgcn_permlane32_swap(__float_as_uint(x), __float_as_uint(x), false, false);
  return fmaxf(__uint_as_float(t[0]), __uint_as_float(t[1]));
}
__device__ __forceinline__ float xrow_sum(float x) {
  auto s = __builtin_amdgcn_permlane16_swap(__float_as_uint(x), __float_as_uint(x), false, false);
  x = __uint_as_float(s[0]) + __uint_as_float(s[1]);
  auto t = __builtin_amdgcn_permlane32_swap(__float_as_uint(x), __float_as_uint(x), false, false);
  return __uint_as_float(t[0]) + __uint_as_float(t[1]);
}
__device__ __forceinline__ v4i mkrsrc(const void* p, unsigned bytes) {
  const unsigned long long a = (unsigned long long)p;
  v4i r; r[0] = (int)(unsigned)a; r[1] = (int)((unsigned)(a >> 32) & 0xffffu); r[2] = (int)bytes; r[3] = 0x00020000; return r;
}
__device__ __forceinline__ int lds_word(LAS unsigned char* p) { RELOAD(); const int v = __builtin_amdgcn_readfirstlane(*(LAS int*)p); RELOAD(); return v; }
__device__ __forceinline__ void lds_inc(LAS unsigned char* p) {
  unsigned long long ex;
  asm volatile("s_mov_b64 %0, exec\n\ts_mov_b64 exec, 1\n\tds_add_u32 %1, %2\n\ts_mov_b64 exec, %0" : "=&s"(ex) : "v"((unsigned)(size_t)p), "v"(1u) : "memory");
}
#define DMA4(V, R, S) "buffer_load_dwordx4 " V ", " R ", " S " offen nt lds\n\tbuffer_load_dwordx4 " V ", " R ", " S " offen offset:1024 nt lds\n\t" \
                      "buffer_load_dwordx4 " V ", " R ", " S " offen offset:2048 nt lds\n\tbuffer_load_dwordx4 " V ", " R ", " S " offen offset:3072 nt lds\n\t"
__device__ __forceinline__ void issue_two(v4i r, unsigned vv, unsigned so, unsigned lds) {
  unsigned t;
  asm volatile("s_mov_b32 m0, %4\n\ts_add_u32 %0, %3, 0x1000\n\t" DMA4("%2", "%1", "%3") "s_add_u32 m0, m0, 0x1000\n\ts_nop 0\n\t" DMA4("%2", "%1", "%0")
               "s_add_u32 m0, m0, 0x1000\n\ts_add_u32 %0, %0, 0x1000\n\t" DMA4("%2", "%1", "%0") "s_add_u32 m0, m0, 0x1000\n\ts_add_u32 %0, %0, 0x1000\n\t" DMA4("%2", "%1", "%0")
               : "=&s"(t) : "s"(r), "v"(vv), "s"(so), "s"(lds) : "memory", "scc");
}
__device__ __forceinline__ void dma1_nt(v4i r, unsigned voff, unsigned soff, unsigned lds) {
  asm volatile("s_mov_b32 m0, %3\n\ts_nop 0\n\tbuffer_load_dwordx4 %1, %0, %2 offen nt lds" :: "s"(r), "v"(voff), "s"(soff), "s"(lds) : "memory");
}
__device__ __forceinline__ void dma1(v4i r, unsigned voff, unsigned soff, unsigned lds) {
  asm volatile("s_mov_b32 m0, %3\n\ts_nop 0\n\tbuffer_load_dwordx4 %1, %0, %2 offen lds" :: "s"(r), "v"(voff), "s"(soff), "s"(lds) : "memory");
}
template <int W> __device__ __forceinline__ void stream_units(v4i rs, unsigned vv, unsigned u, unsigned ue, unsigned so, unsigned ra, unsigned uc16, int hold, int msp, int lat, int ms2) {
  unsigned ta, tb, st = 8192u, fo = ((u + 1u) & 3u) * 8u + C_FREE;
  asm volatile(
      "s_mov_b32 m0, %[m0i]\n\t"
      "v_mov_b32 v2, %[ld0]\n"
      "LT%=:\n\t"
      "s_waitcnt vmcnt(%[win])\n\t"
      DMA4("%[v0]", "%[rs]", "%[so]")
      "s_add_u32 m0, m0, 0x1000\n\t"
      "s_memrealtime s[40:41]\n\t"
      DMA4("%[v1]", "%[rs]", "%[so]")
      "ds_read_b64 v[4:5], %[fo]\n\t"
      "s_add_u32 m0, m0, 0x1000\n\t"
      "s_add_u32 %[so], %[so], %[st]\n\t"
      "s_and_b32 m0, m0, 0x1ffff\n\t"
      "s_and_b32 %[so], %[so], 0x1fffff\n\t"
      "s_xor_b32 %[st], %[st], 0xc000\n\t"
      "s_add_u32 %[ra], %[ra], %[uc]\n\t"
      "v_add_u32 v2, 1, v2\n\t"
      "v_add_u32 %[fo], 8, %[fo]\n\t"
      "s_add_u32 %[u], %[u], 1\n\t"
      "v_and_b32 %[fo], %[fm], %[fo]\n\t"
      "s_waitcnt lgkmcnt(0)\n\t"
      "v_mov_b32 v3, s40\n\t"
      "v_readfirstlane_b32 %[a], v5\n\t"
      "s_lshr_b32 s41, %[ra], 4\n\t"
      "v_readfirstlane_b32 %[b], v4\n\t"
      "ds_write_b64 %[vl], v[2:3]\n\t"
      "s_cmp_eq_u32 %[u], %[ue]\n\t"
      "s_cbranch_scc1 LE%=\n\t"
      "s_add_u32 %[a], %[a], s41\n\t"
      "s_sub_i32 %[a], %[a], s40\n\t"
      "s_cmp_lt_i32 %[a], %[lat]\n\t"
      "s_cselect_b32 s41, %[ms2], %[msp]\n\t"
      "s_min_i32 %[a], %[a], %[hold]\n\t"
      "s_max_i32 %[a], %[a], s41\n\t"
      "s_add_u32 %[a], %[a], s40\n\t"
      "s_cmp_lt_u32 %[b], %[u]\n\t"
      "s_cbranch_scc1 LS%=\n"
      "LW%=:\n\t"
      "s_memrealtime s[40:41]\n\t"
      "s_waitcnt lgkmcnt(0)\n\t"
      "s_sub_i32 %[b], %[a], s40\n\t"
      "s_cmp_gt_i32 %[b], 0\n\t"
      "s_cbranch_scc0 LT%=\n\t"
      "s_branch LW%=\n"
      "LS%=:\n\t"
      "s_and_b32 %[b], %[u], 3\n\t"
      "s_lshl_b32 %[b], %[b], 3\n\t"
      "s_add_u32 %[b], %[b], %[fb]\n\t"
      "v_mov_b32 v5, %[b]\n"
      "LR%=:\n\t"
      "s_sleep 1\n\t"
      "ds_read_b32 v4, v5\n\t"
      "s_waitcnt lgkmcnt(0)\n\t"
      "v_readfirstlane_b32 %[b], v4\n\t"
      "s_cmp_lt_u32 %[b], %[u]\n\t"
      "s_cbranch_scc1 LR%=\n\t"
      "s_branch LW%=\n"
      "LE%=:"
      : [so] "+s"(so), [ra] "+s"(ra), [u] "+s"(u), [st] "+s"(st), [fo] "+v"(fo), [a] "=&s"(ta), [b] "=&s"(tb)
      : [rs] "s"(rs), [v0] "v"(vv), [v1] "v"(vv + 4096u), [vl] "v"((unsigned)C_LAND), [ue] "s"(ue), [uc] "s"(uc16), [hold] "s"(hold), [msp] "s"(msp), [lat] "s"(lat), [ms2] "s"(ms2),
        [m0i] "s"((u & 15u) * SLOT), [ld0] "s"(u - 1u - LAG), [win] "n"(W), [fm] "s"(~32u), [fb] "s"((unsigned)C_FREE)
      : "memory", "scc", "vcc", "v2", "v3", "v4", "v5", "s40", "s41");
}
static_assert((C_FREE & 32) == 0, "the record pointer wraps by clearing bit 5");

__device__ __forceinline__ f32x4 mfma32(v4u a, v4u b, f32x4 c) {
  return __builtin_amdgcn_mfma_f32_16x16x32_bf16(__builtin_bit_cast(bf16x8, a), __builtin_bit_cast(bf16x8, b), c, 0, 0, 0);
}
__device__ __forceinline__ v4u tr_frag(LAS unsigned char* p, int hi) {
  const v2u lo = __builtin_bit_cast(v2u, __builtin_amdgcn_ds_read_tr16_b64_v4i16((LAS s16x4*)p));
  const v2u up = __builtin_bit_cast(v2u, __builtin_amdgcn_ds_read_tr16_b64_v4i16((LAS s16x4*)(p + hi)));
  return v4u{lo[0], lo[1], up[0], up[1]};
}
__device__ __forceinline__ float score16(const v4u* K, v4u qf, unsigned j0) {
  float sc[4];
#pragma unroll
  for (int u = 0; u < 4; ++u) {
    const f32x4 d = mfma32(K[u], qf, f32x4{0.f, 0.f, 0.f, 0.f});
    sc[u] = ((dpp<0x00>(d[0]) + dpp<0x55>(d[1])) + dpp<0xAA>(d[2])) + dpp<0xFF>(d[3]);
  }
  return (j0 & 2) ? ((j0 & 1) ? sc[3] : sc[2]) : ((j0 & 1) ? sc[1] : sc[0]);
}
__device__ __forceinline__ void p_operands(unsigned k0, unsigned k1, unsigned mask0, unsigned mask1, v4u (&bo)[4]) {
  bo[0] = v4u{dppu<0x00>(k0) & mask0, dppu<0x00>(k0) & mask1, dppu<0x55>(k0) & mask0, dppu<0x55>(k0) & mask1};
  bo[1] = v4u{dppu<0xAA>(k0) & mask0, dppu<0xAA>(k0) & mask1, dppu<0xFF>(k0) & mask0, dppu<0xFF>(k0) & mask1};
  bo[2] = v4u{dppu<0x00>(k1) & mask0, dppu<0x00>(k1) & mask1, dppu<0x55>(k1) & mask0, dppu<0x55>(k1) & mask1};
  bo[3] = v4u{dppu<0xAA>(k1) & mask0, dppu<0xAA>(k1) & mask1, dppu<0xFF>(k1) & mask0, dppu<0xFF>(k1) & mask1};
  asm volatile("" : "+v"(bo[0]), "+v"(bo[1]), "+v"(bo[2]), "+v"(bo[3]));
}
#define RD2(A, P, O0, O1) "ds_read2st64_b64 %[" A "], %[" P "] offset0:" O0 " offset1:" O1 "\n\t"
#define MF(N, C, A, B) "s_waitcnt lgkmcnt(" N ")\n\tv_mfma_f32_16x16x32_bf16 %[" C "], %[" A "], %[" B "], %[" C "]\n\t"
__device__ __forceinline__ void pv32(unsigned p, const v4u (&bo)[4], f32x4& acc0, f32x4& acc1) {
  v4u a0, a1, a2, a3, a4, a5, a6, a7;
  asm volatile(RD2("a0", "p0", "0", "2") RD2("a1", "p1", "0", "2") RD2("a2", "p0", "4", "6") RD2("a3", "p1", "4", "6")
               RD2("a4", "p0", "8", "10") RD2("a5", "p1", "8", "10") RD2("a6", "p0", "12", "14") RD2("a7", "p1", "12", "14")
               MF("7", "c0", "a0", "b0") MF("6", "c1", "a1", "b0") MF("5", "c0", "a2", "b1") MF("4", "c1", "a3", "b1")
               MF("3", "c0", "a4", "b2") MF("2", "c1", "a5", "b2") MF("1", "c0", "a6", "b3") MF("0", "c1", "a7", "b3") "s_nop 7"
      : [c0] "+v"(acc0), [c1] "+v"(acc1), [a0] "=&v"(a0), [a1] "=&v"(a1), [a2] "=&v"(a2), [a3] "=&v"(a3), [a4] "=&v"(a4), [a5] "=&v"(a5), [a6] "=&v"(a6), [a7] "=&v"(a7)
      : [p0] "v"(p), [p1] "v"(p + 8), [b0] "v"(bo[0]), [b1] "v"(bo[1]), [b2] "v"(bo[2]), [b3] "v"(bo[3]) : "memory");
}
__device__ __forceinline__ v4u tag_bytes(unsigned tag) { return v4u{tag & 0xffu, (tag >> 8) & 0xffu, (tag >> 16) & 0xffu, tag >> 24}; }
__device__ __forceinline__ v4u tag4(f32x4 v, v4u tb) { return (__builtin_bit_cast(v4u, v) & 0xffffff00u) | tb; }
__device__ __forceinline__ unsigned next_tag(unsigned e) { return ((e & 0x7f7f7f7fu) + 0x51276b35u) ^ (e & 0x80808080u); }
__device__ __forceinline__ unsigned or4(v4u u) { return (u[0] | u[1]) | (u[2] | u[3]); }

__global__ void __launch_bounds__(NTHR, 2) gqa_fused(const unsigned char* __restrict__ x, const unsigned char* __restrict__ kc, const unsigned char* __restrict__ vc,
                                                     const unsigned char* __restrict__ wq, const unsigned char* __restrict__ wo, unsigned char* __restrict__ out,
                                                     unsigned char* ws) {
  __shared__ __attribute__((aligned(16))) unsigned char smem[L_TOT];
  LAS unsigned char* L = (LAS unsigned char*)smem;
  unsigned long long t64;
  unsigned pf, pa;
  {
    unsigned long long pc, ex;
    unsigned clen;
    asm volatile("s_memrealtime %0\n\ts_getpc_b64 %1\nLgqa_pc:\n\t"
                 "s_mov_b32 %2, Lgqa_end_a-Lgqa_pc+160\n\ts_max_u32 %2, %2, Lgqa_end_b-Lgqa_pc+160\n\ts_max_u32 %2, %2, Lgqa_end_c-Lgqa_pc+160\n\t"
                 "s_max_u32 %2, %2, Lgqa_end_d-Lgqa_pc+160\n\ts_max_u32 %2, %2, Lgqa_end_e-Lgqa_pc+160\n\t"
                 "v_lshlrev_b32 %5, 7, %6\n\tv_add_u32 %5, 0xffffc000, %5\n\tv_cmp_gt_u32 vcc, %2, %5\n\t"
                 "s_and_saveexec_b64 %3, vcc\n\ts_cbranch_execz Lgqa_nopf\n\tglobal_load_dword %4, %5, %1 offset:-32\nLgqa_nopf:\n\ts_mov_b64 exec, %3"
                 : "=&s"(t64), "=&s"(pc), "=&s"(clen), "=&s"(ex), "=&v"(pf), "=&v"(pa) : "v"(threadIdx.x) : "scc", "vcc", "memory");
  }
  const int lane = threadIdx.x & 63, wv = __builtin_amdgcn_readfirstlane(threadIdx.x >> 6);
  const int j = blockIdx.x, xg = j & 7, idx = j >> 3;
  const int pr = (idx >> 2) * 8 + ((xg + (idx >> 2)) & 7), s = idx & 3;
  const int ch = 4 * xg + (idx >> 3), nb = idx & 7;
  const int g = lane >> 4;
  const unsigned so0 = (unsigned)(pr >> 1) * 65536u + s * 16384u;
  const unsigned vv = lane * 16;
  const unsigned ra0 = ((unsigned)(j * 37) & 31u) * PHASE >> 1;
  asm volatile("s_waitcnt lgkmcnt(0)" : "+s"(t64) : "s"(x), "s"(kc), "s"(vc), "s"(wq), "s"(wo), "s"(out), "s"(ws), "s"(so0), "v"(vv));
  const unsigned t0 = (unsigned)t64;
  const size_t pbase = (size_t)pr * (SK * DH * 2);

  const v4i rk = mkrsrc(kc + pbase, SK * DH * 2);
  if (wv == 0) {
    __builtin_amdgcn_s_setprio(3);
#pragma unroll
    for (int u = 0; u < NFIRST; u += 2) issue_two(rk, vv, (so0 + (u >> 1) * 65536u) & 0x1fffffu, u * SLOT);
    __builtin_amdgcn_sched_barrier(0);
    const unsigned keep = (unsigned)((lane & 7) - 4) >> 31;
    *(LAS v2u*)(L + L_CTL + (lane & 7) * 8) = v2u{(12u + (lane & 7)) * keep, (t0 + LINE0) * keep};
    asm volatile("s_waitcnt lgkmcnt(0)" ::: "memory");
  }
  unsigned e0, e1, e2;
  if (wv == 1) asm volatile("s_load_dword %0, %3, 0x0\n\ts_load_dword %1, %4, 0x0\n\ts_load_dword %2, %5, %6"
                            : "=&s"(e0), "=&s"(e1), "=&s"(e2) : "s"(ws), "s"(x), "s"(wq), "s"((j >> 6) * (128 * 8192) + (j & 63) * 128) : "memory");
  asm volatile("s_barrier" ::: "memory");
  if (wv == 0) stream_units<WIN0>(rk, vv, NFIRST, UW0, (so0 + (NFIRST >> 1) * 65536u) & 0x1fffffu, NFIRST * UC16 + ra0, UC16, HOLD, MINSP, -LATE, MINSP2);
  const __amdgpu_buffer_rsrc_t rw = __builtin_amdgcn_make_buffer_rsrc((void*)ws, 0, W_END, 0x00020000);
  const unsigned j0 = lane & 3;
  const int h = (lane >> 2) & 3;
  const unsigned mask0 = j0 == 0 ? 0x0000ffffu : (j0 == 1 ? 0xffff0000u : 0u);
  const unsigned mask1 = j0 == 2 ? 0x0000ffffu : (j0 == 3 ? 0xffff0000u : 0u);

  if (wv == 0) {
    const v4i rv = mkrsrc(vc + pbase, SK * DH * 2);
#pragma unroll 1
    for (int pass = 0; pass < 2; ++pass) {
      const unsigned ub = pass ? 64u : (unsigned)UW0;
      while ((unsigned)lds_word(L + C_FREE + (ub & 3u) * 8) < ub) __builtin_amdgcn_s_sleep(1);
      const v4i rs = {pass ? rv[0] : rk[0], pass ? rv[1] : rk[1], rk[2], rk[3]};
      stream_units<WIN>(rs, vv, ub, pass ? (unsigned)UPREP : 64u, (so0 + (ub >> 1) * 65536u) & 0x1fffffu, ub * UC16 + ra0, UC16, HOLD, MINSP, -LATE, MINSP2);
    }
    while (lds_word(L + C_FREE + 24) < 64 + 4 * MLROW + 3 + 16) __builtin_amdgcn_s_sleep(1);
    v4u bo[NFIN][4], tb;
    unsigned vo = h * 512 + g * 64 + j0 * 16;
    float scale;
    {
      v2u pl[NFIN];
#pragma unroll
      for (int k = 0; k < NFIN; ++k) pl[k] = *(LAS v2u*)(L + L_PL + (4 - NFIN + k) * 512 + lane * 8);
      tb = tag_bytes(*(LAS unsigned*)(L + C_TAG));
      scale = *(LAS float*)(L + L_ML + 128 + h * 4);
      const int fr = *(LAS int*)(L + C_FREE + (UPREP & 3) * 8);
#pragma unroll
      for (int k = 0; k < NFIN; ++k) p_operands(pl[k][0], pl[k][1], mask0, mask1, bo[k]);
      asm volatile("" : "+v"(vo), "+v"(tb), "+v"(scale));
      if (__builtin_amdgcn_readfirstlane(fr) < UPREP) { while (lds_word(L + C_FREE + (UPREP & 3) * 8) < UPREP) __builtin_amdgcn_s_sleep(1); }
    }
    stream_units<WIN>(rv, vv, UPREP, NUNIT, (so0 + ((UPREP - 64) >> 1) * 65536u) & 0x1fffffu, UPREP * UC16 + ra0, UC16, HOLD, MINSP, -LATE, MINSP2);
    const int so = W_PA + (pr * 4 + s) * PA_REC;
    f32x4 acc0 = {0.f, 0.f, 0.f, 0.f}, acc1 = {0.f, 0.f, 0.f, 0.f};
    static_assert(NFIN >= 2 && LAG <= 3, "the drain below starts at three units outstanding");
    f32x4 s0, s1;
#pragma unroll
    for (int k = 0; k < 3; ++k) {
      if (k == 0) WAITVM(24); else if (k == 1) WAITVM(16); else WAITVM(8);
      if (k < 2) *(LAS int*)(L + C_LAND) = NUNIT - 3 + k;
      if (k >= 4 - NFIN) pv32((12 + k) * SLOT + vv, bo[k >= 4 - NFIN ? k - (4 - NFIN) : 0], acc0, acc1);
      if (k == MPOS) {
        while (lds_word(L + C_NA) < 4) {}
        f32x4 a[8];
#pragma unroll
        for (int u = 0; u < 8; ++u) a[u] = *(LAS f32x4*)(L + L_ACC + vv + u * 1024);
        __builtin_amdgcn_sched_barrier(0);
        s0 = ((a[0] + a[2]) + (a[4] + a[6])) * scale; s1 = ((a[1] + a[3]) + (a[5] + a[7])) * scale;
        asm volatile("" : "+v"(s0), "+v"(s1) : "v"(14 * SLOT + vv), "v"(14 * SLOT + vv + 8), "v"(15 * SLOT + vv), "v"(15 * SLOT + vv + 8), "s"(so), "s"(0xffffff00u));
      }
    }
    WAITVM(0);
    pv32(15 * SLOT + vv, bo[NFIN - 1], acc0, acc1);
    const v4u o0 = tag4(acc0 * scale + s0, tb), o1 = tag4(acc1 * scale + s1, tb);
    __builtin_amdgcn_raw_buffer_store_b128(o0, rw, vo, so, AUX_SC1);
    __builtin_amdgcn_raw_buffer_store_b128(o1, rw, vo + 256, so, AUX_SC1);
    asm volatile("s_nop 1" :: "v"(o0), "v"(o1) : "memory");
    *(LAS int*)(L + C_LAND) = NUNIT;
    asm volatile("Lgqa_end_a:" ::: "memory");
    return;
  }

  if (wv >= 2) {
    const int w = wv - 2;
    const unsigned vk = (h * 16 + g * 4 + j0) * 16;
    asm volatile("s_waitcnt vmcnt(0)" :: "v"(pf), "v"(pa) : "memory");
    f32x32 sc;
    float m;
    {
      v4u P[NREG][8];
#pragma unroll
      for (int r = 0; r < NREG; ++r) {
        while (lds_word(L + C_LAND) <= 4 * r + w) __builtin_amdgcn_s_sleep(2);
#pragma unroll
        for (int u = 0; u < 8; ++u) P[r][u] = *(LAS v4u*)(L + ((4 * r + w) & 15) * SLOT + vk + u * 1024);
        asm volatile("" :: "v"(P[r][0]), "v"(P[r][1]), "v"(P[r][2]), "v"(P[r][3]), "v"(P[r][4]), "v"(P[r][5]), "v"(P[r][6]), "v"(P[r][7]) : "memory");
        *(LAS int*)(L + C_FREE + w * 8) = 4 * r + w + 16;
      }
      while (lds_word(L + C_READY) == 0) __builtin_amdgcn_s_sleep(2);
      const v4u qf = *(LAS v4u*)(L + L_Q + vk);
      m = -INFINITY;
#pragma unroll
      for (int r = 0; r < NREG; ++r) {
        const float s0 = score16(&P[r][0], qf, j0), s1 = score16(&P[r][4], qf, j0);
        sc[2 * r] = s0; sc[2 * r + 1] = s1; m = fmaxf(m, fmaxf(s0, s1));
      }
#pragma unroll 1
      for (int n = NREG; n < 16; ++n) {
        while (lds_word(L + C_LAND) <= 4 * n + w) __builtin_amdgcn_s_sleep(2);
        v4u K[8];
#pragma unroll
        for (int u = 0; u < 8; ++u) K[u] = *(LAS v4u*)(L + ((4 * n + w) & 15) * SLOT + vk + u * 1024);
        asm volatile("" :: "v"(K[0]), "v"(K[1]), "v"(K[2]), "v"(K[3]), "v"(K[4]), "v"(K[5]), "v"(K[6]), "v"(K[7]) : "memory");
        *(LAS int*)(L + C_FREE + w * 8) = 4 * n + w + 16;
        const float s0 = score16(&K[0], qf, j0), s1 = score16(&K[4], qf, j0);
        sc[2 * n] = s0; sc[2 * n + 1] = s1; m = fmaxf(m, fmaxf(s0, s1));
      }
    }
    const v4u tb = tag_bytes((unsigned)lds_word(L + C_TAG));
    m = fmaxf(m, dpp<0xB1>(m)); m = fmaxf(m, dpp<0x4E>(m)); m = xrow_max(m);
    *(LAS float*)(L + L_ML + h * 16 + w * 4) = m;
    lds_inc(L + C_NM);
    while (lds_word(L + C_NM) < 4) __builtin_amdgcn_s_sleep(1);
    {
      const f32x4 mw = *(LAS f32x4*)(L + L_ML + h * 16);
      m = fmaxf(fmaxf(mw[0], mw[1]), fmaxf(mw[2], mw[3]));
      float l = 0.f;
#pragma unroll
      for (int n = 0; n < 32; ++n) { const float p = __builtin_amdgcn_exp2f((sc[n] - m) * C2); l += p; sc[n] = __uint_as_float(cvtpk(p, p)); }
      l += dpp<0xB1>(l); l += dpp<0x4E>(l); l = xrow_sum(l);
      *(LAS float*)(L + L_ML + 64 + h * 16 + w * 4) = l;
      *(LAS v2u*)(L + L_PL + w * 512 + lane * 8) = v2u{__float_as_uint(sc[30]), __float_as_uint(sc[31])};
      lds_inc(L + C_NL);
    }
    f32x4 acc0 = {0.f, 0.f, 0.f, 0.f}, acc1 = {0.f, 0.f, 0.f, 0.f};
#pragma unroll 1
    for (int n = 0; n < (w < 4 - NFIN ? 16 : 15); ++n) {
      if (w == 3 && n == 1) {
        while (lds_word(L + C_NL) < 4) __builtin_amdgcn_s_sleep(1);
        const f32x4 lw = *(LAS f32x4*)(L + L_ML + 64 + h * 16);
        if (lane < 16 && j0 == 0) __builtin_amdgcn_raw_buffer_store_b128(tag4(f32x4{m, (lw[0] + lw[1]) + (lw[2] + lw[3]), __uint_as_float(__float_as_uint(m) << 24), 0.f}, tb), rw, h * 16, W_ML + (pr * 4 + s) * 128, AUX_SC1);
      }
      if (w == 3 && n == MLROW) {
        v4u ml;
        for (unsigned spin = 0; spin < SPIN_CAP; ++spin) {
          RELOAD();
          ml = __builtin_amdgcn_raw_buffer_load_b128(rw, j0 * 128 + h * 16, W_ML + pr * 512, AUX_SC1) ^ tb;
          if (__all((or4(ml) & 0xffu) == 0u)) break;
        }
        const float ms = __uint_as_float(ml[0] | (ml[2] >> 24));
        float M = fmaxf(ms, dpp<0xB1>(ms)); M = fmaxf(M, dpp<0x4E>(M));
        float ls = __uint_as_float(ml[1]) * __builtin_amdgcn_exp2f((ms - M) * C2);
        ls += dpp<0xB1>(ls); ls += dpp<0x4E>(ls);
        if (lane < 16 && j0 == 0) *(LAS float*)(L + L_ML + 128 + h * 4) = __builtin_amdgcn_exp2f((m - M) * C2) * __builtin_amdgcn_rcpf(ls);
      }
      v4u bo[4];
      p_operands(__float_as_uint(sc[2 * n]), __float_as_uint(sc[2 * n + 1]), mask0, mask1, bo);
      while (lds_word(L + C_LAND) <= 64 + 4 * n + w) __builtin_amdgcn_s_sleep(1);
      pv32(((4 * n + w) & 15) * SLOT + vv, bo, acc0, acc1);
      *(LAS int*)(L + C_FREE + w * 8) = 64 + 4 * n + w + 16;
    }
    *(LAS f32x4*)(L + L_ACC + w * 2048 + vv) = acc0; *(LAS f32x4*)(L + L_ACC + w * 2048 + 1024 + vv) = acc1;
    lds_inc(L + C_NA);
    const int bb = 2 * w + (lane >> 5), c = lane & 31;
    unsigned vg = bb * (HKV * 4 * PA_REC) + c * 16, vy = (lane & 7) * (32 * 2048) + g * 16;
    const int sg = W_PA + (ch >> 2) * (4 * PA_REC) + (ch & 3) * 512, sy = W_YB + (nb * 4 + w) * 2048 + ch * 64;
    LAS unsigned char* ap = L + L_W + (8 * g + ((lane & 15) >> 2)) * 128 + (16 * w + 4 * j0) * 2;
    LAS unsigned char* op = L + L_OT + bb * OT_ROW + c * 8;
    LAS unsigned char* bp = L + L_OT + (lane & 7) * OT_ROW + g * 16;
    if (lane & 8) vy = 0x40000000u;
    v4u tv = tb;
    asm volatile("" : "+v"(vg), "+v"(vy), "+v"(ap), "+v"(op), "+v"(bp), "+v"(tv) : "s"(sg), "s"(sy));
    while (lds_word(L + C_LAND) < GPOS) __builtin_amdgcn_s_sleep(1);
    {
      const unsigned tp = now() + D1;
      unsigned tg = (unsigned)lds_word(L + C_FREE + 4) + (NUNIT * UC16 >> 4) + D1G;
      tg = tp + (unsigned)min(max((int)(tg - tp), 0), 200);
      while ((int)(now() - tg) < 0) {}
    }
    v4u a[4], af[4], bf[4];
#pragma unroll
    for (int u = 0; u < 4; ++u) a[u] = __builtin_amdgcn_raw_buffer_load_b128(rw, vg, sg + u * PA_REC, AUX_SC1);
    while (lds_word(L + C_WO) == 0) {}
#pragma unroll
    for (int ks = 0; ks < 4; ++ks) af[ks] = tr_frag(ap + ks * 4096, 512);
    asm volatile("" : "+v"(af[0]), "+v"(af[1]), "+v"(af[2]), "+v"(af[3]));
#pragma unroll
    for (int u = 0; u < 4; ++u) a[u] ^= tv;
    asm volatile("" : "+v"(a[0]), "+v"(a[1]), "+v"(a[2]), "+v"(a[3]));
    if (__builtin_expect(!__all((or4((a[0] | a[1]) | (a[2] | a[3])) & 0xffu) == 0u), 0)) {
      for (unsigned spin = 0; spin < SPIN_CAP; ++spin) {
        RELOAD();
#pragma unroll
        for (int u = 0; u < 4; ++u) a[u] = __builtin_amdgcn_raw_buffer_load_b128(rw, vg, sg + u * PA_REC, AUX_SC1) ^ tv;
        if (__all((or4((a[0] | a[1]) | (a[2] | a[3])) & 0xffu) == 0u)) break;
      }
      asm volatile("Lgqa_end_d:" ::: "memory");
    }
    const f32x4 o = (__builtin_bit_cast(f32x4, a[0]) + __builtin_bit_cast(f32x4, a[1])) + (__builtin_bit_cast(f32x4, a[2]) + __builtin_bit_cast(f32x4, a[3]));
    *(LAS v2u*)op = v2u{cvtpk(o[0], o[1]), cvtpk(o[2], o[3])};
    BARRIER();
#pragma unroll
    for (int ks = 0; ks < 4; ++ks) bf[ks] = *(LAS v4u*)(bp + ks * 64);
    __builtin_amdgcn_sched_barrier(0);
    f32x4 y = {0.f, 0.f, 0.f, 0.f};
#pragma unroll
    for (int ks = 0; ks < 4; ++ks) y = mfma32(af[ks], bf[ks], y);
    __builtin_amdgcn_raw_buffer_store_b128(tag4(y, tv), rw, vy, sy, AUX_SC1);
    asm volatile("Lgqa_end_b:" ::: "memory");
    return;
  }

  __builtin_amdgcn_s_setprio(2);
  unsigned tag;
  {
    constexpr int NKS = 4 / NCH;
    const bool prod = (idx & (NCH - 1)) == NCH - 1;
    const int pid = (idx / NCH) * 8 + xg, cb = pid & 63, kq = pid >> 6;
    v4u tq;
    if (prod) {
      const v4i rx = mkrsrc(x, NB * DM * 2), rq = mkrsrc(wq, DM * HQ * DH * 2);
      const unsigned vx = (lane & 7) * 1024 + (lane >> 3) * 16, vq = (lane >> 3) * 8192 + (lane & 7) * 16;
      const bool hi = (lane & 8) != 0;
      const int vs = (lane & 7) * (HKV * NKS * 2048) + (hi ? 64 : 0) + g * 16, ss = W_QG + (cb >> 3) * (NKS * 2048) + kq * 2048 + (cb & 7) * 256;
      f32x4 qa[4];
#pragma unroll
      for (int t = 0; t < 4; ++t) qa[t] = f32x4{0.f, 0.f, 0.f, 0.f};
      LAS unsigned char* ap = L + L_W + (8 * g + ((lane & 15) >> 2)) * 128 + j0 * 8;
      LAS unsigned char* bp = L + L_X + g * 128 + (lane & 7) * 16;
#pragma unroll 1
      for (int c = kq * NCH; c < kq * NCH + NCH; ++c) {
        dma1(rx, vx, c * 256, L_X);
        dma1(rx, vx, c * 256 + 128, L_X + 1024);
#pragma unroll 1
        for (int u = 0; u < 16; ++u) dma1_nt(rq, vq, (c * 16 + u) * 65536 + cb * 128, L_W + u * 1024);
        WAITVM(0);
#pragma unroll
        for (int ks = 0; ks < 4; ++ks) {
          const v4u xb = *(LAS v4u*)(bp + ks * 512);
#pragma unroll
          for (int t = 0; t < 4; ++t) qa[t] = mfma32(tr_frag(ap + ks * 4096 + t * 32, 512), xb, qa[t]);
        }
        asm volatile("" : "+v"(qa[0]), "+v"(qa[1]), "+v"(qa[2]), "+v"(qa[3]) :: "memory");
      }
      asm volatile("s_waitcnt lgkmcnt(0)" : "+s"(e0) :: "memory");
      tag = next_tag(e0);
      tq = tag_bytes(tag);
      const v4u o0 = tag4(f32x4{hi ? qa[1][0] : qa[0][0], hi ? qa[1][1] : qa[0][1], hi ? qa[1][2] : qa[0][2], hi ? qa[1][3] : qa[0][3]}, tq);
      const v4u o1 = tag4(f32x4{hi ? qa[3][0] : qa[2][0], hi ? qa[3][1] : qa[2][1], hi ? qa[3][2] : qa[2][2], hi ? qa[3][3] : qa[2][3]}, tq);
      __builtin_amdgcn_raw_buffer_store_b128(o0, rw, vs, ss, AUX_SC1);
      __builtin_amdgcn_raw_buffer_store_b128(o1, rw, vs + 128, ss, AUX_SC1);
      asm volatile("s_nop 1" :: "v"(o0), "v"(o1) : "memory");
    }
    asm volatile("s_waitcnt lgkmcnt(0)" : "+s"(e0) : "s"(e1), "s"(e2) : "memory");
    tag = next_tag(e0);
    tq = tag_bytes(tag);
    while ((int)(now() - t0) < QPOLL) __builtin_amdgcn_s_sleep(2);
    const int so = W_QG + pr * (NKS * 2048);
    v4u a[2 * NKS];
    for (unsigned spin = 0; spin < SPIN_CAP; ++spin) {
      RELOAD();
      v4u bad = {0u, 0u, 0u, 0u};
#pragma unroll
      for (int u = 0; u < 2 * NKS; ++u) { a[u] = __builtin_amdgcn_raw_buffer_load_b128(rw, vv + (u & 3) * 1024, so + (u >> 2) * 4096, AUX_SC1) ^ tq; bad |= a[u]; }
      if (__all((or4(bad) & 0xffu) == 0u)) break;
      __builtin_amdgcn_s_sleep(12);
    }
#pragma unroll
    for (int u = 0; u < 2; ++u) {
      f32x4 v = __builtin_bit_cast(f32x4, a[u]);
#pragma unroll
      for (int p = 1; p < NKS; ++p) v += __builtin_bit_cast(f32x4, a[2 * p + u]);
      *(LAS v2u*)(L + L_Q + u * 512 + lane * 8) = v2u{cvtpk(v[0], v[1]), cvtpk(v[2], v[3])};
    }
    RELOAD();
    *(LAS v2u*)(L + C_READY) = v2u{1u, tag};
  }
  if (lane == 0) {
    const unsigned a = __builtin_nontemporal_load((const GAS unsigned*)(vc + pbase + so0));
    const unsigned b2 = __builtin_nontemporal_load((const GAS unsigned*)(wo + ch * 128 * 1024 + nb * 128));
    asm volatile("" :: "v"(a), "v"(b2));
  }
  {
    unsigned k = now() / FB_P + 1u;
    float af = 0.f, dr = 0.f;
    int nv = 0;
#pragma unroll 1
    for (int it = 0; it < 40; ++it, ++k) {
      const unsigned tk = k * FB_P;
      while ((int)(now() - tk) < 0) __builtin_amdgcn_s_sleep(2);
      RELOAD();
      const v2u pr2 = *(LAS v2u*)(L + C_LAND);
      const int landed = __builtin_amdgcn_readfirstlane(pr2[0]);
      if (landed >= FBEND) break;
      const v2u fb = __builtin_amdgcn_raw_buffer_load_b64(rw, (lane & (FB_NREP - 1)) * 8, W_FB + ((k - 1u) & 63u) * FB_SLOT, AUX_SC1);
      if (j < FB_NREP && lane == 0) {
        const unsigned a = pr2[1] - (((pr2[0] + LAG) * UC16 + ra0) >> 4) + (unsigned)max((int)(tk - pr2[1]) - (int)(UC16 >> 4), 0);
        __builtin_amdgcn_raw_buffer_store_b64(v2u{a, tag ^ k}, rw, 0, W_FB + (k & 63u) * FB_SLOT + j * 8, AUX_SC1);
      }
      const bool ok = fb[1] == (tag ^ (k - 1u));
      float ps = ok ? (float)(int)(fb[0] - t0) : 0.f, pc = ok ? 1.f : 0.f;
      ps += dpp<0xB1>(ps); ps += dpp<0x4E>(ps); ps += dpp<0x124>(ps); ps += dpp<0x128>(ps); ps = xrow_sum(ps);
      pc += dpp<0xB1>(pc); pc += dpp<0x4E>(pc); pc += dpp<0x124>(pc); pc += dpp<0x128>(pc); pc = xrow_sum(pc);
      if (__builtin_amdgcn_readfirstlane(pc < 48.f)) continue;
      const float as = ps * __builtin_amdgcn_rcpf(pc) - (float)(landed < DSW ? DELTA : DELTA2);
      const float er = as - af;
      if (nv >= FB_I0) dr = fminf(fmaxf(dr + FB_KI * fminf(er - FB_Z, 30.f), 0.f), 16.f);
      af = nv ? af + dr + FB_G * er : as;
      ++nv;
      *(LAS unsigned*)(L + C_FREE + 4 + j0 * 8) = t0 + (unsigned)(int)af;
    }
    if (nv) *(LAS unsigned*)(L + C_FREE + 4 + j0 * 8) = t0 + (unsigned)(int)(af + FB_FIN * fmaxf(dr, 0.f));
  }
  {
    const v4i ro = mkrsrc(wo, HQ * DH * DM * 2);
    const unsigned vo = ((lane >> 5) + 8 * ((lane >> 3) & 3)) * 1024 + (lane & 7) * 16;
    while (lds_word(L + C_LAND) < WOPOS) __builtin_amdgcn_s_sleep(1);
#pragma unroll 1
    for (int u = 0; u < 16; ++u) dma1_nt(ro, vo, (ch * 128 + 32 * ((u >> 1) & 3) + 4 * (u >> 3) + 2 * (u & 1)) * 1024 + nb * 128, L_W + u * 1024);
    const __amdgpu_buffer_rsrc_t rout = __builtin_amdgcn_make_buffer_rsrc((void*)out, 0, NB * DM * 2, 0x00020000);
    v4u tb = tag_bytes(tag);
    unsigned vs = (lane & 28) == 0 ? j0 * 8 + (lane >> 5) * 4 : 0x40000000u;
    asm volatile("s_waitcnt vmcnt(0)" : "+v"(vs), "+v"(tb) :: "memory");
    *(LAS int*)(L + C_WO) = 1;
    BARRIER();
    const unsigned tp = now() + D2;
    while ((int)(now() - tp) < 0) {}
    v4u a0 = __builtin_amdgcn_raw_buffer_load_b128(rw, vv, W_YB + j * 2048, AUX_SC1) ^ tb;
    v4u a1 = __builtin_amdgcn_raw_buffer_load_b128(rw, vv + 1024, W_YB + j * 2048, AUX_SC1) ^ tb;
    asm volatile("" : "+v"(a0), "+v"(a1));
    if (__builtin_expect(!__all((or4(a0 | a1) & 0xffu) == 0u), 0)) {
      for (unsigned spin = 0; spin < SPIN_CAP; ++spin) {
        RELOAD();
        a0 = __builtin_amdgcn_raw_buffer_load_b128(rw, vv, W_YB + j * 2048, AUX_SC1) ^ tb;
        a1 = __builtin_amdgcn_raw_buffer_load_b128(rw, vv + 1024, W_YB + j * 2048, AUX_SC1) ^ tb;
        if (__all((or4(a0 | a1) & 0xffu) == 0u)) break;
      }
      asm volatile("Lgqa_end_e:" ::: "memory");
    }
    const f32x4 y = __builtin_bit_cast(f32x4, a0) + __builtin_bit_cast(f32x4, a1);
    const auto u02 = __builtin_amdgcn_permlane32_swap(__float_as_uint(y[0]), __float_as_uint(y[2]), false, false);
    const auto u13 = __builtin_amdgcn_permlane32_swap(__float_as_uint(y[1]), __float_as_uint(y[3]), false, false);
    float e = __uint_as_float(u02[0]) + __uint_as_float(u02[1]), f = __uint_as_float(u13[0]) + __uint_as_float(u13[1]);
    e += dpp<0x124>(e); f += dpp<0x124>(f); e += dpp<0x128>(e); f += dpp<0x128>(f);
    const auto ue = __builtin_amdgcn_permlane16_swap(__float_as_uint(e), __float_as_uint(e), false, false);
    const auto uf = __builtin_amdgcn_permlane16_swap(__float_as_uint(f), __float_as_uint(f), false, false);
    __builtin_amdgcn_raw_buffer_store_b32(cvtpk(__uint_as_float(ue[0]) + __uint_as_float(ue[1]), __uint_as_float(uf[0]) + __uint_as_float(uf[1])), rout, vs, j * 32, 0);
  }
  if (j == 0 && lane == 0) __builtin_amdgcn_raw_buffer_store_b32(tag, rw, 0, W_E, AUX_SC1);
  asm volatile("Lgqa_end_c:" ::: "memory");
}

extern "C" void kernel_launch(void* const* d_in, const int* in_sizes, int n_in, void* d_out, int out_size, void* d_ws, size_t ws_size,
                              hipStream_t stream) {
  gqa_fused<<<NWG, NTHR, 0, stream>>>((const unsigned char*)d_in[0], (const unsigned char*)d_in[1], (const unsigned char*)d_in[2], (const unsigned char*)d_in[3],
                                      (const unsigned char*)d_in[4], (unsigned char*)d_out, (unsigned char*)d_ws);
}
```

```cpp
#define DIAG 105
#define DSIGN 1
#define QPOLL 550
#include <hip/hip_runtime.h>
#include <hip/hip_bf16.h>
#include <stdint.h>

#define GAS __attribute__((address_space(1)))
#define LAS __attribute__((address_space(3)))
typedef unsigned v4u __attribute__((ext_vector_type(4)));
typedef unsigned v2u __attribute__((ext_vector_type(2)));
typedef int v4i __attribute__((ext_vector_type(4)));
typedef float f32x4 __attribute__((ext_vector_type(4)));
typedef float f32x32 __attribute__((ext_vector_type(32)));
typedef short s16x4 __attribute__((ext_vector_type(4)));
typedef __bf16 bf16x8 __attribute__((ext_vector_type(8)));

constexpr int NB = 8, DM = 512, HQ = 32, HKV = 8, SK = 8192, DH = 128;
constexpr int NWG = 256, NTHR = 384;
constexpr int NUNIT = 128;
constexpr int SLOT = 8192;
constexpr float C2 = 0.088388347648318440f * 1.4426950408889634f;

#define NREG 5
#define NFIRST 4
#define WIN 24
#define UPREP 112
#define NFIN 3
#define MPOS 1
#define MLROW 8
#define NCH 2
#define WIN0 16
#define FB_NREP 32
#define XF 9504
#define TUNE_ARG
#define TUNE_PASS
#define TL0 3.39f
#define UC16 ((unsigned)(TL0 * 128.f))
#define DELTA 14
#define DELTA2 4
#define DSW 650
#define FBEND 110
#define UW0 32
#define HOLD0 30
#define FB_DMIN 0.f
#define FB_FIN 3.f
#define FB_Z 5.f
#define HOLD 30
#define MINSP 20
#define LINE0 65
#define LATE 40
#define MINSP2 16
#define PHASE 28
#define FB_P 192u
#define FB_G 0.5f
#define FB_KI 0.15f
#define FB_I0 2
#define WQT 0
#define WQSP 12
#define WOPOS 125
#define D1 30
#define D1G 134
#define GPOS 126
#define D2 40
constexpr unsigned SPIN_CAP = 1u << 20;
constexpr int LAG = (WIN + 7) / 8;

constexpr int L_Q = 16 * SLOT, L_ACC = L_Q + 1024, L_PL = L_ACC + 4 * 2048, L_DG = L_PL + 2048, L_OT = L_DG + 16, OT_ROW = 272, L_ML = L_OT + 8 * OT_ROW, L_CTL = (L_ML + 144 + 63) / 64 * 64;
constexpr int L_X = L_ACC, L_W = L_CTL + 64, L_TOT = L_W + 16384;
constexpr int C_FREE = L_CTL, C_LAND = L_CTL + 32, C_READY = L_CTL + 40, C_TAG = L_CTL + 44, C_NM = L_CTL + 48, C_NL = L_CTL + 52, C_NA = L_CTL + 56, C_WO = L_CTL + 60;
static_assert(L_CTL % 32 == 0 && L_TOT <= 160 * 1024, "lds map");
constexpr int W_E = 0, W_FB = 1024, FB_SLOT = 512, W_ML = W_FB + 64 * FB_SLOT + 1024, W_QG = W_ML + NWG * 128, W_PA = W_QG + NWG * 2048, PA_REC = 2048, W_YB = W_PA + NWG * PA_REC,
              W_END = W_YB + NWG * 2048;

#define STAMP(i) do { if (DIAG % 100 == (i) && DIAG / 100 == (wv > 2 ? 2 : wv) && lane == 0) (void)__hip_atomic_fetch_max((LAS int*)(L + L_DG), (int)(now() - t0), __ATOMIC_RELAXED, __HIP_MEMORY_SCOPE_WORKGROUP); } while (0)
#define STAMP_OUT() do { if (wv == 1) { while ((int)(now() - t0) < 9000) __builtin_amdgcn_s_sleep(8); const int x_ = lds_word(L + L_DG); const int tg_ = DSIGN >= 0 ? 10000 + DSIGN * 10 * x_ : 70000 - 10 * x_; while ((int)(now() - t0) < tg_) __builtin_amdgcn_s_sleep(4); } } while (0)

#define STR_(x) #x
#define STRX_(x) STR_(x)
#define WAITVM(n) asm volatile("s_waitcnt vmcnt(" STR_(n) ")" ::: "memory")
#define BARRIER() asm volatile("s_waitcnt lgkmcnt(0)\n\ts_barrier" ::: "memory")
#define RELOAD() asm volatile("" ::: "memory")
constexpr int AUX_SC1 = 16;

__device__ __forceinline__ unsigned now() { return (unsigned)__builtin_amdgcn_s_memrealtime(); }
__device__ __forceinline__ unsigned cvtpk(float lo, float hi) {
  typedef __bf16 bf16x2_t __attribute__((ext_vector_type(2)));
  typedef float f32x2_t __attribute__((ext_vector_type(2)));
  return __builtin_bit_cast(unsigned, __builtin_convertvector(f32x2_t{lo, hi}, bf16x2_t));
}
template <int CTRL> __device__ __forceinline__ unsigned dppu(unsigned x) { return (unsigned)__builtin_amdgcn_mov_dpp((int)x, CTRL, 0xf, 0xf, true); }
template <int CTRL> __device__ __forceinline__ float dpp(float x) { return __uint_as_float(dppu<CTRL>(__float_as_uint(x))); }
__device__ __forceinline__ float xrow_max(float x) {
  auto s = __builtin_amdgcn_permlane16_swap(__float_as_uint(x), __float_as_uint(x), false, false);
  x = fmaxf(__uint_as_float(s[0]), __uint_as_float(s[1]));
  auto t = __builtin_amdgcn_permlane32_swap(__float_as_uint(x), __float_as_uint(x), false, false);
  return fmaxf(__uint_as_float(t[0]), __uint_as_float(t[1]));
}
__device__ __forceinline__ float xrow_sum(float x) {
  auto s = __builtin_amdgcn_permlane16_swap(__float_as_uint(x), __float_as_uint(x), false, false);
  x = __uint_as_float(s[0]) + __uint_as_float(s[1]);
  auto t = __builtin_amdgcn_permlane32_swap(__float_as_uint(x), __float_as_uint(x), false, false);
  return __uint_as_float(t[0]) + __uint_as_float(t[1]);
}
__device__ __forceinline__ v4i mkrsrc(const void* p, unsigned bytes) {
  const unsigned long long a = (unsigned long long)p;
  v4i r; r[0] = (int)(unsigned)a; r[1] = (int)((unsigned)(a >> 32) & 0xffffu); r[2] = (int)bytes; r[3] = 0x00020000; return r;
}
__device__ __forceinline__ int lds_word(LAS unsigned char* p) { RELOAD(); const int v = __builtin_amdgcn_readfirstlane(*(LAS int*)p); RELOAD(); return v; }
__device__ __forceinline__ void lds_inc(LAS unsigned char* p) {
  unsigned long long ex;
  asm volatile("s_mov_b64 %0, exec\n\ts_mov_b64 exec, 1\n\tds_add_u32 %1, %2\n\ts_mov_b64 exec, %0" : "=&s"(ex) : "v"((unsigned)(size_t)p), "v"(1u) : "memory");
}
#define DMA4(V, R, S) "buffer_load_dwordx4 " V ", " R ", " S " offen nt lds\n\tbuffer_load_dwordx4 " V ", " R ", " S " offen offset:1024 nt lds\n\t" \
                      "buffer_load_dwordx4 " V ", " R ", " S " offen offset:2048 nt lds\n\tbuffer_load_dwordx4 " V ", " R ", " S " offen offset:3072 nt lds\n\t"
__device__ __forceinline__ void issue_two(v4i r, unsigned vv, unsigned so, unsigned lds) {
  unsigned t;
  asm volatile("s_mov_b32 m0, %4\n\ts_add_u32 %0, %3, 0x1000\n\t" DMA4("%2", "%1", "%3") "s_add_u32 m0, m0, 0x1000\n\ts_nop 0\n\t" DMA4("%2", "%1", "%0")
               "s_add_u32 m0, m0, 0x1000\n\ts_add_u32 %0, %0, 0x1000\n\t" DMA4("%2", "%1", "%0") "s_add_u32 m0, m0, 0x1000\n\ts_add_u32 %0, %0, 0x1000\n\t" DMA4("%2", "%1", "%0")
               : "=&s"(t) : "s"(r), "v"(vv), "s"(so), "s"(lds) : "memory", "scc");
}
__device__ __forceinline__ void dma1_nt(v4i r, unsigned voff, unsigned soff, unsigned lds) {
  asm volatile("s_mov_b32 m0, %3\n\ts_nop 0\n\tbuffer_load_dwordx4 %1, %0, %2 offen nt lds" :: "s"(r), "v"(voff), "s"(soff), "s"(lds) : "memory");
}
__device__ __forceinline__ void dma1(v4i r, unsigned voff, unsigned soff, unsigned lds) {
  asm volatile("s_mov_b32 m0, %3\n\ts_nop 0\n\tbuffer_load_dwordx4 %1, %0, %2 offen lds" :: "s"(r), "v"(voff), "s"(soff), "s"(lds) : "memory");
}
struct Dbg { unsigned d0, d1; };
template <int W> __device__ __forceinline__ void stream_units(v4i rs, unsigned vv, unsigned u, unsigned ue, unsigned so, unsigned ra, unsigned uc16, int hold, int msp, int lat, int ms2, Dbg& dbg, int lane) {
  unsigned ta, tb, st = 8192u, fo = ((u + 1u) & 3u) * 8u + C_FREE;
#define STAMP_ASM
#define STAMP_OPS
#define STAMP_INS
  asm volatile(
      "s_mov_b32 m0, %[m0i]\n\t"
      "v_mov_b32 v2, %[ld0]\n"
      "LT%=:\n\t"
      "s_waitcnt vmcnt(%[win])\n\t"
      DMA4("%[v0]", "%[rs]", "%[so]")
      "s_add_u32 m0, m0, 0x1000\n\t"
      "s_memrealtime s[40:41]\n\t"
      DMA4("%[v1]", "%[rs]", "%[so]")
      "ds_read_b64 v[4:5], %[fo]\n\t"
      "s_add_u32 m0, m0, 0x1000\n\t"
      "s_add_u32 %[so], %[so], %[st]\n\t"
      "s_and_b32 m0, m0, 0x1ffff\n\t"
      "s_and_b32 %[so], %[so], 0x1fffff\n\t"
      "s_xor_b32 %[st], %[st], 0xc000\n\t"
      "s_add_u32 %[ra], %[ra], %[uc]\n\t"
      "v_add_u32 v2, 1, v2\n\t"
      "v_add_u32 %[fo], 8, %[fo]\n\t"
      "s_add_u32 %[u], %[u], 1\n\t"
      "v_and_b32 %[fo], %[fm], %[fo]\n\t"
      "s_waitcnt lgkmcnt(0)\n\t"
      "v_mov_b32 v3, s40\n\t"
      "v_readfirstlane_b32 %[a], v5\n\t"
      "s_lshr_b32 s41, %[ra], 4\n\t"
      "v_readfirstlane_b32 %[b], v4\n\t"
      "ds_write_b64 %[vl], v[2:3]\n\t"
      STAMP_ASM
      "s_cmp_eq_u32 %[u], %[ue]\n\t"
      "s_cbranch_scc1 LE%=\n\t"
      "s_add_u32 %[a], %[a], s41\n\t"
      "s_sub_i32 %[a], %[a], s40\n\t"
      "s_cmp_lt_i32 %[a], %[lat]\n\t"
      "s_cselect_b32 s41, %[ms2], %[msp]\n\t"
      "s_min_i32 %[a], %[a], %[hold]\n\t"
      "s_max_i32 %[a], %[a], s41\n\t"
      "s_add_u32 %[a], %[a], s40\n\t"
      "s_cmp_lt_u32 %[b], %[u]\n\t"
      "s_cbranch_scc1 LS%=\n"
      "LW%=:\n\t"
      "s_memrealtime s[40:41]\n\t"
      "s_waitcnt lgkmcnt(0)\n\t"
      "s_sub_i32 %[b], %[a], s40\n\t"
      "s_cmp_gt_i32 %[b], 0\n\t"
      "s_cbranch_scc0 LT%=\n\t"
      "s_branch LW%=\n"
      "LS%=:\n\t"
      "s_and_b32 %[b], %[u], 3\n\t"
      "s_lshl_b32 %[b], %[b], 3\n\t"
      "s_add_u32 %[b], %[b], %[fb]\n\t"
      "v_mov_b32 v5, %[b]\n"
      "LR%=:\n\t"
      "s_sleep 1\n\t"
      "ds_read_b32 v4, v5\n\t"
      "s_waitcnt lgkmcnt(0)\n\t"
      "v_readfirstlane_b32 %[b], v4\n\t"
      "s_cmp_lt_u32 %[b], %[u]\n\t"
      "s_cbranch_scc1 LR%=\n\t"
      "s_branch LW%=\n"
      "LE%=:"
      : [so] "+s"(so), [ra] "+s"(ra), [u] "+s"(u), [st] "+s"(st), [fo] "+v"(fo), [a] "=&s"(ta), [b] "=&s"(tb) STAMP_OPS
      : [rs] "s"(rs), [v0] "v"(vv), [v1] "v"(vv + 4096u), [vl] "v"((unsigned)C_LAND), [ue] "s"(ue), [uc] "s"(uc16), [hold] "s"(hold), [msp] "s"(msp), [lat] "s"(lat), [ms2] "s"(ms2),
        [m0i] "s"((u & 15u) * SLOT), [ld0] "s"(u - 1u - LAG), [win] "n"(W), [fm] "s"(~32u), [fb] "s"((unsigned)C_FREE) STAMP_INS
      : "memory", "scc", "vcc", "v2", "v3", "v4", "v5", "s40", "s41");
}
static_assert((C_FREE & 32) == 0, "the record pointer wraps by clearing bit 5");

__device__ __forceinline__ f32x4 mfma32(v4u a, v4u b, f32x4 c) {
  return __builtin_amdgcn_mfma_f32_16x16x32_bf16(__builtin_bit_cast(bf16x8, a), __builtin_bit_cast(bf16x8, b), c, 0, 0, 0);
}
__device__ __forceinline__ v4u tr_frag(LAS unsigned char* p, int hi) {
  const v2u lo = __builtin_bit_cast(v2u, __builtin_amdgcn_ds_read_tr16_b64_v4i16((LAS s16x4*)p));
  const v2u up = __builtin_bit_cast(v2u, __builtin_amdgcn_ds_read_tr16_b64_v4i16((LAS s16x4*)(p + hi)));
  return v4u{lo[0], lo[1], up[0], up[1]};
}
__device__ __forceinline__ float score16(const v4u* K, v4u qf, unsigned j0) {
  float sc[4];
#pragma unroll
  for (int u = 0; u < 4; ++u) {
    const f32x4 d = mfma32(K[u], qf, f32x4{0.f, 0.f, 0.f, 0.f});
    sc[u] = ((dpp<0x00>(d[0]) + dpp<0x55>(d[1])) + dpp<0xAA>(d[2])) + dpp<0xFF>(d[3]);
  }
  return (j0 & 2) ? ((j0 & 1) ? sc[3] : sc[2]) : ((j0 & 1) ? sc[1] : sc[0]);
}
__device__ __forceinline__ void p_operands(unsigned k0, unsigned k1, unsigned mask0, unsigned mask1, v4u (&bo)[4]) {
  bo[0] = v4u{dppu<0x00>(k0) & mask0, dppu<0x00>(k0) & mask1, dppu<0x55>(k0) & mask0, dppu<0x55>(k0) & mask1};
  bo[1] = v4u{dppu<0xAA>(k0) & mask0, dppu<0xAA>(k0) & mask1, dppu<0xFF>(k0) & mask0, dppu<0xFF>(k0) & mask1};
  bo[2] = v4u{dppu<0x00>(k1) & mask0, dppu<0x00>(k1) & mask1, dppu<0x55>(k1) & mask0, dppu<0x55>(k1) & mask1};
  bo[3] = v4u{dppu<0xAA>(k1) & mask0, dppu<0xAA>(k1) & mask1, dppu<0xFF>(k1) & mask0, dppu<0xFF>(k1) & mask1};
  asm volatile("" : "+v"(bo[0]), "+v"(bo[1]), "+v"(bo[2]), "+v"(bo[3]));
}
#define RD2(A, P, O0, O1) "ds_read2st64_b64 %[" A "], %[" P "] offset0:" O0 " offset1:" O1 "\n\t"
#define MF(N, C, A, B) "s_waitcnt lgkmcnt(" N ")\n\tv_mfma_f32_16x16x32_bf16 %[" C "], %[" A "], %[" B "], %[" C "]\n\t"
__device__ __forceinline__ void pv32(unsigned p, const v4u (&bo)[4], f32x4& acc0, f32x4& acc1) {
  v4u a0, a1, a2, a3, a4, a5, a6, a7;
  asm volatile(RD2("a0", "p0", "0", "2") RD2("a1", "p1", "0", "2") RD2("a2", "p0", "4", "6") RD2("a3", "p1", "4", "6")
               RD2("a4", "p0", "8", "10") RD2("a5", "p1", "8", "10") RD2("a6", "p0", "12", "14") RD2("a7", "p1", "12", "14")
               MF("7", "c0", "a0", "b0") MF("6", "c1", "a1", "b0") MF("5", "c0", "a2", "b1") MF("4", "c1", "a3", "b1")
               MF("3", "c0", "a4", "b2") MF("2", "c1", "a5", "b2") MF("1", "c0", "a6", "b3") MF("0", "c1", "a7", "b3") "s_nop 7"
      : [c0] "+v"(acc0), [c1] "+v"(acc1), [a0] "=&v"(a0), [a1] "=&v"(a1), [a2] "=&v"(a2), [a3] "=&v"(a3), [a4] "=&v"(a4), [a5] "=&v"(a5), [a6] "=&v"(a6), [a7] "=&v"(a7)
      : [p0] "v"(p), [p1] "v"(p + 8), [b0] "v"(bo[0]), [b1] "v"(bo[1]), [b2] "v"(bo[2]), [b3] "v"(bo[3]) : "memory");
}
__device__ __forceinline__ v4u tag_bytes(unsigned tag) { return v4u{tag & 0xffu, (tag >> 8) & 0xffu, (tag >> 16) & 0xffu, tag >> 24}; }
__device__ __forceinline__ v4u tag4(f32x4 v, v4u tb) { return (__builtin_bit_cast(v4u, v) & 0xffffff00u) | tb; }
__device__ __forceinline__ unsigned or4(v4u u) { return (u[0] | u[1]) | (u[2] | u[3]); }

__global__ void __launch_bounds__(NTHR, 2) gqa_fused(const unsigned char* __restrict__ x, const unsigned char* __restrict__ kc, const unsigned char* __restrict__ vc,
                                                     const unsigned char* __restrict__ wq, const unsigned char* __restrict__ wo, unsigned char* __restrict__ out,
                                                     unsigned char* ws TUNE_ARG) {
  __shared__ __attribute__((aligned(16))) unsigned char smem[L_TOT];
  LAS unsigned char* L = (LAS unsigned char*)smem;
  unsigned long long t64;
  unsigned pf, pa;
  unsigned long long pc;
  unsigned clen;
  {
    unsigned long long ex;
    asm volatile("s_memrealtime %0\n\ts_getpc_b64 %1\nLgqa_pc:\n\t"
                 "s_mov_b32 %2, Lgqa_end_a-Lgqa_pc+160\n\ts_max_u32 %2, %2, Lgqa_end_b-Lgqa_pc+160\n\ts_max_u32 %2, %2, Lgqa_end_c-Lgqa_pc+160\n\t"
                 "s_max_u32 %2, %2, Lgqa_end_d-Lgqa_pc+160\n\ts_max_u32 %2, %2, Lgqa_end_e-Lgqa_pc+160\n\t"
                 "v_lshlrev_b32 %5, 7, %6\n\tv_add_u32 %5, 0xffffc000, %5\n\tv_cmp_gt_u32 vcc, %2, %5\n\t"
                 "s_and_saveexec_b64 %3, vcc\n\ts_cbranch_execz Lgqa_nopf\n\tglobal_load_dword %4, %5, %1 offset:-32\nLgqa_nopf:\n\ts_mov_b64 exec, %3"
                 : "=&s"(t64), "=&s"(pc), "=&s"(clen), "=&s"(ex), "=&v"(pf), "=&v"(pa) : "v"(threadIdx.x) : "scc", "vcc", "memory");
  }
  const int lane = threadIdx.x & 63, wv = __builtin_amdgcn_readfirstlane(threadIdx.x >> 6);
  const int j = blockIdx.x, xg = j & 7, idx = j >> 3;
  const int yy = (xg - idx) & 7, pr = (XF & 16) ? (idx >> 2) * 8 + xg : (XF & 128) ? xg * 8 + (idx >> 2) : (XF & 256) ? (idx >> 2) * 8 + ((xg + (idx >> 2)) & 7) : 2 * idx + (yy >> 2), s = (XF & (16 | 128 | 256)) ? (idx & 3) : yy & 3;
  const int ch = 4 * xg + (idx >> 3), nb = idx & 7;
  const int g = lane >> 4;
  const unsigned so0 = (unsigned)(pr >> 1) * 65536u + s * 16384u;
  const unsigned vv = lane * 16;
  const unsigned ra0 = ((unsigned)(j * 37) & 31u) * PHASE >> 1;
  asm volatile("s_waitcnt lgkmcnt(0)" : "+s"(t64) : "s"(x), "s"(kc), "s"(vc), "s"(wq), "s"(wo), "s"(out), "s"(ws), "s"(so0), "v"(vv));
  const unsigned t0 = (unsigned)t64;
  const size_t pbase = (size_t)pr * (SK * DH * 2);
  Dbg dbg = {0u, 0u};

  const v4i rk = mkrsrc(kc + pbase, SK * DH * 2);
  if (wv == 0) {
    __builtin_amdgcn_s_setprio(3);
#pragma unroll
    for (int u = 0; u < NFIRST; u += 2) issue_two(rk, vv, (so0 + (u >> 1) * 65536u) & 0x1fffffu, u * SLOT);
    __builtin_amdgcn_sched_barrier(0);
    const unsigned keep = (unsigned)((lane & 7) - 4) >> 31;
    *(LAS v2u*)(L + L_CTL + (lane & 7) * 8) = v2u{(12u + (lane & 7)) * keep, (t0 + LINE0) * keep};
    *(LAS int*)(L + L_DG) = 0;
    STAMP(1);
    asm volatile("s_waitcnt lgkmcnt(0)" ::: "memory");
  }
  unsigned e0, e1, e2;
  if (wv == 1) asm volatile("s_load_dword %0, %3, 0x0\n\ts_load_dword %1, %4, 0x0\n\ts_load_dword %2, %5, %6"
                            : "=&s"(e0), "=&s"(e1), "=&s"(e2) : "s"(ws), "s"((XF & 1024) ? x : ws), "s"((XF & 1024) ? wq : ws), "s"((XF & 1024) ? (j >> 6) * (128 * 8192) + (j & 63) * 128 : 0) : "memory");
  asm volatile("s_barrier" ::: "memory");
  if (wv == 0) stream_units<WIN0>(rk, vv, NFIRST, UW0, (so0 + (NFIRST >> 1) * 65536u) & 0x1fffffu, NFIRST * UC16 + ra0, UC16, HOLD0, MINSP, -LATE, MINSP2, dbg, lane);
  const __amdgpu_buffer_rsrc_t rw = __builtin_amdgcn_make_buffer_rsrc((void*)ws, 0, W_END, 0x00020000);
  const unsigned j0 = lane & 3;
  const int h = (lane >> 2) & 3;
  const unsigned mask0 = j0 == 0 ? 0x0000ffffu : (j0 == 1 ? 0xffff0000u : 0u);
  const unsigned mask1 = j0 == 2 ? 0x0000ffffu : (j0 == 3 ? 0xffff0000u : 0u);

  if (wv == 0) {
    const v4i rv = mkrsrc(vc + pbase, SK * DH * 2);
#pragma unroll 1
    for (int pass = 0; pass < 2; ++pass) {
      const unsigned ub = pass ? 64u : (unsigned)UW0;
      while ((unsigned)lds_word(L + C_FREE + (ub & 3u) * 8) < ub) __builtin_amdgcn_s_sleep(1);
      const v4i rs = {pass ? rv[0] : rk[0], pass ? rv[1] : rk[1], rk[2], rk[3]};
      stream_units<WIN>(rs, vv, ub, pass ? (unsigned)UPREP : 64u, (so0 + (ub >> 1) * 65536u) & 0x1fffffu, ub * UC16 + ra0, UC16, HOLD, MINSP, -LATE, MINSP2, dbg, lane);
    }
    while (lds_word(L + C_FREE + 24) < 64 + 4 * MLROW + 3 + 16) __builtin_amdgcn_s_sleep(1);
    v4u bo[NFIN][4], tb;
    unsigned vo = h * 512 + g * 64 + j0 * 16;
    float scale;
    {
      v2u pl[NFIN];
#pragma unroll
      for (int k = 0; k < NFIN; ++k) pl[k] = *(LAS v2u*)(L + L_PL + (4 - NFIN + k) * 512 + lane * 8);
      tb = tag_bytes(*(LAS unsigned*)(L + C_TAG));
      scale = *(LAS float*)(L + L_ML + 128 + h * 4);
      const int fr = *(LAS int*)(L + C_FREE + (UPREP & 3) * 8);
#pragma unroll
      for (int k = 0; k < NFIN; ++k) p_operands(pl[k][0], pl[k][1], mask0, mask1, bo[k]);
      asm volatile("" : "+v"(vo), "+v"(tb), "+v"(scale));
      if (__builtin_amdgcn_readfirstlane(fr) < UPREP) { while (lds_word(L + C_FREE + (UPREP & 3) * 8) < UPREP) __builtin_amdgcn_s_sleep(1); }
    }
    stream_units<WIN>(rv, vv, UPREP, NUNIT, (so0 + ((UPREP - 64) >> 1) * 65536u) & 0x1fffffu, UPREP * UC16 + ra0, UC16, HOLD, MINSP, -LATE, MINSP2, dbg, lane);
    STAMP(2);
    const int so = W_PA + (pr * 4 + s) * PA_REC;
    f32x4 acc0 = {0.f, 0.f, 0.f, 0.f}, acc1 = {0.f, 0.f, 0.f, 0.f};
    static_assert(NFIN >= 2 && LAG <= 3, "the drain below starts at three units outstanding");
    f32x4 s0, s1;
#pragma unroll
    for (int k = 0; k < 3; ++k) {
      if (k == 0) WAITVM(24); else if (k == 1) WAITVM(16); else WAITVM(8);
      if (k < 2) *(LAS int*)(L + C_LAND) = NUNIT - 3 + k;
      if (k >= 4 - NFIN) pv32((12 + k) * SLOT + vv, bo[k >= 4 - NFIN ? k - (4 - NFIN) : 0], acc0, acc1);
      if (k == MPOS) {
        while (lds_word(L + C_NA) < 4) {}
        f32x4 a[8];
#pragma unroll
        for (int u = 0; u < 8; ++u) a[u] = *(LAS f32x4*)(L + L_ACC + vv + u * 1024);
        __builtin_amdgcn_sched_barrier(0);
        s0 = ((a[0] + a[2]) + (a[4] + a[6])) * scale; s1 = ((a[1] + a[3]) + (a[5] + a[7])) * scale;
        asm volatile("" : "+v"(s0), "+v"(s1) : "v"(14 * SLOT + vv), "v"(14 * SLOT + vv + 8), "v"(15 * SLOT + vv), "v"(15 * SLOT + vv + 8), "s"(so), "s"(0xffffff00u));
      }
    }
    STAMP(4);
    WAITVM(0);
    STAMP(3);
    pv32(15 * SLOT + vv, bo[NFIN - 1], acc0, acc1);
    const v4u o0 = tag4(acc0 * scale + s0, tb), o1 = tag4(acc1 * scale + s1, tb);
    __builtin_amdgcn_raw_buffer_store_b128(o0, rw, vo, so, AUX_SC1);
    __builtin_amdgcn_raw_buffer_store_b128(o1, rw, vo + 256, so, AUX_SC1);
    asm volatile("s_nop 1" :: "v"(o0), "v"(o1) : "memory");
    *(LAS int*)(L + C_LAND) = NUNIT;
    asm volatile("Lgqa_end_a:" ::: "memory");
    STAMP(5);
    STAMP_OUT();
    return;
  }

  if (wv >= 2) {
    const int w = wv - 2;
    const unsigned vk = (h * 16 + g * 4 + j0) * 16;
    asm volatile("s_waitcnt vmcnt(0)" :: "v"(pf), "v"(pa) : "memory");
    f32x32 sc;
    float m;
    {
      v4u P[NREG][8];
#pragma unroll
      for (int r = 0; r < NREG; ++r) {
        while (lds_word(L + C_LAND) <= 4 * r + w) __builtin_amdgcn_s_sleep(2);
#pragma unroll
        for (int u = 0; u < 8; ++u) P[r][u] = *(LAS v4u*)(L + ((4 * r + w) & 15) * SLOT + vk + u * 1024);
        asm volatile("" :: "v"(P[r][0]), "v"(P[r][1]), "v"(P[r][2]), "v"(P[r][3]), "v"(P[r][4]), "v"(P[r][5]), "v"(P[r][6]), "v"(P[r][7]) : "memory");
        *(LAS int*)(L + C_FREE + w * 8) = 4 * r + w + 16;
      }
      STAMP(1);
      while (lds_word(L + C_READY) == 0) __builtin_amdgcn_s_sleep(2);
      STAMP(2);
      const v4u qf = *(LAS v4u*)(L + L_Q + vk);
      m = -INFINITY;
#pragma unroll
      for (int r = 0; r < NREG; ++r) {
        const float s0 = score16(&P[r][0], qf, j0), s1 = score16(&P[r][4], qf, j0);
        sc[2 * r] = s0; sc[2 * r + 1] = s1; m = fmaxf(m, fmaxf(s0, s1));
      }
#pragma unroll 1
      for (int n = NREG; n < 16; ++n) {
        while (lds_word(L + C_LAND) <= 4 * n + w) __builtin_amdgcn_s_sleep(2);
        v4u K[8];
#pragma unroll
        for (int u = 0; u < 8; ++u) K[u] = *(LAS v4u*)(L + ((4 * n + w) & 15) * SLOT + vk + u * 1024);
        asm volatile("" :: "v"(K[0]), "v"(K[1]), "v"(K[2]), "v"(K[3]), "v"(K[4]), "v"(K[5]), "v"(K[6]), "v"(K[7]) : "memory");
        *(LAS int*)(L + C_FREE + w * 8) = 4 * n + w + 16;
        const float s0 = score16(&K[0], qf, j0), s1 = score16(&K[4], qf, j0);
        sc[2 * n] = s0; sc[2 * n + 1] = s1; m = fmaxf(m, fmaxf(s0, s1));
      }
    }
    STAMP(10);
    const v4u tb = tag_bytes((unsigned)lds_word(L + C_TAG));
    m = fmaxf(m, dpp<0xB1>(m)); m = fmaxf(m, dpp<0x4E>(m)); m = xrow_max(m);
    *(LAS float*)(L + L_ML + h * 16 + w * 4) = m;
    lds_inc(L + C_NM);
    while (lds_word(L + C_NM) < 4) __builtin_amdgcn_s_sleep(1);
    {
      const f32x4 mw = *(LAS f32x4*)(L + L_ML + h * 16);
      m = fmaxf(fmaxf(mw[0], mw[1]), fmaxf(mw[2], mw[3]));
      float l = 0.f;
#pragma unroll
      for (int n = 0; n < 32; ++n) { const float p = __builtin_amdgcn_exp2f((sc[n] - m) * C2); l += p; sc[n] = __uint_as_float(cvtpk(p, p)); }
      l += dpp<0xB1>(l); l += dpp<0x4E>(l); l = xrow_sum(l);
      *(LAS float*)(L + L_ML + 64 + h * 16 + w * 4) = l;
      *(LAS v2u*)(L + L_PL + w * 512 + lane * 8) = v2u{__float_as_uint(sc[30]), __float_as_uint(sc[31])};
      lds_inc(L + C_NL);
    }
    STAMP(11);
    f32x4 acc0 = {0.f, 0.f, 0.f, 0.f}, acc1 = {0.f, 0.f, 0.f, 0.f};
#pragma unroll 1
    for (int n = 0; n < (w < 4 - NFIN ? 16 : 15); ++n) {
      if (w == 3 && n == 1) {
        while (lds_word(L + C_NL) < 4) __builtin_amdgcn_s_sleep(1);
        const f32x4 lw = *(LAS f32x4*)(L + L_ML + 64 + h * 16);
        if (lane < 16 && j0 == 0) __builtin_amdgcn_raw_buffer_store_b128(tag4(f32x4{m, (lw[0] + lw[1]) + (lw[2] + lw[3]), __uint_as_float(__float_as_uint(m) << 24), 0.f}, tb), rw, h * 16, W_ML + (pr * 4 + s) * 128, AUX_SC1);
      }
      if (w == 3 && n == MLROW) {
        v4u ml;
        for (unsigned spin = 0; spin < SPIN_CAP; ++spin) {
          RELOAD();
          ml = __builtin_amdgcn_raw_buffer_load_b128(rw, j0 * 128 + h * 16, W_ML + pr * 512, AUX_SC1) ^ tb;
          if (__all((or4(ml) & 0xffu) == 0u)) break;
        }
        const float ms = __uint_as_float(ml[0] | (ml[2] >> 24));
        float M = fmaxf(ms, dpp<0xB1>(ms)); M = fmaxf(M, dpp<0x4E>(M));
        float ls = __uint_as_float(ml[1]) * __builtin_amdgcn_exp2f((ms - M) * C2);
        ls += dpp<0xB1>(ls); ls += dpp<0x4E>(ls);
        if (lane < 16 && j0 == 0) *(LAS float*)(L + L_ML + 128 + h * 4) = __builtin_amdgcn_exp2f((m - M) * C2) * __builtin_amdgcn_rcpf(ls);
      }
      v4u bo[4];
      p_operands(__float_as_uint(sc[2 * n]), __float_as_uint(sc[2 * n + 1]), mask0, mask1, bo);
      while (lds_word(L + C_LAND) <= 64 + 4 * n + w) __builtin_amdgcn_s_sleep(1);
      pv32(((4 * n + w) & 15) * SLOT + vv, bo, acc0, acc1);
      *(LAS int*)(L + C_FREE + w * 8) = 64 + 4 * n + w + 16;
    }
    *(LAS f32x4*)(L + L_ACC + w * 2048 + vv) = acc0; *(LAS f32x4*)(L + L_ACC + w * 2048 + 1024 + vv) = acc1;
    lds_inc(L + C_NA);
    STAMP(3);
    const int bb = 2 * w + (lane >> 5), c = lane & 31;
    unsigned vg = bb * (HKV * 4 * PA_REC) + c * 16, vy = (lane & 7) * (32 * 2048) + g * 16;
    const int sg = W_PA + (ch >> 2) * (4 * PA_REC) + (ch & 3) * 512, sy = W_YB + (nb * 4 + w) * 2048 + ch * 64;
    LAS unsigned char* ap = L + L_W + (8 * g + ((lane & 15) >> 2)) * 128 + (16 * w + 4 * j0) * 2;
    LAS unsigned char* op = L + L_OT + bb * OT_ROW + c * 8;
    LAS unsigned char* bp = L + L_OT + (lane & 7) * OT_ROW + g * 16;
    if (lane & 8) vy = 0x40000000u;
    v4u tv = tb;
    asm volatile("" : "+v"(vg), "+v"(vy), "+v"(ap), "+v"(op), "+v"(bp), "+v"(tv) : "s"(sg), "s"(sy));
    while (lds_word(L + C_LAND) < GPOS) __builtin_amdgcn_s_sleep(1);
    STAMP(5);
    {
      const unsigned tp = now() + D1;
      unsigned tg = (unsigned)lds_word(L + C_FREE + 4) + (NUNIT * UC16 >> 4) + D1G;
      tg = tp + (unsigned)min(max((int)(tg - tp), 0), 200);
      while ((int)(now() - tg) < 0) {}
    }
    STAMP(4);
    v4u a[4], af[4], bf[4];
#pragma unroll
    for (int u = 0; u < 4; ++u) a[u] = __builtin_amdgcn_raw_buffer_load_b128(rw, vg, sg + u * PA_REC, AUX_SC1);
    while (lds_word(L + C_WO) == 0) {}
#pragma unroll
    for (int ks = 0; ks < 4; ++ks) af[ks] = tr_frag(ap + ks * 4096, 512);
    asm volatile("" : "+v"(af[0]), "+v"(af[1]), "+v"(af[2]), "+v"(af[3]));
#pragma unroll
    for (int u = 0; u < 4; ++u) a[u] ^= tv;
    asm volatile("" : "+v"(a[0]), "+v"(a[1]), "+v"(a[2]), "+v"(a[3]));
    if (__builtin_expect(!__all((or4((a[0] | a[1]) | (a[2] | a[3])) & 0xffu) == 0u), 0)) {
      for (unsigned spin = 0; spin < SPIN_CAP; ++spin) {
        RELOAD();
#pragma unroll
        for (int u = 0; u < 4; ++u) a[u] = __builtin_amdgcn_raw_buffer_load_b128(rw, vg, sg + u * PA_REC, AUX_SC1) ^ tv;
        if (__all((or4((a[0] | a[1]) | (a[2] | a[3])) & 0xffu) == 0u)) break;
      }
      asm volatile("Lgqa_end_d:" ::: "memory");
    }
    STAMP(6);
    const f32x4 o = (__builtin_bit_cast(f32x4, a[0]) + __builtin_bit_cast(f32x4, a[1])) + (__builtin_bit_cast(f32x4, a[2]) + __builtin_bit_cast(f32x4, a[3]));
    *(LAS v2u*)op = v2u{cvtpk(o[0], o[1]), cvtpk(o[2], o[3])};
    BARRIER();
    STAMP(7);
#pragma unroll
    for (int ks = 0; ks < 4; ++ks) bf[ks] = *(LAS v4u*)(bp + ks * 64);
    __builtin_amdgcn_sched_barrier(0);
    f32x4 y = {0.f, 0.f, 0.f, 0.f};
#pragma unroll
    for (int ks = 0; ks < 4; ++ks) y = mfma32(af[ks], bf[ks], y);
    __builtin_amdgcn_raw_buffer_store_b128(tag4(y, tv), rw, vy, sy, AUX_SC1);
    asm volatile("Lgqa_end_b:" ::: "memory");
    STAMP(8);
    STAMP_OUT();
    return;
  }

  __builtin_amdgcn_s_setprio(2);
  unsigned tag;
  {
    constexpr int NKS = 4 / NCH;
    const bool prod = (idx & (NCH - 1)) == NCH - 1;
    const int pid = (idx / NCH) * 8 + xg, cb = pid & 63, kq = pid >> 6;
    if (XF & 4096) asm volatile("s_waitcnt lgkmcnt(0)" ::: "memory");
    v4u tq;
    if (prod) {
      const v4i rx = mkrsrc(x, NB * DM * 2), rq = mkrsrc(wq, (XF & 8) ? 0 : DM * HQ * DH * 2);
      const unsigned vx = (lane & 7) * 1024 + (lane >> 3) * 16, vq = (lane >> 3) * 8192 + (lane & 7) * 16;
      const bool hi = (lane & 8) != 0;
      const int vs = (lane & 7) * (HKV * NKS * 2048) + (hi ? 64 : 0) + g * 16, ss = W_QG + (cb >> 3) * (NKS * 2048) + kq * 2048 + (cb & 7) * 256;
      f32x4 qa[4];
#pragma unroll
      for (int t = 0; t < 4; ++t) qa[t] = f32x4{0.f, 0.f, 0.f, 0.f};
      LAS unsigned char* ap = L + L_W + (8 * g + ((lane & 15) >> 2)) * 128 + j0 * 8;
      LAS unsigned char* bp = L + L_X + g * 128 + (lane & 7) * 16;
#pragma unroll 1
      for (int c = kq * NCH; c < kq * NCH + NCH; ++c) {
        dma1(rx, vx, c * 256, L_X);
        dma1(rx, vx, c * 256 + 128, L_X + 1024);
#pragma unroll 1
        for (int u = 0; u < 16; ++u) dma1_nt(rq, vq, (c * 16 + u) * 65536 + cb * 128, L_W + u * 1024);
        STAMP(5);
        WAITVM(0);
        STAMP(10);
#pragma unroll
        for (int ks = 0; ks < 4; ++ks) {
          const v4u xb = *(LAS v4u*)(bp + ks * 512);
#pragma unroll
          for (int t = 0; t < 4; ++t) qa[t] = mfma32(tr_frag(ap + ks * 4096 + t * 32, 512), xb, qa[t]);
        }
        asm volatile("" : "+v"(qa[0]), "+v"(qa[1]), "+v"(qa[2]), "+v"(qa[3]) :: "memory");
      }
      STAMP(1);
      asm volatile("s_waitcnt lgkmcnt(0)" : "+s"(e0) :: "memory");
      tag = ((e0 & 0x7f7f7f7fu) + 0x51276b35u) ^ (e0 & 0x80808080u);
      tq = tag_bytes(tag);
      const v4u o0 = tag4(f32x4{hi ? qa[1][0] : qa[0][0], hi ? qa[1][1] : qa[0][1], hi ? qa[1][2] : qa[0][2], hi ? qa[1][3] : qa[0][3]}, tq);
      const v4u o1 = tag4(f32x4{hi ? qa[3][0] : qa[2][0], hi ? qa[3][1] : qa[2][1], hi ? qa[3][2] : qa[2][2], hi ? qa[3][3] : qa[2][3]}, tq);
      if (!(XF & 1)) {
        __builtin_amdgcn_raw_buffer_store_b128(o0, rw, vs, ss, AUX_SC1);
        __builtin_amdgcn_raw_buffer_store_b128(o1, rw, vs + 128, ss, AUX_SC1);
      }
      asm volatile("s_nop 1" :: "v"(o0), "v"(o1) : "memory");
      STAMP(2);
    }
    asm volatile("s_waitcnt lgkmcnt(0)" : "+s"(e0) : "s"(e1), "s"(e2) : "memory");
    tag = ((e0 & 0x7f7f7f7fu) + 0x51276b35u) ^ (e0 & 0x80808080u);
    tq = tag_bytes(tag);
    while ((int)(now() - t0) < QPOLL) __builtin_amdgcn_s_sleep(2);
    const int so = W_QG + pr * (NKS * 2048);
    v4u a[2 * NKS];
    for (unsigned spin = 0; spin < SPIN_CAP; ++spin) {
      RELOAD();
      v4u bad = {0u, 0u, 0u, 0u};
#pragma unroll
      for (int u = 0; u < 2 * NKS; ++u) { a[u] = __builtin_amdgcn_raw_buffer_load_b128(rw, vv + (u & 3) * 1024, so + (u >> 2) * 4096, AUX_SC1) ^ tq; bad |= a[u]; }
      if ((XF & 3) || __all((or4(bad) & 0xffu) == 0u)) break;
      if (XF & 8192) __builtin_amdgcn_s_sleep(12);
      STAMP(15);
    }
#pragma unroll
    for (int u = 0; u < 2; ++u) {
      f32x4 v = __builtin_bit_cast(f32x4, a[u]);
#pragma unroll
      for (int p = 1; p < NKS; ++p) v += __builtin_bit_cast(f32x4, a[2 * p + u]);
      *(LAS v2u*)(L + L_Q + u * 512 + lane * 8) = v2u{cvtpk(v[0], v[1]), cvtpk(v[2], v[3])};
    }
    RELOAD();
    *(LAS v2u*)(L + C_READY) = v2u{1u, tag};
  }
  STAMP(3);
  if ((XF & 32) && lane == 0) {
    const unsigned a = __builtin_nontemporal_load((const GAS unsigned*)(vc + pbase + so0));
    const unsigned b2 = __builtin_nontemporal_load((const GAS unsigned*)(wo + ch * 128 * 1024 + nb * 128));
    const unsigned c = (XF & 2048) ? __builtin_nontemporal_load((const GAS unsigned*)(out + j * 32)) : 0u;
    asm volatile("" :: "v"(a), "v"(b2), "v"(c));
  }
  {
    unsigned k = now() / FB_P + 1u;
    float af = 0.f, dr = 0.f;
    int nv = 0;
#pragma unroll 1
    for (int it = 0; it < 40; ++it, ++k) {
      const unsigned tk = k * FB_P;
      while ((int)(now() - tk) < 0) __builtin_amdgcn_s_sleep(2);
      RELOAD();
      const v2u pr2 = *(LAS v2u*)(L + C_LAND);
      const int landed = __builtin_amdgcn_readfirstlane(pr2[0]);
      if (landed >= FBEND) break;
      const v2u fb = __builtin_amdgcn_raw_buffer_load_b64(rw, (lane & (FB_NREP - 1)) * 8, W_FB + ((k - 1u) & 63u) * FB_SLOT, AUX_SC1);
      if (j < FB_NREP && lane == 0) {
        const unsigned a = pr2[1] - (((pr2[0] + LAG) * UC16 + ra0) >> 4) + (unsigned)max((int)(tk - pr2[1]) - (int)(UC16 >> 4), 0);
        __builtin_amdgcn_raw_buffer_store_b64(v2u{a, tag ^ k}, rw, 0, W_FB + (k & 63u) * FB_SLOT + j * 8, AUX_SC1);
      }
      const bool ok = fb[1] == (tag ^ (k - 1u));
      float ps = ok ? (float)(int)(fb[0] - t0) : 0.f, pc = ok ? 1.f : 0.f;
      ps += dpp<0xB1>(ps); ps += dpp<0x4E>(ps); ps += dpp<0x124>(ps); ps += dpp<0x128>(ps); ps = xrow_sum(ps);
      pc += dpp<0xB1>(pc); pc += dpp<0x4E>(pc); pc += dpp<0x124>(pc); pc += dpp<0x128>(pc); pc = xrow_sum(pc);
      if (__builtin_amdgcn_readfirstlane(pc < 48.f)) continue;
      const float as = ps * __builtin_amdgcn_rcpf(pc) - (float)(landed < DSW / 8 ? DELTA : DELTA2);
      const float er = as - af;
      if (nv >= FB_I0) dr = fminf(fmaxf(dr + FB_KI * fminf(er - FB_Z, 30.f), FB_DMIN), 16.f);
      af = nv ? af + dr + FB_G * er : as;
      ++nv;
      *(LAS unsigned*)(L + C_FREE + 4 + j0 * 8) = t0 + (unsigned)(int)af;
    }
    if (nv) *(LAS unsigned*)(L + C_FREE + 4 + j0 * 8) = t0 + (unsigned)(int)(af + FB_FIN * fmaxf(dr, 0.f));
  }
  {
    const v4i ro = mkrsrc(wo, HQ * DH * DM * 2);
    const unsigned vo = ((lane >> 5) + 8 * ((lane >> 3) & 3)) * 1024 + (lane & 7) * 16;
    while (lds_word(L + C_LAND) < WOPOS) __builtin_amdgcn_s_sleep(1);
#pragma unroll 1
    for (int u = 0; u < 16; ++u) dma1_nt(ro, vo, (ch * 128 + 32 * ((u >> 1) & 3) + 4 * (u >> 3) + 2 * (u & 1)) * 1024 + nb * 128, L_W + u * 1024);
    const __amdgpu_buffer_rsrc_t rout = __builtin_amdgcn_make_buffer_rsrc((void*)out, 0, NB * DM * 2, 0x00020000);
    v4u tb = tag_bytes(tag);
    unsigned vs = (lane & 28) == 0 ? j0 * 8 + (lane >> 5) * 4 : 0x40000000u;
    asm volatile("s_waitcnt vmcnt(0)" : "+v"(vs), "+v"(tb) :: "memory");
    *(LAS int*)(L + C_WO) = 1;
    STAMP(11);
    BARRIER();
    STAMP(7);
    const unsigned tp = now() + D2;
    while ((int)(now() - tp) < 0) {}
    v4u a0 = __builtin_amdgcn_raw_buffer_load_b128(rw, vv, W_YB + j * 2048, AUX_SC1) ^ tb;
    v4u a1 = __builtin_amdgcn_raw_buffer_load_b128(rw, vv + 1024, W_YB + j * 2048, AUX_SC1) ^ tb;
    asm volatile("" : "+v"(a0), "+v"(a1));
    if (__builtin_expect(!__all((or4(a0 | a1) & 0xffu) == 0u), 0)) {
      for (unsigned spin = 0; spin < SPIN_CAP; ++spin) {
        RELOAD();
        a0 = __builtin_amdgcn_raw_buffer_load_b128(rw, vv, W_YB + j * 2048, AUX_SC1) ^ tb;
        a1 = __builtin_amdgcn_raw_buffer_load_b128(rw, vv + 1024, W_YB + j * 2048, AUX_SC1) ^ tb;
        if (__all((or4(a0 | a1) & 0xffu) == 0u)) break;
      }
      asm volatile("Lgqa_end_e:" ::: "memory");
    }
    STAMP(8);
    const f32x4 y = __builtin_bit_cast(f32x4, a0) + __builtin_bit_cast(f32x4, a1);
    const auto u02 = __builtin_amdgcn_permlane32_swap(__float_as_uint(y[0]), __float_as_uint(y[2]), false, false);
    const auto u13 = __builtin_amdgcn_permlane32_swap(__float_as_uint(y[1]), __float_as_uint(y[3]), false, false);
    float e = __uint_as_float(u02[0]) + __uint_as_float(u02[1]), f = __uint_as_float(u13[0]) + __uint_as_float(u13[1]);
    e += dpp<0x124>(e); f += dpp<0x124>(f); e += dpp<0x128>(e); f += dpp<0x128>(f);
    const auto ue = __builtin_amdgcn_permlane16_swap(__float_as_uint(e), __float_as_uint(e), false, false);
    const auto uf = __builtin_amdgcn_permlane16_swap(__float_as_uint(f), __float_as_uint(f), false, false);
    __builtin_amdgcn_raw_buffer_store_b32(cvtpk(__uint_as_float(ue[0]) + __uint_as_float(ue[1]), __uint_as_float(uf[0]) + __uint_as_float(uf[1])), rout, vs, j * 32, 0);
  }
  if (j == 0 && lane == 0) __builtin_amdgcn_raw_buffer_store_b32(tag, rw, 0, W_E, AUX_SC1);
  asm volatile("Lgqa_end_c:" ::: "memory");
  STAMP(9);
  STAMP_OUT();
}

extern "C" void kernel_launch(void* const* d_in, const int* in_sizes, int n_in, void* d_out, int out_size, void* d_ws, size_t ws_size,
                              hipStream_t stream) {
  gqa_fused<<<NWG, NTHR, 0, stream>>>((const unsigned char*)d_in[0], (const unsigned char*)d_in[1], (const unsigned char*)d_in[2], (const unsigned char*)d_in[3],
                                      (const unsigned char*)d_in[4], (unsigned char*)d_out, (unsigned char*)d_ws TUNE_PASS);
}
```

```cpp
#include <hip/hip_runtime.h>
#include <hip/hip_bf16.h>
#include <stdint.h>

#define GAS __attribute__((address_space(1)))
#define LAS __attribute__((address_space(3)))
typedef unsigned v4u __attribute__((ext_vector_type(4)));
typedef unsigned v2u __attribute__((ext_vector_type(2)));
typedef int v4i __attribute__((ext_vector_type(4)));
typedef float f32x4 __attribute__((ext_vector_type(4)));
typedef float f32x32 __attribute__((ext_vector_type(32)));
typedef short s16x4 __attribute__((ext_vector_type(4)));
typedef __bf16 bf16x8 __attribute__((ext_vector_type(8)));

constexpr int NB = 8, DM = 512, HQ = 32, HKV = 8, SK = 8192, DH = 128;
constexpr int NWG = 256, NTHR = 384;
constexpr int NUNIT = 128;
constexpr int SLOT = 8192;
constexpr float C2 = 0.088388347648318440f * 1.4426950408889634f;

constexpr int NREG = 5;
constexpr int NFIRST = 4;
constexpr int NCH = 2;
constexpr int MLROW = 8;
constexpr int UPREP = 112;
constexpr int NFIN = 3;
constexpr int MPOS = 1;
constexpr int WIN0 = 16, UW0 = 32, WIN = 24;
constexpr unsigned UC16 = (unsigned)(3.39f * 128.f);
constexpr int LINE0 = 65;
constexpr unsigned PHASE = 28;
constexpr int HOLD = 30, MINSP = 20;
constexpr int LATE = 40, MINSP2 = 16;
constexpr int FB_NREP = 32, FBEND = 110;
constexpr unsigned FB_P = 192u;
constexpr int DELTA = 14, DELTA2 = 4, DSW = 81;
constexpr float FB_G = 0.5f, FB_KI = 0.15f, FB_Z = 5.f, FB_FIN = 3.f;
constexpr int FB_I0 = 2;
constexpr int QPOLL = 600;
constexpr int WOPOS = 125;
constexpr int GPOS = 126, D1 = 30, D1G = 134;
constexpr int D2 = 40;
constexpr unsigned SPIN_CAP = 1u << 20;
constexpr int LAG = (WIN + 7) / 8;

constexpr int L_Q = 16 * SLOT, L_ACC = L_Q + 1024, L_PL = L_ACC + 4 * 2048, L_OT = L_PL + 2048, OT_ROW = 272, L_ML = L_OT + 8 * OT_ROW, L_CTL = (L_ML + 144 + 63) / 64 * 64;
constexpr int L_X = L_ACC, L_W = L_CTL + 64, L_TOT = L_W + 16384;
constexpr int C_FREE = L_CTL, C_LAND = L_CTL + 32, C_READY = L_CTL + 40, C_TAG = L_CTL + 44, C_NM = L_CTL + 48, C_NL = L_CTL + 52, C_NA = L_CTL + 56, C_WO = L_CTL + 60;
static_assert(L_CTL % 32 == 0 && L_TOT <= 160 * 1024, "lds map");
constexpr int W_E = 0, W_FB = 1024, FB_SLOT = 512, W_ML = W_FB + 64 * FB_SLOT + 1024, W_QG = W_ML + NWG * 128, W_PA = W_QG + NWG * 2048, PA_REC = 2048, W_YB = W_PA + NWG * PA_REC,
              W_END = W_YB + NWG * 2048;


#define STR_(x) #x
#define WAITVM(n) asm volatile("s_waitcnt vmcnt(" STR_(n) ")" ::: "memory")
#define BARRIER() asm volatile("s_waitcnt lgkmcnt(0)\n\ts_barrier" ::: "memory")
#define RELOAD() asm volatile("" ::: "memory")
constexpr int AUX_SC1 = 16;

__device__ __forceinline__ unsigned now() { return (unsigned)__builtin_amdgcn_s_memrealtime(); }
__device__ __forceinline__ unsigned cvtpk(float lo, float hi) {
  typedef __bf16 bf16x2_t __attribute__((ext_vector_type(2)));
  typedef float f32x2_t __attribute__((ext_vector_type(2)));
  return __builtin_bit_cast(unsigned, __builtin_convertvector(f32x2_t{lo, hi}, bf16x2_t));
}
template <int CTRL> __device__ __forceinline__ unsigned dppu(unsigned x) { return (unsigned)__builtin_amdgcn_mov_dpp((int)x, CTRL, 0xf, 0xf, true); }
template <int CTRL> __device__ __forceinline__ float dpp(float x) { return __uint_as_float(dppu<CTRL>(__float_as_uint(x))); }
__device__ __forceinline__ float xrow_max(float x) {
  auto s = __builtin_amdgcn_permlane16_swap(__float_as_uint(x), __float_as_uint(x), false, false);
  x = fmaxf(__uint_as_float(s[0]), __uint_as_float(s[1]));
  auto t = __builtin_amdgcn_permlane32_swap(__float_as_uint(x), __float_as_uint(x), false, false);
  return fmaxf(__uint_as_float(t[0]), __uint_as_float(t[1]));
}
__device__ __forceinline__ float xrow_sum(float x) {
  auto s = __builtin_amdgcn_permlane16_swap(__float_as_uint(x), __float_as_uint(x), false, false);
  x = __uint_as_float(s[0]) + __uint_as_float(s[1]);
  auto t = __builtin_amdgcn_permlane32_swap(__float_as_uint(x), __float_as_uint(x), false, false);
  return __uint_as_float(t[0]) + __uint_as_float(t[1]);
}
__device__ __forceinline__ v4i mkrsrc(const void* p, unsigned bytes) {
  const unsigned long long a = (unsigned long long)p;
  v4i r; r[0] = (int)(unsigned)a; r[1] = (int)((unsigned)(a >> 32) & 0xffffu); r[2] = (int)bytes; r[3] = 0x00020000; return r;
}
__device__ __forceinline__ int lds_word(LAS unsigned char* p) { RELOAD(); const int v = __builtin_amdgcn_readfirstlane(*(LAS int*)p); RELOAD(); return v; }
__device__ __forceinline__ void lds_inc(LAS unsigned char* p) {
  unsigned long long ex;
  asm volatile("s_mov_b64 %0, exec\n\ts_mov_b64 exec, 1\n\tds_add_u32 %1, %2\n\ts_mov_b64 exec, %0" : "=&s"(ex) : "v"((unsigned)(size_t)p), "v"(1u) : "memory");
}
#define DMA4(V, R, S) "buffer_load_dwordx4 " V ", " R ", " S " offen nt lds\n\tbuffer_load_dwordx4 " V ", " R ", " S " offen offset:1024 nt lds\n\t" \
                      "buffer_load_dwordx4 " V ", " R ", " S " offen offset:2048 nt lds\n\tbuffer_load_dwordx4 " V ", " R ", " S " offen offset:3072 nt lds\n\t"
__device__ __forceinline__ void issue_two(v4i r, unsigned vv, unsigned so, unsigned lds) {
  unsigned t;
  asm volatile("s_mov_b32 m0, %4\n\ts_add_u32 %0, %3, 0x1000\n\t" DMA4("%2", "%1", "%3") "s_add_u32 m0, m0, 0x1000\n\ts_nop 0\n\t" DMA4("%2", "%1", "%0")
               "s_add_u32 m0, m0, 0x1000\n\ts_add_u32 %0, %0, 0x1000\n\t" DMA4("%2", "%1", "%0") "s_add_u32 m0, m0, 0x1000\n\ts_add_u32 %0, %0, 0x1000\n\t" DMA4("%2", "%1", "%0")
               : "=&s"(t) : "s"(r), "v"(vv), "s"(so), "s"(lds) : "memory", "scc");
}
__device__ __forceinline__ void dma1_nt(v4i r, unsigned voff, unsigned soff, unsigned lds) {
  asm volatile("s_mov_b32 m0, %3\n\ts_nop 0\n\tbuffer_load_dwordx4 %1, %0, %2 offen nt lds" :: "s"(r), "v"(voff), "s"(soff), "s"(lds) : "memory");
}
__device__ __forceinline__ void dma1(v4i r, unsigned voff, unsigned soff, unsigned lds) {
  asm volatile("s_mov_b32 m0, %3\n\ts_nop 0\n\tbuffer_load_dwordx4 %1, %0, %2 offen lds" :: "s"(r), "v"(voff), "s"(soff), "s"(lds) : "memory");
}
template <int W> __device__ __forceinline__ void stream_units(v4i rs, unsigned vv, unsigned u, unsigned ue, unsigned so, unsigned ra, unsigned uc16, int hold, int msp, int lat, int ms2) {
  unsigned ta, tb, st = 8192u, fo = ((u + 1u) & 3u) * 8u + C_FREE;
  asm volatile(
      "s_mov_b32 m0, %[m0i]\n\t"
      "v_mov_b32 v2, %[ld0]\n"
      "LT%=:\n\t"
      "s_waitcnt vmcnt(%[win])\n\t"
      DMA4("%[v0]", "%[rs]", "%[so]")
      "s_add_u32 m0, m0, 0x1000\n\t"
      "s_memrealtime s[40:41]\n\t"
      DMA4("%[v1]", "%[rs]", "%[so]")
      "ds_read_b64 v[4:5], %[fo]\n\t"
      "s_add_u32 m0, m0, 0x1000\n\t"
      "s_add_u32 %[so], %[so], %[st]\n\t"
      "s_and_b32 m0, m0, 0x1ffff\n\t"
      "s_and_b32 %[so], %[so], 0x1fffff\n\t"
      "s_xor_b32 %[st], %[st], 0xc000\n\t"
      "s_add_u32 %[ra], %[ra], %[uc]\n\t"
      "v_add_u32 v2, 1, v2\n\t"
      "v_add_u32 %[fo], 8, %[fo]\n\t"
      "s_add_u32 %[u], %[u], 1\n\t"
      "v_and_b32 %[fo], %[fm], %[fo]\n\t"
      "s_waitcnt lgkmcnt(0)\n\t"
      "v_mov_b32 v3, s40\n\t"
      "v_readfirstlane_b32 %[a], v5\n\t"
      "s_lshr_b32 s41, %[ra], 4\n\t"
      "v_readfirstlane_b32 %[b], v4\n\t"
      "ds_write_b64 %[vl], v[2:3]\n\t"
      "s_cmp_eq_u32 %[u], %[ue]\n\t"
      "s_cbranch_scc1 LE%=\n\t"
      "s_add_u32 %[a], %[a], s41\n\t"
      "s_sub_i32 %[a], %[a], s40\n\t"
      "s_cmp_lt_i32 %[a], %[lat]\n\t"
      "s_cselect_b32 s41, %[ms2], %[msp]\n\t"
      "s_min_i32 %[a], %[a], %[hold]\n\t"
      "s_max_i32 %[a], %[a], s41\n\t"
      "s_add_u32 %[a], %[a], s40\n\t"
      "s_cmp_lt_u32 %[b], %[u]\n\t"
      "s_cbranch_scc1 LS%=\n"
      "LW%=:\n\t"
      "s_memrealtime s[40:41]\n\t"
      "s_waitcnt lgkmcnt(0)\n\t"
      "s_sub_i32 %[b], %[a], s40\n\t"
      "s_cmp_gt_i32 %[b], 0\n\t"
      "s_cbranch_scc0 LT%=\n\t"
      "s_branch LW%=\n"
      "LS%=:\n\t"
      "s_and_b32 %[b], %[u], 3\n\t"
      "s_lshl_b32 %[b], %[b], 3\n\t"
      "s_add_u32 %[b], %[b], %[fb]\n\t"
      "v_mov_b32 v5, %[b]\n"
      "LR%=:\n\t"
      "s_sleep 1\n\t"
      "ds_read_b32 v4, v5\n\t"
      "s_waitcnt lgkmcnt(0)\n\t"
      "v_readfirstlane_b32 %[b], v4\n\t"
      "s_cmp_lt_u32 %[b], %[u]\n\t"
      "s_cbranch_scc1 LR%=\n\t"
      "s_branch LW%=\n"
      "LE%=:"
      : [so] "+&s"(so), [ra] "+&s"(ra), [u] "+&s"(u), [st] "+&s"(st), [fo] "+&v"(fo), [a] "=&s"(ta), [b] "=&s"(tb)
      : [rs] "s"(rs), [v0] "v"(vv), [v1] "v"(vv + 4096u), [vl] "v"((unsigned)C_LAND), [ue] "s"(ue), [uc] "s"(uc16), [hold] "s"(hold), [msp] "s"(msp), [lat] "s"(lat), [ms2] "s"(ms2),
        [m0i] "s"((u & 15u) * SLOT), [ld0] "s"(u - 1u - LAG), [win] "n"(W), [fm] "s"(~32u), [fb] "s"((unsigned)C_FREE)
      : "memory", "scc", "vcc", "v2", "v3", "v4", "v5", "s40", "s41");
}
static_assert((C_FREE & 32) == 0, "the record pointer wraps by clearing bit 5");

__device__ __forceinline__ f32x4 mfma32(v4u a, v4u b, f32x4 c) {
  return __builtin_amdgcn_mfma_f32_16x16x32_bf16(__builtin_bit_cast(bf16x8, a), __builtin_bit_cast(bf16x8, b), c, 0, 0, 0);
}
__device__ __forceinline__ v4u tr_frag(LAS unsigned char* p, int hi) {
  const v2u lo = __builtin_bit_cast(v2u, __builtin_amdgcn_ds_read_tr16_b64_v4i16((LAS s16x4*)p));
  const v2u up = __builtin_bit_cast(v2u, __builtin_amdgcn_ds_read_tr16_b64_v4i16((LAS s16x4*)(p + hi)));
  return v4u{lo[0], lo[1], up[0], up[1]};
}
__device__ __forceinline__ float score16(const v4u* K, v4u qf, unsigned j0) {
  float sc[4];
#pragma unroll
  for (int u = 0; u < 4; ++u) {
    const f32x4 d = mfma32(K[u], qf, f32x4{0.f, 0.f, 0.f, 0.f});
    sc[u] = ((dpp<0x00>(d[0]) + dpp<0x55>(d[1])) + dpp<0xAA>(d[2])) + dpp<0xFF>(d[3]);
  }
  return (j0 & 2) ? ((j0 & 1) ? sc[3] : sc[2]) : ((j0 & 1) ? sc[1] : sc[0]);
}
__device__ __forceinline__ void p_operands(unsigned k0, unsigned k1, unsigned mask0, unsigned mask1, v4u (&bo)[4]) {
  bo[0] = v4u{dppu<0x00>(k0) & mask0, dppu<0x00>(k0) & mask1, dppu<0x55>(k0) & mask0, dppu<0x55>(k0) & mask1};
  bo[1] = v4u{dppu<0xAA>(k0) & mask0, dppu<0xAA>(k0) & mask1, dppu<0xFF>(k0) & mask0, dppu<0xFF>(k0) & mask1};
  bo[2] = v4u{dppu<0x00>(k1) & mask0, dppu<0x00>(k1) & mask1, dppu<0x55>(k1) & mask0, dppu<0x55>(k1) & mask1};
  bo[3] = v4u{dppu<0xAA>(k1) & mask0, dppu<0xAA>(k1) & mask1, dppu<0xFF>(k1) & mask0, dppu<0xFF>(k1) & mask1};
  asm volatile("" : "+v"(bo[0]), "+v"(bo[1]), "+v"(bo[2]), "+v"(bo[3]));
}
#define RD2(A, P, O0, O1) "ds_read2st64_b64 %[" A "], %[" P "] offset0:" O0 " offset1:" O1 "\n\t"
#define MF(N, C, A, B) "s_waitcnt lgkmcnt(" N ")\n\tv_mfma_f32_16x16x32_bf16 %[" C "], %[" A "], %[" B "], %[" C "]\n\t"
__device__ __forceinline__ void pv32(unsigned p, const v4u (&bo)[4], f32x4& acc0, f32x4& acc1) {
  v4u a0, a1, a2, a3, a4, a5, a6, a7;
  asm volatile(RD2("a0", "p0", "0", "2") RD2("a1", "p1", "0", "2") RD2("a2", "p0", "4", "6") RD2("a3", "p1", "4", "6")
               RD2("a4", "p0", "8", "10") RD2("a5", "p1", "8", "10") RD2("a6", "p0", "12", "14") RD2("a7", "p1", "12", "14")
               MF("7", "c0", "a0", "b0") MF("6", "c1", "a1", "b0") MF("5", "c0", "a2", "b1") MF("4", "c1", "a3", "b1")
               MF("3", "c0", "a4", "b2") MF("2", "c1", "a5", "b2") MF("1", "c0", "a6", "b3") MF("0", "c1", "a7", "b3") "s_nop 7"
      : [c0] "+&v"(acc0), [c1] "+&v"(acc1), [a0] "=&v"(a0), [a1] "=&v"(a1), [a2] "=&v"(a2), [a3] "=&v"(a3), [a4] "=&v"(a4), [a5] "=&v"(a5), [a6] "=&v"(a6), [a7] "=&v"(a7)
      : [p0] "v"(p), [p1] "v"(p + 8), [b0] "v"(bo[0]), [b1] "v"(bo[1]), [b2] "v"(bo[2]), [b3] "v"(bo[3]) : "memory");
}
__device__ __forceinline__ v4u tag_bytes(unsigned tag) { return v4u{tag & 0xffu, (tag >> 8) & 0xffu, (tag >> 16) & 0xffu, tag >> 24}; }
__device__ __forceinline__ v4u tag4(f32x4 v, v4u tb) { return (__builtin_bit_cast(v4u, v) & 0xffffff00u) | tb; }
__device__ __forceinline__ unsigned next_tag(unsigned e) { return ((e & 0x7f7f7f7fu) + 0x51276b35u) ^ (e & 0x80808080u); }
__device__ __forceinline__ unsigned or4(v4u u) { return (u[0] | u[1]) | (u[2] | u[3]); }

__global__ void __launch_bounds__(NTHR, 2) gqa_fused(const unsigned char* __restrict__ x, const unsigned char* __restrict__ kc, const unsigned char* __restrict__ vc,
                                                     const unsigned char* __restrict__ wq, const unsigned char* __restrict__ wo, unsigned char* __restrict__ out,
                                                     unsigned char* ws) {
  __shared__ __attribute__((aligned(16))) unsigned char smem[L_TOT];
  LAS unsigned char* L = (LAS unsigned char*)smem;
  unsigned long long t64;
  unsigned pf, pa;
  {
    unsigned long long pc, ex;
    unsigned clen;
    asm volatile("s_memrealtime %0\n\ts_getpc_b64 %1\nLgqa_pc:\n\t"
                 "s_mov_b32 %2, Lgqa_end_a-Lgqa_pc+160\n\ts_max_u32 %2, %2, Lgqa_end_b-Lgqa_pc+160\n\ts_max_u32 %2, %2, Lgqa_end_c-Lgqa_pc+160\n\t"
                 "s_max_u32 %2, %2, Lgqa_end_d-Lgqa_pc+160\n\ts_max_u32 %2, %2, Lgqa_end_e-Lgqa_pc+160\n\t"
                 "v_lshlrev_b32 %5, 7, %6\n\tv_add_u32 %5, 0xffffc000, %5\n\tv_cmp_gt_u32 vcc, %2, %5\n\t"
                 "s_and_saveexec_b64 %3, vcc\n\ts_cbranch_execz Lgqa_nopf\n\tglobal_load_dword %4, %5, %1 offset:-32\nLgqa_nopf:\n\ts_mov_b64 exec, %3"
                 : "=&s"(t64), "=&s"(pc), "=&s"(clen), "=&s"(ex), "=&v"(pf), "=&v"(pa) : "v"(threadIdx.x) : "scc", "vcc", "memory");
  }
  const int lane = threadIdx.x & 63, wv = __builtin_amdgcn_readfirstlane(threadIdx.x >> 6);
  const int j = blockIdx.x, xg = j & 7, idx = j >> 3;
  const int pr = (idx >> 2) * 8 + ((xg + (idx >> 2)) & 7), s = idx & 3;
  const int ch = 4 * xg + (idx >> 3), nb = idx & 7;
  const int g = lane >> 4;
  const unsigned so0 = (unsigned)(pr >> 1) * 65536u + s * 16384u;
  const unsigned vv = lane * 16;
  const unsigned ra0 = ((unsigned)(j * 37) & 31u) * PHASE >> 1;
  asm volatile("s_waitcnt lgkmcnt(0)" : "+s"(t64) : "s"(x), "s"(kc), "s"(vc), "s"(wq), "s"(wo), "s"(out), "s"(ws), "s"(so0), "v"(vv));
  const unsigned t0 = (unsigned)t64;
  const size_t pbase = (size_t)pr * (SK * DH * 2);

  const v4i rk = mkrsrc(kc + pbase, SK * DH * 2);
  if (wv == 0) {
    __builtin_amdgcn_s_setprio(3);
#pragma unroll
    for (int u = 0; u < NFIRST; u += 2) issue_two(rk, vv, (so0 + (u >> 1) * 65536u) & 0x1fffffu, u * SLOT);
    __builtin_amdgcn_sched_barrier(0);
    const unsigned keep = (unsigned)((lane & 7) - 4) >> 31;
    *(LAS v2u*)(L + L_CTL + (lane & 7) * 8) = v2u{(12u + (lane & 7)) * keep, (t0 + LINE0) * keep};
    asm volatile("s_waitcnt lgkmcnt(0)" ::: "memory");
  }
  unsigned e0, e1, e2;
  if (wv == 1) asm volatile("s_load_dword %0, %3, 0x0\n\ts_load_dword %1, %4, 0x0\n\ts_load_dword %2, %5, %6"
                            : "=&s"(e0), "=&s"(e1), "=&s"(e2) : "s"(ws), "s"(x), "s"(wq), "s"((j >> 6) * (128 * 8192) + (j & 63) * 128) : "memory");
  asm volatile("s_barrier" ::: "memory");
  if (wv == 0) stream_units<WIN0>(rk, vv, NFIRST, UW0, (so0 + (NFIRST >> 1) * 65536u) & 0x1fffffu, NFIRST * UC16 + ra0, UC16, HOLD, MINSP, -LATE, MINSP2);
  const __amdgpu_buffer_rsrc_t rw = __builtin_amdgcn_make_buffer_rsrc((void*)ws, 0, W_END, 0x00020000);
  const unsigned j0 = lane & 3;
  const int h = (lane >> 2) & 3;
  const unsigned mask0 = j0 == 0 ? 0x0000ffffu : (j0 == 1 ? 0xffff0000u : 0u);
  const unsigned mask1 = j0 == 2 ? 0x0000ffffu : (j0 == 3 ? 0xffff0000u : 0u);

  if (wv == 0) {
    const v4i rv = mkrsrc(vc + pbase, SK * DH * 2);
#pragma unroll 1
    for (int pass = 0; pass < 2; ++pass) {
      const unsigned ub = pass ? 64u : (unsigned)UW0;
      while ((unsigned)lds_word(L + C_FREE + (ub & 3u) * 8) < ub) __builtin_amdgcn_s_sleep(1);
      const v4i rs = {pass ? rv[0] : rk[0], pass ? rv[1] : rk[1], rk[2], rk[3]};
      stream_units<WIN>(rs, vv, ub, pass ? (unsigned)UPREP : 64u, (so0 + (ub >> 1) * 65536u) & 0x1fffffu, ub * UC16 + ra0, UC16, HOLD, MINSP, -LATE, MINSP2);
    }
    while (lds_word(L + C_FREE + 24) < 64 + 4 * MLROW + 3 + 16) __builtin_amdgcn_s_sleep(1);
    v4u bo[NFIN][4], tb;
    unsigned vo = h * 512 + g * 64 + j0 * 16;
    float scale;
    {
      v2u pl[NFIN];
#pragma unroll
      for (int k = 0; k < NFIN; ++k) pl[k] = *(LAS v2u*)(L + L_PL + (4 - NFIN + k) * 512 + lane * 8);
      tb = tag_bytes(*(LAS unsigned*)(L + C_TAG));
      scale = *(LAS float*)(L + L_ML + 128 + h * 4);
      const int fr = *(LAS int*)(L + C_FREE + (UPREP & 3) * 8);
#pragma unroll
      for (int k = 0; k < NFIN; ++k) p_operands(pl[k][0], pl[k][1], mask0, mask1, bo[k]);
      asm volatile("" : "+v"(vo), "+v"(tb), "+v"(scale));
      if (__builtin_amdgcn_readfirstlane(fr) < UPREP) { while (lds_word(L + C_FREE + (UPREP & 3) * 8) < UPREP) __builtin_amdgcn_s_sleep(1); }
    }
    stream_units<WIN>(rv, vv, UPREP, NUNIT, (so0 + ((UPREP - 64) >> 1) * 65536u) & 0x1fffffu, UPREP * UC16 + ra0, UC16, HOLD, MINSP, -LATE, MINSP2);
    const int so = W_PA + (pr * 4 + s) * PA_REC;
    f32x4 acc0 = {0.f, 0.f, 0.f, 0.f}, acc1 = {0.f, 0.f, 0.f, 0.f};
    static_assert(NFIN >= 2 && LAG <= 3, "the drain below starts at three units outstanding");
    f32x4 s0, s1;
#pragma unroll
    for (int k = 0; k < 3; ++k) {
      if (k == 0) WAITVM(24); else if (k == 1) WAITVM(16); else WAITVM(8);
      if (k < 2) *(LAS int*)(L + C_LAND) = NUNIT - 3 + k;
      if (k >= 4 - NFIN) pv32((12 + k) * SLOT + vv, bo[k >= 4 - NFIN ? k - (4 - NFIN) : 0], acc0, acc1);
      if (k == MPOS) {
        while (lds_word(L + C_NA) < 4) {}
        f32x4 a[8];
#pragma unroll
        for (int u = 0; u < 8; ++u) a[u] = *(LAS f32x4*)(L + L_ACC + vv + u * 1024);
        __builtin_amdgcn_sched_barrier(0);
        s0 = ((a[0] + a[2]) + (a[4] + a[6])) * scale; s1 = ((a[1] + a[3]) + (a[5] + a[7])) * scale;
        asm volatile("" : "+v"(s0), "+v"(s1) : "v"(14 * SLOT + vv), "v"(14 * SLOT + vv + 8), "v"(15 * SLOT + vv), "v"(15 * SLOT + vv + 8), "s"(so), "s"(0xffffff00u));
      }
    }
    WAITVM(0);
    pv32(15 * SLOT + vv, bo[NFIN - 1], acc0, acc1);
    const v4u o0 = tag4(acc0 * scale + s0, tb), o1 = tag4(acc1 * scale + s1, tb);
    __builtin_amdgcn_raw_buffer_store_b128(o0, rw, vo, so, AUX_SC1);
    __builtin_amdgcn_raw_buffer_store_b128(o1, rw, vo + 256, so, AUX_SC1);
    asm volatile("s_nop 1" :: "v"(o0), "v"(o1) : "memory");
    *(LAS int*)(L + C_LAND) = NUNIT;
    asm volatile("Lgqa_end_a:" ::: "memory");
    return;
  }

  if (wv >= 2) {
    const int w = wv - 2;
    const unsigned vk = (h * 16 + g * 4 + j0) * 16;
    asm volatile("s_waitcnt vmcnt(0)" :: "v"(pf), "v"(pa) : "memory");
    f32x32 sc;
    float m;
    {
      v4u P[NREG][8];
#pragma unroll
      for (int r = 0; r < NREG; ++r) {
        while (lds_word(L + C_LAND) <= 4 * r + w) __builtin_amdgcn_s_sleep(2);
#pragma unroll
        for (int u = 0; u < 8; ++u) P[r][u] = *(LAS v4u*)(L + ((4 * r + w) & 15) * SLOT + vk + u * 1024);
        asm volatile("" :: "v"(P[r][0]), "v"(P[r][1]), "v"(P[r][2]), "v"(P[r][3]), "v"(P[r][4]), "v"(P[r][5]), "v"(P[r][6]), "v"(P[r][7]) : "memory");
        *(LAS int*)(L + C_FREE + w * 8) = 4 * r + w + 16;
      }
      while (lds_word(L + C_READY) == 0) __builtin_amdgcn_s_sleep(2);
      const v4u qf = *(LAS v4u*)(L + L_Q + vk);
      m = -INFINITY;
#pragma unroll
      for (int r = 0; r < NREG; ++r) {
        const float s0 = score16(&P[r][0], qf, j0), s1 = score16(&P[r][4], qf, j0);
        sc[2 * r] = s0; sc[2 * r + 1] = s1; m = fmaxf(m, fmaxf(s0, s1));
      }
#pragma unroll 1
      for (int n = NREG; n < 16; ++n) {
        while (lds_word(L + C_LAND) <= 4 * n + w) __builtin_amdgcn_s_sleep(2);
        v4u K[8];
#pragma unroll
        for (int u = 0; u < 8; ++u) K[u] = *(LAS v4u*)(L + ((4 * n + w) & 15) * SLOT + vk + u * 1024);
        asm volatile("" :: "v"(K[0]), "v"(K[1]), "v"(K[2]), "v"(K[3]), "v"(K[4]), "v"(K[5]), "v"(K[6]), "v"(K[7]) : "memory");
        *(LAS int*)(L + C_FREE + w * 8) = 4 * n + w + 16;
        const float s0 = score16(&K[0], qf, j0), s1 = score16(&K[4], qf, j0);
        sc[2 * n] = s0; sc[2 * n + 1] = s1; m = fmaxf(m, fmaxf(s0, s1));
      }
    }
    const v4u tb = tag_bytes((unsigned)lds_word(L + C_TAG));
    m = fmaxf(m, dpp<0xB1>(m)); m = fmaxf(m, dpp<0x4E>(m)); m = xrow_max(m);
    *(LAS float*)(L + L_ML + h * 16 + w * 4) = m;
    lds_inc(L + C_NM);
    while (lds_word(L + C_NM) < 4) __builtin_amdgcn_s_sleep(1);
    {
      const f32x4 mw = *(LAS f32x4*)(L + L_ML + h * 16);
      m = fmaxf(fmaxf(mw[0], mw[1]), fmaxf(mw[2], mw[3]));
      float l = 0.f;
#pragma unroll
      for (int n = 0; n < 32; ++n) { const float p = __builtin_amdgcn_exp2f((sc[n] - m) * C2); l += p; sc[n] = __uint_as_float(cvtpk(p, p)); }
      l += dpp<0xB1>(l); l += dpp<0x4E>(l); l = xrow_sum(l);
      *(LAS float*)(L + L_ML + 64 + h * 16 + w * 4) = l;
      *(LAS v2u*)(L + L_PL + w * 512 + lane * 8) = v2u{__float_as_uint(sc[30]), __float_as_uint(sc[31])};
      lds_inc(L + C_NL);
    }
    f32x4 acc0 = {0.f, 0.f, 0.f, 0.f}, acc1 = {0.f, 0.f, 0.f, 0.f};
#pragma unroll 1
    for (int n = 0; n < (w < 4 - NFIN ? 16 : 15); ++n) {
      if (w == 3 && n == 1) {
        while (lds_word(L + C_NL) < 4) __builtin_amdgcn_s_sleep(1);
        const f32x4 lw = *(LAS f32x4*)(L + L_ML + 64 + h * 16);
        if (lane < 16 && j0 == 0) __builtin_amdgcn_raw_buffer_store_b128(tag4(f32x4{m, (lw[0] + lw[1]) + (lw[2] + lw[3]), __uint_as_float(__float_as_uint(m) << 24), 0.f}, tb), rw, h * 16, W_ML + (pr * 4 + s) * 128, AUX_SC1);
      }
      if (w == 3 && n == MLROW) {
        v4u ml;
        for (unsigned spin = 0; spin < SPIN_CAP; ++spin) {
          RELOAD();
          ml = __builtin_amdgcn_raw_buffer_load_b128(rw, j0 * 128 + h * 16, W_ML + pr * 512, AUX_SC1) ^ tb;
          if (__all((or4(ml) & 0xffu) == 0u)) break;
        }
        const float ms = __uint_as_float(ml[0] | (ml[2] >> 24));
        float M = fmaxf(ms, dpp<0xB1>(ms)); M = fmaxf(M, dpp<0x4E>(M));
        float ls = __uint_as_float(ml[1]) * __builtin_amdgcn_exp2f((ms - M) * C2);
        ls += dpp<0xB1>(ls); ls += dpp<0x4E>(ls);
        if (lane < 16 && j0 == 0) *(LAS float*)(L + L_ML + 128 + h * 4) = __builtin_amdgcn_exp2f((m - M) * C2) * __builtin_amdgcn_rcpf(ls);
      }
      v4u bo[4];
      p_operands(__float_as_uint(sc[2 * n]), __float_as_uint(sc[2 * n + 1]), mask0, mask1, bo);
      while (lds_word(L + C_LAND) <= 64 + 4 * n + w) __builtin_amdgcn_s_sleep(1);
      pv32(((4 * n + w) & 15) * SLOT + vv, bo, acc0, acc1);
      *(LAS int*)(L + C_FREE + w * 8) = 64 + 4 * n + w + 16;
    }
    *(LAS f32x4*)(L + L_ACC + w * 2048 + vv) = acc0; *(LAS f32x4*)(L + L_ACC + w * 2048 + 1024 + vv) = acc1;
    lds_inc(L + C_NA);
    const int bb = 2 * w + (lane >> 5), c = lane & 31;
    unsigned vg = bb * (HKV * 4 * PA_REC) + c * 16, vy = (lane & 7) * (32 * 2048) + g * 16;
    const int sg = W_PA + (ch >> 2) * (4 * PA_REC) + (ch & 3) * 512, sy = W_YB + (nb * 4 + w) * 2048 + ch * 64;
    LAS unsigned char* ap = L + L_W + (8 * g + ((lane & 15) >> 2)) * 128 + (16 * w + 4 * j0) * 2;
    LAS unsigned char* op = L + L_OT + bb * OT_ROW + c * 8;
    LAS unsigned char* bp = L + L_OT + (lane & 7) * OT_ROW + g * 16;
    if (lane & 8) vy = 0x40000000u;
    v4u tv = tb;
    asm volatile("" : "+v"(vg), "+v"(vy), "+v"(ap), "+v"(op), "+v"(bp), "+v"(tv) : "s"(sg), "s"(sy));
    while (lds_word(L + C_LAND) < GPOS) __builtin_amdgcn_s_sleep(1);
    {
      const unsigned tp = now() + D1;
      unsigned tg = (unsigned)lds_word(L + C_FREE + 4) + (NUNIT * UC16 >> 4) + D1G;
      tg = tp + (unsigned)min(max((int)(tg - tp), 0), 200);
      while ((int)(now() - tg) < 0) {}
    }
    v4u a[4], af[4], bf[4];
#pragma unroll
    for (int u = 0; u < 4; ++u) a[u] = __builtin_amdgcn_raw_buffer_load_b128(rw, vg, sg + u * PA_REC, AUX_SC1);
    while (lds_word(L + C_WO) == 0) {}
#pragma unroll
    for (int ks = 0; ks < 4; ++ks) af[ks] = tr_frag(ap + ks * 4096, 512);
    asm volatile("" : "+v"(af[0]), "+v"(af[1]), "+v"(af[2]), "+v"(af[3]));
#pragma unroll
    for (int u = 0; u < 4; ++u) a[u] ^= tv;
    asm volatile("" : "+v"(a[0]), "+v"(a[1]), "+v"(a[2]), "+v"(a[3]));
    if (__builtin_expect(!__all((or4((a[0] | a[1]) | (a[2] | a[3])) & 0xffu) == 0u), 0)) {
      for (unsigned spin = 0; spin < SPIN_CAP; ++spin) {
        RELOAD();
#pragma unroll
        for (int u = 0; u < 4; ++u) a[u] = __builtin_amdgcn_raw_buffer_load_b128(rw, vg, sg + u * PA_REC, AUX_SC1) ^ tv;
        if (__all((or4((a[0] | a[1]) | (a[2] | a[3])) & 0xffu) == 0u)) break;
      }
      asm volatile("Lgqa_end_d:" ::: "memory");
    }
    const f32x4 o = (__builtin_bit_cast(f32x4, a[0]) + __builtin_bit_cast(f32x4, a[1])) + (__builtin_bit_cast(f32x4, a[2]) + __builtin_bit_cast(f32x4, a[3]));
    *(LAS v2u*)op = v2u{cvtpk(o[0], o[1]), cvtpk(o[2], o[3])};
    BARRIER();
#pragma unroll
    for (int ks = 0; ks < 4; ++ks) bf[ks] = *(LAS v4u*)(bp + ks * 64);
    __builtin_amdgcn_sched_barrier(0);
    f32x4 y = {0.f, 0.f, 0.f, 0.f};
#pragma unroll
    for (int ks = 0; ks < 4; ++ks) y = mfma32(af[ks], bf[ks], y);
    __builtin_amdgcn_raw_buffer_store_b128(tag4(y, tv), rw, vy, sy, AUX_SC1);
    asm volatile("Lgqa_end_b:" ::: "memory");
    return;
  }

  __builtin_amdgcn_s_setprio(2);
  unsigned tag;
  {
    constexpr int NKS = 4 / NCH;
    const bool prod = (idx & (NCH - 1)) == NCH - 1;
    const int pid = (idx / NCH) * 8 + xg, cb = pid & 63, kq = pid >> 6;
    v4u tq;
    if (prod) {
      const v4i rx = mkrsrc(x, NB * DM * 2), rq = mkrsrc(wq, DM * HQ * DH * 2);
      const unsigned vx = (lane & 7) * 1024 + (lane >> 3) * 16, vq = (lane >> 3) * 8192 + (lane & 7) * 16;
      const bool hi = (lane & 8) != 0;
      const int vs = (lane & 7) * (HKV * NKS * 2048) + (hi ? 64 : 0) + g * 16, ss = W_QG + (cb >> 3) * (NKS * 2048) + kq * 2048 + (cb & 7) * 256;
      f32x4 qa[4];
#pragma unroll
      for (int t = 0; t < 4; ++t) qa[t] = f32x4{0.f, 0.f, 0.f, 0.f};
      LAS unsigned char* ap = L + L_W + (8 * g + ((lane & 15) >> 2)) * 128 + j0 * 8;
      LAS unsigned char* bp = L + L_X + g * 128 + (lane & 7) * 16;
#pragma unroll 1
      for (int c = kq * NCH; c < kq * NCH + NCH; ++c) {
        dma1(rx, vx, c * 256, L_X);
        dma1(rx, vx, c * 256 + 128, L_X + 1024);
#pragma unroll 1
        for (int u = 0; u < 16; ++u) dma1_nt(rq, vq, (c * 16 + u) * 65536 + cb * 128, L_W + u * 1024);
        WAITVM(0);
#pragma unroll
        for (int ks = 0; ks < 4; ++ks) {
          const v4u xb = *(LAS v4u*)(bp + ks * 512);
#pragma unroll
          for (int t = 0; t < 4; ++t) qa[t] = mfma32(tr_frag(ap + ks * 4096 + t * 32, 512), xb, qa[t]);
        }
        asm volatile("" : "+v"(qa[0]), "+v"(qa[1]), "+v"(qa[2]), "+v"(qa[3]) :: "memory");
      }
      asm volatile("s_waitcnt lgkmcnt(0)" : "+s"(e0) :: "memory");
      tag = next_tag(e0);
      tq = tag_bytes(tag);
      const v4u o0 = tag4(f32x4{hi ? qa[1][0] : qa[0][0], hi ? qa[1][1] : qa[0][1], hi ? qa[1][2] : qa[0][2], hi ? qa[1][3] : qa[0][3]}, tq);
      const v4u o1 = tag4(f32x4{hi ? qa[3][0] : qa[2][0], hi ? qa[3][1] : qa[2][1], hi ? qa[3][2] : qa[2][2], hi ? qa[3][3] : qa[2][3]}, tq);
      __builtin_amdgcn_raw_buffer_store_b128(o0, rw, vs, ss, AUX_SC1);
      __builtin_amdgcn_raw_buffer_store_b128(o1, rw, vs + 128, ss, AUX_SC1);
      asm volatile("s_nop 1" :: "v"(o0), "v"(o1) : "memory");
    }
    asm volatile("s_waitcnt lgkmcnt(0)" : "+s"(e0) : "s"(e1), "s"(e2) : "memory");
    tag = next_tag(e0);
    tq = tag_bytes(tag);
    {
      unsigned f0, f1;
      asm volatile("s_load_dword %0, %2, %3\n\ts_load_dword %1, %4, %5\n\ts_waitcnt lgkmcnt(0)" : "=&s"(f0), "=&s"(f1) : "s"(vc + pbase), "s"(so0), "s"(wo), "s"(ch * 128 * 1024 + nb * 128) : "memory");
    }
    while ((int)(now() - t0) < QPOLL) __builtin_amdgcn_s_sleep(2);
    const int so = W_QG + pr * (NKS * 2048);
    v4u a[2 * NKS];
    for (unsigned spin = 0; spin < SPIN_CAP; ++spin) {
      RELOAD();
      v4u bad = {0u, 0u, 0u, 0u};
#pragma unroll
      for (int u = 0; u < 2 * NKS; ++u) { a[u] = __builtin_amdgcn_raw_buffer_load_b128(rw, vv + (u & 3) * 1024, so + (u >> 2) * 4096, AUX_SC1) ^ tq; bad |= a[u]; }
      if (__all((or4(bad) & 0xffu) == 0u)) break;
      __builtin_amdgcn_s_sleep(12);
    }
#pragma unroll
    for (int u = 0; u < 2; ++u) {
      f32x4 v = __builtin_bit_cast(f32x4, a[u]);
#pragma unroll
      for (int p = 1; p < NKS; ++p) v += __builtin_bit_cast(f32x4, a[2 * p + u]);
      *(LAS v2u*)(L + L_Q + u * 512 + lane * 8) = v2u{cvtpk(v[0], v[1]), cvtpk(v[2], v[3])};
    }
    RELOAD();
    *(LAS v2u*)(L + C_READY) = v2u{1u, tag};
  }
  {
    unsigned k = now() / FB_P + 1u;
    float af = 0.f, dr = 0.f;
    int nv = 0;
#pragma unroll 1
    for (int it = 0; it < 40; ++it, ++k) {
      const unsigned tk = k * FB_P;
      while ((int)(now() - tk) < 0) __builtin_amdgcn_s_sleep(2);
      RELOAD();
      const v2u pr2 = *(LAS v2u*)(L + C_LAND);
      const int landed = __builtin_amdgcn_readfirstlane(pr2[0]);
      if (landed >= FBEND) break;
      const v2u fb = __builtin_amdgcn_raw_buffer_load_b64(rw, (lane & (FB_NREP - 1)) * 8, W_FB + ((k - 1u) & 63u) * FB_SLOT, AUX_SC1);
      if (j < FB_NREP && lane == 0) {
        const unsigned a = pr2[1] - (((pr2[0] + LAG) * UC16 + ra0) >> 4) + (unsigned)max((int)(tk - pr2[1]) - (int)(UC16 >> 4), 0);
        __builtin_amdgcn_raw_buffer_store_b64(v2u{a, tag ^ k}, rw, 0, W_FB + (k & 63u) * FB_SLOT + j * 8, AUX_SC1);
      }
      const bool ok = fb[1] == (tag ^ (k - 1u));
      float ps = ok ? (float)(int)(fb[0] - t0) : 0.f, pc = ok ? 1.f : 0.f;
      ps += dpp<0xB1>(ps); ps += dpp<0x4E>(ps); ps += dpp<0x124>(ps); ps += dpp<0x128>(ps); ps = xrow_sum(ps);
      pc += dpp<0xB1>(pc); pc += dpp<0x4E>(pc); pc += dpp<0x124>(pc); pc += dpp<0x128>(pc); pc = xrow_sum(pc);
      if (__builtin_amdgcn_readfirstlane(pc < 48.f)) continue;
      const float as = ps * __builtin_amdgcn_rcpf(pc) - (float)(landed < DSW ? DELTA : DELTA2);
      const float er = as - af;
      if (nv >= FB_I0) dr = fminf(fmaxf(dr + FB_KI * fminf(er - FB_Z, 30.f), 0.f), 16.f);
      af = nv ? af + dr + FB_G * er : as;
      ++nv;
      *(LAS unsigned*)(L + C_FREE + 4 + j0 * 8) = t0 + (unsigned)(int)af;
    }
    if (nv) *(LAS unsigned*)(L + C_FREE + 4 + j0 * 8) = t0 + (unsigned)(int)(af + FB_FIN * fmaxf(dr, 0.f));
  }
  {
    const v4i ro = mkrsrc(wo, HQ * DH * DM * 2);
    const unsigned vo = ((lane >> 5) + 8 * ((lane >> 3) & 3)) * 1024 + (lane & 7) * 16;
    while (lds_word(L + C_LAND) < WOPOS) __builtin_amdgcn_s_sleep(1);
#pragma unroll 1
    for (int u = 0; u < 16; ++u) dma1_nt(ro, vo, (ch * 128 + 32 * ((u >> 1) & 3) + 4 * (u >> 3) + 2 * (u & 1)) * 1024 + nb * 128, L_W + u * 1024);
    const __amdgpu_buffer_rsrc_t rout = __builtin_amdgcn_make_buffer_rsrc((void*)out, 0, NB * DM * 2, 0x00020000);
    v4u tb = tag_bytes(tag);
    unsigned vs = (lane & 28) == 0 ? j0 * 8 + (lane >> 5) * 4 : 0x40000000u;
    asm volatile("s_waitcnt vmcnt(0)" : "+v"(vs), "+v"(tb) :: "memory");
    *(LAS int*)(L + C_WO) = 1;
    BARRIER();
    const unsigned tp = now() + D2;
    while ((int)(now() - tp) < 0) {}
    v4u a0 = __builtin_amdgcn_raw_buffer_load_b128(rw, vv, W_YB + j * 2048, AUX_SC1) ^ tb;
    v4u a1 = __builtin_amdgcn_raw_buffer_load_b128(rw, vv + 1024, W_YB + j * 2048, AUX_SC1) ^ tb;
    asm volatile("" : "+v"(a0), "+v"(a1));
    if (__builtin_expect(!__all((or4(a0 | a1) & 0xffu) == 0u), 0)) {
      for (unsigned spin = 0; spin < SPIN_CAP; ++spin) {
        RELOAD();
        a0 = __builtin_amdgcn_raw_buffer_load_b128(rw, vv, W_YB + j * 2048, AUX_SC1) ^ tb;
        a1 = __builtin_amdgcn_raw_buffer_load_b128(rw, vv + 1024, W_YB + j * 2048, AUX_SC1) ^ tb;
        if (__all((or4(a0 | a1) & 0xffu) == 0u)) break;
      }
      asm volatile("Lgqa_end_e:" ::: "memory");
    }
    const f32x4 y = __builtin_bit_cast(f32x4, a0) + __builtin_bit_cast(f32x4, a1);
    const auto u02 = __builtin_amdgcn_permlane32_swap(__float_as_uint(y[0]), __float_as_uint(y[2]), false, false);
    const auto u13 = __builtin_amdgcn_permlane32_swap(__float_as_uint(y[1]), __float_as_uint(y[3]), false, false);
    float e = __uint_as_float(u02[0]) + __uint_as_float(u02[1]), f = __uint_as_float(u13[0]) + __uint_as_float(u13[1]);
    e += dpp<0x124>(e); f += dpp<0x124>(f); e += dpp<0x128>(e); f += dpp<0x128>(f);
    const auto ue = __builtin_amdgcn_permlane16_swap(__float_as_uint(e), __float_as_uint(e), false, false);
    const auto uf = __builtin_amdgcn_permlane16_swap(__float_as_uint(f), __float_as_uint(f), false, false);
    __builtin_amdgcn_raw_buffer_store_b32(cvtpk(__uint_as_float(ue[0]) + __uint_as_float(ue[1]), __uint_as_float(uf[0]) + __uint_as_float(uf[1])), rout, vs, j * 32, 0);
  }
  if (j == 0 && lane == 0) __builtin_amdgcn_raw_buffer_store_b32(tag, rw, 0, W_E, AUX_SC1);
  asm volatile("Lgqa_end_c:" ::: "memory");
}

extern "C" void kernel_launch(void* const* d_in, const int* in_sizes, int n_in, void* d_out, int out_size, void* d_ws, size_t ws_size,
                              hipStream_t stream) {
  gqa_fused<<<NWG, NTHR, 0, stream>>>((const unsigned char*)d_in[0], (const unsigned char*)d_in[1], (const unsigned char*)d_in[2], (const unsigned char*)d_in[3],
                                      (const unsigned char*)d_in[4], (unsigned char*)d_out, (unsigned char*)d_ws);
}
```

```cpp
#define DG 0
#define MEAN 2
#include <hip/hip_runtime.h>
#include <hip/hip_bf16.h>
#include <stdint.h>

#define GAS __attribute__((address_space(1)))
#define LAS __attribute__((address_space(3)))
typedef unsigned v4u __attribute__((ext_vector_type(4)));
typedef unsigned v2u __attribute__((ext_vector_type(2)));
typedef int v4i __attribute__((ext_vector_type(4)));
typedef float f32x4 __attribute__((ext_vector_type(4)));
typedef float f32x32 __attribute__((ext_vector_type(32)));
typedef short s16x4 __attribute__((ext_vector_type(4)));
typedef __bf16 bf16x8 __attribute__((ext_vector_type(8)));

constexpr int NB = 8, DM = 512, HQ = 32, HKV = 8, SK = 8192, DH = 128;
constexpr int NWG = 256, NTHR = 384;
constexpr int NUNIT = 128;
constexpr int SLOT = 8192;
constexpr float C2 = 0.088388347648318440f * 1.4426950408889634f;

constexpr int NREG = 5;
constexpr int NFIRST = 4;
constexpr int NCH = 2;
constexpr int MLROW = 8;
constexpr int UPREP = 112;
constexpr int NFIN = 3;
constexpr int MPOS = 1;
constexpr int WIN0 = 16, UW0 = 32, WIN = 24;
constexpr unsigned UC16 = (unsigned)(3.39f * 128.f);
constexpr int LINE0 = 65;
constexpr unsigned PHASE = 28;
constexpr int HOLD = 30, MINSP = 20;
constexpr int LATE = 40, MINSP2 = 16;
constexpr int FB_NREP = 32, FBEND = 110;
constexpr unsigned FB_P = 192u;
constexpr int DELTA = 14, DELTA2 = 4, DSW = 81;
constexpr float FB_G = 0.5f, FB_KI = 0.15f, FB_Z = 5.f, FB_FIN = 3.f;
constexpr int FB_I0 = 2;
constexpr int QPOLL = 600;
constexpr int WOPOS = 125;
constexpr int GPOS = 126, D1 = 30, D1G = 138;
constexpr int D2 = 40;
constexpr unsigned SPIN_CAP = 1u << 20;
constexpr int LAG = (WIN + 7) / 8;

constexpr int L_Q = 16 * SLOT, L_ACC = L_Q + 1024, L_PL = L_ACC + 4 * 2048, L_OT = L_PL + 2048, OT_ROW = 272, L_ML = L_OT + 8 * OT_ROW, L_CTL = (L_ML + 144 + 63) / 64 * 64;
constexpr int L_X = L_ACC, L_W = L_CTL + 64, L_TOT = L_W + 16384;
constexpr int C_FREE = L_CTL, C_LAND = L_CTL + 32, C_READY = L_CTL + 40, C_TAG = L_CTL + 44, C_NM = L_CTL + 48, C_NL = L_CTL + 52, C_NA = L_CTL + 56, C_WO = L_CTL + 60;
static_assert(L_CTL % 32 == 0 && L_TOT <= 160 * 1024, "lds map");
constexpr int W_E = 0, W_FB = 1024, FB_SLOT = 512, W_ML = W_FB + 64 * FB_SLOT + 1024, W_QG = W_ML + NWG * 128, W_PA = W_QG + NWG * 2048, PA_REC = 2048, W_YB = W_PA + NWG * PA_REC,
              W_END = W_YB + NWG * 2048;


#define STR_(x) #x
#define WAITVM(n) asm volatile("s_waitcnt vmcnt(" STR_(n) ")" ::: "memory")
#define BARRIER() asm volatile("s_waitcnt lgkmcnt(0)\n\ts_barrier" ::: "memory")
#define RELOAD() asm volatile("" ::: "memory")
constexpr int AUX_SC1 = 16;

__device__ __forceinline__ unsigned now() { return (unsigned)__builtin_amdgcn_s_memrealtime(); }
__device__ __forceinline__ unsigned cvtpk(float lo, float hi) {
  typedef __bf16 bf16x2_t __attribute__((ext_vector_type(2)));
  typedef float f32x2_t __attribute__((ext_vector_type(2)));
  return __builtin_bit_cast(unsigned, __builtin_convertvector(f32x2_t{lo, hi}, bf16x2_t));
}
template <int CTRL> __device__ __forceinline__ unsigned dppu(unsigned x) { return (unsigned)__builtin_amdgcn_mov_dpp((int)x, CTRL, 0xf, 0xf, true); }
template <int CTRL> __device__ __forceinline__ float dpp(float x) { return __uint_as_float(dppu<CTRL>(__float_as_uint(x))); }
__device__ __forceinline__ float xrow_max(float x) {
  auto s = __builtin_amdgcn_permlane16_swap(__float_as_uint(x), __float_as_uint(x), false, false);
  x = fmaxf(__uint_as_float(s[0]), __uint_as_float(s[1]));
  auto t = __builtin_amdgcn_permlane32_swap(__float_as_uint(x), __float_as_uint(x), false, false);
  return fmaxf(__uint_as_float(t[0]), __uint_as_float(t[1]));
}
__device__ __forceinline__ float xrow_sum(float x) {
  auto s = __builtin_amdgcn_permlane16_swap(__float_as_uint(x), __float_as_uint(x), false, false);
  x = __uint_as_float(s[0]) + __uint_as_float(s[1]);
  auto t = __builtin_amdgcn_permlane32_swap(__float_as_uint(x), __float_as_uint(x), false, false);
  return __uint_as_float(t[0]) + __uint_as_float(t[1]);
}
__device__ __forceinline__ v4i mkrsrc(const void* p, unsigned bytes) {
  const unsigned long long a = (unsigned long long)p;
  v4i r; r[0] = (int)(unsigned)a; r[1] = (int)((unsigned)(a >> 32) & 0xffffu); r[2] = (int)bytes; r[3] = 0x00020000; return r;
}
__device__ __forceinline__ int lds_word(LAS unsigned char* p) { RELOAD(); const int v = __builtin_amdgcn_readfirstlane(*(LAS int*)p); RELOAD(); return v; }
__device__ __forceinline__ void lds_inc(LAS unsigned char* p) {
  unsigned long long ex;
  asm volatile("s_mov_b64 %0, exec\n\ts_mov_b64 exec, 1\n\tds_add_u32 %1, %2\n\ts_mov_b64 exec, %0" : "=&s"(ex) : "v"((unsigned)(size_t)p), "v"(1u) : "memory");
}
#define DMA4(V, R, S) "buffer_load_dwordx4 " V ", " R ", " S " offen nt lds\n\tbuffer_load_dwordx4 " V ", " R ", " S " offen offset:1024 nt lds\n\t" \
                      "buffer_load_dwordx4 " V ", " R ", " S " offen offset:2048 nt lds\n\tbuffer_load_dwordx4 " V ", " R ", " S " offen offset:3072 nt lds\n\t"
__device__ __forceinline__ void issue_two(v4i r, unsigned vv, unsigned so, unsigned lds) {
  unsigned t;
  asm volatile("s_mov_b32 m0, %4\n\ts_add_u32 %0, %3, 0x1000\n\t" DMA4("%2", "%1", "%3") "s_add_u32 m0, m0, 0x1000\n\ts_nop 0\n\t" DMA4("%2", "%1", "%0")
               "s_add_u32 m0, m0, 0x1000\n\ts_add_u32 %0, %0, 0x1000\n\t" DMA4("%2", "%1", "%0") "s_add_u32 m0, m0, 0x1000\n\ts_add_u32 %0, %0, 0x1000\n\t" DMA4("%2", "%1", "%0")
               : "=&s"(t) : "s"(r), "v"(vv), "s"(so), "s"(lds) : "memory", "scc");
}
__device__ __forceinline__ void dma1_nt(v4i r, unsigned voff, unsigned soff, unsigned lds) {
  asm volatile("s_mov_b32 m0, %3\n\ts_nop 0\n\tbuffer_load_dwordx4 %1, %0, %2 offen nt lds" :: "s"(r), "v"(voff), "s"(soff), "s"(lds) : "memory");
}
__device__ __forceinline__ void dma1(v4i r, unsigned voff, unsigned soff, unsigned lds) {
  asm volatile("s_mov_b32 m0, %3\n\ts_nop 0\n\tbuffer_load_dwordx4 %1, %0, %2 offen lds" :: "s"(r), "v"(voff), "s"(soff), "s"(lds) : "memory");
}
template <int W> __device__ __forceinline__ void stream_units(v4i rs, unsigned vv, unsigned u, unsigned ue, unsigned so, unsigned ra, unsigned uc16, int hold, int msp, int lat, int ms2) {
  unsigned ta, tb, st = 8192u, fo = ((u + 1u) & 3u) * 8u + C_FREE;
  asm volatile(
      "s_mov_b32 m0, %[m0i]\n\t"
      "v_mov_b32 v2, %[ld0]\n"
      "LT%=:\n\t"
      "s_waitcnt vmcnt(%[win])\n\t"
      DMA4("%[v0]", "%[rs]", "%[so]")
      "s_add_u32 m0, m0, 0x1000\n\t"
      "s_memrealtime s[40:41]\n\t"
      DMA4("%[v1]", "%[rs]", "%[so]")
      "ds_read_b64 v[4:5], %[fo]\n\t"
      "s_add_u32 m0, m0, 0x1000\n\t"
      "s_add_u32 %[so], %[so], %[st]\n\t"
      "s_and_b32 m0, m0, 0x1ffff\n\t"
      "s_and_b32 %[so], %[so], 0x1fffff\n\t"
      "s_xor_b32 %[st], %[st], 0xc000\n\t"
      "s_add_u32 %[ra], %[ra], %[uc]\n\t"
      "v_add_u32 v2, 1, v2\n\t"
      "v_add_u32 %[fo], 8, %[fo]\n\t"
      "s_add_u32 %[u], %[u], 1\n\t"
      "v_and_b32 %[fo], %[fm], %[fo]\n\t"
      "s_waitcnt lgkmcnt(0)\n\t"
      "v_mov_b32 v3, s40\n\t"
      "v_readfirstlane_b32 %[a], v5\n\t"
      "s_lshr_b32 s41, %[ra], 4\n\t"
      "v_readfirstlane_b32 %[b], v4\n\t"
      "ds_write_b64 %[vl], v[2:3]\n\t"
      "s_cmp_eq_u32 %[u], %[ue]\n\t"
      "s_cbranch_scc1 LE%=\n\t"
      "s_add_u32 %[a], %[a], s41\n\t"
      "s_sub_i32 %[a], %[a], s40\n\t"
      "s_cmp_lt_i32 %[a], %[lat]\n\t"
      "s_cselect_b32 s41, %[ms2], %[msp]\n\t"
      "s_min_i32 %[a], %[a], %[hold]\n\t"
      "s_max_i32 %[a], %[a], s41\n\t"
      "s_add_u32 %[a], %[a], s40\n\t"
      "s_cmp_lt_u32 %[b], %[u]\n\t"
      "s_cbranch_scc1 LS%=\n"
      "LW%=:\n\t"
      "s_memrealtime s[40:41]\n\t"
      "s_waitcnt lgkmcnt(0)\n\t"
      "s_sub_i32 %[b], %[a], s40\n\t"
      "s_cmp_gt_i32 %[b], 0\n\t"
      "s_cbranch_scc0 LT%=\n\t"
      "s_branch LW%=\n"
      "LS%=:\n\t"
      "s_and_b32 %[b], %[u], 3\n\t"
      "s_lshl_b32 %[b], %[b], 3\n\t"
      "s_add_u32 %[b], %[b], %[fb]\n\t"
      "v_mov_b32 v5, %[b]\n"
      "LR%=:\n\t"
      "s_sleep 1\n\t"
      "ds_read_b32 v4, v5\n\t"
      "s_waitcnt lgkmcnt(0)\n\t"
      "v_readfirstlane_b32 %[b], v4\n\t"
      "s_cmp_lt_u32 %[b], %[u]\n\t"
      "s_cbranch_scc1 LR%=\n\t"
      "s_branch LW%=\n"
      "LE%=:"
      : [so] "+&s"(so), [ra] "+&s"(ra), [u] "+&s"(u), [st] "+&s"(st), [fo] "+&v"(fo), [a] "=&s"(ta), [b] "=&s"(tb)
      : [rs] "s"(rs), [v0] "v"(vv), [v1] "v"(vv + 4096u), [vl] "v"((unsigned)C_LAND), [ue] "s"(ue), [uc] "s"(uc16), [hold] "s"(hold), [msp] "s"(msp), [lat] "s"(lat), [ms2] "s"(ms2),
        [m0i] "s"((u & 15u) * SLOT), [ld0] "s"(u - 1u - LAG), [win] "n"(W), [fm] "s"(~32u), [fb] "s"((unsigned)C_FREE)
      : "memory", "scc", "vcc", "v2", "v3", "v4", "v5", "s40", "s41");
}
static_assert((C_FREE & 32) == 0, "the record pointer wraps by clearing bit 5");

__device__ __forceinline__ f32x4 mfma32(v4u a, v4u b, f32x4 c) {
  return __builtin_amdgcn_mfma_f32_16x16x32_bf16(__builtin_bit_cast(bf16x8, a), __builtin_bit_cast(bf16x8, b), c, 0, 0, 0);
}
__device__ __forceinline__ v4u tr_frag(LAS unsigned char* p, int hi) {
  const v2u lo = __builtin_bit_cast(v2u, __builtin_amdgcn_ds_read_tr16_b64_v4i16((LAS s16x4*)p));
  const v2u up = __builtin_bit_cast(v2u, __builtin_amdgcn_ds_read_tr16_b64_v4i16((LAS s16x4*)(p + hi)));
  return v4u{lo[0], lo[1], up[0], up[1]};
}
__device__ __forceinline__ float score16(const v4u* K, v4u qf, unsigned j0) {
  float sc[4];
#pragma unroll
  for (int u = 0; u < 4; ++u) {
    const f32x4 d = mfma32(K[u], qf, f32x4{0.f, 0.f, 0.f, 0.f});
    sc[u] = ((dpp<0x00>(d[0]) + dpp<0x55>(d[1])) + dpp<0xAA>(d[2])) + dpp<0xFF>(d[3]);
  }
  return (j0 & 2) ? ((j0 & 1) ? sc[3] : sc[2]) : ((j0 & 1) ? sc[1] : sc[0]);
}
__device__ __forceinline__ void p_operands(unsigned k0, unsigned k1, unsigned mask0, unsigned mask1, v4u (&bo)[4]) {
  bo[0] = v4u{dppu<0x00>(k0) & mask0, dppu<0x00>(k0) & mask1, dppu<0x55>(k0) & mask0, dppu<0x55>(k0) & mask1};
  bo[1] = v4u{dppu<0xAA>(k0) & mask0, dppu<0xAA>(k0) & mask1, dppu<0xFF>(k0) & mask0, dppu<0xFF>(k0) & mask1};
  bo[2] = v4u{dppu<0x00>(k1) & mask0, dppu<0x00>(k1) & mask1, dppu<0x55>(k1) & mask0, dppu<0x55>(k1) & mask1};
  bo[3] = v4u{dppu<0xAA>(k1) & mask0, dppu<0xAA>(k1) & mask1, dppu<0xFF>(k1) & mask0, dppu<0xFF>(k1) & mask1};
  asm volatile("" : "+v"(bo[0]), "+v"(bo[1]), "+v"(bo[2]), "+v"(bo[3]));
}
#define RD2(A, P, O0, O1) "ds_read2st64_b64 %[" A "], %[" P "] offset0:" O0 " offset1:" O1 "\n\t"
#define MF(N, C, A, B) "s_waitcnt lgkmcnt(" N ")\n\tv_mfma_f32_16x16x32_bf16 %[" C "], %[" A "], %[" B "], %[" C "]\n\t"
__device__ __forceinline__ void pv32(unsigned p, const v4u (&bo)[4], f32x4& acc0, f32x4& acc1) {
  v4u a0, a1, a2, a3, a4, a5, a6, a7;
  asm volatile(RD2("a0", "p0", "0", "2") RD2("a1", "p1", "0", "2") RD2("a2", "p0", "4", "6") RD2("a3", "p1", "4", "6")
               RD2("a4", "p0", "8", "10") RD2("a5", "p1", "8", "10") RD2("a6", "p0", "12", "14") RD2("a7", "p1", "12", "14")
               MF("7", "c0", "a0", "b0") MF("6", "c1", "a1", "b0") MF("5", "c0", "a2", "b1") MF("4", "c1", "a3", "b1")
               MF("3", "c0", "a4", "b2") MF("2", "c1", "a5", "b2") MF("1", "c0", "a6", "b3") MF("0", "c1", "a7", "b3") "s_nop 7"
      : [c0] "+&v"(acc0), [c1] "+&v"(acc1), [a0] "=&v"(a0), [a1] "=&v"(a1), [a2] "=&v"(a2), [a3] "=&v"(a3), [a4] "=&v"(a4), [a5] "=&v"(a5), [a6] "=&v"(a6), [a7] "=&v"(a7)
      : [p0] "v"(p), [p1] "v"(p + 8), [b0] "v"(bo[0]), [b1] "v"(bo[1]), [b2] "v"(bo[2]), [b3] "v"(bo[3]) : "memory");
}
__device__ __forceinline__ v4u tag_bytes(unsigned tag) { return v4u{tag & 0xffu, (tag >> 8) & 0xffu, (tag >> 16) & 0xffu, tag >> 24}; }
__device__ __forceinline__ v4u tag4(f32x4 v, v4u tb) { return (__builtin_bit_cast(v4u, v) & 0xffffff00u) | tb; }
__device__ __forceinline__ unsigned next_tag(unsigned e) { return ((e & 0x7f7f7f7fu) + 0x51276b35u) ^ (e & 0x80808080u); }
__device__ __forceinline__ unsigned or4(v4u u) { return (u[0] | u[1]) | (u[2] | u[3]); }

__global__ void __launch_bounds__(NTHR, 2) gqa_fused(const unsigned char* __restrict__ x, const unsigned char* __restrict__ kc, const unsigned char* __restrict__ vc,
                                                     const unsigned char* __restrict__ wq, const unsigned char* __restrict__ wo, unsigned char* __restrict__ out,
                                                     unsigned char* ws) {
  __shared__ __attribute__((aligned(16))) unsigned char smem[L_TOT];
  LAS unsigned char* L = (LAS unsigned char*)smem;
  unsigned long long t64;
  unsigned pf, pa;
  {
    unsigned long long pc, ex;
    unsigned clen;
    asm volatile("s_memrealtime %0\n\ts_getpc_b64 %1\nLgqa_pc:\n\t"
                 "s_mov_b32 %2, Lgqa_end_a-Lgqa_pc+160\n\ts_max_u32 %2, %2, Lgqa_end_b-Lgqa_pc+160\n\ts_max_u32 %2, %2, Lgqa_end_c-Lgqa_pc+160\n\t"
                 "s_max_u32 %2, %2, Lgqa_end_d-Lgqa_pc+160\n\ts_max_u32 %2, %2, Lgqa_end_e-Lgqa_pc+160\n\t"
                 "v_lshlrev_b32 %5, 7, %6\n\tv_add_u32 %5, 0xffffc000, %5\n\tv_cmp_gt_u32 vcc, %2, %5\n\t"
                 "s_and_saveexec_b64 %3, vcc\n\ts_cbranch_execz Lgqa_nopf\n\tglobal_load_dword %4, %5, %1 offset:-32\nLgqa_nopf:\n\ts_mov_b64 exec, %3"
                 : "=&s"(t64), "=&s"(pc), "=&s"(clen), "=&s"(ex), "=&v"(pf), "=&v"(pa) : "v"(threadIdx.x) : "scc", "vcc", "memory");
  }
  const int lane = threadIdx.x & 63, wv = __builtin_amdgcn_readfirstlane(threadIdx.x >> 6);
  const int j = blockIdx.x, xg = j & 7, idx = j >> 3;
  const int pr = (idx >> 2) * 8 + ((xg + (idx >> 2)) & 7), s = idx & 3;
  const int ch = 4 * xg + (idx >> 3), nb = idx & 7;
  const int g = lane >> 4;
  const unsigned so0 = (unsigned)(pr >> 1) * 65536u + s * 16384u;
  const unsigned vv = lane * 16;
  const unsigned ra0 = ((unsigned)(j * 37) & 31u) * PHASE >> 1;
  asm volatile("s_waitcnt lgkmcnt(0)" : "+s"(t64) : "s"(x), "s"(kc), "s"(vc), "s"(wq), "s"(wo), "s"(out), "s"(ws), "s"(so0), "v"(vv));
  const unsigned t0 = (unsigned)t64;
  const size_t pbase = (size_t)pr * (SK * DH * 2);

  const v4i rk = mkrsrc(kc + pbase, SK * DH * 2);
  if (wv == 0) {
    __builtin_amdgcn_s_setprio(3);
#pragma unroll
    for (int u = 0; u < NFIRST; u += 2) issue_two(rk, vv, (so0 + (u >> 1) * 65536u) & 0x1fffffu, u * SLOT);
    __builtin_amdgcn_sched_barrier(0);
    const unsigned keep = (unsigned)((lane & 7) - 4) >> 31;
    *(LAS v2u*)(L + L_CTL + (lane & 7) * 8) = v2u{(12u + (lane & 7)) * keep, (t0 + LINE0) * keep};
    *(LAS unsigned*)(L + L_CTL - 16) = now();
    asm volatile("s_waitcnt lgkmcnt(0)" ::: "memory");
  }
  unsigned e0, e1, e2;
  if (wv == 1) asm volatile("s_load_dword %0, %3, 0x0\n\ts_load_dword %1, %4, 0x0\n\ts_load_dword %2, %5, %6"
                            : "=&s"(e0), "=&s"(e1), "=&s"(e2) : "s"(ws), "s"(x), "s"(wq), "s"((j >> 6) * (128 * 8192) + (j & 63) * 128) : "memory");
  asm volatile("s_barrier" ::: "memory");
  if (wv == 0) stream_units<WIN0>(rk, vv, NFIRST, UW0, (so0 + (NFIRST >> 1) * 65536u) & 0x1fffffu, NFIRST * UC16 + ra0, UC16, HOLD, MINSP, -LATE, MINSP2);
  const __amdgpu_buffer_rsrc_t rw = __builtin_amdgcn_make_buffer_rsrc((void*)ws, 0, W_END, 0x00020000);
  const unsigned j0 = lane & 3;
  const int h = (lane >> 2) & 3;
  const unsigned mask0 = j0 == 0 ? 0x0000ffffu : (j0 == 1 ? 0xffff0000u : 0u);
  const unsigned mask1 = j0 == 2 ? 0x0000ffffu : (j0 == 3 ? 0xffff0000u : 0u);

  if (wv == 0) {
    const v4i rv = mkrsrc(vc + pbase, SK * DH * 2);
#pragma unroll 1
    for (int pass = 0; pass < 2; ++pass) {
      const unsigned ub = pass ? 64u : (unsigned)UW0;
      while ((unsigned)lds_word(L + C_FREE + (ub & 3u) * 8) < ub) __builtin_amdgcn_s_sleep(1);
      const v4i rs = {pass ? rv[0] : rk[0], pass ? rv[1] : rk[1], rk[2], rk[3]};
      stream_units<WIN>(rs, vv, ub, pass ? (unsigned)UPREP : 64u, (so0 + (ub >> 1) * 65536u) & 0x1fffffu, ub * UC16 + ra0, UC16, HOLD, MINSP, -LATE, MINSP2);
    }
    while (lds_word(L + C_FREE + 24) < 64 + 4 * MLROW + 3 + 16) __builtin_amdgcn_s_sleep(1);
    v4u bo[NFIN][4], tb;
    unsigned vo = h * 512 + g * 64 + j0 * 16;
    float scale;
    {
      v2u pl[NFIN];
#pragma unroll
      for (int k = 0; k < NFIN; ++k) pl[k] = *(LAS v2u*)(L + L_PL + (4 - NFIN + k) * 512 + lane * 8);
      tb = tag_bytes(*(LAS unsigned*)(L + C_TAG));
      scale = *(LAS float*)(L + L_ML + 128 + h * 4);
      const int fr = *(LAS int*)(L + C_FREE + (UPREP & 3) * 8);
#pragma unroll
      for (int k = 0; k < NFIN; ++k) p_operands(pl[k][0], pl[k][1], mask0, mask1, bo[k]);
      asm volatile("" : "+v"(vo), "+v"(tb), "+v"(scale));
      if (__builtin_amdgcn_readfirstlane(fr) < UPREP) { while (lds_word(L + C_FREE + (UPREP & 3) * 8) < UPREP) __builtin_amdgcn_s_sleep(1); }
    }
    stream_units<WIN>(rv, vv, UPREP, NUNIT, (so0 + ((UPREP - 64) >> 1) * 65536u) & 0x1fffffu, UPREP * UC16 + ra0, UC16, HOLD, MINSP, -LATE, MINSP2);
    const int so = W_PA + (pr * 4 + s) * PA_REC;
    f32x4 acc0 = {0.f, 0.f, 0.f, 0.f}, acc1 = {0.f, 0.f, 0.f, 0.f};
    static_assert(NFIN >= 2 && LAG <= 3, "the drain below starts at three units outstanding");
    f32x4 s0, s1;
#pragma unroll
    for (int k = 0; k < 3; ++k) {
      if (k == 0) WAITVM(24); else if (k == 1) WAITVM(16); else WAITVM(8);
      if (k < 2) *(LAS int*)(L + C_LAND) = NUNIT - 3 + k;
      if (k >= 4 - NFIN) pv32((12 + k) * SLOT + vv, bo[k >= 4 - NFIN ? k - (4 - NFIN) : 0], acc0, acc1);
      if (k == MPOS) {
        while (lds_word(L + C_NA) < 4) {}
        f32x4 a[8];
#pragma unroll
        for (int u = 0; u < 8; ++u) a[u] = *(LAS f32x4*)(L + L_ACC + vv + u * 1024);
        __builtin_amdgcn_sched_barrier(0);
        s0 = ((a[0] + a[2]) + (a[4] + a[6])) * scale; s1 = ((a[1] + a[3]) + (a[5] + a[7])) * scale;
        asm volatile("" : "+v"(s0), "+v"(s1) : "v"(14 * SLOT + vv), "v"(14 * SLOT + vv + 8), "v"(15 * SLOT + vv), "v"(15 * SLOT + vv + 8), "s"(so), "s"(0xffffff00u));
      }
    }
    WAITVM(0);
    pv32(15 * SLOT + vv, bo[NFIN - 1], acc0, acc1);
    const v4u o0 = tag4(acc0 * scale + s0, tb), o1 = tag4(acc1 * scale + s1, tb);
    __builtin_amdgcn_raw_buffer_store_b128(o0, rw, vo, so, AUX_SC1);
    __builtin_amdgcn_raw_buffer_store_b128(o1, rw, vo + 256, so, AUX_SC1);
    asm volatile("s_nop 1" :: "v"(o0), "v"(o1) : "memory");
    *(LAS int*)(L + C_LAND) = NUNIT;
    asm volatile("Lgqa_end_a:" ::: "memory");
    return;
  }

  if (wv >= 2) {
    const int w = wv - 2;
    const unsigned vk = (h * 16 + g * 4 + j0) * 16;
    asm volatile("s_waitcnt vmcnt(0)" :: "v"(pf), "v"(pa) : "memory");
    f32x32 sc;
    float m;
    {
      v4u P[NREG][8];
#pragma unroll
      for (int r = 0; r < NREG; ++r) {
        while (lds_word(L + C_LAND) <= 4 * r + w) __builtin_amdgcn_s_sleep(2);
        if (w == 0 && r == 0) *(LAS unsigned*)(L + L_CTL - 12) = now();
        if (w == 0 && r == 2) *(LAS unsigned*)(L + L_CTL - 8) = now();
        if (w == 0 && r == 4) *(LAS unsigned*)(L + L_CTL - 4) = now();
#pragma unroll
        for (int u = 0; u < 8; ++u) P[r][u] = *(LAS v4u*)(L + ((4 * r + w) & 15) * SLOT + vk + u * 1024);
        asm volatile("" :: "v"(P[r][0]), "v"(P[r][1]), "v"(P[r][2]), "v"(P[r][3]), "v"(P[r][4]), "v"(P[r][5]), "v"(P[r][6]), "v"(P[r][7]) : "memory");
        *(LAS int*)(L + C_FREE + w * 8) = 4 * r + w + 16;
      }
      while (lds_word(L + C_READY) == 0) __builtin_amdgcn_s_sleep(2);
      const v4u qf = *(LAS v4u*)(L + L_Q + vk);
      m = -INFINITY;
#pragma unroll
      for (int r = 0; r < NREG; ++r) {
        const float s0 = score16(&P[r][0], qf, j0), s1 = score16(&P[r][4], qf, j0);
        sc[2 * r] = s0; sc[2 * r + 1] = s1; m = fmaxf(m, fmaxf(s0, s1));
      }
#pragma unroll 1
      for (int n = NREG; n < 16; ++n) {
        while (lds_word(L + C_LAND) <= 4 * n + w) __builtin_amdgcn_s_sleep(2);
        v4u K[8];
#pragma unroll
        for (int u = 0; u < 8; ++u) K[u] = *(LAS v4u*)(L + ((4 * n + w) & 15) * SLOT + vk + u * 1024);
        asm volatile("" :: "v"(K[0]), "v"(K[1]), "v"(K[2]), "v"(K[3]), "v"(K[4]), "v"(K[5]), "v"(K[6]), "v"(K[7]) : "memory");
        *(LAS int*)(L + C_FREE + w * 8) = 4 * n + w + 16;
        const float s0 = score16(&K[0], qf, j0), s1 = score16(&K[4], qf, j0);
        sc[2 * n] = s0; sc[2 * n + 1] = s1; m = fmaxf(m, fmaxf(s0, s1));
      }
    }
    const v4u tb = tag_bytes((unsigned)lds_word(L + C_TAG));
    m = fmaxf(m, dpp<0xB1>(m)); m = fmaxf(m, dpp<0x4E>(m)); m = xrow_max(m);
    *(LAS float*)(L + L_ML + h * 16 + w * 4) = m;
    lds_inc(L + C_NM);
    while (lds_word(L + C_NM) < 4) __builtin_amdgcn_s_sleep(1);
    {
      const f32x4 mw = *(LAS f32x4*)(L + L_ML + h * 16);
      m = fmaxf(fmaxf(mw[0], mw[1]), fmaxf(mw[2], mw[3]));
      float l = 0.f;
#pragma unroll
      for (int n = 0; n < 32; ++n) { const float p = __builtin_amdgcn_exp2f((sc[n] - m) * C2); l += p; sc[n] = __uint_as_float(cvtpk(p, p)); }
      l += dpp<0xB1>(l); l += dpp<0x4E>(l); l = xrow_sum(l);
      *(LAS float*)(L + L_ML + 64 + h * 16 + w * 4) = l;
      *(LAS v2u*)(L + L_PL + w * 512 + lane * 8) = v2u{__float_as_uint(sc[30]), __float_as_uint(sc[31])};
      lds_inc(L + C_NL);
    }
    f32x4 acc0 = {0.f, 0.f, 0.f, 0.f}, acc1 = {0.f, 0.f, 0.f, 0.f};
#pragma unroll 1
    for (int n = 0; n < (w < 4 - NFIN ? 16 : 15); ++n) {
      if (w == 3 && n == 1) {
        while (lds_word(L + C_NL) < 4) __builtin_amdgcn_s_sleep(1);
        const f32x4 lw = *(LAS f32x4*)(L + L_ML + 64 + h * 16);
        if (lane < 16 && j0 == 0) __builtin_amdgcn_raw_buffer_store_b128(tag4(f32x4{m, (lw[0] + lw[1]) + (lw[2] + lw[3]), __uint_as_float(__float_as_uint(m) << 24), 0.f}, tb), rw, h * 16, W_ML + (pr * 4 + s) * 128, AUX_SC1);
      }
      if (w == 3 && n == MLROW) {
        v4u ml;
        for (unsigned spin = 0; spin < SPIN_CAP; ++spin) {
          RELOAD();
          ml = __builtin_amdgcn_raw_buffer_load_b128(rw, j0 * 128 + h * 16, W_ML + pr * 512, AUX_SC1) ^ tb;
          if (__all((or4(ml) & 0xffu) == 0u)) break;
        }
        const float ms = __uint_as_float(ml[0] | (ml[2] >> 24));
        float M = fmaxf(ms, dpp<0xB1>(ms)); M = fmaxf(M, dpp<0x4E>(M));
        float ls = __uint_as_float(ml[1]) * __builtin_amdgcn_exp2f((ms - M) * C2);
        ls += dpp<0xB1>(ls); ls += dpp<0x4E>(ls);
        if (lane < 16 && j0 == 0) *(LAS float*)(L + L_ML + 128 + h * 4) = __builtin_amdgcn_exp2f((m - M) * C2) * __builtin_amdgcn_rcpf(ls);
      }
      v4u bo[4];
      p_operands(__float_as_uint(sc[2 * n]), __float_as_uint(sc[2 * n + 1]), mask0, mask1, bo);
      while (lds_word(L + C_LAND) <= 64 + 4 * n + w) __builtin_amdgcn_s_sleep(1);
      pv32(((4 * n + w) & 15) * SLOT + vv, bo, acc0, acc1);
      *(LAS int*)(L + C_FREE + w * 8) = 64 + 4 * n + w + 16;
    }
    *(LAS f32x4*)(L + L_ACC + w * 2048 + vv) = acc0; *(LAS f32x4*)(L + L_ACC + w * 2048 + 1024 + vv) = acc1;
    lds_inc(L + C_NA);
    const int bb = 2 * w + (lane >> 5), c = lane & 31;
    unsigned vg = bb * (HKV * 4 * PA_REC) + c * 16, vy = (lane & 7) * (32 * 2048) + g * 16;
    const int sg = W_PA + (ch >> 2) * (4 * PA_REC) + (ch & 3) * 512, sy = W_YB + (nb * 4 + w) * 2048 + ch * 64;
    LAS unsigned char* ap = L + L_W + (8 * g + ((lane & 15) >> 2)) * 128 + (16 * w + 4 * j0) * 2;
    LAS unsigned char* op = L + L_OT + bb * OT_ROW + c * 8;
    LAS unsigned char* bp = L + L_OT + (lane & 7) * OT_ROW + g * 16;
    if (lane & 8) vy = 0x40000000u;
    v4u tv = tb;
    asm volatile("" : "+v"(vg), "+v"(vy), "+v"(ap), "+v"(op), "+v"(bp), "+v"(tv) : "s"(sg), "s"(sy));
    while (lds_word(L + C_LAND) < GPOS) __builtin_amdgcn_s_sleep(1);
    {
      const unsigned tp = now() + D1;
      unsigned tg = (unsigned)lds_word(L + C_FREE + 4) + (NUNIT * UC16 >> 4) + D1G;
      tg = tp + (unsigned)min(max((int)(tg - tp), 0), 200);
      while ((int)(now() - tg) < 0) {}
    }
    v4u a[4], af[4], bf[4];
#pragma unroll
    for (int u = 0; u < 4; ++u) a[u] = __builtin_amdgcn_raw_buffer_load_b128(rw, vg, sg + u * PA_REC, AUX_SC1);
    while (lds_word(L + C_WO) == 0) {}
#pragma unroll
    for (int ks = 0; ks < 4; ++ks) af[ks] = tr_frag(ap + ks * 4096, 512);
    asm volatile("" : "+v"(af[0]), "+v"(af[1]), "+v"(af[2]), "+v"(af[3]));
#pragma unroll
    for (int u = 0; u < 4; ++u) a[u] ^= tv;
    asm volatile("" : "+v"(a[0]), "+v"(a[1]), "+v"(a[2]), "+v"(a[3]));
    if (__builtin_expect(!__all((or4((a[0] | a[1]) | (a[2] | a[3])) & 0xffu) == 0u), 0)) {
      for (unsigned spin = 0; spin < SPIN_CAP; ++spin) {
        RELOAD();
#pragma unroll
        for (int u = 0; u < 4; ++u) a[u] = __builtin_amdgcn_raw_buffer_load_b128(rw, vg, sg + u * PA_REC, AUX_SC1) ^ tv;
        if (__all((or4((a[0] | a[1]) | (a[2] | a[3])) & 0xffu) == 0u)) break;
      }
      asm volatile("Lgqa_end_d:" ::: "memory");
    }
    const f32x4 o = (__builtin_bit_cast(f32x4, a[0]) + __builtin_bit_cast(f32x4, a[1])) + (__builtin_bit_cast(f32x4, a[2]) + __builtin_bit_cast(f32x4, a[3]));
    *(LAS v2u*)op = v2u{cvtpk(o[0], o[1]), cvtpk(o[2], o[3])};
    BARRIER();
#pragma unroll
    for (int ks = 0; ks < 4; ++ks) bf[ks] = *(LAS v4u*)(bp + ks * 64);
    __builtin_amdgcn_sched_barrier(0);
    f32x4 y = {0.f, 0.f, 0.f, 0.f};
#pragma unroll
    for (int ks = 0; ks < 4; ++ks) y = mfma32(af[ks], bf[ks], y);
    __builtin_amdgcn_raw_buffer_store_b128(tag4(y, tv), rw, vy, sy, AUX_SC1);
    asm volatile("Lgqa_end_b:" ::: "memory");
    return;
  }

  __builtin_amdgcn_s_setprio(2);
  unsigned tag;
  {
    constexpr int NKS = 4 / NCH;
    const bool prod = (idx & (NCH - 1)) == NCH - 1;
    const int pid = (idx / NCH) * 8 + xg, cb = pid & 63, kq = pid >> 6;
    v4u tq;
    if (prod) {
      const v4i rx = mkrsrc(x, NB * DM * 2), rq = mkrsrc(wq, DM * HQ * DH * 2);
      const unsigned vx = (lane & 7) * 1024 + (lane >> 3) * 16, vq = (lane >> 3) * 8192 + (lane & 7) * 16;
      const bool hi = (lane & 8) != 0;
      const int vs = (lane & 7) * (HKV * NKS * 2048) + (hi ? 64 : 0) + g * 16, ss = W_QG + (cb >> 3) * (NKS * 2048) + kq * 2048 + (cb & 7) * 256;
      f32x4 qa[4];
#pragma unroll
      for (int t = 0; t < 4; ++t) qa[t] = f32x4{0.f, 0.f, 0.f, 0.f};
      LAS unsigned char* ap = L + L_W + (8 * g + ((lane & 15) >> 2)) * 128 + j0 * 8;
      LAS unsigned char* bp = L + L_X + g * 128 + (lane & 7) * 16;
#pragma unroll 1
      for (int c = kq * NCH; c < kq * NCH + NCH; ++c) {
        dma1(rx, vx, c * 256, L_X);
        dma1(rx, vx, c * 256 + 128, L_X + 1024);
#pragma unroll 1
        for (int u = 0; u < 16; ++u) dma1_nt(rq, vq, (c * 16 + u) * 65536 + cb * 128, L_W + u * 1024);
        WAITVM(0);
#pragma unroll
        for (int ks = 0; ks < 4; ++ks) {
          const v4u xb = *(LAS v4u*)(bp + ks * 512);
#pragma unroll
          for (int t = 0; t < 4; ++t) qa[t] = mfma32(tr_frag(ap + ks * 4096 + t * 32, 512), xb, qa[t]);
        }
        asm volatile("" : "+v"(qa[0]), "+v"(qa[1]), "+v"(qa[2]), "+v"(qa[3]) :: "memory");
      }
      asm volatile("s_waitcnt lgkmcnt(0)" : "+s"(e0) :: "memory");
      tag = next_tag(e0);
      tq = tag_bytes(tag);
      const v4u o0 = tag4(f32x4{hi ? qa[1][0] : qa[0][0], hi ? qa[1][1] : qa[0][1], hi ? qa[1][2] : qa[0][2], hi ? qa[1][3] : qa[0][3]}, tq);
      const v4u o1 = tag4(f32x4{hi ? qa[3][0] : qa[2][0], hi ? qa[3][1] : qa[2][1], hi ? qa[3][2] : qa[2][2], hi ? qa[3][3] : qa[2][3]}, tq);
      __builtin_amdgcn_raw_buffer_store_b128(o0, rw, vs, ss, AUX_SC1);
      __builtin_amdgcn_raw_buffer_store_b128(o1, rw, vs + 128, ss, AUX_SC1);
      asm volatile("s_nop 1" :: "v"(o0), "v"(o1) : "memory");
    }
    asm volatile("s_waitcnt lgkmcnt(0)" : "+s"(e0) : "s"(e1), "s"(e2) : "memory");
    tag = next_tag(e0);
    tq = tag_bytes(tag);
    {
      unsigned f0, f1;
      asm volatile("s_load_dword %0, %2, %3\n\ts_load_dword %1, %4, %5\n\ts_waitcnt lgkmcnt(0)" : "=&s"(f0), "=&s"(f1) : "s"(vc + pbase), "s"(so0), "s"(wo), "s"(ch * 128 * 1024 + nb * 128) : "memory");
    }
    while ((int)(now() - t0) < QPOLL) __builtin_amdgcn_s_sleep(2);
    const int so = W_QG + pr * (NKS * 2048);
    v4u a[2 * NKS];
    for (unsigned spin = 0; spin < SPIN_CAP; ++spin) {
      RELOAD();
      v4u bad = {0u, 0u, 0u, 0u};
#pragma unroll
      for (int u = 0; u < 2 * NKS; ++u) { a[u] = __builtin_amdgcn_raw_buffer_load_b128(rw, vv + (u & 3) * 1024, so + (u >> 2) * 4096, AUX_SC1) ^ tq; bad |= a[u]; }
      if (__all((or4(bad) & 0xffu) == 0u)) break;
      __builtin_amdgcn_s_sleep(12);
    }
#pragma unroll
    for (int u = 0; u < 2; ++u) {
      f32x4 v = __builtin_bit_cast(f32x4, a[u]);
#pragma unroll
      for (int p = 1; p < NKS; ++p) v += __builtin_bit_cast(f32x4, a[2 * p + u]);
      *(LAS v2u*)(L + L_Q + u * 512 + lane * 8) = v2u{cvtpk(v[0], v[1]), cvtpk(v[2], v[3])};
    }
    RELOAD();
    *(LAS v2u*)(L + C_READY) = v2u{1u, tag};
  }
  {
    unsigned k = now() / FB_P + 1u;
    float af = 0.f, dr = 0.f;
    int nv = 0;
#pragma unroll 1
    for (int it = 0; it < 40; ++it, ++k) {
      const unsigned tk = k * FB_P;
      while ((int)(now() - tk) < 0) __builtin_amdgcn_s_sleep(2);
      RELOAD();
      const v2u pr2 = *(LAS v2u*)(L + C_LAND);
      const int landed = __builtin_amdgcn_readfirstlane(pr2[0]);
      if (landed >= FBEND) break;
      const v2u fb = __builtin_amdgcn_raw_buffer_load_b64(rw, (lane & (FB_NREP - 1)) * 8, W_FB + ((k - 1u) & 63u) * FB_SLOT, AUX_SC1);
      if (j < FB_NREP && lane == 0) {
        const unsigned a = pr2[1] - (((pr2[0] + LAG) * UC16 + ra0) >> 4) + (unsigned)max((int)(tk - pr2[1]) - (int)(UC16 >> 4), 0);
        __builtin_amdgcn_raw_buffer_store_b64(v2u{a, tag ^ k}, rw, 0, W_FB + (k & 63u) * FB_SLOT + j * 8, AUX_SC1);
      }
      const bool ok = fb[1] == (tag ^ (k - 1u));
      float ps = ok ? (float)(int)(fb[0] - t0) : 0.f, pc = ok ? 1.f : 0.f;
      ps += dpp<0xB1>(ps); ps += dpp<0x4E>(ps); ps += dpp<0x124>(ps); ps += dpp<0x128>(ps); ps = xrow_sum(ps);
      pc += dpp<0xB1>(pc); pc += dpp<0x4E>(pc); pc += dpp<0x124>(pc); pc += dpp<0x128>(pc); pc = xrow_sum(pc);
      if (__builtin_amdgcn_readfirstlane(pc < 48.f)) continue;
      const float as = ps * __builtin_amdgcn_rcpf(pc) - (float)(landed < DSW ? DELTA : DELTA2);
      const float er = as - af;
      if (nv >= FB_I0) dr = fminf(fmaxf(dr + FB_KI * fminf(er - FB_Z, 30.f), 0.f), 16.f);
      af = nv ? af + dr + FB_G * er : as;
      ++nv;
      *(LAS unsigned*)(L + C_FREE + 4 + j0 * 8) = t0 + (unsigned)(int)af;
    }
    if (nv) *(LAS unsigned*)(L + C_FREE + 4 + j0 * 8) = t0 + (unsigned)(int)(af + FB_FIN * fmaxf(dr, 0.f));
  }
  {
    const v4i ro = mkrsrc(wo, HQ * DH * DM * 2);
    const unsigned vo = ((lane >> 5) + 8 * ((lane >> 3) & 3)) * 1024 + (lane & 7) * 16;
    while (lds_word(L + C_LAND) < WOPOS) __builtin_amdgcn_s_sleep(1);
#pragma unroll 1
    for (int u = 0; u < 16; ++u) dma1_nt(ro, vo, (ch * 128 + 32 * ((u >> 1) & 3) + 4 * (u >> 3) + 2 * (u & 1)) * 1024 + nb * 128, L_W + u * 1024);
    const __amdgpu_buffer_rsrc_t rout = __builtin_amdgcn_make_buffer_rsrc((void*)out, 0, NB * DM * 2, 0x00020000);
    v4u tb = tag_bytes(tag);
    unsigned vs = (lane & 28) == 0 ? j0 * 8 + (lane >> 5) * 4 : 0x40000000u;
    asm volatile("s_waitcnt vmcnt(0)" : "+v"(vs), "+v"(tb) :: "memory");
    *(LAS int*)(L + C_WO) = 1;
    BARRIER();
    const unsigned tp = now() + D2;
    while ((int)(now() - tp) < 0) {}
    v4u a0 = __builtin_amdgcn_raw_buffer_load_b128(rw, vv, W_YB + j * 2048, AUX_SC1) ^ tb;
    v4u a1 = __builtin_amdgcn_raw_buffer_load_b128(rw, vv + 1024, W_YB + j * 2048, AUX_SC1) ^ tb;
    asm volatile("" : "+v"(a0), "+v"(a1));
    if (__builtin_expect(!__all((or4(a0 | a1) & 0xffu) == 0u), 0)) {
      for (unsigned spin = 0; spin < SPIN_CAP; ++spin) {
        RELOAD();
        a0 = __builtin_amdgcn_raw_buffer_load_b128(rw, vv, W_YB + j * 2048, AUX_SC1) ^ tb;
        a1 = __builtin_amdgcn_raw_buffer_load_b128(rw, vv + 1024, W_YB + j * 2048, AUX_SC1) ^ tb;
        if (__all((or4(a0 | a1) & 0xffu) == 0u)) break;
      }
      asm volatile("Lgqa_end_e:" ::: "memory");
    }
    const f32x4 y = __builtin_bit_cast(f32x4, a0) + __builtin_bit_cast(f32x4, a1);
    const auto u02 = __builtin_amdgcn_permlane32_swap(__float_as_uint(y[0]), __float_as_uint(y[2]), false, false);
    const auto u13 = __builtin_amdgcn_permlane32_swap(__float_as_uint(y[1]), __float_as_uint(y[3]), false, false);
    float e = __uint_as_float(u02[0]) + __uint_as_float(u02[1]), f = __uint_as_float(u13[0]) + __uint_as_float(u13[1]);
    e += dpp<0x124>(e); f += dpp<0x124>(f); e += dpp<0x128>(e); f += dpp<0x128>(f);
    const auto ue = __builtin_amdgcn_permlane16_swap(__float_as_uint(e), __float_as_uint(e), false, false);
    const auto uf = __builtin_amdgcn_permlane16_swap(__float_as_uint(f), __float_as_uint(f), false, false);
    __builtin_amdgcn_raw_buffer_store_b32(cvtpk(__uint_as_float(ue[0]) + __uint_as_float(ue[1]), __uint_as_float(uf[0]) + __uint_as_float(uf[1])), rout, vs, j * 32, 0);
  }
  if (j == 0 && lane == 0) __builtin_amdgcn_raw_buffer_store_b32(tag, rw, 0, W_E, AUX_SC1);
  asm volatile("Lgqa_end_c:" ::: "memory");
  const int tot = (int)(now() - t0);
  { const unsigned v = DG == 0 ? t0 : (unsigned)lds_word(L + L_CTL - 20 + 4 * DG);
    if (lane == 0) __hip_atomic_store((GAS unsigned long long*)(ws + (3 << 20)) + j, ((unsigned long long)v << 32) | t0, __ATOMIC_RELAXED, __HIP_MEMORY_SCOPE_AGENT); }
  if (j == 0) {
    while ((int)(now() - t0) < 7000) __builtin_amdgcn_s_sleep(4);
    int lo = 1 << 30, hi = -(1 << 30), sm = 0;
    for (int z = 0; z < 4; ++z) { const unsigned long long a = __hip_atomic_load((GAS unsigned long long*)(ws + (3 << 20)) + z * 64 + lane, __ATOMIC_RELAXED, __HIP_MEMORY_SCOPE_AGENT);
      lo = min(lo, (int)((unsigned)a - t0)); const int d = (int)((unsigned)(a >> 32) - t0); hi = max(hi, d); sm += d; }
    for (int z = 1; z < 64; z <<= 1) { lo = min(lo, __shfl_xor(lo, z)); hi = max(hi, __shfl_xor(hi, z)); sm += __shfl_xor(sm, z); }
    const int dgx = MEAN == 2 ? 0 : (MEAN ? sm / 256 : hi) - lo;
    const int q = min(max((tot - lo - 3700) / 25, 0), 40);
    const int tg = lo + 10000 + q * 10000 + 10 * min(max(__builtin_amdgcn_readfirstlane(dgx), 0), 850);
    while ((int)(now() - t0) < __builtin_amdgcn_readfirstlane(tg)) __builtin_amdgcn_s_sleep(4);
  }
}

extern "C" void kernel_launch(void* const* d_in, const int* in_sizes, int n_in, void* d_out, int out_size, void* d_ws, size_t ws_size,
                              hipStream_t stream) {
  gqa_fused<<<NWG, NTHR, 0, stream>>>((const unsigned char*)d_in[0], (const unsigned char*)d_in[1], (const unsigned char*)d_in[2], (const unsigned char*)d_in[3],
                                      (const unsigned char*)d_in[4], (unsigned char*)d_out, (unsigned char*)d_ws);
}
```

```cpp
#include <hip/hip_runtime.h>
#include <hip/hip_bf16.h>
#include <stdint.h>

#define GAS __attribute__((address_space(1)))
#define LAS __attribute__((address_space(3)))
typedef unsigned v4u __attribute__((ext_vector_type(4)));
typedef unsigned v2u __attribute__((ext_vector_type(2)));
typedef int v4i __attribute__((ext_vector_type(4)));
typedef float f32x4 __attribute__((ext_vector_type(4)));
typedef float f32x32 __attribute__((ext_vector_type(32)));
typedef short s16x4 __attribute__((ext_vector_type(4)));
typedef __bf16 bf16x8 __attribute__((ext_vector_type(8)));

constexpr int NB = 8, DM = 512, HQ = 32, HKV = 8, SK = 8192, DH = 128;
constexpr int NWG = 256, NTHR = 384;
constexpr int NUNIT = 128;
constexpr int SLOT = 8192;
constexpr float C2 = 0.088388347648318440f * 1.4426950408889634f;

constexpr int NREG = 5;
constexpr int NFIRST = 4;
constexpr int NCH = 2;
constexpr int MLROW = 8;
constexpr int UPREP = 112;
constexpr int NFIN = 3;
constexpr int MPOS = 1;
constexpr int WIN0 = 16, UW0 = 32, WIN = 24;
constexpr unsigned UC16 = (unsigned)(3.39f * 128.f);
constexpr int LINE0 = 65;
constexpr unsigned PHASE = 28;
constexpr int HOLD = 30, MINSP = 20;
constexpr int LATE = 40, MINSP2 = 16;
constexpr int FB_NREP = 32, FBEND = 110;
constexpr unsigned FB_P = 192u;
constexpr int DELTA = 14, DELTA2 = 4, DSW = 81;
constexpr float FB_G = 0.5f, FB_KI = 0.15f, FB_Z = 5.f, FB_FIN = 3.f;
constexpr int FB_I0 = 2;
constexpr int QPOLL = 600;
constexpr int WOPOS = 125;
constexpr int GPOS = 126, D1 = 30, D1G = 138;
constexpr int D2 = 40;
constexpr unsigned SPIN_CAP = 1u << 20;
constexpr int LAG = (WIN + 7) / 8;

constexpr int L_Q = 16 * SLOT, L_ACC = L_Q + 1024, L_PL = L_ACC + 4 * 2048, L_OT = L_PL + 2048, OT_ROW = 272, L_ML = L_OT + 8 * OT_ROW, L_CTL = (L_ML + 144 + 63) / 64 * 64;
constexpr int L_X = L_ACC, L_W = L_CTL + 64, L_TOT = L_W + 16384;
constexpr int C_FREE = L_CTL, C_LAND = L_CTL + 32, C_READY = L_CTL + 40, C_TAG = L_CTL + 44, C_NM = L_CTL + 48, C_NL = L_CTL + 52, C_NA = L_CTL + 56, C_WO = L_CTL + 60;
static_assert(L_CTL % 32 == 0 && L_TOT <= 160 * 1024, "lds map");
constexpr int W_E = 0, W_FB = 1024, FB_SLOT = 512, W_ML = W_FB + 64 * FB_SLOT + 1024, W_QG = W_ML + NWG * 128, W_PA = W_QG + NWG * 2048, PA_REC = 2048, W_YB = W_PA + NWG * PA_REC,
              W_END = W_YB + NWG * 2048;


#define STR_(x) #x
#define WAITVM(n) asm volatile("s_waitcnt vmcnt(" STR_(n) ")" ::: "memory")
#define BARRIER() asm volatile("s_waitcnt lgkmcnt(0)\n\ts_barrier" ::: "memory")
#define RELOAD() asm volatile("" ::: "memory")
constexpr int AUX_SC1 = 16;

__device__ __forceinline__ unsigned now() { return (unsigned)__builtin_amdgcn_s_memrealtime(); }
__device__ __forceinline__ unsigned cvtpk(float lo, float hi) {
  typedef __bf16 bf16x2_t __attribute__((ext_vector_type(2)));
  typedef float f32x2_t __attribute__((ext_vector_type(2)));
  return __builtin_bit_cast(unsigned, __builtin_convertvector(f32x2_t{lo, hi}, bf16x2_t));
}
template <int CTRL> __device__ __forceinline__ unsigned dppu(unsigned x) { return (unsigned)__builtin_amdgcn_mov_dpp((int)x, CTRL, 0xf, 0xf, true); }
template <int CTRL> __device__ __forceinline__ float dpp(float x) { return __uint_as_float(dppu<CTRL>(__float_as_uint(x))); }
__device__ __forceinline__ float xrow_max(float x) {
  auto s = __builtin_amdgcn_permlane16_swap(__float_as_uint(x), __float_as_uint(x), false, false);
  x = fmaxf(__uint_as_float(s[0]), __uint_as_float(s[1]));
  auto t = __builtin_amdgcn_permlane32_swap(__float_as_uint(x), __float_as_uint(x), false, false);
  return fmaxf(__uint_as_float(t[0]), __uint_as_float(t[1]));
}
__device__ __forceinline__ float xrow_sum(float x) {
  auto s = __builtin_amdgcn_permlane16_swap(__float_as_uint(x), __float_as_uint(x), false, false);
  x = __uint_as_float(s[0]) + __uint_as_float(s[1]);
  auto t = __builtin_amdgcn_permlane32_swap(__float_as_uint(x), __float_as_uint(x), false, false);
  return __uint_as_float(t[0]) + __uint_as_float(t[1]);
}
__device__ __forceinline__ v4i mkrsrc(const void* p, unsigned bytes) {
  const unsigned long long a = (unsigned long long)p;
  v4i r; r[0] = (int)(unsigned)a; r[1] = (int)((unsigned)(a >> 32) & 0xffffu); r[2] = (int)bytes; r[3] = 0x00020000; return r;
}
__device__ __forceinline__ int lds_word(LAS unsigned char* p) { RELOAD(); const int v = __builtin_amdgcn_readfirstlane(*(LAS int*)p); RELOAD(); return v; }
__device__ __forceinline__ void lds_inc(LAS unsigned char* p) {
  unsigned long long ex;
  asm volatile("s_mov_b64 %0, exec\n\ts_mov_b64 exec, 1\n\tds_add_u32 %1, %2\n\ts_mov_b64 exec, %0" : "=&s"(ex) : "v"((unsigned)(size_t)p), "v"(1u) : "memory");
}
#define DMA4(V, R, S) "buffer_load_dwordx4 " V ", " R ", " S " offen nt lds\n\tbuffer_load_dwordx4 " V ", " R ", " S " offen offset:1024 nt lds\n\t" \
                      "buffer_load_dwordx4 " V ", " R ", " S " offen offset:2048 nt lds\n\tbuffer_load_dwordx4 " V ", " R ", " S " offen offset:3072 nt lds\n\t"
__device__ __forceinline__ void issue_two(v4i r, unsigned vv, unsigned so, unsigned lds) {
  unsigned t;
  asm volatile("s_mov_b32 m0, %4\n\ts_add_u32 %0, %3, 0x1000\n\t" DMA4("%2", "%1", "%3") "s_add_u32 m0, m0, 0x1000\n\ts_nop 0\n\t" DMA4("%2", "%1", "%0")
               "s_add_u32 m0, m0, 0x1000\n\ts_add_u32 %0, %0, 0x1000\n\t" DMA4("%2", "%1", "%0") "s_add_u32 m0, m0, 0x1000\n\ts_add_u32 %0, %0, 0x1000\n\t" DMA4("%2", "%1", "%0")
               : "=&s"(t) : "s"(r), "v"(vv), "s"(so), "s"(lds) : "memory", "scc");
}
__device__ __forceinline__ void dma1_nt(v4i r, unsigned voff, unsigned soff, unsigned lds) {
  asm volatile("s_mov_b32 m0, %3\n\ts_nop 0\n\tbuffer_load_dwordx4 %1, %0, %2 offen nt lds" :: "s"(r), "v"(voff), "s"(soff), "s"(lds) : "memory");
}
__device__ __forceinline__ void dma1(v4i r, unsigned voff, unsigned soff, unsigned lds) {
  asm volatile("s_mov_b32 m0, %3\n\ts_nop 0\n\tbuffer_load_dwordx4 %1, %0, %2 offen lds" :: "s"(r), "v"(voff), "s"(soff), "s"(lds) : "memory");
}
template <int W> __device__ __forceinline__ void stream_units(v4i rs, unsigned vv, unsigned u, unsigned ue, unsigned so, unsigned ra, unsigned uc16, int hold, int msp, int lat, int ms2) {
  unsigned ta, tb, st = 8192u, fo = ((u + 1u) & 3u) * 8u + C_FREE;
  asm volatile(
      "s_mov_b32 m0, %[m0i]\n\t"
      "v_mov_b32 v2, %[ld0]\n"
      "LT%=:\n\t"
      "s_waitcnt vmcnt(%[win])\n\t"
      DMA4("%[v0]", "%[rs]", "%[so]")
      "s_add_u32 m0, m0, 0x1000\n\t"
      "s_memrealtime s[40:41]\n\t"
      DMA4("%[v1]", "%[rs]", "%[so]")
      "ds_read_b64 v[4:5], %[fo]\n\t"
      "s_add_u32 m0, m0, 0x1000\n\t"
      "s_add_u32 %[so], %[so], %[st]\n\t"
      "s_and_b32 m0, m0, 0x1ffff\n\t"
      "s_and_b32 %[so], %[so], 0x1fffff\n\t"
      "s_xor_b32 %[st], %[st], 0xc000\n\t"
      "s_add_u32 %[ra], %[ra], %[uc]\n\t"
      "v_add_u32 v2, 1, v2\n\t"
      "v_add_u32 %[fo], 8, %[fo]\n\t"
      "s_add_u32 %[u], %[u], 1\n\t"
      "v_and_b32 %[fo], %[fm], %[fo]\n\t"
      "s_waitcnt lgkmcnt(0)\n\t"
      "v_mov_b32 v3, s40\n\t"
      "v_readfirstlane_b32 %[a], v5\n\t"
      "s_lshr_b32 s41, %[ra], 4\n\t"
      "v_readfirstlane_b32 %[b], v4\n\t"
      "ds_write_b64 %[vl], v[2:3]\n\t"
      "s_cmp_eq_u32 %[u], %[ue]\n\t"
      "s_cbranch_scc1 LE%=\n\t"
      "s_add_u32 %[a], %[a], s41\n\t"
      "s_sub_i32 %[a], %[a], s40\n\t"
      "s_cmp_lt_i32 %[a], %[lat]\n\t"
      "s_cselect_b32 s41, %[ms2], %[msp]\n\t"
      "s_min_i32 %[a], %[a], %[hold]\n\t"
      "s_max_i32 %[a], %[a], s41\n\t"
      "s_add_u32 %[a], %[a], s40\n\t"
      "s_cmp_lt_u32 %[b], %[u]\n\t"
      "s_cbranch_scc1 LS%=\n"
      "LW%=:\n\t"
      "s_memrealtime s[40:41]\n\t"
      "s_waitcnt lgkmcnt(0)\n\t"
      "s_sub_i32 %[b], %[a], s40\n\t"
      "s_cmp_gt_i32 %[b], 0\n\t"
      "s_cbranch_scc0 LT%=\n\t"
      "s_branch LW%=\n"
      "LS%=:\n\t"
      "s_and_b32 %[b], %[u], 3\n\t"
      "s_lshl_b32 %[b], %[b], 3\n\t"
      "s_add_u32 %[b], %[b], %[fb]\n\t"
      "v_mov_b32 v5, %[b]\n"
      "LR%=:\n\t"
      "s_sleep 1\n\t"
      "ds_read_b32 v4, v5\n\t"
      "s_waitcnt lgkmcnt(0)\n\t"
      "v_readfirstlane_b32 %[b], v4\n\t"
      "s_cmp_lt_u32 %[b], %[u]\n\t"
      "s_cbranch_scc1 LR%=\n\t"
      "s_branch LW%=\n"
      "LE%=:"
      : [so] "+&s"(so), [ra] "+&s"(ra), [u] "+&s"(u), [st] "+&s"(st), [fo] "+&v"(fo), [a] "=&s"(ta), [b] "=&s"(tb)
      : [rs] "s"(rs), [v0] "v"(vv), [v1] "v"(vv + 4096u), [vl] "v"((unsigned)C_LAND), [ue] "s"(ue), [uc] "s"(uc16), [hold] "s"(hold), [msp] "s"(msp), [lat] "s"(lat), [ms2] "s"(ms2),
        [m0i] "s"((u & 15u) * SLOT), [ld0] "s"(u - 1u - LAG), [win] "n"(W), [fm] "s"(~32u), [fb] "s"((unsigned)C_FREE)
      : "memory", "scc", "vcc", "v2", "v3", "v4", "v5", "s40", "s41");
}
static_assert((C_FREE & 32) == 0, "the record pointer wraps by clearing bit 5");

__device__ __forceinline__ f32x4 mfma32(v4u a, v4u b, f32x4 c) {
  return __builtin_amdgcn_mfma_f32_16x16x32_bf16(__builtin_bit_cast(bf16x8, a), __builtin_bit_cast(bf16x8, b), c, 0, 0, 0);
}
__device__ __forceinline__ v4u tr_frag(LAS unsigned char* p, int hi) {
  const v2u lo = __builtin_bit_cast(v2u, __builtin_amdgcn_ds_read_tr16_b64_v4i16((LAS s16x4*)p));
  const v2u up = __builtin_bit_cast(v2u, __builtin_amdgcn_ds_read_tr16_b64_v4i16((LAS s16x4*)(p + hi)));
  return v4u{lo[0], lo[1], up[0], up[1]};
}
__device__ __forceinline__ float score16(const v4u* K, v4u qf, unsigned j0) {
  float sc[4];
#pragma unroll
  for (int u = 0; u < 4; ++u) {
    const f32x4 d = mfma32(K[u], qf, f32x4{0.f, 0.f, 0.f, 0.f});
    sc[u] = ((dpp<0x00>(d[0]) + dpp<0x55>(d[1])) + dpp<0xAA>(d[2])) + dpp<0xFF>(d[3]);
  }
  return (j0 & 2) ? ((j0 & 1) ? sc[3] : sc[2]) : ((j0 & 1) ? sc[1] : sc[0]);
}
__device__ __forceinline__ void p_operands(unsigned k0, unsigned k1, unsigned mask0, unsigned mask1, v4u (&bo)[4]) {
  bo[0] = v4u{dppu<0x00>(k0) & mask0, dppu<0x00>(k0) & mask1, dppu<0x55>(k0) & mask0, dppu<0x55>(k0) & mask1};
  bo[1] = v4u{dppu<0xAA>(k0) & mask0, dppu<0xAA>(k0) & mask1, dppu<0xFF>(k0) & mask0, dppu<0xFF>(k0) & mask1};
  bo[2] = v4u{dppu<0x00>(k1) & mask0, dppu<0x00>(k1) & mask1, dppu<0x55>(k1) & mask0, dppu<0x55>(k1) & mask1};
  bo[3] = v4u{dppu<0xAA>(k1) & mask0, dppu<0xAA>(k1) & mask1, dppu<0xFF>(k1) & mask0, dppu<0xFF>(k1) & mask1};
  asm volatile("" : "+v"(bo[0]), "+v"(bo[1]), "+v"(bo[2]), "+v"(bo[3]));
}
#define RD2(A, P, O0, O1) "ds_read2st64_b64 %[" A "], %[" P "] offset0:" O0 " offset1:" O1 "\n\t"
#define MF(N, C, A, B) "s_waitcnt lgkmcnt(" N ")\n\tv_mfma_f32_16x16x32_bf16 %[" C "], %[" A "], %[" B "], %[" C "]\n\t"
__device__ __forceinline__ void pv32(unsigned p, const v4u (&bo)[4], f32x4& acc0, f32x4& acc1) {
  v4u a0, a1, a2, a3, a4, a5, a6, a7;
  asm volatile(RD2("a0", "p0", "0", "2") RD2("a1", "p1", "0", "2") RD2("a2", "p0", "4", "6") RD2("a3", "p1", "4", "6")
               RD2("a4", "p0", "8", "10") RD2("a5", "p1", "8", "10") RD2("a6", "p0", "12", "14") RD2("a7", "p1", "12", "14")
               MF("7", "c0", "a0", "b0") MF("6", "c1", "a1", "b0") MF("5", "c0", "a2", "b1") MF("4", "c1", "a3", "b1")
               MF("3", "c0", "a4", "b2") MF("2", "c1", "a5", "b2") MF("1", "c0", "a6", "b3") MF("0", "c1", "a7", "b3") "s_nop 7"
      : [c0] "+&v"(acc0), [c1] "+&v"(acc1), [a0] "=&v"(a0), [a1] "=&v"(a1), [a2] "=&v"(a2), [a3] "=&v"(a3), [a4] "=&v"(a4), [a5] "=&v"(a5), [a6] "=&v"(a6), [a7] "=&v"(a7)
      : [p0] "v"(p), [p1] "v"(p + 8), [b0] "v"(bo[0]), [b1] "v"(bo[1]), [b2] "v"(bo[2]), [b3] "v"(bo[3]) : "memory");
}
__device__ __forceinline__ v4u tag_bytes(unsigned tag) { return v4u{tag & 0xffu, (tag >> 8) & 0xffu, (tag >> 16) & 0xffu, tag >> 24}; }
__device__ __forceinline__ v4u tag4(f32x4 v, v4u tb) { return (__builtin_bit_cast(v4u, v) & 0xffffff00u) | tb; }
__device__ __forceinline__ unsigned next_tag(unsigned e) { return ((e & 0x7f7f7f7fu) + 0x51276b35u) ^ (e & 0x80808080u); }
__device__ __forceinline__ unsigned or4(v4u u) { return (u[0] | u[1]) | (u[2] | u[3]); }

__global__ void __launch_bounds__(NTHR, 2) gqa_fused(const unsigned char* __restrict__ x, const unsigned char* __restrict__ kc, const unsigned char* __restrict__ vc,
                                                     const unsigned char* __restrict__ wq, const unsigned char* __restrict__ wo, unsigned char* __restrict__ out,
                                                     unsigned char* ws) {
  __shared__ __attribute__((aligned(16))) unsigned char smem[L_TOT];
  LAS unsigned char* L = (LAS unsigned char*)smem;
  unsigned long long t64;
  unsigned pf, pa;
  {
    unsigned long long pc, ex;
    unsigned clen;
    asm volatile("s_memrealtime %0\n\ts_getpc_b64 %1\nLgqa_pc:\n\t"
                 "s_mov_b32 %2, Lgqa_end_a-Lgqa_pc+160\n\ts_max_u32 %2, %2, Lgqa_end_b-Lgqa_pc+160\n\ts_max_u32 %2, %2, Lgqa_end_c-Lgqa_pc+160\n\t"
                 "s_max_u32 %2, %2, Lgqa_end_d-Lgqa_pc+160\n\ts_max_u32 %2, %2, Lgqa_end_e-Lgqa_pc+160\n\t"
                 "v_lshlrev_b32 %5, 7, %6\n\tv_add_u32 %5, 0xffffc000, %5\n\tv_cmp_gt_u32 vcc, %2, %5\n\t"
                 "s_and_saveexec_b64 %3, vcc\n\ts_cbranch_execz Lgqa_nopf\n\tglobal_load_dword %4, %5, %1 offset:-32\nLgqa_nopf:\n\ts_mov_b64 exec, %3"
                 : "=&s"(t64), "=&s"(pc), "=&s"(clen), "=&s"(ex), "=&v"(pf), "=&v"(pa) : "v"(threadIdx.x) : "scc", "vcc", "memory");
  }
  const int lane = threadIdx.x & 63, wv = __builtin_amdgcn_readfirstlane(threadIdx.x >> 6);
  const int j = blockIdx.x, xg = j & 7, idx = j >> 3;
  const int pr = (idx >> 2) * 8 + ((xg + (idx >> 2)) & 7), s = idx & 3;
  const int ch = 4 * xg + (idx >> 3), nb = idx & 7;
  const int jf = xg * 32 + idx;
  const int g = lane >> 4;
  const unsigned so0 = (unsigned)(pr >> 1) * 65536u + s * 16384u;
  const unsigned vv = lane * 16;
  const unsigned ra0 = ((unsigned)(j * 37) & 31u) * PHASE >> 1;
  asm volatile("s_waitcnt lgkmcnt(0)" : "+s"(t64) : "s"(x), "s"(kc), "s"(vc), "s"(wq), "s"(wo), "s"(out), "s"(ws), "s"(so0), "v"(vv));
  const unsigned t0 = (unsigned)t64;
  const size_t pbase = (size_t)pr * (SK * DH * 2);

  const v4i rk = mkrsrc(kc + pbase, SK * DH * 2);
  if (wv == 0) {
    __builtin_amdgcn_s_setprio(3);
#pragma unroll
    for (int u = 0; u < NFIRST; u += 2) issue_two(rk, vv, (so0 + (u >> 1) * 65536u) & 0x1fffffu, u * SLOT);
    __builtin_amdgcn_sched_barrier(0);
    const unsigned keep = (unsigned)((lane & 7) - 4) >> 31;
    *(LAS v2u*)(L + L_CTL + (lane & 7) * 8) = v2u{(12u + (lane & 7)) * keep, (t0 + LINE0) * keep};
    asm volatile("s_waitcnt lgkmcnt(0)" ::: "memory");
  }
  unsigned e0, e1, e2;
  if (wv == 1) asm volatile("s_load_dword %0, %3, 0x0\n\ts_load_dword %1, %4, 0x0\n\ts_load_dword %2, %5, %6"
                            : "=&s"(e0), "=&s"(e1), "=&s"(e2) : "s"(ws), "s"(x), "s"(wq), "s"((j >> 6) * (128 * 8192) + (j & 63) * 128) : "memory");
  asm volatile("s_barrier" ::: "memory");
  if (wv == 0) stream_units<WIN0>(rk, vv, NFIRST, UW0, (so0 + (NFIRST >> 1) * 65536u) & 0x1fffffu, NFIRST * UC16 + ra0, UC16, HOLD, MINSP, -LATE, MINSP2);
  const __amdgpu_buffer_rsrc_t rw = __builtin_amdgcn_make_buffer_rsrc((void*)ws, 0, W_END, 0x00020000);
  const unsigned j0 = lane & 3;
  const int h = (lane >> 2) & 3;
  const unsigned mask0 = j0 == 0 ? 0x0000ffffu : (j0 == 1 ? 0xffff0000u : 0u);
  const unsigned mask1 = j0 == 2 ? 0x0000ffffu : (j0 == 3 ? 0xffff0000u : 0u);

  if (wv == 0) {
    const v4i rv = mkrsrc(vc + pbase, SK * DH * 2);
#pragma unroll 1
    for (int pass = 0; pass < 2; ++pass) {
      const unsigned ub = pass ? 64u : (unsigned)UW0;
      while ((unsigned)lds_word(L + C_FREE + (ub & 3u) * 8) < ub) __builtin_amdgcn_s_sleep(1);
      const v4i rs = {pass ? rv[0] : rk[0], pass ? rv[1] : rk[1], rk[2], rk[3]};
      stream_units<WIN>(rs, vv, ub, pass ? (unsigned)UPREP : 64u, (so0 + (ub >> 1) * 65536u) & 0x1fffffu, ub * UC16 + ra0, UC16, HOLD, MINSP, -LATE, MINSP2);
    }
    while (lds_word(L + C_FREE + 24) < 64 + 4 * MLROW + 3 + 16) __builtin_amdgcn_s_sleep(1);
    v4u bo[NFIN][4], tb;
    unsigned vo = h * 512 + g * 64 + j0 * 16;
    float scale;
    {
      v2u pl[NFIN];
#pragma unroll
      for (int k = 0; k < NFIN; ++k) pl[k] = *(LAS v2u*)(L + L_PL + (4 - NFIN + k) * 512 + lane * 8);
      tb = tag_bytes(*(LAS unsigned*)(L + C_TAG));
      scale = *(LAS float*)(L + L_ML + 128 + h * 4);
      const int fr = *(LAS int*)(L + C_FREE + (UPREP & 3) * 8);
#pragma unroll
      for (int k = 0; k < NFIN; ++k) p_operands(pl[k][0], pl[k][1], mask0, mask1, bo[k]);
      asm volatile("" : "+v"(vo), "+v"(tb), "+v"(scale));
      if (__builtin_amdgcn_readfirstlane(fr) < UPREP) { while (lds_word(L + C_FREE + (UPREP & 3) * 8) < UPREP) __builtin_amdgcn_s_sleep(1); }
    }
    stream_units<WIN>(rv, vv, UPREP, NUNIT, (so0 + ((UPREP - 64) >> 1) * 65536u) & 0x1fffffu, UPREP * UC16 + ra0, UC16, HOLD, MINSP, -LATE, MINSP2);
    const int so = W_PA + (pr * 4 + s) * PA_REC;
    f32x4 acc0 = {0.f, 0.f, 0.f, 0.f}, acc1 = {0.f, 0.f, 0.f, 0.f};
    static_assert(NFIN >= 2 && LAG <= 3, "the drain below starts at three units outstanding");
    f32x4 s0, s1;
#pragma unroll
    for (int k = 0; k < 3; ++k) {
      if (k == 0) WAITVM(24); else if (k == 1) WAITVM(16); else WAITVM(8);
      if (k < 2) *(LAS int*)(L + C_LAND) = NUNIT - 3 + k;
      if (k >= 4 - NFIN) pv32((12 + k) * SLOT + vv, bo[k >= 4 - NFIN ? k - (4 - NFIN) : 0], acc0, acc1);
      if (k == MPOS) {
        while (lds_word(L + C_NA) < 4) {}
        f32x4 a[8];
#pragma unroll
        for (int u = 0; u < 8; ++u) a[u] = *(LAS f32x4*)(L + L_ACC + vv + u * 1024);
        __builtin_amdgcn_sched_barrier(0);
        s0 = ((a[0] + a[2]) + (a[4] + a[6])) * scale; s1 = ((a[1] + a[3]) + (a[5] + a[7])) * scale;
        asm volatile("" : "+v"(s0), "+v"(s1) : "v"(14 * SLOT + vv), "v"(14 * SLOT + vv + 8), "v"(15 * SLOT + vv), "v"(15 * SLOT + vv + 8), "s"(so), "s"(0xffffff00u));
      }
    }
    WAITVM(0);
    pv32(15 * SLOT + vv, bo[NFIN - 1], acc0, acc1);
    const v4u o0 = tag4(acc0 * scale + s0, tb), o1 = tag4(acc1 * scale + s1, tb);
    __builtin_amdgcn_raw_buffer_store_b128(o0, rw, vo, so, AUX_SC1);
    __builtin_amdgcn_raw_buffer_store_b128(o1, rw, vo + 256, so, AUX_SC1);
    asm volatile("s_nop 1" :: "v"(o0), "v"(o1) : "memory");
    *(LAS int*)(L + C_LAND) = NUNIT;
    asm volatile("Lgqa_end_a:" ::: "memory");
    return;
  }

  if (wv >= 2) {
    const int w = wv - 2;
    const unsigned vk = (h * 16 + g * 4 + j0) * 16;
    asm volatile("s_waitcnt vmcnt(0)" :: "v"(pf), "v"(pa) : "memory");
    f32x32 sc;
    float m;
    {
      v4u P[NREG][8];
#pragma unroll
      for (int r = 0; r < NREG; ++r) {
        while (lds_word(L + C_LAND) <= 4 * r + w) __builtin_amdgcn_s_sleep(2);
#pragma unroll
        for (int u = 0; u < 8; ++u) P[r][u] = *(LAS v4u*)(L + ((4 * r + w) & 15) * SLOT + vk + u * 1024);
        asm volatile("" :: "v"(P[r][0]), "v"(P[r][1]), "v"(P[r][2]), "v"(P[r][3]), "v"(P[r][4]), "v"(P[r][5]), "v"(P[r][6]), "v"(P[r][7]) : "memory");
        *(LAS int*)(L + C_FREE + w * 8) = 4 * r + w + 16;
      }
      while (lds_word(L + C_READY) == 0) __builtin_amdgcn_s_sleep(2);
      const v4u qf = *(LAS v4u*)(L + L_Q + vk);
      m = -INFINITY;
#pragma unroll
      for (int r = 0; r < NREG; ++r) {
        const float s0 = score16(&P[r][0], qf, j0), s1 = score16(&P[r][4], qf, j0);
        sc[2 * r] = s0; sc[2 * r + 1] = s1; m = fmaxf(m, fmaxf(s0, s1));
      }
#pragma unroll 1
      for (int n = NREG; n < 16; ++n) {
        while (lds_word(L + C_LAND) <= 4 * n + w) __builtin_amdgcn_s_sleep(2);
        v4u K[8];
#pragma unroll
        for (int u = 0; u < 8; ++u) K[u] = *(LAS v4u*)(L + ((4 * n + w) & 15) * SLOT + vk + u * 1024);
        asm volatile("" :: "v"(K[0]), "v"(K[1]), "v"(K[2]), "v"(K[3]), "v"(K[4]), "v"(K[5]), "v"(K[6]), "v"(K[7]) : "memory");
        *(LAS int*)(L + C_FREE + w * 8) = 4 * n + w + 16;
        const float s0 = score16(&K[0], qf, j0), s1 = score16(&K[4], qf, j0);
        sc[2 * n] = s0; sc[2 * n + 1] = s1; m = fmaxf(m, fmaxf(s0, s1));
      }
    }
    const v4u tb = tag_bytes((unsigned)lds_word(L + C_TAG));
    m = fmaxf(m, dpp<0xB1>(m)); m = fmaxf(m, dpp<0x4E>(m)); m = xrow_max(m);
    *(LAS float*)(L + L_ML + h * 16 + w * 4) = m;
    lds_inc(L + C_NM);
    while (lds_word(L + C_NM) < 4) __builtin_amdgcn_s_sleep(1);
    {
      const f32x4 mw = *(LAS f32x4*)(L + L_ML + h * 16);
      m = fmaxf(fmaxf(mw[0], mw[1]), fmaxf(mw[2], mw[3]));
      float l = 0.f;
#pragma unroll
      for (int n = 0; n < 32; ++n) { const float p = __builtin_amdgcn_exp2f((sc[n] - m) * C2); l += p; sc[n] = __uint_as_float(cvtpk(p, p)); }
      l += dpp<0xB1>(l); l += dpp<0x4E>(l); l = xrow_sum(l);
      *(LAS float*)(L + L_ML + 64 + h * 16 + w * 4) = l;
      *(LAS v2u*)(L + L_PL + w * 512 + lane * 8) = v2u{__float_as_uint(sc[30]), __float_as_uint(sc[31])};
      lds_inc(L + C_NL);
    }
    f32x4 acc0 = {0.f, 0.f, 0.f, 0.f}, acc1 = {0.f, 0.f, 0.f, 0.f};
#pragma unroll 1
    for (int n = 0; n < (w < 4 - NFIN ? 16 : 15); ++n) {
      if (w == 3 && n == 1) {
        while (lds_word(L + C_NL) < 4) __builtin_amdgcn_s_sleep(1);
        const f32x4 lw = *(LAS f32x4*)(L + L_ML + 64 + h * 16);
        if (lane < 16 && j0 == 0) __builtin_amdgcn_raw_buffer_store_b128(tag4(f32x4{m, (lw[0] + lw[1]) + (lw[2] + lw[3]), __uint_as_float(__float_as_uint(m) << 24), 0.f}, tb), rw, h * 16, W_ML + (pr * 4 + s) * 128, AUX_SC1);
      }
      if (w == 3 && n == MLROW) {
        v4u ml;
        for (unsigned spin = 0; spin < SPIN_CAP; ++spin) {
          RELOAD();
          ml = __builtin_amdgcn_raw_buffer_load_b128(rw, j0 * 128 + h * 16, W_ML + pr * 512, AUX_SC1) ^ tb;
          if (__all((or4(ml) & 0xffu) == 0u)) break;
        }
        const float ms = __uint_as_float(ml[0] | (ml[2] >> 24));
        float M = fmaxf(ms, dpp<0xB1>(ms)); M = fmaxf(M, dpp<0x4E>(M));
        float ls = __uint_as_float(ml[1]) * __builtin_amdgcn_exp2f((ms - M) * C2);
        ls += dpp<0xB1>(ls); ls += dpp<0x4E>(ls);
        if (lane < 16 && j0 == 0) *(LAS float*)(L + L_ML + 128 + h * 4) = __builtin_amdgcn_exp2f((m - M) * C2) * __builtin_amdgcn_rcpf(ls);
      }
      v4u bo[4];
      p_operands(__float_as_uint(sc[2 * n]), __float_as_uint(sc[2 * n + 1]), mask0, mask1, bo);
      while (lds_word(L + C_LAND) <= 64 + 4 * n + w) __builtin_amdgcn_s_sleep(1);
      pv32(((4 * n + w) & 15) * SLOT + vv, bo, acc0, acc1);
      *(LAS int*)(L + C_FREE + w * 8) = 64 + 4 * n + w + 16;
    }
    *(LAS f32x4*)(L + L_ACC + w * 2048 + vv) = acc0; *(LAS f32x4*)(L + L_ACC + w * 2048 + 1024 + vv) = acc1;
    lds_inc(L + C_NA);
    const int bb = 2 * w + (lane >> 5), c = lane & 31;
    unsigned vg = bb * (HKV * 4 * PA_REC) + c * 16, vy = (lane & 7) * (32 * 2048) + g * 16;
    const int sg = W_PA + (ch >> 2) * (4 * PA_REC) + (ch & 3) * 512, sy = W_YB + (nb * 4 + w) * 2048 + ch * 64;
    LAS unsigned char* ap = L + L_W + (8 * g + ((lane & 15) >> 2)) * 128 + (16 * w + 4 * j0) * 2;
    LAS unsigned char* op = L + L_OT + bb * OT_ROW + c * 8;
    LAS unsigned char* bp = L + L_OT + (lane & 7) * OT_ROW + g * 16;
    if (lane & 8) vy = 0x40000000u;
    v4u tv = tb;
    asm volatile("" : "+v"(vg), "+v"(vy), "+v"(ap), "+v"(op), "+v"(bp), "+v"(tv) : "s"(sg), "s"(sy));
    while (lds_word(L + C_LAND) < GPOS) __builtin_amdgcn_s_sleep(1);
    {
      const unsigned tp = now() + D1;
      unsigned tg = (unsigned)lds_word(L + C_FREE + 4) + (NUNIT * UC16 >> 4) + D1G;
      tg = tp + (unsigned)min(max((int)(tg - tp), 0), 200);
      while ((int)(now() - tg) < 0) {}
    }
    v4u a[4], af[4], bf[4];
#pragma unroll
    for (int u = 0; u < 4; ++u) a[u] = __builtin_amdgcn_raw_buffer_load_b128(rw, vg, sg + u * PA_REC, AUX_SC1);
    while (lds_word(L + C_WO) == 0) {}
#pragma unroll
    for (int ks = 0; ks < 4; ++ks) af[ks] = tr_frag(ap + ks * 4096, 512);
    asm volatile("" : "+v"(af[0]), "+v"(af[1]), "+v"(af[2]), "+v"(af[3]));
#pragma unroll
    for (int u = 0; u < 4; ++u) a[u] ^= tv;
    asm volatile("" : "+v"(a[0]), "+v"(a[1]), "+v"(a[2]), "+v"(a[3]));
    if (__builtin_expect(!__all((or4((a[0] | a[1]) | (a[2] | a[3])) & 0xffu) == 0u), 0)) {
      for (unsigned spin = 0; spin < SPIN_CAP; ++spin) {
        RELOAD();
#pragma unroll
        for (int u = 0; u < 4; ++u) a[u] = __builtin_amdgcn_raw_buffer_load_b128(rw, vg, sg + u * PA_REC, AUX_SC1) ^ tv;
        if (__all((or4((a[0] | a[1]) | (a[2] | a[3])) & 0xffu) == 0u)) break;
      }
      asm volatile("Lgqa_end_d:" ::: "memory");
    }
    const f32x4 o = (__builtin_bit_cast(f32x4, a[0]) + __builtin_bit_cast(f32x4, a[1])) + (__builtin_bit_cast(f32x4, a[2]) + __builtin_bit_cast(f32x4, a[3]));
    *(LAS v2u*)op = v2u{cvtpk(o[0], o[1]), cvtpk(o[2], o[3])};
    BARRIER();
#pragma unroll
    for (int ks = 0; ks < 4; ++ks) bf[ks] = *(LAS v4u*)(bp + ks * 64);
    __builtin_amdgcn_sched_barrier(0);
    f32x4 y = {0.f, 0.f, 0.f, 0.f};
#pragma unroll
    for (int ks = 0; ks < 4; ++ks) y = mfma32(af[ks], bf[ks], y);
    __builtin_amdgcn_raw_buffer_store_b128(tag4(y, tv), rw, vy, sy, AUX_SC1);
    asm volatile("Lgqa_end_b:" ::: "memory");
    return;
  }

  __builtin_amdgcn_s_setprio(2);
  unsigned tag;
  {
    constexpr int NKS = 4 / NCH;
    const bool prod = (idx & (NCH - 1)) == NCH - 1;
    const int pid = (idx / NCH) * 8 + xg, cb = pid & 63, kq = pid >> 6;
    v4u tq;
    if (prod) {
      const v4i rx = mkrsrc(x, NB * DM * 2), rq = mkrsrc(wq, DM * HQ * DH * 2);
      const unsigned vx = (lane & 7) * 1024 + (lane >> 3) * 16, vq = (lane >> 3) * 8192 + (lane & 7) * 16;
      const bool hi = (lane & 8) != 0;
      const int vs = (lane & 7) * (HKV * NKS * 2048) + (hi ? 64 : 0) + g * 16, ss = W_QG + (cb >> 3) * (NKS * 2048) + kq * 2048 + (cb & 7) * 256;
      f32x4 qa[4];
#pragma unroll
      for (int t = 0; t < 4; ++t) qa[t] = f32x4{0.f, 0.f, 0.f, 0.f};
      LAS unsigned char* ap = L + L_W + (8 * g + ((lane & 15) >> 2)) * 128 + j0 * 8;
      LAS unsigned char* bp = L + L_X + g * 128 + (lane & 7) * 16;
#pragma unroll 1
      for (int c = kq * NCH; c < kq * NCH + NCH; ++c) {
        dma1(rx, vx, c * 256, L_X);
        dma1(rx, vx, c * 256 + 128, L_X + 1024);
#pragma unroll 1
        for (int u = 0; u < 16; ++u) dma1_nt(rq, vq, (c * 16 + u) * 65536 + cb * 128, L_W + u * 1024);
        WAITVM(0);
#pragma unroll
        for (int ks = 0; ks < 4; ++ks) {
          const v4u xb = *(LAS v4u*)(bp + ks * 512);
#pragma unroll
          for (int t = 0; t < 4; ++t) qa[t] = mfma32(tr_frag(ap + ks * 4096 + t * 32, 512), xb, qa[t]);
        }
        asm volatile("" : "+v"(qa[0]), "+v"(qa[1]), "+v"(qa[2]), "+v"(qa[3]) :: "memory");
      }
      asm volatile("s_waitcnt lgkmcnt(0)" : "+s"(e0) :: "memory");
      tag = next_tag(e0);
      tq = tag_bytes(tag);
      const v4u o0 = tag4(f32x4{hi ? qa[1][0] : qa[0][0], hi ? qa[1][1] : qa[0][1], hi ? qa[1][2] : qa[0][2], hi ? qa[1][3] : qa[0][3]}, tq);
      const v4u o1 = tag4(f32x4{hi ? qa[3][0] : qa[2][0], hi ? qa[3][1] : qa[2][1], hi ? qa[3][2] : qa[2][2], hi ? qa[3][3] : qa[2][3]}, tq);
      __builtin_amdgcn_raw_buffer_store_b128(o0, rw, vs, ss, AUX_SC1);
      __builtin_amdgcn_raw_buffer_store_b128(o1, rw, vs + 128, ss, AUX_SC1);
      asm volatile("s_nop 1" :: "v"(o0), "v"(o1) : "memory");
    }
    asm volatile("s_waitcnt lgkmcnt(0)" : "+s"(e0) : "s"(e1), "s"(e2) : "memory");
    tag = next_tag(e0);
    tq = tag_bytes(tag);
    {
      unsigned f0, f1;
      asm volatile("s_load_dword %0, %2, %3\n\ts_load_dword %1, %4, %5\n\ts_waitcnt lgkmcnt(0)" : "=&s"(f0), "=&s"(f1) : "s"(vc + pbase), "s"(so0), "s"(wo), "s"(ch * 128 * 1024 + nb * 128) : "memory");
    }
    if (lane == 0) *(GAS unsigned*)(out + jf * 32) = 0u;
    while ((int)(now() - t0) < QPOLL) __builtin_amdgcn_s_sleep(2);
    const int so = W_QG + pr * (NKS * 2048);
    v4u a[2 * NKS];
    for (unsigned spin = 0; spin < SPIN_CAP; ++spin) {
      RELOAD();
      v4u bad = {0u, 0u, 0u, 0u};
#pragma unroll
      for (int u = 0; u < 2 * NKS; ++u) { a[u] = __builtin_amdgcn_raw_buffer_load_b128(rw, vv + (u & 3) * 1024, so + (u >> 2) * 4096, AUX_SC1) ^ tq; bad |= a[u]; }
      if (__all((or4(bad) & 0xffu) == 0u)) break;
      __builtin_amdgcn_s_sleep(12);
    }
#pragma unroll
    for (int u = 0; u < 2; ++u) {
      f32x4 v = __builtin_bit_cast(f32x4, a[u]);
#pragma unroll
      for (int p = 1; p < NKS; ++p) v += __builtin_bit_cast(f32x4, a[2 * p + u]);
      *(LAS v2u*)(L + L_Q + u * 512 + lane * 8) = v2u{cvtpk(v[0], v[1]), cvtpk(v[2], v[3])};
    }
    RELOAD();
    *(LAS v2u*)(L + C_READY) = v2u{1u, tag};
  }
  {
    unsigned k = now() / FB_P + 1u;
    float af = 0.f, dr = 0.f;
    int nv = 0;
#pragma unroll 1
    for (int it = 0; it < 40; ++it, ++k) {
      const unsigned tk = k * FB_P;
      while ((int)(now() - tk) < 0) __builtin_amdgcn_s_sleep(2);
      RELOAD();
      const v2u pr2 = *(LAS v2u*)(L + C_LAND);
      const int landed = __builtin_amdgcn_readfirstlane(pr2[0]);
      if (landed >= FBEND) break;
      const v2u fb = __builtin_amdgcn_raw_buffer_load_b64(rw, (lane & (FB_NREP - 1)) * 8, W_FB + ((k - 1u) & 63u) * FB_SLOT, AUX_SC1);
      if (j < FB_NREP && lane == 0) {
        const unsigned a = pr2[1] - (((pr2[0] + LAG) * UC16 + ra0) >> 4) + (unsigned)max((int)(tk - pr2[1]) - (int)(UC16 >> 4), 0);
        __builtin_amdgcn_raw_buffer_store_b64(v2u{a, tag ^ k}, rw, 0, W_FB + (k & 63u) * FB_SLOT + j * 8, AUX_SC1);
      }
      const bool ok = fb[1] == (tag ^ (k - 1u));
      float ps = ok ? (float)(int)(fb[0] - t0) : 0.f, pc = ok ? 1.f : 0.f;
      ps += dpp<0xB1>(ps); ps += dpp<0x4E>(ps); ps += dpp<0x124>(ps); ps += dpp<0x128>(ps); ps = xrow_sum(ps);
      pc += dpp<0xB1>(pc); pc += dpp<0x4E>(pc); pc += dpp<0x124>(pc); pc += dpp<0x128>(pc); pc = xrow_sum(pc);
      if (__builtin_amdgcn_readfirstlane(pc < 48.f)) continue;
      const float as = ps * __builtin_amdgcn_rcpf(pc) - (float)(landed < DSW ? DELTA : DELTA2);
      const float er = as - af;
      if (nv >= FB_I0) dr = fminf(fmaxf(dr + FB_KI * fminf(er - FB_Z, 30.f), 0.f), 16.f);
      af = nv ? af + dr + FB_G * er : as;
      ++nv;
      *(LAS unsigned*)(L + C_FREE + 4 + j0 * 8) = t0 + (unsigned)(int)af;
    }
    if (nv) *(LAS unsigned*)(L + C_FREE + 4 + j0 * 8) = t0 + (unsigned)(int)(af + FB_FIN * fmaxf(dr, 0.f));
  }
  {
    const v4i ro = mkrsrc(wo, HQ * DH * DM * 2);
    const unsigned vo = ((lane >> 5) + 8 * ((lane >> 3) & 3)) * 1024 + (lane & 7) * 16;
    while (lds_word(L + C_LAND) < WOPOS) __builtin_amdgcn_s_sleep(1);
#pragma unroll 1
    for (int u = 0; u < 16; ++u) dma1_nt(ro, vo, (ch * 128 + 32 * ((u >> 1) & 3) + 4 * (u >> 3) + 2 * (u & 1)) * 1024 + nb * 128, L_W + u * 1024);
    const __amdgpu_buffer_rsrc_t rout = __builtin_amdgcn_make_buffer_rsrc((void*)out, 0, NB * DM * 2, 0x00020000);
    v4u tb = tag_bytes(tag);
    unsigned vs = (lane & 28) == 0 ? j0 * 8 + (lane >> 5) * 4 : 0x40000000u;
    asm volatile("s_waitcnt vmcnt(0)" : "+v"(vs), "+v"(tb) :: "memory");
    *(LAS int*)(L + C_WO) = 1;
    BARRIER();
    const unsigned tp = now() + D2;
    while ((int)(now() - tp) < 0) {}
    v4u a0 = __builtin_amdgcn_raw_buffer_load_b128(rw, vv, W_YB + jf * 2048, AUX_SC1) ^ tb;
    v4u a1 = __builtin_amdgcn_raw_buffer_load_b128(rw, vv + 1024, W_YB + jf * 2048, AUX_SC1) ^ tb;
    asm volatile("" : "+v"(a0), "+v"(a1));
    if (__builtin_expect(!__all((or4(a0 | a1) & 0xffu) == 0u), 0)) {
      for (unsigned spin = 0; spin < SPIN_CAP; ++spin) {
        RELOAD();
        a0 = __builtin_amdgcn_raw_buffer_load_b128(rw, vv, W_YB + jf * 2048, AUX_SC1) ^ tb;
        a1 = __builtin_amdgcn_raw_buffer_load_b128(rw, vv + 1024, W_YB + jf * 2048, AUX_SC1) ^ tb;
        if (__all((or4(a0 | a1) & 0xffu) == 0u)) break;
      }
      asm volatile("Lgqa_end_e:" ::: "memory");
    }
    const f32x4 y = __builtin_bit_cast(f32x4, a0) + __builtin_bit_cast(f32x4, a1);
    const auto u02 = __builtin_amdgcn_permlane32_swap(__float_as_uint(y[0]), __float_as_uint(y[2]), false, false);
    const auto u13 = __builtin_amdgcn_permlane32_swap(__float_as_uint(y[1]), __float_as_uint(y[3]), false, false);
    float e = __uint_as_float(u02[0]) + __uint_as_float(u02[1]), f = __uint_as_float(u13[0]) + __uint_as_float(u13[1]);
    e += dpp<0x124>(e); f += dpp<0x124>(f); e += dpp<0x128>(e); f += dpp<0x128>(f);
    const auto ue = __builtin_amdgcn_permlane16_swap(__float_as_uint(e), __float_as_uint(e), false, false);
    const auto uf = __builtin_amdgcn_permlane16_swap(__float_as_uint(f), __float_as_uint(f), false, false);
    __builtin_amdgcn_raw_buffer_store_b32(cvtpk(__uint_as_float(ue[0]) + __uint_as_float(ue[1]), __uint_as_float(uf[0]) + __uint_as_float(uf[1])), rout, vs, jf * 32, 0);
  }
  if (j == 0 && lane == 0) __builtin_amdgcn_raw_buffer_store_b32(tag, rw, 0, W_E, AUX_SC1);
  asm volatile("Lgqa_end_c:" ::: "memory");
}

extern "C" void kernel_launch(void* const* d_in, const int* in_sizes, int n_in, void* d_out, int out_size, void* d_ws, size_t ws_size,
                              hipStream_t stream) {
  gqa_fused<<<NWG, NTHR, 0, stream>>>((const unsigned char*)d_in[0], (const unsigned char*)d_in[1], (const unsigned char*)d_in[2], (const unsigned char*)d_in[3],
                                      (const unsigned char*)d_in[4], (unsigned char*)d_out, (unsigned char*)d_ws);
}
```
